```python
import torch

def _softmax(x, axis=-1):
    return torch.softmax(x, dim=axis)
def _gelu(x):
    return torch.nn.functional.gelu(x, approximate="tanh")
def _silu(x):
    return torch.nn.functional.silu(x)
def _sigmoid(x):
    return torch.sigmoid(x)
def _layernorm(x, axis=-1, eps=1e-5):
    m = x.mean(dim=axis, keepdim=True)
    v = x.var(dim=axis, keepdim=True, unbiased=False)
    return (x - m) / torch.sqrt(v + eps)
def _scatter_add(tgt, idx, src, axis=0):
    return tgt.clone().index_add_(axis, idx, src)
def _segment_sum(src, seg, n):
    out = torch.zeros((n,) + src.shape[1:], dtype=src.dtype)
    return out.index_add_(0, seg, src)
def _pad(x, pw):
    flat = tuple(v for pair in reversed(pw) for v in pair)
    return torch.nn.functional.pad(x, flat)

kernel_name = 'sequential_preLN__compressed_kv_cla__mixer_token_mlp__prompt'

def setup_inputs():

    _w = (lambda a: (lambda t: t.to(torch.bfloat16) if t.is_floating_point() else t)(torch.as_tensor(a)))
    def _A_setup():
        B, H, Sq, Sk, D, M = 1, 32, 2048, 2048, 128, 512
        Dc = 4 * D
        return {
            "x": _w((lambda *s: torch.randn(*s, dtype=torch.bfloat16) * 0.05)(B, Sq, M)),
            "c_kv": _w((lambda *s: torch.randn(*s, dtype=torch.bfloat16) * 0.2)(B, Sk, Dc)),
            "w_q": _w((lambda *s: torch.randn(*s, dtype=torch.bfloat16) * 3.2)(M, H, D) * (M ** -0.5)),
            "w_uk": _w((lambda *s: torch.randn(*s, dtype=torch.bfloat16) * 6.4)(Dc, H, D) * (Dc ** -0.5)),
            "w_uv": _w((lambda *s: torch.randn(*s, dtype=torch.bfloat16) * 0.2)(Dc, H, D) * (Dc ** -0.5)),
            "w_o": _w((lambda *s: torch.randn(*s, dtype=torch.bfloat16) * 0.2)(H, D, M) * ((H * D) ** -0.5)),
        }
    _A_in = _A_setup()

    def _B_setup():
        B, S, D, F = 1, 2048, 512, 1792
        return {
            "x": _w((lambda *s: torch.randn(*s, dtype=torch.bfloat16) * 0.05)(B, S, D)),
            "w1": _w((lambda *s: torch.randn(*s, dtype=torch.bfloat16) * 0.4)(S, F) * (S ** -0.5)),
            "b1": _w((lambda *s: torch.randn(*s, dtype=torch.bfloat16) * 3.2)(F) * 0.02),
            "w2": _w((lambda *s: torch.randn(*s, dtype=torch.bfloat16) * 0.2)(F, S) * (F ** -0.5)),
            "b2": _w((lambda *s: torch.randn(*s, dtype=torch.bfloat16) * 0.4)(S) * 0.02),
        }
    _B_in = _B_setup()

    return {
        "A_x": _A_in["x"],
        "A_c_kv": _A_in["c_kv"],
        "A_w_q": _A_in["w_q"],
        "A_w_uk": _A_in["w_uk"],
        "A_w_uv": _A_in["w_uv"],
        "A_w_o": _A_in["w_o"],
        "B_x": _B_in["x"],
        "B_w1": _B_in["w1"],
        "B_b1": _B_in["b1"],
        "B_w2": _B_in["w2"],
        "B_b2": _B_in["b2"],
    }

def reference(A_x, A_c_kv, A_w_q, A_w_uk, A_w_uv, A_w_o, B_x, B_w1, B_b1, B_w2, B_b2):
    def _A_ref(x, c_kv, w_q, w_uk, w_uv, w_o):
        B, Sq, M = x.shape[0], x.shape[1], x.shape[2]
        Sk = c_kv.shape[1]
        H, D = w_q.shape[1], w_q.shape[2]
        q = torch.einsum("bqm,mhd->bhqd", x, w_q)
        k = torch.einsum("bkc,chd->bhkd", c_kv, w_uk)
        v = torch.einsum("bkc,chd->bhkd", c_kv, w_uv)
        scores = torch.einsum("bhqd,bhkd->bhqk", q, k) * (D ** -0.5)
        off = Sk - Sq
        qi = torch.arange(Sq)[:, None]
        kj = torch.arange(Sk)[None, :]
        cmask = kj <= (qi + off)
        scores = torch.where(cmask[None, None], scores, -1e9)
        p = _softmax(scores, axis=-1)
        ctx = torch.einsum("bhqk,bhkd->bhqd", p, v)
        return torch.einsum("bhqd,hdm->bqm", ctx, w_o)

    def _B_ref(x, w1, b1, w2, b2):
        B, S, D = x.shape
        h = _layernorm(x, axis=-1)
        u = torch.einsum("bsd,sf->bdf", h, w1) + b1
        u = _gelu(u)
        y = torch.einsum("bdf,fs->bds", u, w2) + b2
        return (lambda x, *d: x.permute(*d))(y, 0, 2, 1)

    x = A_x
    h = x + _A_ref(_layernorm(x, axis=-1), A_c_kv, A_w_q, A_w_uk, A_w_uv, A_w_o)
    h2 = h + _B_ref(_layernorm(h, axis=-1), B_w1, B_b1, B_w2, B_b2)
    return h2
```

```cpp
#include <hip/hip_runtime.h>
#include <hip/hip_bf16.h>
#include <stdint.h>
typedef unsigned short u16;
typedef short bf16x8 __attribute__((ext_vector_type(8)));
typedef float f32x4 __attribute__((ext_vector_type(4)));
typedef float f32x16 __attribute__((ext_vector_type(16)));
__device__ __forceinline__ float bf2f(u16 v){ return __uint_as_float(((unsigned)v)<<16); }
__device__ __forceinline__ u16 f2bf(float f){ __bf16 b=(__bf16)f; return __builtin_bit_cast(u16,b); }

__global__ void ln_kernel(const u16* __restrict__ x, u16* __restrict__ y, int times){
  int row = blockIdx.x*4 + (threadIdx.x>>6), lane = threadIdx.x&63;
  float v[8];
  bf16x8 r = *(const bf16x8*)(x + (size_t)row*512 + lane*8);
  for(int i=0;i<8;i++) v[i]=bf2f((u16)r[i]);
  for(int t=0;t<times;t++){
    float s=0; for(int i=0;i<8;i++) s+=v[i];
    for(int o=32;o;o>>=1) s+=__shfl_xor(s,o);
    float m=s*(1.f/512);
    float q=0; for(int i=0;i<8;i++){ v[i]-=m; q+=v[i]*v[i]; }
    for(int o=32;o;o>>=1) q+=__shfl_xor(q,o);
    float rs=1.f/sqrtf(q*(1.f/512)+1e-5f);
    for(int i=0;i<8;i++) v[i]*=rs;
  }
  bf16x8 o; for(int i=0;i<8;i++) o[i]=(short)f2bf(v[i]);
  *(bf16x8*)(y+(size_t)row*512+lane*8)=o;
}
template<int MODE>
__global__ __launch_bounds__(256) void gemm(const u16* __restrict__ A, long sam, long sak, const u16* __restrict__ B, long sbk, long sbn,
   u16* __restrict__ C, int ldc, int K, const u16* __restrict__ res, const u16* __restrict__ bias){
  __shared__ u16 As[64][40], Bs[64][40];
  int m0=blockIdx.y*64, n0=blockIdx.x*64, tid=threadIdx.x, w=tid>>6, lane=tid&63;
  int wr=w>>1, wc=w&1;
  f32x16 acc={};
  for(int k0=0;k0<K;k0+=32){
    for(int i=tid;i<64*32;i+=256){
      int r,c;
      if(sak==1){ r=i>>5; c=i&31; } else { r=i&63; c=i>>6; }
      As[r][c]=A[(m0+r)*sam+(k0+c)*sak];
      if(sbk==1){ r=i>>5; c=i&31; } else { r=i&63; c=i>>6; }
      Bs[r][c]=B[(k0+c)*sbk+(n0+r)*sbn];
    }
    __syncthreads();
    for(int kk=0;kk<32;kk+=16){
      bf16x8 a=*(const bf16x8*)&As[wr*32+(lane&31)][kk+8*(lane>>5)];
      bf16x8 b=*(const bf16x8*)&Bs[wc*32+(lane&31)][kk+8*(lane>>5)];
      acc=__builtin_amdgcn_mfma_f32_32x32x16_bf16(a,b,acc,0,0,0);
    }
    __syncthreads();
  }
  for(int r=0;r<16;r++){
    int m=m0+wr*32+(r&3)+8*(r>>2)+4*(lane>>5), n=n0+wc*32+(lane&31);
    float v=acc[r];
    if(MODE==1) v+=bf2f(res[(size_t)m*ldc+n]);
    if(MODE==2){ v+=bf2f(bias[n]); float t=0.7978845608028654f*(v+0.044715f*v*v*v); v=0.5f*v*(1.f+tanhf(t)); }
    if(MODE==3) v+=bf2f(res[(size_t)m*ldc+n])+bf2f(bias[m]);
    C[(size_t)m*ldc+n]=f2bf(v);
  }
}
__global__ __launch_bounds__(64) void attn(const u16* __restrict__ q,const u16* __restrict__ k,const u16* __restrict__ v,u16* __restrict__ o){
  int qb=63-(blockIdx.x>>5), h=blockIdx.x&31, lane=threadIdx.x, r32=lane&31, hi=lane>>5;
  bf16x8 qr[8];
  for(int d=0;d<8;d++) qr[d]=*(const bf16x8*)(q+(size_t)(qb*32+r32)*4096+h*128+d*16+hi*8);
  f32x16 O[4]={}; float m=-1e30f,l=0;
  const float C=0.08838834764831845f*1.4426950408889634f;
  for(int t=0;t<=qb;t++){
    f32x16 s={};
    for(int d=0;d<8;d++){ bf16x8 kf=*(const bf16x8*)(k+(size_t)(t*32+r32)*4096+h*128+d*16+hi*8); s=__builtin_amdgcn_mfma_f32_32x32x16_bf16(kf,qr[d],s,0,0,0);}
    if(t==qb) for(int r=0;r<16;r++){ int key=(r&3)+8*(r>>2)+4*hi; if(key>r32) s[r]=-INFINITY; }
    float mx=s[0]; for(int r=1;r<16;r++) mx=fmaxf(mx,s[r]); mx=fmaxf(mx,__shfl_xor(mx,32));
    float mn=fmaxf(m,mx), al=exp2f((m-mn)*C); m=mn;
    float ps=0; for(int r=0;r<16;r++){ s[r]=exp2f((s[r]-mn)*C); ps+=s[r]; }
    ps+=__shfl_xor(ps,32); l=l*al+ps;
    for(int d=0;d<4;d++) for(int r=0;r<16;r++) O[d][r]*=al;
    for(int ks=0;ks<2;ks++){
      bf16x8 p; for(int j=0;j<8;j++) p[j]=(short)f2bf(s[8*ks+j]);
      for(int d=0;d<4;d++){
        bf16x8 vf; for(int j=0;j<8;j++){ int key=16*ks+8*(j>>2)+4*hi+(j&3); vf[j]=(short)v[(size_t)(t*32+key)*4096+h*128+d*32+r32]; }
        O[d]=__builtin_amdgcn_mfma_f32_32x32x16_bf16(vf,p,O[d],0,0,0);
      }
    }
  }
  float il=1.f/l;
  for(int d=0;d<4;d++) for(int r=0;r<16;r++){ int dd=d*32+(r&3)+8*(r>>2)+4*hi; o[(size_t)(qb*32+r32)*4096+h*128+dd]=f2bf(O[d][r]*il); }
}
extern "C" void kernel_launch(void* const* d_in, const int* in_sizes, int n_in, void* d_out, int out_size, void* d_ws, size_t ws_size, hipStream_t stream){
  const u16 *x=(const u16*)d_in[0],*ckv=(const u16*)d_in[1],*wq=(const u16*)d_in[2],*wuk=(const u16*)d_in[3],*wuv=(const u16*)d_in[4],*wo=(const u16*)d_in[5];
  const u16 *w1=(const u16*)d_in[7],*b1=(const u16*)d_in[8],*w2=(const u16*)d_in[9],*b2=(const u16*)d_in[10];
  u16* ws=(u16*)d_ws; size_t M1=1<<20;
  u16 *xn=ws, *q=ws+M1, *k=q+8*M1, *v=k+8*M1, *ctx=v+8*M1, *h=ctx+8*M1, *hn=h+M1, *u=hn+M1;
  ln_kernel<<<512,256,0,stream>>>(x,xn,1);
  gemm<0><<<dim3(64,32),256,0,stream>>>(xn,512,1,wq,4096,1,q,4096,512,nullptr,nullptr);
  gemm<0><<<dim3(64,32),256,0,stream>>>(ckv,512,1,wuk,4096,1,k,4096,512,nullptr,nullptr);
  gemm<0><<<dim3(64,32),256,0,stream>>>(ckv,512,1,wuv,4096,1,v,4096,512,nullptr,nullptr);
  attn<<<2048,64,0,stream>>>(q,k,v,ctx);
  gemm<1><<<dim3(8,32),256,0,stream>>>(ctx,4096,1,wo,512,1,h,512,4096,x,nullptr);
  ln_kernel<<<512,256,0,stream>>>(h,hn,2);
  gemm<2><<<dim3(28,8),256,0,stream>>>(hn,1,512,w1,1792,1,u,1792,2048,nullptr,b1);
  gemm<3><<<dim3(8,32),256,0,stream>>>(w2,1,2048,u,1,1792,(u16*)d_out,512,1792,h,b2);
}
```

```cpp
#include <hip/hip_runtime.h>

struct Buf { const void* base; unsigned bytes; unsigned fmt; };
struct Args { Buf b[12]; };
__global__ __launch_bounds__(256, 1) void fused_block(Args args) {
  __shared__ __attribute__((aligned(16))) char lds[163840];
  const unsigned wid = __builtin_amdgcn_readfirstlane(threadIdx.x >> 6);
  const unsigned bid = blockIdx.x;
  auto ka = __builtin_amdgcn_kernarg_segment_ptr();
  asm volatile(
"s_load_dwordx16 s[12:27], %[ka], 0\n"
"s_load_dwordx16 s[28:43], %[ka], 64\n"
"s_load_dwordx16 s[44:59], %[ka], 128\n"
"s_mov_b32 s60, %[bid]\n"
"s_brev_b32 s61, %[wid]\n"
"s_lshr_b32 s61, s61, 30\n"
"v_mbcnt_lo_u32_b32 v0, -1, 0\n"
"v_mbcnt_hi_u32_b32 v0, -1, v0\n"
"v_lshlrev_b32 v1, 4, v0\n"
"v_and_b32 v2, 15, v0\n"
"v_lshrrev_b32 v3, 4, v0\n"
"s_lshr_b32 s66, s61, 1\n"
"s_and_b32 s67, s61, 1\n"
"s_mov_b32 s69, 400000\n"
"s_and_b32 s76, s60, 7\n"
"s_lshl_b32 s76, s76, 2\n"
"s_lshr_b32 s77, s60, 6\n"
"s_add_u32 s62, s76, s77\n"
"s_bfe_u32 s63, s60, 0x30003\n"
"s_sub_u32 s76, 15, s63\n"
"s_bitcmp1_b32 s62, 0\n"
"s_cselect_b32 s70, s76, s63\n"
"s_cselect_b32 s71, s63, s76\n"
"s_getpc_b64 s[8:9]\n"
"PC_1_%=:\n"
"s_add_u32 s8, s8, KV0_%=-PC_1_%=\n"
"s_addc_u32 s9, s9, 0\n"
"s_mov_b32 s10, KEND_%=-KV0_%=\n"
"s_mov_b32 s11, 0x20000\n"
"s_lshr_b32 s98, s60, 3\n"
"s_lshl_b32 s98, s98, 2\n"
"s_add_u32 s98, s98, s61\n"
"s_lshl_b32 s98, s98, 7\n"
"v_and_b32 v5, 7, v0\n"
"v_lshlrev_b32 v5, 14, v5\n"
"buffer_load_dword v6, v5, s[8:11], s98 offen\n"
"v_and_b32 v61, 7, v0\n"
"v_lshrrev_b32 v62, 3, v0\n"
"v_lshlrev_b32 v63, 10, v61\n"
"v_lshl_add_u32 v63, v62, 4, v63\n"
"s_lshl_b32 s94, s60, 13\n"
"s_lshl_b32 s93, s61, 8\n"
"s_add_u32 s94, s94, s93\n"
"s_mov_b32 s95, 917504\n"
"v_lshrrev_b32 v11, 1, v62\n"
"v_xor_b32 v11, v11, v61\n"
"v_lshlrev_b32 v11, 4, v11\n"
"v_lshl_add_u32 v11, v62, 10, v11\n"
"v_xor_b32 v12, 64, v11\n"
"v_add_u32 v14, 0, v11\n"
"v_add_u32 v15, 7168, v12\n"
"v_add_u32 v16, 14336, v11\n"
"v_add_u32 v17, 21504, v12\n"
"v_add_u32 v18, 32768, v11\n"
"v_add_u32 v19, 39936, v12\n"
"v_add_u32 v20, 47104, v11\n"
"v_add_u32 v21, 54272, v12\n"
"v_lshlrev_b32 v11, 1, v3\n"
"v_xor_b32 v11, v11, v2\n"
"v_lshlrev_b32 v11, 4, v11\n"
"v_lshl_add_u32 v11, v3, 13, v11\n"
"v_xor_b32 v12, 128, v11\n"
"v_add_u32 v22, 0, v11\n"
"v_add_u32 v23, 31744, v11\n"
"v_add_u32 v30, 63488, v12\n"
"v_add_u32 v31, 95232, v12\n"
"s_sub_u32 s91, 15, s63\n"
"s_lshl_b32 s72, s61, 19\n"
"s_lshl_b32 s92, s63, 16\n"
"s_add_u32 s72, s72, s92\n"
"s_lshl_b32 s73, s61, 17\n"
"s_lshl_b32 s87, s62, 8\n"
"s_add_u32 s73, s73, s87\n"
"s_lshl_b32 s78, s61, 13\n"
"s_add_u32 s79, s78, 4096\n"
"s_lshl_b32 s80, s61, 12\n"
"s_add_u32 s80, s80, 65536\n"
"s_add_u32 s81, s80, 16384\n"
"s_mov_b32 s74, 0\n"
"v_lshlrev_b32 v55, 7, v2\n"
"s_lshl_b32 s96, s60, 2\n"
"s_add_u32 s96, s96, s61\n"
"s_lshl_b32 s96, s96, 11\n"
"s_waitcnt lgkmcnt(0)\n"
"s_add_u32 m0, s74, s78\n"
"s_nop 0\n"
"buffer_load_dwordx4 v14, s[16:19], s72 offen offset:0 lds\n"
"buffer_load_dwordx4 v15, s[16:19], s72 offen offset:1024 lds\n"
"buffer_load_dwordx4 v16, s[16:19], s72 offen offset:2048 lds\n"
"buffer_load_dwordx4 v17, s[16:19], s72 offen offset:3072 lds\n"
"s_add_u32 m0, s74, s79\n"
"s_nop 0\n"
"buffer_load_dwordx4 v18, s[16:19], s72 offen offset:0 lds\n"
"buffer_load_dwordx4 v19, s[16:19], s72 offen offset:1024 lds\n"
"buffer_load_dwordx4 v20, s[16:19], s72 offen offset:2048 lds\n"
"buffer_load_dwordx4 v21, s[16:19], s72 offen offset:3072 lds\n"
"s_add_u32 m0, s74, s80\n"
"s_nop 0\n"
"buffer_load_dwordx4 v22, s[24:27], s73 offen offset:0 lds\n"
"buffer_load_dwordx4 v23, s[24:27], s73 offen offset:1024 lds\n"
"buffer_load_dwordx4 v30, s[24:27], s73 offen offset:2048 lds\n"
"buffer_load_dwordx4 v31, s[24:27], s73 offen offset:3072 lds\n"
"s_add_u32 m0, s74, s81\n"
"s_nop 0\n"
"buffer_load_dwordx4 v22, s[28:31], s73 offen offset:0 lds\n"
"buffer_load_dwordx4 v23, s[28:31], s73 offen offset:1024 lds\n"
"buffer_load_dwordx4 v30, s[28:31], s73 offen offset:2048 lds\n"
"buffer_load_dwordx4 v31, s[28:31], s73 offen offset:3072 lds\n"
"s_add_u32 s72, s72, 128\n"
"s_add_u32 s73, s73, 524288\n"
"s_mov_b32 s74, 32768\n"
"v_lshlrev_b32 v58, 8, v62\n"
"v_lshl_add_u32 v58, v61, 4, v58\n"
"s_lshl_b32 s92, s61, 3\n"
"s_add_u32 s92, s92, 131072\n"
"v_lshl_add_u32 v59, v61, 5, s92\n"
"v_lshlrev_b32 v60, 5, v61\n"
"v_add_u32 v60, 0x20000, v60\n"
"s_and_b32 s92, s62, 3\n"
"s_lshl_b32 s92, s92, 19\n"
"s_lshl_b32 s93, s63, 16\n"
"s_add_u32 s92, s92, s93\n"
"s_add_u32 s93, s93, s87\n"
"s_lshl_b32 s89, s67, 15\n"
"s_add_u32 s92, s92, s89\n"
"v_bfe_u32 v11, v0, 1, 3\n"
"v_lshlrev_b32 v11, 13, v11\n"
"v_and_b32 v12, 1, v0\n"
"v_lshl_add_u32 v11, v12, 7, v11\n"
"v_and_b32 v12, 31, v0\n"
"v_lshlrev_b32 v12, 10, v12\n"
"s_movk_i32 s90, 128\n"
"s_cmp_eq_u32 s66, 0\n"
"s_cselect_b32 s89, s92, s93\n"
"s_cselect_b32 s90, s90, 524288\n"
"s_cselect_b64 vcc, -1, 0\n"
"v_cndmask_b32 v27, v11, v12, vcc\n"
"v_lshrrev_b32 v12, 5, v0\n"
"v_mad_u32_u24 v57, v12, s90, v27\n"
"v_and_b32 v26, 0x6080, v11\n"
"v_lshl_add_u32 v26, v12, 20, v26\n"
"s_lshl_b32 s75, s63, 17\n"
"s_lshl_b32 s92, s61, 15\n"
"s_add_u32 s75, s75, s92\n"
"s_add_u32 s75, s75, s87\n"
"v_lshrrev_b32 v11, 1, v2\n"
"v_xor_b32 v11, v11, v3\n"
"v_lshlrev_b32 v11, 4, v11\n"
"v_lshl_add_u32 v11, v2, 7, v11\n"
"s_lshl_b32 s92, s66, 12\n"
"v_add_u32 v8, s92, v11\n"
"v_xor_b32 v9, 64, v8\n"
"v_lshrrev_b32 v11, 2, v2\n"
"v_and_b32 v12, 1, v3\n"
"v_lshl_or_b32 v12, v12, 2, v11\n"
"v_lshl_add_u32 v11, v3, 3, v11\n"
"v_lshlrev_b32 v11, 8, v11\n"
"v_lshl_add_u32 v11, v12, 5, v11\n"
"v_and_b32 v12, 3, v0\n"
"v_lshl_add_u32 v4, v12, 3, v11\n"
"s_lshl_b32 s92, s67, 7\n"
"s_add_u32 s92, s92, 65536\n"
"s_add_u32 s93, s92, 0\n"
"v_xor_b32 v10, s93, v4\n"
"s_add_u32 s93, s92, 32\n"
"v_xor_b32 v11, s93, v4\n"
"s_add_u32 s93, s92, 64\n"
"v_xor_b32 v12, s93, v4\n"
"s_add_u32 s93, s92, 96\n"
"v_xor_b32 v13, s93, v4\n"
"s_mov_b64 s[6:7], s[56:57]\n"
"s_cmp_eq_u32 s67, 0\n"
"s_cselect_b64 s[56:57], s[24:25], s[28:29]\n"
"s_cmp_eq_u32 s66, 0\n"
"s_cselect_b64 s[56:57], s[16:17], s[56:57]\n"
"s_cselect_b32 s58, s18, s26\n"
"s_branch KV0_%=\n"
".p2align 12\n"
"PADRET_%=:\n"
"s_setpc_b64 s[10:11]\n"
"KV0_%=:\n"
"s_getpc_b64 s[8:9]\n"
"WM_2_%=:\n"
"s_add_u32 s10, s8, WM_2_%=r-WM_2_%=\n"
"s_addc_u32 s11, s9, 0\n"
"s_mov_b32 s98, PG1_%=-WM_2_%=\n"
"s_mov_b32 s99, PG3_%=-WM_2_%=\n"
"s_cmp_eq_u32 s67, 0\n"
"s_cselect_b32 s98, s98, PG2_%=-WM_2_%=\n"
"s_cselect_b32 s99, s99, PG4_%=-WM_2_%=\n"
"s_cmp_eq_u32 s66, 0\n"
"s_cselect_b32 s98, s98, s99\n"
"s_ashr_i32 s99, s98, 31\n"
"s_add_u32 s8, s8, s98\n"
"s_addc_u32 s9, s9, s99\n"
"s_mov_b64 exec, 0\n"
"s_setpc_b64 s[8:9]\n"
"WM_2_%=r:\n"
"s_mov_b64 exec, -1\n"
"s_getpc_b64 s[8:9]\n"
"WM_3_%=:\n"
"s_add_u32 s10, s8, WM_3_%=r-WM_3_%=\n"
"s_addc_u32 s11, s9, 0\n"
"s_mov_b32 s98, PG5_%=-WM_3_%=\n"
"s_mov_b32 s99, PG7_%=-WM_3_%=\n"
"s_cmp_eq_u32 s67, 0\n"
"s_cselect_b32 s98, s98, PG6_%=-WM_3_%=\n"
"s_cselect_b32 s99, s99, PG8_%=-WM_3_%=\n"
"s_cmp_eq_u32 s66, 0\n"
"s_cselect_b32 s98, s98, s99\n"
"s_ashr_i32 s99, s98, 31\n"
"s_add_u32 s8, s8, s98\n"
"s_addc_u32 s9, s9, s99\n"
"s_mov_b64 exec, 0\n"
"s_setpc_b64 s[8:9]\n"
"WM_3_%=r:\n"
"s_mov_b64 exec, -1\n"
"s_getpc_b64 s[8:9]\n"
"WM_4_%=:\n"
"s_add_u32 s10, s8, WM_4_%=r-WM_4_%=\n"
"s_addc_u32 s11, s9, 0\n"
"s_mov_b32 s98, PG9_%=-WM_4_%=\n"
"s_mov_b32 s99, PG11_%=-WM_4_%=\n"
"s_cmp_eq_u32 s67, 0\n"
"s_cselect_b32 s98, s98, PG10_%=-WM_4_%=\n"
"s_cselect_b32 s99, s99, PG12_%=-WM_4_%=\n"
"s_cmp_eq_u32 s66, 0\n"
"s_cselect_b32 s98, s98, s99\n"
"s_ashr_i32 s99, s98, 31\n"
"s_add_u32 s8, s8, s98\n"
"s_addc_u32 s9, s9, s99\n"
"s_mov_b64 exec, 0\n"
"s_setpc_b64 s[8:9]\n"
"WM_4_%=r:\n"
"s_mov_b64 exec, -1\n"
"s_getpc_b64 s[8:9]\n"
"WM_5_%=:\n"
"s_add_u32 s10, s8, WM_5_%=r-WM_5_%=\n"
"s_addc_u32 s11, s9, 0\n"
"s_mov_b32 s98, ODD0_%=-WM_5_%=\n"
"s_mov_b32 s99, EPI_%=-WM_5_%=\n"
"s_cmp_eq_u32 s67, 0\n"
"s_cselect_b32 s98, s98, ODD1_%=-WM_5_%=\n"
"s_cselect_b32 s99, s99, TAIL0_%=-WM_5_%=\n"
"s_cmp_eq_u32 s66, 0\n"
"s_cselect_b32 s98, s98, s99\n"
"s_ashr_i32 s99, s98, 31\n"
"s_add_u32 s8, s8, s98\n"
"s_addc_u32 s9, s9, s99\n"
"s_mov_b64 exec, 0\n"
"s_setpc_b64 s[8:9]\n"
"WM_5_%=r:\n"
"s_mov_b64 exec, -1\n"
"s_getpc_b64 s[8:9]\n"
"WM_6_%=:\n"
"s_add_u32 s10, s8, WM_6_%=r-WM_6_%=\n"
"s_addc_u32 s11, s9, 0\n"
"s_mov_b32 s98, PG13_%=-WM_6_%=\n"
"s_mov_b32 s99, PG15_%=-WM_6_%=\n"
"s_cmp_eq_u32 s67, 0\n"
"s_cselect_b32 s98, s98, PG14_%=-WM_6_%=\n"
"s_cselect_b32 s99, s99, PG16_%=-WM_6_%=\n"
"s_cmp_eq_u32 s66, 0\n"
"s_cselect_b32 s98, s98, s99\n"
"s_ashr_i32 s99, s98, 31\n"
"s_add_u32 s8, s8, s98\n"
"s_addc_u32 s9, s9, s99\n"
"s_mov_b64 exec, 0\n"
"s_setpc_b64 s[8:9]\n"
"WM_6_%=r:\n"
"s_mov_b64 exec, -1\n"
"buffer_load_dword v54, v55, s[16:19], s96 offen\n"
"s_add_u32 s89, s89, s90\n"
"s_add_u32 s89, s89, s90\n"
"s_add_u32 m0, s74, s78\n"
"s_nop 0\n"
"buffer_load_dwordx4 v14, s[16:19], s72 offen offset:0 lds\n"
"buffer_load_dwordx4 v15, s[16:19], s72 offen offset:1024 lds\n"
"buffer_load_dwordx4 v16, s[16:19], s72 offen offset:2048 lds\n"
"buffer_load_dwordx4 v17, s[16:19], s72 offen offset:3072 lds\n"
"s_add_u32 m0, s74, s79\n"
"s_nop 0\n"
"buffer_load_dwordx4 v18, s[16:19], s72 offen offset:0 lds\n"
"buffer_load_dwordx4 v19, s[16:19], s72 offen offset:1024 lds\n"
"buffer_load_dwordx4 v20, s[16:19], s72 offen offset:2048 lds\n"
"buffer_load_dwordx4 v21, s[16:19], s72 offen offset:3072 lds\n"
"s_add_u32 m0, s74, s80\n"
"s_nop 0\n"
"buffer_load_dwordx4 v22, s[24:27], s73 offen offset:0 lds\n"
"buffer_load_dwordx4 v23, s[24:27], s73 offen offset:1024 lds\n"
"buffer_load_dwordx4 v30, s[24:27], s73 offen offset:2048 lds\n"
"buffer_load_dwordx4 v31, s[24:27], s73 offen offset:3072 lds\n"
"s_add_u32 m0, s74, s81\n"
"s_nop 0\n"
"buffer_load_dwordx4 v22, s[28:31], s73 offen offset:0 lds\n"
"buffer_load_dwordx4 v23, s[28:31], s73 offen offset:1024 lds\n"
"buffer_load_dwordx4 v30, s[28:31], s73 offen offset:2048 lds\n"
"buffer_load_dwordx4 v31, s[28:31], s73 offen offset:3072 lds\n"
"s_add_u32 s72, s72, 128\n"
"s_add_u32 s73, s73, 524288\n"
"s_waitcnt vmcnt(17) lgkmcnt(0)\n"
"s_barrier\n"
"ds_read_b128 v[64:67], v8 offset:0\n"
"ds_read_b128 v[68:71], v9 offset:0\n"
"ds_read_b64_tr_b16 a[192:193], v10 offset:0\n"
"ds_read_b64_tr_b16 a[194:195], v10 offset:1024\n"
"ds_read_b64_tr_b16 a[196:197], v10 offset:8192\n"
"ds_read_b64_tr_b16 a[198:199], v10 offset:9216\n"
"ds_read_b64_tr_b16 a[200:201], v11 offset:0\n"
"ds_read_b64_tr_b16 a[202:203], v11 offset:1024\n"
"ds_read_b64_tr_b16 a[204:205], v11 offset:8192\n"
"ds_read_b64_tr_b16 a[206:207], v11 offset:9216\n"
"ds_read_b64_tr_b16 a[208:209], v12 offset:0\n"
"ds_read_b64_tr_b16 a[210:211], v12 offset:1024\n"
"ds_read_b64_tr_b16 a[212:213], v12 offset:8192\n"
"ds_read_b64_tr_b16 a[214:215], v12 offset:9216\n"
"ds_read_b64_tr_b16 a[216:217], v13 offset:0\n"
"ds_read_b64_tr_b16 a[218:219], v13 offset:1024\n"
"ds_read_b64_tr_b16 a[220:221], v13 offset:8192\n"
"ds_read_b64_tr_b16 a[222:223], v13 offset:9216\n"
"ds_read_b128 v[72:75], v8 offset:2048\n"
"ds_read_b128 v[76:79], v9 offset:2048\n"
"s_waitcnt lgkmcnt(15)\n"
"v_mfma_f32_16x16x32_bf16 v[128:131], a[192:195], v[64:67], 0\n"
"s_mov_b32 s74, 0\n"
"ds_read_b128 v[80:83], v8 offset:8192\n"
"ds_read_b128 v[84:87], v9 offset:8192\n"
"s_waitcnt lgkmcnt(15)\n"
"v_mfma_f32_16x16x32_bf16 v[128:131], a[196:199], v[68:71], v[128:131]\n"
"buffer_load_dword v61, v57, s[56:59], s89 offen\n"
"s_waitcnt lgkmcnt(12)\n"
"v_mfma_f32_16x16x32_bf16 v[132:135], a[204:207], v[68:71], 0\n"
"s_add_u32 s89, s89, s90\n"
"ds_read_b128 v[88:91], v8 offset:10240\n"
"ds_read_b128 v[92:95], v9 offset:10240\n"
"v_mfma_f32_16x16x32_bf16 v[132:135], a[200:203], v[64:67], v[132:135]\n"
"s_add_u32 s89, s89, s90\n"
"ds_read_b128 v[96:99], v8 offset:16384\n"
"ds_read_b128 v[100:103], v9 offset:16384\n"
"s_waitcnt lgkmcnt(14)\n"
"v_mfma_f32_16x16x32_bf16 v[136:139], a[208:211], v[64:67], 0\n"
"s_lshr_b32 s88, s60, 1\n"
"ds_read_b128 v[104:107], v8 offset:18432\n"
"ds_read_b128 v[108:111], v9 offset:18432\n"
"s_waitcnt lgkmcnt(14)\n"
"v_mfma_f32_16x16x32_bf16 v[136:139], a[212:215], v[68:71], v[136:139]\n"
"s_lshl_b32 s88, s88, 14\n"
"ds_read_b128 v[112:115], v8 offset:24576\n"
"ds_read_b128 v[116:119], v9 offset:24576\n"
"s_waitcnt lgkmcnt(12)\n"
"v_mfma_f32_16x16x32_bf16 v[140:143], a[220:223], v[68:71], 0\n"
"s_and_b32 s93, s60, 1\n"
"ds_read_b128 v[120:123], v8 offset:26624\n"
"ds_read_b128 v[124:127], v9 offset:26624\n"
"v_mfma_f32_16x16x32_bf16 v[140:143], a[216:219], v[64:67], v[140:143]\n"
"s_lshl_b32 s93, s93, 7\n"
"s_add_u32 s88, s88, s93\n"
"s_lshl_b32 s93, s61, 12\n"
"s_waitcnt lgkmcnt(13)\n"
"v_mfma_f32_16x16x32_bf16 v[156:159], a[216:219], v[72:75], 0\n"
"s_add_u32 s88, s88, s93\n"
"s_add_u32 s88, s88, 1048576\n"
"s_waitcnt lgkmcnt(12)\n"
"v_mfma_f32_16x16x32_bf16 v[156:159], a[220:223], v[76:79], v[156:159]\n"
"v_mfma_f32_16x16x32_bf16 v[152:155], a[212:215], v[76:79], 0\n"
"v_mfma_f32_16x16x32_bf16 v[152:155], a[208:211], v[72:75], v[152:155]\n"
"v_mfma_f32_16x16x32_bf16 v[148:151], a[200:203], v[72:75], 0\n"
"v_mfma_f32_16x16x32_bf16 v[148:151], a[204:207], v[76:79], v[148:151]\n"
"v_mfma_f32_16x16x32_bf16 v[144:147], a[196:199], v[76:79], 0\n"
"v_mfma_f32_16x16x32_bf16 v[144:147], a[192:195], v[72:75], v[144:147]\n"
"s_waitcnt lgkmcnt(11)\n"
"v_mfma_f32_16x16x32_bf16 v[160:163], a[192:195], v[80:83], 0\n"
"s_waitcnt lgkmcnt(10)\n"
"v_mfma_f32_16x16x32_bf16 v[160:163], a[196:199], v[84:87], v[160:163]\n"
"v_mfma_f32_16x16x32_bf16 v[164:167], a[204:207], v[84:87], 0\n"
"v_mfma_f32_16x16x32_bf16 v[164:167], a[200:203], v[80:83], v[164:167]\n"
"v_mfma_f32_16x16x32_bf16 v[168:171], a[208:211], v[80:83], 0\n"
"ds_read_b64_tr_b16 a[224:225], v10 offset:16384\n"
"v_mfma_f32_16x16x32_bf16 v[168:171], a[212:215], v[84:87], v[168:171]\n"
"ds_read_b64_tr_b16 a[226:227], v10 offset:17408\n"
"v_mfma_f32_16x16x32_bf16 v[172:175], a[220:223], v[84:87], 0\n"
"v_mfma_f32_16x16x32_bf16 v[172:175], a[216:219], v[80:83], v[172:175]\n"
"s_waitcnt lgkmcnt(11)\n"
"ds_read_b64_tr_b16 a[228:229], v10 offset:24576\n"
"v_mfma_f32_16x16x32_bf16 v[188:191], a[216:219], v[88:91], 0\n"
"s_waitcnt lgkmcnt(11)\n"
"ds_read_b64_tr_b16 a[230:231], v10 offset:25600\n"
"v_mfma_f32_16x16x32_bf16 v[188:191], a[220:223], v[92:95], v[188:191]\n"
"v_mfma_f32_16x16x32_bf16 v[184:187], a[212:215], v[92:95], 0\n"
"ds_read_b64_tr_b16 a[232:233], v11 offset:16384\n"
"v_mfma_f32_16x16x32_bf16 v[184:187], a[208:211], v[88:91], v[184:187]\n"
"ds_read_b64_tr_b16 a[234:235], v11 offset:17408\n"
"v_mfma_f32_16x16x32_bf16 v[180:183], a[200:203], v[88:91], 0\n"
"v_mfma_f32_16x16x32_bf16 v[180:183], a[204:207], v[92:95], v[180:183]\n"
"ds_read_b64_tr_b16 a[236:237], v11 offset:24576\n"
"v_mfma_f32_16x16x32_bf16 v[176:179], a[196:199], v[92:95], 0\n"
"ds_read_b64_tr_b16 a[238:239], v11 offset:25600\n"
"v_mfma_f32_16x16x32_bf16 v[176:179], a[192:195], v[88:91], v[176:179]\n"
"s_waitcnt lgkmcnt(15)\n"
"v_mfma_f32_16x16x32_bf16 v[192:195], a[192:195], v[96:99], 0\n"
"s_waitcnt lgkmcnt(14)\n"
"ds_read_b64_tr_b16 a[240:241], v12 offset:16384\n"
"v_mfma_f32_16x16x32_bf16 v[192:195], a[196:199], v[100:103], v[192:195]\n"
"v_mfma_f32_16x16x32_bf16 v[196:199], a[204:207], v[100:103], 0\n"
"ds_read_b64_tr_b16 a[242:243], v12 offset:17408\n"
"v_mfma_f32_16x16x32_bf16 v[196:199], a[200:203], v[96:99], v[196:199]\n"
"ds_read_b64_tr_b16 a[244:245], v12 offset:24576\n"
"v_mfma_f32_16x16x32_bf16 v[200:203], a[208:211], v[96:99], 0\n"
"v_mfma_f32_16x16x32_bf16 v[200:203], a[212:215], v[100:103], v[200:203]\n"
"ds_read_b64_tr_b16 a[246:247], v12 offset:25600\n"
"v_mfma_f32_16x16x32_bf16 v[204:207], a[220:223], v[100:103], 0\n"
"ds_read_b64_tr_b16 a[248:249], v13 offset:16384\n"
"v_mfma_f32_16x16x32_bf16 v[204:207], a[216:219], v[96:99], v[204:207]\n"
"s_waitcnt lgkmcnt(15)\n"
"v_mfma_f32_16x16x32_bf16 v[220:223], a[216:219], v[104:107], 0\n"
"s_waitcnt lgkmcnt(15)\n"
"ds_read_b64_tr_b16 a[250:251], v13 offset:17408\n"
"v_mfma_f32_16x16x32_bf16 v[220:223], a[220:223], v[108:111], v[220:223]\n"
"ds_read_b64_tr_b16 a[252:253], v13 offset:24576\n"
"v_mfma_f32_16x16x32_bf16 v[216:219], a[212:215], v[108:111], 0\n"
"v_mfma_f32_16x16x32_bf16 v[216:219], a[208:211], v[104:107], v[216:219]\n"
"ds_read_b64_tr_b16 a[254:255], v13 offset:25600\n"
"v_mfma_f32_16x16x32_bf16 v[212:215], a[200:203], v[104:107], 0\n"
"v_mfma_f32_16x16x32_bf16 v[212:215], a[204:207], v[108:111], v[212:215]\n"
"v_mfma_f32_16x16x32_bf16 v[208:211], a[196:199], v[108:111], 0\n"
"v_mfma_f32_16x16x32_bf16 v[208:211], a[192:195], v[104:107], v[208:211]\n"
"s_waitcnt lgkmcnt(15)\n"
"v_mfma_f32_16x16x32_bf16 v[224:227], a[192:195], v[112:115], 0\n"
"s_waitcnt lgkmcnt(15)\n"
"v_mfma_f32_16x16x32_bf16 v[224:227], a[196:199], v[116:119], v[224:227]\n"
"v_mfma_f32_16x16x32_bf16 v[228:231], a[204:207], v[116:119], 0\n"
"v_mfma_f32_16x16x32_bf16 v[228:231], a[200:203], v[112:115], v[228:231]\n"
"v_mfma_f32_16x16x32_bf16 v[232:235], a[208:211], v[112:115], 0\n"
"v_mfma_f32_16x16x32_bf16 v[232:235], a[212:215], v[116:119], v[232:235]\n"
"v_mfma_f32_16x16x32_bf16 v[236:239], a[220:223], v[116:119], 0\n"
"v_mfma_f32_16x16x32_bf16 v[236:239], a[216:219], v[112:115], v[236:239]\n"
"s_waitcnt lgkmcnt(15)\n"
"v_mfma_f32_16x16x32_bf16 v[252:255], a[216:219], v[120:123], 0\n"
"s_waitcnt lgkmcnt(15)\n"
"v_mfma_f32_16x16x32_bf16 v[252:255], a[220:223], v[124:127], v[252:255]\n"
"v_mfma_f32_16x16x32_bf16 v[248:251], a[212:215], v[124:127], 0\n"
"v_mfma_f32_16x16x32_bf16 v[248:251], a[208:211], v[120:123], v[248:251]\n"
"v_mfma_f32_16x16x32_bf16 v[244:247], a[200:203], v[120:123], 0\n"
"v_mfma_f32_16x16x32_bf16 v[244:247], a[204:207], v[124:127], v[244:247]\n"
"v_mfma_f32_16x16x32_bf16 v[240:243], a[196:199], v[124:127], 0\n"
"v_mfma_f32_16x16x32_bf16 v[240:243], a[192:195], v[120:123], v[240:243]\n"
"s_waitcnt vmcnt(1) lgkmcnt(0)\n"
"s_barrier\n"
"v_mfma_f32_16x16x32_bf16 a[0:3], v[64:67], a[224:227], 0\n"
"s_add_u32 m0, s74, s78\n"
"s_lshl_b32 s96, s60, 2\n"
"ds_read_b64_tr_b16 a[192:193], v10 offset:32768\n"
"v_mfma_f32_16x16x32_bf16 a[0:3], v[68:71], a[228:231], a[0:3]\n"
"buffer_load_dwordx4 v14, s[16:19], s72 offen offset:0 lds\n"
"v_mfma_f32_16x16x32_bf16 a[32:35], v[68:71], a[236:239], 0\n"
"s_add_u32 s96, s96, s61\n"
"s_lshl_b32 s96, s96, 7\n"
"ds_read_b64_tr_b16 a[194:195], v10 offset:33792\n"
"v_mfma_f32_16x16x32_bf16 a[32:35], v[64:67], a[232:235], a[32:35]\n"
"s_add_u32 s96, s96, 262144\n"
"s_lshl_b32 s97, s61, 16\n"
"ds_read_b64_tr_b16 a[196:197], v10 offset:40960\n"
"v_mfma_f32_16x16x32_bf16 a[64:67], v[64:67], a[240:243], 0\n"
"s_lshl_b32 s93, s60, 7\n"
"s_add_u32 s97, s97, s93\n"
"v_lshrrev_b32 v48, 3, v0\n"
"v_mfma_f32_16x16x32_bf16 a[64:67], v[68:71], a[244:247], a[64:67]\n"
"ds_read_b64_tr_b16 a[198:199], v10 offset:41984\n"
"v_min_u32 v49, 2, v48\n"
"v_mfma_f32_16x16x32_bf16 a[96:99], v[68:71], a[252:255], 0\n"
"buffer_load_dwordx4 v15, s[16:19], s72 offen offset:1024 lds\n"
"v_mfma_f32_16x16x32_bf16 a[96:99], v[64:67], a[248:251], a[96:99]\n"
"ds_read_b64_tr_b16 a[200:201], v11 offset:32768\n"
"v_lshl_add_u32 v49, v49, 17, s96\n"
"v_mfma_f32_16x16x32_bf16 a[100:103], v[72:75], a[248:251], 0\n"
"ds_read_b64_tr_b16 a[202:203], v11 offset:33792\n"
"v_and_b32 v50, 1, v48\n"
"v_mfma_f32_16x16x32_bf16 a[100:103], v[76:79], a[252:255], a[100:103]\n"
"v_lshl_add_u32 v50, v50, 15, s97\n"
"v_cmp_gt_u32 vcc, 4, v48\n"
"v_mfma_f32_16x16x32_bf16 a[68:71], v[76:79], a[244:247], 0\n"
"ds_read_b64_tr_b16 a[204:205], v11 offset:40960\n"
"v_cndmask_b32 v49, v50, v49, vcc\n"
"v_mfma_f32_16x16x32_bf16 a[68:71], v[72:75], a[240:243], a[68:71]\n"
"buffer_load_dwordx4 v16, s[16:19], s72 offen offset:2048 lds\n"
"v_mfma_f32_16x16x32_bf16 a[36:39], v[72:75], a[232:235], 0\n"
"ds_read_b64_tr_b16 a[206:207], v11 offset:41984\n"
"v_and_b32 v48, 7, v0\n"
"v_mfma_f32_16x16x32_bf16 a[36:39], v[76:79], a[236:239], a[36:39]\n"
"v_lshl_add_u32 v48, v48, 4, v49\n"
"v_mov_b32 v52, 0\n"
"v_mfma_f32_16x16x32_bf16 a[4:7], v[76:79], a[228:231], 0\n"
"ds_read_b64_tr_b16 a[208:209], v12 offset:32768\n"
"v_mov_b32 v53, 0\n"
"v_mfma_f32_16x16x32_bf16 a[4:7], v[72:75], a[224:227], a[4:7]\n"
"v_mov_b32 v54, 0\n"
"v_mov_b32 v55, 0\n"
"v_mfma_f32_16x16x32_bf16 a[8:11], v[80:83], a[224:227], 0\n"
"ds_read_b64_tr_b16 a[210:211], v12 offset:33792\n"
"v_mfma_f32_16x16x32_bf16 a[8:11], v[84:87], a[228:231], a[8:11]\n"
"ds_read_b64_tr_b16 a[212:213], v12 offset:40960\n"
"v_mfma_f32_16x16x32_bf16 a[40:43], v[84:87], a[236:239], 0\n"
"buffer_load_dwordx4 v17, s[16:19], s72 offen offset:3072 lds\n"
"v_mfma_f32_16x16x32_bf16 a[40:43], v[80:83], a[232:235], a[40:43]\n"
"s_add_u32 m0, s74, s79\n"
"ds_read_b64_tr_b16 a[214:215], v12 offset:41984\n"
"v_mfma_f32_16x16x32_bf16 a[72:75], v[80:83], a[240:243], 0\n"
"v_mfma_f32_16x16x32_bf16 a[72:75], v[84:87], a[244:247], a[72:75]\n"
"ds_read_b64_tr_b16 a[216:217], v13 offset:32768\n"
"v_mfma_f32_16x16x32_bf16 a[104:107], v[84:87], a[252:255], 0\n"
"buffer_load_dwordx4 v18, s[16:19], s72 offen offset:0 lds\n"
"v_mfma_f32_16x16x32_bf16 a[104:107], v[80:83], a[248:251], a[104:107]\n"
"ds_read_b64_tr_b16 a[218:219], v13 offset:33792\n"
"v_mfma_f32_16x16x32_bf16 a[108:111], v[88:91], a[248:251], 0\n"
"ds_read_b64_tr_b16 a[220:221], v13 offset:40960\n"
"v_mfma_f32_16x16x32_bf16 a[108:111], v[92:95], a[252:255], a[108:111]\n"
"v_mfma_f32_16x16x32_bf16 a[76:79], v[92:95], a[244:247], 0\n"
"ds_read_b64_tr_b16 a[222:223], v13 offset:41984\n"
"v_mfma_f32_16x16x32_bf16 a[76:79], v[88:91], a[240:243], a[76:79]\n"
"v_mfma_f32_16x16x32_bf16 a[44:47], v[88:91], a[232:235], 0\n"
"ds_read_b128 a[128:131], v8 offset:32768\n"
"v_mfma_f32_16x16x32_bf16 a[44:47], v[92:95], a[236:239], a[44:47]\n"
"buffer_load_dwordx4 v19, s[16:19], s72 offen offset:1024 lds\n"
"v_mfma_f32_16x16x32_bf16 a[12:15], v[92:95], a[228:231], 0\n"
"ds_read_b128 a[132:135], v9 offset:32768\n"
"v_mfma_f32_16x16x32_bf16 a[12:15], v[88:91], a[224:227], a[12:15]\n"
"v_mfma_f32_16x16x32_bf16 a[16:19], v[96:99], a[224:227], 0\n"
"ds_read_b128 a[136:139], v8 offset:34816\n"
"v_mfma_f32_16x16x32_bf16 a[16:19], v[100:103], a[228:231], a[16:19]\n"
"ds_read_b128 a[140:143], v9 offset:34816\n"
"v_mfma_f32_16x16x32_bf16 a[48:51], v[100:103], a[236:239], 0\n"
"buffer_load_dwordx4 v20, s[16:19], s72 offen offset:2048 lds\n"
"v_mfma_f32_16x16x32_bf16 a[48:51], v[96:99], a[232:235], a[48:51]\n"
"ds_read_b128 a[144:147], v8 offset:40960\n"
"v_mfma_f32_16x16x32_bf16 a[80:83], v[96:99], a[240:243], 0\n"
"v_mfma_f32_16x16x32_bf16 a[80:83], v[100:103], a[244:247], a[80:83]\n"
"ds_read_b128 a[148:151], v9 offset:40960\n"
"v_mfma_f32_16x16x32_bf16 a[112:115], v[100:103], a[252:255], 0\n"
"v_mfma_f32_16x16x32_bf16 a[112:115], v[96:99], a[248:251], a[112:115]\n"
"ds_read_b128 a[152:155], v8 offset:43008\n"
"v_mfma_f32_16x16x32_bf16 a[116:119], v[104:107], a[248:251], 0\n"
"ds_read_b128 a[156:159], v9 offset:43008\n"
"v_mfma_f32_16x16x32_bf16 a[116:119], v[108:111], a[252:255], a[116:119]\n"
"buffer_load_dwordx4 v21, s[16:19], s72 offen offset:3072 lds\n"
"v_mfma_f32_16x16x32_bf16 a[84:87], v[108:111], a[244:247], 0\n"
"s_add_u32 m0, s74, s80\n"
"ds_read_b128 a[160:163], v8 offset:49152\n"
"v_mfma_f32_16x16x32_bf16 a[84:87], v[104:107], a[240:243], a[84:87]\n"
"buffer_store_dwordx4 v[52:55], v48, s[44:47], 0 offen sc1\n"
"v_mfma_f32_16x16x32_bf16 a[52:55], v[104:107], a[232:235], 0\n"
"ds_read_b128 a[164:167], v9 offset:49152\n"
"v_mfma_f32_16x16x32_bf16 a[52:55], v[108:111], a[236:239], a[52:55]\n"
"buffer_load_dwordx4 v22, s[24:27], s73 offen offset:0 lds\n"
"v_mfma_f32_16x16x32_bf16 a[20:23], v[108:111], a[228:231], 0\n"
"ds_read_b128 a[168:171], v8 offset:51200\n"
"v_mfma_f32_16x16x32_bf16 a[20:23], v[104:107], a[224:227], a[20:23]\n"
"v_mfma_f32_16x16x32_bf16 a[24:27], v[112:115], a[224:227], 0\n"
"ds_read_b128 a[172:175], v9 offset:51200\n"
"v_mfma_f32_16x16x32_bf16 a[24:27], v[116:119], a[228:231], a[24:27]\n"
"ds_read_b128 a[176:179], v8 offset:57344\n"
"v_mfma_f32_16x16x32_bf16 a[56:59], v[116:119], a[236:239], 0\n"
"v_mfma_f32_16x16x32_bf16 a[56:59], v[112:115], a[232:235], a[56:59]\n"
"ds_read_b128 a[180:183], v9 offset:57344\n"
"v_mfma_f32_16x16x32_bf16 a[88:91], v[112:115], a[240:243], 0\n"
"buffer_load_dwordx4 v23, s[24:27], s73 offen offset:1024 lds\n"
"v_mfma_f32_16x16x32_bf16 a[88:91], v[116:119], a[244:247], a[88:91]\n"
"ds_read_b128 a[184:187], v8 offset:59392\n"
"v_mfma_f32_16x16x32_bf16 a[120:123], v[116:119], a[252:255], 0\n"
"v_mfma_f32_16x16x32_bf16 a[120:123], v[112:115], a[248:251], a[120:123]\n"
"ds_read_b128 a[188:191], v9 offset:59392\n"
"v_mfma_f32_16x16x32_bf16 a[124:127], v[120:123], a[248:251], 0\n"
"v_mfma_f32_16x16x32_bf16 a[124:127], v[124:127], a[252:255], a[124:127]\n"
"buffer_load_dwordx4 v30, s[24:27], s73 offen offset:2048 lds\n"
"v_mfma_f32_16x16x32_bf16 a[92:95], v[124:127], a[244:247], 0\n"
"v_mfma_f32_16x16x32_bf16 a[92:95], v[120:123], a[240:243], a[92:95]\n"
"v_mfma_f32_16x16x32_bf16 a[60:63], v[120:123], a[232:235], 0\n"
"v_mfma_f32_16x16x32_bf16 a[60:63], v[124:127], a[236:239], a[60:63]\n"
"v_mfma_f32_16x16x32_bf16 a[28:31], v[124:127], a[228:231], 0\n"
"v_mfma_f32_16x16x32_bf16 a[28:31], v[120:123], a[224:227], a[28:31]\n"
"s_waitcnt lgkmcnt(15)\n"
"v_mfma_f32_16x16x32_bf16 v[128:131], a[192:195], a[128:131], v[128:131]\n"
"s_waitcnt lgkmcnt(14)\n"
"ds_read_b64_tr_b16 a[224:225], v10 offset:49152\n"
"v_mfma_f32_16x16x32_bf16 v[128:131], a[196:199], a[132:135], v[128:131]\n"
"buffer_load_dwordx4 v31, s[24:27], s73 offen offset:3072 lds\n"
"v_mfma_f32_16x16x32_bf16 v[132:135], a[204:207], a[132:135], v[132:135]\n"
"s_add_u32 m0, s74, s81\n"
"s_add_u32 s72, s72, 128\n"
"ds_read_b64_tr_b16 a[226:227], v10 offset:50176\n"
"v_mfma_f32_16x16x32_bf16 v[132:135], a[200:203], a[128:131], v[132:135]\n"
"v_mfma_f32_16x16x32_bf16 v[136:139], a[208:211], a[128:131], v[136:139]\n"
"buffer_load_dwordx4 v22, s[28:31], s73 offen offset:0 lds\n"
"v_mfma_f32_16x16x32_bf16 v[136:139], a[212:215], a[132:135], v[136:139]\n"
"ds_read_b64_tr_b16 a[228:229], v10 offset:57344\n"
"v_mfma_f32_16x16x32_bf16 v[140:143], a[220:223], a[132:135], v[140:143]\n"
"v_mfma_f32_16x16x32_bf16 v[140:143], a[216:219], a[128:131], v[140:143]\n"
"buffer_load_dwordx4 v23, s[28:31], s73 offen offset:1024 lds\n"
"s_waitcnt lgkmcnt(15)\n"
"v_mfma_f32_16x16x32_bf16 v[156:159], a[216:219], a[136:139], v[156:159]\n"
"s_waitcnt lgkmcnt(15)\n"
"ds_read_b64_tr_b16 a[230:231], v10 offset:58368\n"
"v_mfma_f32_16x16x32_bf16 v[156:159], a[220:223], a[140:143], v[156:159]\n"
"v_mfma_f32_16x16x32_bf16 v[152:155], a[212:215], a[140:143], v[152:155]\n"
"v_mfma_f32_16x16x32_bf16 v[152:155], a[208:211], a[136:139], v[152:155]\n"
"buffer_load_dwordx4 v30, s[28:31], s73 offen offset:2048 lds\n"
"v_mfma_f32_16x16x32_bf16 v[148:151], a[200:203], a[136:139], v[148:151]\n"
"ds_read_b64_tr_b16 a[232:233], v11 offset:49152\n"
"v_mfma_f32_16x16x32_bf16 v[148:151], a[204:207], a[140:143], v[148:151]\n"
"v_mfma_f32_16x16x32_bf16 v[144:147], a[196:199], a[140:143], v[144:147]\n"
"buffer_load_dwordx4 v31, s[28:31], s73 offen offset:3072 lds\n"
"v_mfma_f32_16x16x32_bf16 v[144:147], a[192:195], a[136:139], v[144:147]\n"
"s_add_u32 s73, s73, 524288\n"
"ds_read_b64_tr_b16 a[234:235], v11 offset:50176\n"
"s_waitcnt lgkmcnt(15)\n"
"v_mfma_f32_16x16x32_bf16 v[160:163], a[192:195], a[144:147], v[160:163]\n"
"s_waitcnt lgkmcnt(15)\n"
"ds_read_b64_tr_b16 a[236:237], v11 offset:57344\n"
"v_mfma_f32_16x16x32_bf16 v[160:163], a[196:199], a[148:151], v[160:163]\n"
"buffer_load_dword v61, v27, s[56:59], s89 offen\n"
"v_mfma_f32_16x16x32_bf16 v[164:167], a[204:207], a[148:151], v[164:167]\n"
"s_add_u32 s89, s89, s90\n"
"s_xor_b32 s74, s74, 0x8000\n"
"s_cmp_eq_u32 s67, 0\n"
"v_mfma_f32_16x16x32_bf16 v[164:167], a[200:203], a[144:147], v[164:167]\n"
"s_cselect_b32 s92, s63, s91\n"
"s_lshl_b32 s93, s92, 11\n"
"ds_read_b64_tr_b16 a[238:239], v11 offset:58368\n"
"v_mfma_f32_16x16x32_bf16 v[168:171], a[208:211], a[144:147], v[168:171]\n"
"s_add_u32 s93, s93, 0\n"
"s_lshl_b32 s82, s92, 17\n"
"v_lshl_add_u32 v7, v2, 7, s93\n"
"v_mfma_f32_16x16x32_bf16 v[168:171], a[212:215], a[148:151], v[168:171]\n"
"buffer_load_dwordx4 v[32:35], v63, s[12:15], s94 offen offset:0\n"
"v_mfma_f32_16x16x32_bf16 v[172:175], a[220:223], a[148:151], v[172:175]\n"
"s_lshl_b32 s93, s66, 14\n"
"s_add_u32 s82, s82, s93\n"
"ds_read_b64_tr_b16 a[240:241], v12 offset:49152\n"
"v_mfma_f32_16x16x32_bf16 v[172:175], a[216:219], a[144:147], v[172:175]\n"
"s_add_u32 s82, s82, 1048576\n"
"s_lshl_b32 s83, s62, 20\n"
"s_lshl_b32 s93, s63, 15\n"
"s_waitcnt lgkmcnt(15)\n"
"v_mfma_f32_16x16x32_bf16 v[188:191], a[216:219], a[152:155], v[188:191]\n"
"buffer_load_dwordx4 v[36:39], v63, s[12:15], s94 offen offset:128\n"
"s_waitcnt lgkmcnt(15)\n"
"v_mfma_f32_16x16x32_bf16 v[188:191], a[220:223], a[156:159], v[188:191]\n"
"s_add_u32 s83, s83, s93\n"
"s_add_u32 s83, s83, 4194304\n"
"ds_read_b64_tr_b16 a[242:243], v12 offset:50176\n"
"v_mfma_f32_16x16x32_bf16 v[184:187], a[212:215], a[156:159], v[184:187]\n"
"s_lshl_b32 s93, s66, 14\n"
"s_add_u32 s83, s83, s93\n"
"s_lshl_b32 s93, s67, 8\n"
"v_mfma_f32_16x16x32_bf16 v[184:187], a[208:211], a[152:155], v[184:187]\n"
"buffer_load_dword v56, off, s[44:47], s95 sc1\n"
"v_mfma_f32_16x16x32_bf16 v[180:183], a[200:203], a[152:155], v[180:183]\n"
"s_add_u32 s84, s83, s93\n"
"s_add_u32 s84, s84, 512\n"
"ds_read_b64_tr_b16 a[244:245], v12 offset:57344\n"
"v_mfma_f32_16x16x32_bf16 v[180:183], a[204:207], a[156:159], v[180:183]\n"
"s_lshl_b32 s93, s67, 13\n"
"s_add_u32 s83, s83, s93\n"
"v_mfma_f32_16x16x32_bf16 v[176:179], a[196:199], a[156:159], v[176:179]\n"
"ds_read_b64_tr_b16 a[246:247], v12 offset:58368\n"
"v_mfma_f32_16x16x32_bf16 v[176:179], a[192:195], a[152:155], v[176:179]\n"
"s_waitcnt lgkmcnt(15)\n"
"v_mfma_f32_16x16x32_bf16 v[192:195], a[192:195], a[160:163], v[192:195]\n"
"s_waitcnt lgkmcnt(15)\n"
"v_mfma_f32_16x16x32_bf16 v[192:195], a[196:199], a[164:167], v[192:195]\n"
"ds_read_b64_tr_b16 a[248:249], v13 offset:49152\n"
"v_mfma_f32_16x16x32_bf16 v[196:199], a[204:207], a[164:167], v[196:199]\n"
"v_mfma_f32_16x16x32_bf16 v[196:199], a[200:203], a[160:163], v[196:199]\n"
"v_mfma_f32_16x16x32_bf16 v[200:203], a[208:211], a[160:163], v[200:203]\n"
"ds_read_b64_tr_b16 a[250:251], v13 offset:50176\n"
"v_mfma_f32_16x16x32_bf16 v[200:203], a[212:215], a[164:167], v[200:203]\n"
"v_mfma_f32_16x16x32_bf16 v[204:207], a[220:223], a[164:167], v[204:207]\n"
"v_mfma_f32_16x16x32_bf16 v[204:207], a[216:219], a[160:163], v[204:207]\n"
"s_waitcnt lgkmcnt(15)\n"
"ds_read_b64_tr_b16 a[252:253], v13 offset:57344\n"
"v_mfma_f32_16x16x32_bf16 v[220:223], a[216:219], a[168:171], v[220:223]\n"
"s_waitcnt lgkmcnt(15)\n"
"v_mfma_f32_16x16x32_bf16 v[220:223], a[220:223], a[172:175], v[220:223]\n"
"v_mfma_f32_16x16x32_bf16 v[216:219], a[212:215], a[172:175], v[216:219]\n"
"ds_read_b64_tr_b16 a[254:255], v13 offset:58368\n"
"v_mfma_f32_16x16x32_bf16 v[216:219], a[208:211], a[168:171], v[216:219]\n"
"v_mfma_f32_16x16x32_bf16 v[212:215], a[200:203], a[168:171], v[212:215]\n"
"v_mfma_f32_16x16x32_bf16 v[212:215], a[204:207], a[172:175], v[212:215]\n"
"v_mfma_f32_16x16x32_bf16 v[208:211], a[196:199], a[172:175], v[208:211]\n"
"v_mfma_f32_16x16x32_bf16 v[208:211], a[192:195], a[168:171], v[208:211]\n"
"s_waitcnt lgkmcnt(15)\n"
"v_mfma_f32_16x16x32_bf16 v[224:227], a[192:195], a[176:179], v[224:227]\n"
"s_waitcnt lgkmcnt(15)\n"
"v_mfma_f32_16x16x32_bf16 v[224:227], a[196:199], a[180:183], v[224:227]\n"
"v_mfma_f32_16x16x32_bf16 v[228:231], a[204:207], a[180:183], v[228:231]\n"
"v_mfma_f32_16x16x32_bf16 v[228:231], a[200:203], a[176:179], v[228:231]\n"
"v_mfma_f32_16x16x32_bf16 v[232:235], a[208:211], a[176:179], v[232:235]\n"
"v_mfma_f32_16x16x32_bf16 v[232:235], a[212:215], a[180:183], v[232:235]\n"
"v_mfma_f32_16x16x32_bf16 v[236:239], a[220:223], a[180:183], v[236:239]\n"
"v_mfma_f32_16x16x32_bf16 v[236:239], a[216:219], a[176:179], v[236:239]\n"
"s_waitcnt lgkmcnt(15)\n"
"v_mfma_f32_16x16x32_bf16 v[252:255], a[216:219], a[184:187], v[252:255]\n"
"s_waitcnt lgkmcnt(15)\n"
"v_mfma_f32_16x16x32_bf16 v[252:255], a[220:223], a[188:191], v[252:255]\n"
"v_mfma_f32_16x16x32_bf16 v[248:251], a[212:215], a[188:191], v[248:251]\n"
"v_mfma_f32_16x16x32_bf16 v[248:251], a[208:211], a[184:187], v[248:251]\n"
"v_mfma_f32_16x16x32_bf16 v[244:247], a[200:203], a[184:187], v[244:247]\n"
"v_mfma_f32_16x16x32_bf16 v[244:247], a[204:207], a[188:191], v[244:247]\n"
"v_mfma_f32_16x16x32_bf16 v[240:243], a[196:199], a[188:191], v[240:243]\n"
"v_mfma_f32_16x16x32_bf16 v[240:243], a[192:195], a[184:187], v[240:243]\n"
"s_waitcnt vmcnt(4) lgkmcnt(0)\n"
"s_barrier\n"
"v_mfma_f32_16x16x32_bf16 a[0:3], a[128:131], a[224:227], a[0:3]\n"
"s_add_u32 m0, s74, s78\n"
"ds_read_b64_tr_b16 a[192:193], v10 offset:0\n"
"v_mfma_f32_16x16x32_bf16 a[0:3], a[132:135], a[228:231], a[0:3]\n"
"buffer_load_dwordx4 v14, s[16:19], s72 offen offset:0 lds\n"
"v_mfma_f32_16x16x32_bf16 a[32:35], a[132:135], a[236:239], a[32:35]\n"
"ds_read_b64_tr_b16 a[194:195], v10 offset:1024\n"
"v_mfma_f32_16x16x32_bf16 a[32:35], a[128:131], a[232:235], a[32:35]\n"
"ds_read_b64_tr_b16 a[196:197], v10 offset:8192\n"
"v_mfma_f32_16x16x32_bf16 a[64:67], a[128:131], a[240:243], a[64:67]\n"
"v_mfma_f32_16x16x32_bf16 a[64:67], a[132:135], a[244:247], a[64:67]\n"
"ds_read_b64_tr_b16 a[198:199], v10 offset:9216\n"
"v_mfma_f32_16x16x32_bf16 a[96:99], a[132:135], a[252:255], a[96:99]\n"
"buffer_load_dwordx4 v15, s[16:19], s72 offen offset:1024 lds\n"
"v_mfma_f32_16x16x32_bf16 a[96:99], a[128:131], a[248:251], a[96:99]\n"
"ds_read_b64_tr_b16 a[200:201], v11 offset:0\n"
"v_mfma_f32_16x16x32_bf16 a[100:103], a[136:139], a[248:251], a[100:103]\n"
"ds_read_b64_tr_b16 a[202:203], v11 offset:1024\n"
"v_mfma_f32_16x16x32_bf16 a[100:103], a[140:143], a[252:255], a[100:103]\n"
"v_mfma_f32_16x16x32_bf16 a[68:71], a[140:143], a[244:247], a[68:71]\n"
"ds_read_b64_tr_b16 a[204:205], v11 offset:8192\n"
"v_mfma_f32_16x16x32_bf16 a[68:71], a[136:139], a[240:243], a[68:71]\n"
"buffer_load_dwordx4 v16, s[16:19], s72 offen offset:2048 lds\n"
"v_mfma_f32_16x16x32_bf16 a[36:39], a[136:139], a[232:235], a[36:39]\n"
"ds_read_b64_tr_b16 a[206:207], v11 offset:9216\n"
"v_mfma_f32_16x16x32_bf16 a[36:39], a[140:143], a[236:239], a[36:39]\n"
"v_mfma_f32_16x16x32_bf16 a[4:7], a[140:143], a[228:231], a[4:7]\n"
"ds_read_b64_tr_b16 a[208:209], v12 offset:0\n"
"v_mfma_f32_16x16x32_bf16 a[4:7], a[136:139], a[224:227], a[4:7]\n"
"v_mfma_f32_16x16x32_bf16 a[8:11], a[144:147], a[224:227], a[8:11]\n"
"ds_read_b64_tr_b16 a[210:211], v12 offset:1024\n"
"v_mfma_f32_16x16x32_bf16 a[8:11], a[148:151], a[228:231], a[8:11]\n"
"ds_read_b64_tr_b16 a[212:213], v12 offset:8192\n"
"v_mfma_f32_16x16x32_bf16 a[40:43], a[148:151], a[236:239], a[40:43]\n"
"buffer_load_dwordx4 v17, s[16:19], s72 offen offset:3072 lds\n"
"v_mfma_f32_16x16x32_bf16 a[40:43], a[144:147], a[232:235], a[40:43]\n"
"s_add_u32 m0, s74, s79\n"
"ds_read_b64_tr_b16 a[214:215], v12 offset:9216\n"
"v_mfma_f32_16x16x32_bf16 a[72:75], a[144:147], a[240:243], a[72:75]\n"
"v_mfma_f32_16x16x32_bf16 a[72:75], a[148:151], a[244:247], a[72:75]\n"
"ds_read_b64_tr_b16 a[216:217], v13 offset:0\n"
"v_mfma_f32_16x16x32_bf16 a[104:107], a[148:151], a[252:255], a[104:107]\n"
"buffer_load_dwordx4 v18, s[16:19], s72 offen offset:0 lds\n"
"v_mfma_f32_16x16x32_bf16 a[104:107], a[144:147], a[248:251], a[104:107]\n"
"ds_read_b64_tr_b16 a[218:219], v13 offset:1024\n"
"v_mfma_f32_16x16x32_bf16 a[108:111], a[152:155], a[248:251], a[108:111]\n"
"ds_read_b64_tr_b16 a[220:221], v13 offset:8192\n"
"v_mfma_f32_16x16x32_bf16 a[108:111], a[156:159], a[252:255], a[108:111]\n"
"v_mfma_f32_16x16x32_bf16 a[76:79], a[156:159], a[244:247], a[76:79]\n"
"ds_read_b64_tr_b16 a[222:223], v13 offset:9216\n"
"v_mfma_f32_16x16x32_bf16 a[76:79], a[152:155], a[240:243], a[76:79]\n"
"v_mfma_f32_16x16x32_bf16 a[44:47], a[152:155], a[232:235], a[44:47]\n"
"ds_read_b128 v[64:67], v8 offset:0\n"
"v_mfma_f32_16x16x32_bf16 a[44:47], a[156:159], a[236:239], a[44:47]\n"
"buffer_load_dwordx4 v19, s[16:19], s72 offen offset:1024 lds\n"
"v_mfma_f32_16x16x32_bf16 a[12:15], a[156:159], a[228:231], a[12:15]\n"
"ds_read_b128 v[68:71], v9 offset:0\n"
"v_mfma_f32_16x16x32_bf16 a[12:15], a[152:155], a[224:227], a[12:15]\n"
"v_mfma_f32_16x16x32_bf16 a[16:19], a[160:163], a[224:227], a[16:19]\n"
"ds_read_b128 v[72:75], v8 offset:2048\n"
"v_mfma_f32_16x16x32_bf16 a[16:19], a[164:167], a[228:231], a[16:19]\n"
"ds_read_b128 v[76:79], v9 offset:2048\n"
"v_mfma_f32_16x16x32_bf16 a[48:51], a[164:167], a[236:239], a[48:51]\n"
"buffer_load_dwordx4 v20, s[16:19], s72 offen offset:2048 lds\n"
"v_mfma_f32_16x16x32_bf16 a[48:51], a[160:163], a[232:235], a[48:51]\n"
"ds_read_b128 v[80:83], v8 offset:8192\n"
"v_mfma_f32_16x16x32_bf16 a[80:83], a[160:163], a[240:243], a[80:83]\n"
"v_mfma_f32_16x16x32_bf16 a[80:83], a[164:167], a[244:247], a[80:83]\n"
"ds_read_b128 v[84:87], v9 offset:8192\n"
"s_nop 0\n"
"PG1_%=:\n"
"s_cbranch_execz PADRET_%=\n"
"v_mfma_f32_16x16x32_bf16 a[112:115], a[164:167], a[252:255], a[112:115]\n"
"v_mfma_f32_16x16x32_bf16 a[112:115], a[160:163], a[248:251], a[112:115]\n"
"ds_read_b128 v[88:91], v8 offset:10240\n"
"v_mfma_f32_16x16x32_bf16 a[116:119], a[168:171], a[248:251], a[116:119]\n"
"ds_read_b128 v[92:95], v9 offset:10240\n"
"v_mfma_f32_16x16x32_bf16 a[116:119], a[172:175], a[252:255], a[116:119]\n"
"buffer_load_dwordx4 v21, s[16:19], s72 offen offset:3072 lds\n"
"v_mfma_f32_16x16x32_bf16 a[84:87], a[172:175], a[244:247], a[84:87]\n"
"s_add_u32 m0, s74, s80\n"
"ds_read_b128 v[96:99], v8 offset:16384\n"
"v_mfma_f32_16x16x32_bf16 a[84:87], a[168:171], a[240:243], a[84:87]\n"
"v_mfma_f32_16x16x32_bf16 a[52:55], a[168:171], a[232:235], a[52:55]\n"
"ds_read_b128 v[100:103], v9 offset:16384\n"
"v_mfma_f32_16x16x32_bf16 a[52:55], a[172:175], a[236:239], a[52:55]\n"
"buffer_load_dwordx4 v22, s[24:27], s73 offen offset:0 lds\n"
"v_mfma_f32_16x16x32_bf16 a[20:23], a[172:175], a[228:231], a[20:23]\n"
"ds_read_b128 v[104:107], v8 offset:18432\n"
"v_mfma_f32_16x16x32_bf16 a[20:23], a[168:171], a[224:227], a[20:23]\n"
"v_mfma_f32_16x16x32_bf16 a[24:27], a[176:179], a[224:227], a[24:27]\n"
"ds_read_b128 v[108:111], v9 offset:18432\n"
"v_mfma_f32_16x16x32_bf16 a[24:27], a[180:183], a[228:231], a[24:27]\n"
"ds_read_b128 v[112:115], v8 offset:24576\n"
"v_mfma_f32_16x16x32_bf16 a[56:59], a[180:183], a[236:239], a[56:59]\n"
"v_mfma_f32_16x16x32_bf16 a[56:59], a[176:179], a[232:235], a[56:59]\n"
"ds_read_b128 v[116:119], v9 offset:24576\n"
"v_mfma_f32_16x16x32_bf16 a[88:91], a[176:179], a[240:243], a[88:91]\n"
"buffer_load_dwordx4 v23, s[24:27], s73 offen offset:1024 lds\n"
"v_mfma_f32_16x16x32_bf16 a[88:91], a[180:183], a[244:247], a[88:91]\n"
"ds_read_b128 v[120:123], v8 offset:26624\n"
"v_mfma_f32_16x16x32_bf16 a[120:123], a[180:183], a[252:255], a[120:123]\n"
"v_mfma_f32_16x16x32_bf16 a[120:123], a[176:179], a[248:251], a[120:123]\n"
"ds_read_b128 v[124:127], v9 offset:26624\n"
"v_mfma_f32_16x16x32_bf16 a[124:127], a[184:187], a[248:251], a[124:127]\n"
"v_mfma_f32_16x16x32_bf16 a[124:127], a[188:191], a[252:255], a[124:127]\n"
"buffer_load_dwordx4 v30, s[24:27], s73 offen offset:2048 lds\n"
"v_mfma_f32_16x16x32_bf16 a[92:95], a[188:191], a[244:247], a[92:95]\n"
"v_mfma_f32_16x16x32_bf16 a[92:95], a[184:187], a[240:243], a[92:95]\n"
"v_mfma_f32_16x16x32_bf16 a[60:63], a[184:187], a[232:235], a[60:63]\n"
"v_mfma_f32_16x16x32_bf16 a[60:63], a[188:191], a[236:239], a[60:63]\n"
"v_mfma_f32_16x16x32_bf16 a[28:31], a[188:191], a[228:231], a[28:31]\n"
"v_mfma_f32_16x16x32_bf16 a[28:31], a[184:187], a[224:227], a[28:31]\n"
"s_waitcnt lgkmcnt(15)\n"
"v_mfma_f32_16x16x32_bf16 v[128:131], a[192:195], v[64:67], v[128:131]\n"
"s_add_u32 s72, s72, 128\n"
"s_add_u32 s95, s75, 2097152\n"
"ds_read_b64_tr_b16 a[224:225], v10 offset:16384\n"
"s_waitcnt lgkmcnt(15)\n"
"v_mfma_f32_16x16x32_bf16 v[128:131], a[196:199], v[68:71], v[128:131]\n"
"buffer_load_dwordx4 v31, s[24:27], s73 offen offset:3072 lds\n"
"v_mfma_f32_16x16x32_bf16 v[132:135], a[204:207], v[68:71], v[132:135]\n"
"s_add_u32 m0, s74, s81\n"
"s_xor_b32 s74, s74, 0x8000\n"
"ds_read_b64_tr_b16 a[226:227], v10 offset:17408\n"
"v_mfma_f32_16x16x32_bf16 v[132:135], a[200:203], v[64:67], v[132:135]\n"
"v_mfma_f32_16x16x32_bf16 v[136:139], a[208:211], v[64:67], v[136:139]\n"
"v_mfma_f32_16x16x32_bf16 v[136:139], a[212:215], v[68:71], v[136:139]\n"
"buffer_load_dwordx4 v22, s[28:31], s73 offen offset:0 lds\n"
"v_mfma_f32_16x16x32_bf16 v[140:143], a[220:223], v[68:71], v[140:143]\n"
"ds_read_b64_tr_b16 a[228:229], v10 offset:24576\n"
"v_mfma_f32_16x16x32_bf16 v[140:143], a[216:219], v[64:67], v[140:143]\n"
"s_waitcnt lgkmcnt(15)\n"
"v_mfma_f32_16x16x32_bf16 v[156:159], a[216:219], v[72:75], v[156:159]\n"
"s_waitcnt lgkmcnt(15)\n"
"ds_read_b64_tr_b16 a[230:231], v10 offset:25600\n"
"v_mfma_f32_16x16x32_bf16 v[156:159], a[220:223], v[76:79], v[156:159]\n"
"buffer_load_dwordx4 v23, s[28:31], s73 offen offset:1024 lds\n"
"v_mfma_f32_16x16x32_bf16 v[152:155], a[212:215], v[76:79], v[152:155]\n"
"v_mfma_f32_16x16x32_bf16 v[152:155], a[208:211], v[72:75], v[152:155]\n"
"ds_read_b64_tr_b16 a[232:233], v11 offset:16384\n"
"v_mfma_f32_16x16x32_bf16 v[148:151], a[200:203], v[72:75], v[148:151]\n"
"v_mfma_f32_16x16x32_bf16 v[148:151], a[204:207], v[76:79], v[148:151]\n"
"buffer_load_dwordx4 v30, s[28:31], s73 offen offset:2048 lds\n"
"v_mfma_f32_16x16x32_bf16 v[144:147], a[196:199], v[76:79], v[144:147]\n"
"ds_read_b64_tr_b16 a[234:235], v11 offset:17408\n"
"v_mfma_f32_16x16x32_bf16 v[144:147], a[192:195], v[72:75], v[144:147]\n"
"s_waitcnt lgkmcnt(15)\n"
"v_mfma_f32_16x16x32_bf16 v[160:163], a[192:195], v[80:83], v[160:163]\n"
"s_waitcnt lgkmcnt(15)\n"
"ds_read_b64_tr_b16 a[236:237], v11 offset:24576\n"
"v_mfma_f32_16x16x32_bf16 v[160:163], a[196:199], v[84:87], v[160:163]\n"
"v_mfma_f32_16x16x32_bf16 v[164:167], a[204:207], v[84:87], v[164:167]\n"
"buffer_load_dwordx4 v31, s[28:31], s73 offen offset:3072 lds\n"
"v_mfma_f32_16x16x32_bf16 v[164:167], a[200:203], v[80:83], v[164:167]\n"
"s_add_u32 s73, s73, 524288\n"
"ds_read_b64_tr_b16 a[238:239], v11 offset:25600\n"
"v_mfma_f32_16x16x32_bf16 v[168:171], a[208:211], v[80:83], v[168:171]\n"
"v_mfma_f32_16x16x32_bf16 v[168:171], a[212:215], v[84:87], v[168:171]\n"
"v_mfma_f32_16x16x32_bf16 v[172:175], a[220:223], v[84:87], v[172:175]\n"
"buffer_load_dword v61, v27, s[56:59], s89 offen\n"
"v_mfma_f32_16x16x32_bf16 v[172:175], a[216:219], v[80:83], v[172:175]\n"
"s_add_u32 s89, s89, s90\n"
"ds_read_b64_tr_b16 a[240:241], v12 offset:16384\n"
"s_waitcnt lgkmcnt(15)\n"
"v_mfma_f32_16x16x32_bf16 v[188:191], a[216:219], v[88:91], v[188:191]\n"
"s_waitcnt lgkmcnt(15)\n"
"v_mfma_f32_16x16x32_bf16 v[188:191], a[220:223], v[92:95], v[188:191]\n"
"ds_read_b64_tr_b16 a[242:243], v12 offset:17408\n"
"v_mfma_f32_16x16x32_bf16 v[184:187], a[212:215], v[92:95], v[184:187]\n"
"buffer_load_dword v62, v26, s[20:23], s95 offen\n"
"v_mfma_f32_16x16x32_bf16 v[184:187], a[208:211], v[88:91], v[184:187]\n"
"v_mfma_f32_16x16x32_bf16 v[180:183], a[200:203], v[88:91], v[180:183]\n"
"ds_read_b64_tr_b16 a[244:245], v12 offset:24576\n"
"v_mfma_f32_16x16x32_bf16 v[180:183], a[204:207], v[92:95], v[180:183]\n"
"v_mfma_f32_16x16x32_bf16 v[176:179], a[196:199], v[92:95], v[176:179]\n"
"ds_read_b64_tr_b16 a[246:247], v12 offset:25600\n"
"v_mfma_f32_16x16x32_bf16 v[176:179], a[192:195], v[88:91], v[176:179]\n"
"s_waitcnt lgkmcnt(15)\n"
"v_mfma_f32_16x16x32_bf16 v[192:195], a[192:195], v[96:99], v[192:195]\n"
"s_waitcnt lgkmcnt(15)\n"
"v_mfma_f32_16x16x32_bf16 v[192:195], a[196:199], v[100:103], v[192:195]\n"
"ds_read_b64_tr_b16 a[248:249], v13 offset:16384\n"
"v_mfma_f32_16x16x32_bf16 v[196:199], a[204:207], v[100:103], v[196:199]\n"
"v_mfma_f32_16x16x32_bf16 v[196:199], a[200:203], v[96:99], v[196:199]\n"
"v_mfma_f32_16x16x32_bf16 v[200:203], a[208:211], v[96:99], v[200:203]\n"
"ds_read_b64_tr_b16 a[250:251], v13 offset:17408\n"
"v_mfma_f32_16x16x32_bf16 v[200:203], a[212:215], v[100:103], v[200:203]\n"
"v_mfma_f32_16x16x32_bf16 v[204:207], a[220:223], v[100:103], v[204:207]\n"
"v_mfma_f32_16x16x32_bf16 v[204:207], a[216:219], v[96:99], v[204:207]\n"
"s_waitcnt lgkmcnt(15)\n"
"ds_read_b64_tr_b16 a[252:253], v13 offset:24576\n"
"v_mfma_f32_16x16x32_bf16 v[220:223], a[216:219], v[104:107], v[220:223]\n"
"s_waitcnt lgkmcnt(15)\n"
"v_mfma_f32_16x16x32_bf16 v[220:223], a[220:223], v[108:111], v[220:223]\n"
"v_mfma_f32_16x16x32_bf16 v[216:219], a[212:215], v[108:111], v[216:219]\n"
"ds_read_b64_tr_b16 a[254:255], v13 offset:25600\n"
"v_mfma_f32_16x16x32_bf16 v[216:219], a[208:211], v[104:107], v[216:219]\n"
"v_mfma_f32_16x16x32_bf16 v[212:215], a[200:203], v[104:107], v[212:215]\n"
"v_mfma_f32_16x16x32_bf16 v[212:215], a[204:207], v[108:111], v[212:215]\n"
"v_mfma_f32_16x16x32_bf16 v[208:211], a[196:199], v[108:111], v[208:211]\n"
"v_mfma_f32_16x16x32_bf16 v[208:211], a[192:195], v[104:107], v[208:211]\n"
"s_waitcnt lgkmcnt(15)\n"
"v_mfma_f32_16x16x32_bf16 v[224:227], a[192:195], v[112:115], v[224:227]\n"
"s_waitcnt lgkmcnt(15)\n"
"v_mfma_f32_16x16x32_bf16 v[224:227], a[196:199], v[116:119], v[224:227]\n"
"v_mfma_f32_16x16x32_bf16 v[228:231], a[204:207], v[116:119], v[228:231]\n"
"v_mfma_f32_16x16x32_bf16 v[228:231], a[200:203], v[112:115], v[228:231]\n"
"v_mfma_f32_16x16x32_bf16 v[232:235], a[208:211], v[112:115], v[232:235]\n"
"v_mfma_f32_16x16x32_bf16 v[232:235], a[212:215], v[116:119], v[232:235]\n"
"v_mfma_f32_16x16x32_bf16 v[236:239], a[220:223], v[116:119], v[236:239]\n"
"v_mfma_f32_16x16x32_bf16 v[236:239], a[216:219], v[112:115], v[236:239]\n"
"s_waitcnt lgkmcnt(15)\n"
"v_mfma_f32_16x16x32_bf16 v[252:255], a[216:219], v[120:123], v[252:255]\n"
"s_waitcnt lgkmcnt(15)\n"
"v_mfma_f32_16x16x32_bf16 v[252:255], a[220:223], v[124:127], v[252:255]\n"
"v_mfma_f32_16x16x32_bf16 v[248:251], a[212:215], v[124:127], v[248:251]\n"
"v_mfma_f32_16x16x32_bf16 v[248:251], a[208:211], v[120:123], v[248:251]\n"
"v_mfma_f32_16x16x32_bf16 v[244:247], a[200:203], v[120:123], v[244:247]\n"
"v_mfma_f32_16x16x32_bf16 v[244:247], a[204:207], v[124:127], v[244:247]\n"
"v_mfma_f32_16x16x32_bf16 v[240:243], a[196:199], v[124:127], v[240:243]\n"
"v_mfma_f32_16x16x32_bf16 v[240:243], a[192:195], v[120:123], v[240:243]\n"
"s_waitcnt vmcnt(2) lgkmcnt(0)\n"
"s_barrier\n"
"v_mfma_f32_16x16x32_bf16 a[0:3], v[64:67], a[224:227], a[0:3]\n"
"s_add_u32 m0, s74, s78\n"
"ds_read_b64_tr_b16 a[192:193], v10 offset:32768\n"
"v_readfirstlane_b32 s68, v56\n"
"v_mfma_f32_16x16x32_bf16 a[0:3], v[68:71], a[228:231], a[0:3]\n"
"buffer_load_dwordx4 v14, s[16:19], s72 offen offset:0 lds\n"
"v_mfma_f32_16x16x32_bf16 a[32:35], v[68:71], a[236:239], a[32:35]\n"
"s_xor_b32 s64, s68, 0xa5c3f00d\n"
"ds_read_b64_tr_b16 a[194:195], v10 offset:33792\n"
"v_lshlrev_b32 v40, 16, v32\n"
"v_mfma_f32_16x16x32_bf16 a[32:35], v[64:67], a[232:235], a[32:35]\n"
"s_xor_b32 s65, s68, 0x3c6ef372\n"
"ds_read_b64_tr_b16 a[196:197], v10 offset:40960\n"
"v_and_b32 v41, 0xffff0000, v32\n"
"v_mfma_f32_16x16x32_bf16 a[64:67], v[64:67], a[240:243], a[64:67]\n"
"s_add_u32 s65, s65, 0x9e3779b9\n"
"v_lshlrev_b32 v42, 16, v33\n"
"v_and_b32 v43, 0xffff0000, v33\n"
"v_mfma_f32_16x16x32_bf16 a[64:67], v[68:71], a[244:247], a[64:67]\n"
"ds_read_b64_tr_b16 a[198:199], v10 offset:41984\n"
"v_lshlrev_b32 v44, 16, v34\n"
"v_mfma_f32_16x16x32_bf16 a[96:99], v[68:71], a[252:255], a[96:99]\n"
"buffer_load_dwordx4 v15, s[16:19], s72 offen offset:1024 lds\n"
"v_mfma_f32_16x16x32_bf16 a[96:99], v[64:67], a[248:251], a[96:99]\n"
"ds_read_b64_tr_b16 a[200:201], v11 offset:32768\n"
"v_and_b32 v45, 0xffff0000, v34\n"
"v_mfma_f32_16x16x32_bf16 a[100:103], v[72:75], a[248:251], a[100:103]\n"
"ds_read_b64_tr_b16 a[202:203], v11 offset:33792\n"
"v_lshlrev_b32 v46, 16, v35\n"
"v_mfma_f32_16x16x32_bf16 a[100:103], v[76:79], a[252:255], a[100:103]\n"
"v_and_b32 v47, 0xffff0000, v35\n"
"v_lshlrev_b32 v48, 16, v36\n"
"v_mfma_f32_16x16x32_bf16 a[68:71], v[76:79], a[244:247], a[68:71]\n"
"ds_read_b64_tr_b16 a[204:205], v11 offset:40960\n"
"v_and_b32 v49, 0xffff0000, v36\n"
"v_mfma_f32_16x16x32_bf16 a[68:71], v[72:75], a[240:243], a[68:71]\n"
"buffer_load_dwordx4 v16, s[16:19], s72 offen offset:2048 lds\n"
"v_mfma_f32_16x16x32_bf16 a[36:39], v[72:75], a[232:235], a[36:39]\n"
"ds_read_b64_tr_b16 a[206:207], v11 offset:41984\n"
"v_lshlrev_b32 v50, 16, v37\n"
"v_mfma_f32_16x16x32_bf16 a[36:39], v[76:79], a[236:239], a[36:39]\n"
"v_and_b32 v51, 0xffff0000, v37\n"
"v_lshlrev_b32 v52, 16, v38\n"
"v_mfma_f32_16x16x32_bf16 a[4:7], v[76:79], a[228:231], a[4:7]\n"
"ds_read_b64_tr_b16 a[208:209], v12 offset:32768\n"
"v_and_b32 v53, 0xffff0000, v38\n"
"v_mfma_f32_16x16x32_bf16 a[4:7], v[72:75], a[224:227], a[4:7]\n"
"v_lshlrev_b32 v54, 16, v39\n"
"v_and_b32 v55, 0xffff0000, v39\n"
"v_mfma_f32_16x16x32_bf16 a[8:11], v[80:83], a[224:227], a[8:11]\n"
"ds_read_b64_tr_b16 a[210:211], v12 offset:33792\n"
"v_add_f32 v32, v40, v41\n"
"v_mfma_f32_16x16x32_bf16 a[8:11], v[84:87], a[228:231], a[8:11]\n"
"ds_read_b64_tr_b16 a[212:213], v12 offset:40960\n"
"v_mul_f32 v33, v40, v40\n"
"v_mfma_f32_16x16x32_bf16 a[40:43], v[84:87], a[236:239], a[40:43]\n"
"buffer_load_dwordx4 v17, s[16:19], s72 offen offset:3072 lds\n"
"v_mfma_f32_16x16x32_bf16 a[40:43], v[80:83], a[232:235], a[40:43]\n"
"s_add_u32 m0, s74, s79\n"
"ds_read_b64_tr_b16 a[214:215], v12 offset:41984\n"
"v_fmac_f32 v33, v41, v41\n"
"v_mfma_f32_16x16x32_bf16 a[72:75], v[80:83], a[240:243], a[72:75]\n"
"v_add_f32 v32, v32, v42\n"
"v_fmac_f32 v33, v42, v42\n"
"v_mfma_f32_16x16x32_bf16 a[72:75], v[84:87], a[244:247], a[72:75]\n"
"ds_read_b64_tr_b16 a[216:217], v13 offset:32768\n"
"v_add_f32 v32, v32, v43\n"
"v_mfma_f32_16x16x32_bf16 a[104:107], v[84:87], a[252:255], a[104:107]\n"
"buffer_load_dwordx4 v18, s[16:19], s72 offen offset:0 lds\n"
"v_mfma_f32_16x16x32_bf16 a[104:107], v[80:83], a[248:251], a[104:107]\n"
"ds_read_b64_tr_b16 a[218:219], v13 offset:33792\n"
"v_fmac_f32 v33, v43, v43\n"
"v_mfma_f32_16x16x32_bf16 a[108:111], v[88:91], a[248:251], a[108:111]\n"
"ds_read_b64_tr_b16 a[220:221], v13 offset:40960\n"
"v_add_f32 v32, v32, v44\n"
"v_mfma_f32_16x16x32_bf16 a[108:111], v[92:95], a[252:255], a[108:111]\n"
"v_fmac_f32 v33, v44, v44\n"
"v_add_f32 v32, v32, v45\n"
"v_mfma_f32_16x16x32_bf16 a[76:79], v[92:95], a[244:247], a[76:79]\n"
"ds_read_b64_tr_b16 a[222:223], v13 offset:41984\n"
"v_fmac_f32 v33, v45, v45\n"
"v_mfma_f32_16x16x32_bf16 a[76:79], v[88:91], a[240:243], a[76:79]\n"
"v_add_f32 v32, v32, v46\n"
"v_fmac_f32 v33, v46, v46\n"
"v_mfma_f32_16x16x32_bf16 a[44:47], v[88:91], a[232:235], a[44:47]\n"
"ds_read_b128 a[128:131], v8 offset:32768\n"
"v_add_f32 v32, v32, v47\n"
"v_mfma_f32_16x16x32_bf16 a[44:47], v[92:95], a[236:239], a[44:47]\n"
"buffer_load_dwordx4 v19, s[16:19], s72 offen offset:1024 lds\n"
"v_mfma_f32_16x16x32_bf16 a[12:15], v[92:95], a[228:231], a[12:15]\n"
"ds_read_b128 a[132:135], v9 offset:32768\n"
"v_fmac_f32 v33, v47, v47\n"
"v_mfma_f32_16x16x32_bf16 a[12:15], v[88:91], a[224:227], a[12:15]\n"
"v_add_f32 v32, v32, v48\n"
"v_fmac_f32 v33, v48, v48\n"
"v_mfma_f32_16x16x32_bf16 a[16:19], v[96:99], a[224:227], a[16:19]\n"
"ds_read_b128 a[136:139], v8 offset:34816\n"
"v_add_f32 v32, v32, v49\n"
"v_mfma_f32_16x16x32_bf16 a[16:19], v[100:103], a[228:231], a[16:19]\n"
"ds_read_b128 a[140:143], v9 offset:34816\n"
"v_fmac_f32 v33, v49, v49\n"
"v_mfma_f32_16x16x32_bf16 a[48:51], v[100:103], a[236:239], a[48:51]\n"
"buffer_load_dwordx4 v20, s[16:19], s72 offen offset:2048 lds\n"
"v_mfma_f32_16x16x32_bf16 a[48:51], v[96:99], a[232:235], a[48:51]\n"
"ds_read_b128 a[144:147], v8 offset:40960\n"
"v_add_f32 v32, v32, v50\n"
"v_mfma_f32_16x16x32_bf16 a[80:83], v[96:99], a[240:243], a[80:83]\n"
"v_fmac_f32 v33, v50, v50\n"
"v_add_f32 v32, v32, v51\n"
"v_mfma_f32_16x16x32_bf16 a[80:83], v[100:103], a[244:247], a[80:83]\n"
"ds_read_b128 a[148:151], v9 offset:40960\n"
"v_fmac_f32 v33, v51, v51\n"
"v_mfma_f32_16x16x32_bf16 a[112:115], v[100:103], a[252:255], a[112:115]\n"
"v_add_f32 v32, v32, v52\n"
"v_fmac_f32 v33, v52, v52\n"
"v_mfma_f32_16x16x32_bf16 a[112:115], v[96:99], a[248:251], a[112:115]\n"
"ds_read_b128 a[152:155], v8 offset:43008\n"
"v_add_f32 v32, v32, v53\n"
"v_mfma_f32_16x16x32_bf16 a[116:119], v[104:107], a[248:251], a[116:119]\n"
"ds_read_b128 a[156:159], v9 offset:43008\n"
"v_fmac_f32 v33, v53, v53\n"
"v_mfma_f32_16x16x32_bf16 a[116:119], v[108:111], a[252:255], a[116:119]\n"
"buffer_load_dwordx4 v21, s[16:19], s72 offen offset:3072 lds\n"
"v_mfma_f32_16x16x32_bf16 a[84:87], v[108:111], a[244:247], a[84:87]\n"
"s_add_u32 m0, s74, s80\n"
"ds_read_b128 a[160:163], v8 offset:49152\n"
"v_add_f32 v32, v32, v54\n"
"v_mfma_f32_16x16x32_bf16 a[84:87], v[104:107], a[240:243], a[84:87]\n"
"v_fmac_f32 v33, v54, v54\n"
"v_add_f32 v32, v32, v55\n"
"v_mfma_f32_16x16x32_bf16 a[52:55], v[104:107], a[232:235], a[52:55]\n"
"ds_read_b128 a[164:167], v9 offset:49152\n"
"v_fmac_f32 v33, v55, v55\n"
"v_mfma_f32_16x16x32_bf16 a[52:55], v[108:111], a[236:239], a[52:55]\n"
"buffer_load_dwordx4 v22, s[24:27], s73 offen offset:0 lds\n"
"v_mfma_f32_16x16x32_bf16 a[20:23], v[108:111], a[228:231], a[20:23]\n"
"ds_read_b128 a[168:171], v8 offset:51200\n"
"v_add_f32_dpp v32, v32, v32 row_ror:8 row_mask:0xf bank_mask:0xf\n"
"v_mfma_f32_16x16x32_bf16 a[20:23], v[104:107], a[224:227], a[20:23]\n"
"v_add_f32_dpp v33, v33, v33 row_ror:8 row_mask:0xf bank_mask:0xf\n"
"v_mov_b32 v34, v32\n"
"v_mfma_f32_16x16x32_bf16 a[24:27], v[112:115], a[224:227], a[24:27]\n"
"ds_read_b128 a[172:175], v9 offset:51200\n"
"v_mov_b32 v35, v33\n"
"v_mfma_f32_16x16x32_bf16 a[24:27], v[116:119], a[228:231], a[24:27]\n"
"ds_read_b128 a[176:179], v8 offset:57344\n"
"v_permlane16_swap_b32 v32, v34\n"
"v_mfma_f32_16x16x32_bf16 a[56:59], v[116:119], a[236:239], a[56:59]\n"
"v_permlane16_swap_b32 v33, v35\n"
"v_add_f32 v32, v32, v34\n"
"v_mfma_f32_16x16x32_bf16 a[56:59], v[112:115], a[232:235], a[56:59]\n"
"ds_read_b128 a[180:183], v9 offset:57344\n"
"v_add_f32 v33, v33, v35\n"
"v_mfma_f32_16x16x32_bf16 a[88:91], v[112:115], a[240:243], a[88:91]\n"
"buffer_load_dwordx4 v23, s[24:27], s73 offen offset:1024 lds\n"
"v_mfma_f32_16x16x32_bf16 a[88:91], v[116:119], a[244:247], a[88:91]\n"
"ds_read_b128 a[184:187], v8 offset:59392\n"
"v_mov_b32 v34, v32\n"
"v_mfma_f32_16x16x32_bf16 a[120:123], v[116:119], a[252:255], a[120:123]\n"
"v_mov_b32 v35, v33\n"
"v_permlane32_swap_b32 v32, v34\n"
"v_mfma_f32_16x16x32_bf16 a[120:123], v[112:115], a[248:251], a[120:123]\n"
"ds_read_b128 a[188:191], v9 offset:59392\n"
"v_permlane32_swap_b32 v33, v35\n"
"v_mfma_f32_16x16x32_bf16 a[124:127], v[120:123], a[248:251], a[124:127]\n"
"v_add_f32 v32, v32, v34\n"
"v_add_f32 v33, v33, v35\n"
"v_mfma_f32_16x16x32_bf16 a[124:127], v[124:127], a[252:255], a[124:127]\n"
"buffer_load_dwordx4 v30, s[24:27], s73 offen offset:2048 lds\n"
"v_mfma_f32_16x16x32_bf16 a[92:95], v[124:127], a[244:247], a[92:95]\n"
"ds_write_b64 v59, v[32:33]\n"
"v_mfma_f32_16x16x32_bf16 a[92:95], v[120:123], a[240:243], a[92:95]\n"
"v_mfma_f32_16x16x32_bf16 a[60:63], v[120:123], a[232:235], a[60:63]\n"
"v_mfma_f32_16x16x32_bf16 a[60:63], v[124:127], a[236:239], a[60:63]\n"
"v_mfma_f32_16x16x32_bf16 a[28:31], v[124:127], a[228:231], a[28:31]\n"
"v_mfma_f32_16x16x32_bf16 a[28:31], v[120:123], a[224:227], a[28:31]\n"
"s_waitcnt lgkmcnt(15)\n"
"v_mfma_f32_16x16x32_bf16 v[128:131], a[192:195], a[128:131], v[128:131]\n"
"s_add_u32 s72, s72, 128\n"
"s_add_u32 s95, s75, 0\n"
"ds_read_b64_tr_b16 a[224:225], v10 offset:49152\n"
"s_waitcnt lgkmcnt(15)\n"
"v_mfma_f32_16x16x32_bf16 v[128:131], a[196:199], a[132:135], v[128:131]\n"
"buffer_load_dwordx4 v31, s[24:27], s73 offen offset:3072 lds\n"
"v_mfma_f32_16x16x32_bf16 v[132:135], a[204:207], a[132:135], v[132:135]\n"
"s_add_u32 m0, s74, s81\n"
"s_xor_b32 s74, s74, 0x8000\n"
"ds_read_b64_tr_b16 a[226:227], v10 offset:50176\n"
"v_mfma_f32_16x16x32_bf16 v[132:135], a[200:203], a[128:131], v[132:135]\n"
"s_and_b32 s93, s62, 3\n"
"s_lshl_b32 s93, s93, 15\n"
"s_add_u32 s94, s82, s93\n"
"v_mfma_f32_16x16x32_bf16 v[136:139], a[208:211], a[128:131], v[136:139]\n"
"s_lshl_b32 s76, s61, 18\n"
"s_add_u32 s76, s76, s87\n"
"v_lshlrev_b32 v57, 7, v0\n"
"v_mfma_f32_16x16x32_bf16 v[136:139], a[212:215], a[132:135], v[136:139]\n"
"buffer_load_dwordx4 v22, s[28:31], s73 offen offset:0 lds\n"
"v_mfma_f32_16x16x32_bf16 v[140:143], a[220:223], a[132:135], v[140:143]\n"
"ds_read_b64_tr_b16 a[228:229], v10 offset:57344\n"
"v_mfma_f32_16x16x32_bf16 v[140:143], a[216:219], a[128:131], v[140:143]\n"
"s_waitcnt lgkmcnt(15)\n"
"v_mfma_f32_16x16x32_bf16 v[156:159], a[216:219], a[136:139], v[156:159]\n"
"s_waitcnt lgkmcnt(15)\n"
"ds_read_b64_tr_b16 a[230:231], v10 offset:58368\n"
"v_mfma_f32_16x16x32_bf16 v[156:159], a[220:223], a[140:143], v[156:159]\n"
"buffer_load_dwordx4 v23, s[28:31], s73 offen offset:1024 lds\n"
"v_mfma_f32_16x16x32_bf16 v[152:155], a[212:215], a[140:143], v[152:155]\n"
"v_mfma_f32_16x16x32_bf16 v[152:155], a[208:211], a[136:139], v[152:155]\n"
"ds_read_b64_tr_b16 a[232:233], v11 offset:49152\n"
"v_mfma_f32_16x16x32_bf16 v[148:151], a[200:203], a[136:139], v[148:151]\n"
"v_mfma_f32_16x16x32_bf16 v[148:151], a[204:207], a[140:143], v[148:151]\n"
"buffer_load_dwordx4 v30, s[28:31], s73 offen offset:2048 lds\n"
"v_mfma_f32_16x16x32_bf16 v[144:147], a[196:199], a[140:143], v[144:147]\n"
"ds_read_b64_tr_b16 a[234:235], v11 offset:50176\n"
"v_mfma_f32_16x16x32_bf16 v[144:147], a[192:195], a[136:139], v[144:147]\n"
"s_waitcnt lgkmcnt(15)\n"
"v_mfma_f32_16x16x32_bf16 v[160:163], a[192:195], a[144:147], v[160:163]\n"
"s_waitcnt lgkmcnt(15)\n"
"ds_read_b64_tr_b16 a[236:237], v11 offset:57344\n"
"v_mfma_f32_16x16x32_bf16 v[160:163], a[196:199], a[148:151], v[160:163]\n"
"v_mfma_f32_16x16x32_bf16 v[164:167], a[204:207], a[148:151], v[164:167]\n"
"buffer_load_dwordx4 v31, s[28:31], s73 offen offset:3072 lds\n"
"v_mfma_f32_16x16x32_bf16 v[164:167], a[200:203], a[144:147], v[164:167]\n"
"s_add_u32 s73, s73, 524288\n"
"ds_read_b64_tr_b16 a[238:239], v11 offset:58368\n"
"v_mfma_f32_16x16x32_bf16 v[168:171], a[208:211], a[144:147], v[168:171]\n"
"v_mfma_f32_16x16x32_bf16 v[168:171], a[212:215], a[148:151], v[168:171]\n"
"v_mfma_f32_16x16x32_bf16 v[172:175], a[220:223], a[148:151], v[172:175]\n"
"buffer_load_dword v61, v27, s[56:59], s89 offen\n"
"v_mfma_f32_16x16x32_bf16 v[172:175], a[216:219], a[144:147], v[172:175]\n"
"s_add_u32 s89, s89, s90\n"
"ds_read_b64_tr_b16 a[240:241], v12 offset:49152\n"
"s_waitcnt lgkmcnt(15)\n"
"v_mfma_f32_16x16x32_bf16 v[188:191], a[216:219], a[152:155], v[188:191]\n"
"s_waitcnt lgkmcnt(15)\n"
"v_mfma_f32_16x16x32_bf16 v[188:191], a[220:223], a[156:159], v[188:191]\n"
"ds_read_b64_tr_b16 a[242:243], v12 offset:50176\n"
"v_mfma_f32_16x16x32_bf16 v[184:187], a[212:215], a[156:159], v[184:187]\n"
"buffer_load_dword v62, v26, s[20:23], s95 offen\n"
"v_mfma_f32_16x16x32_bf16 v[184:187], a[208:211], a[152:155], v[184:187]\n"
"v_mfma_f32_16x16x32_bf16 v[180:183], a[200:203], a[152:155], v[180:183]\n"
"ds_read_b64_tr_b16 a[244:245], v12 offset:57344\n"
"v_mfma_f32_16x16x32_bf16 v[180:183], a[204:207], a[156:159], v[180:183]\n"
"v_mfma_f32_16x16x32_bf16 v[176:179], a[196:199], a[156:159], v[176:179]\n"
"ds_read_b64_tr_b16 a[246:247], v12 offset:58368\n"
"v_mfma_f32_16x16x32_bf16 v[176:179], a[192:195], a[152:155], v[176:179]\n"
"s_waitcnt lgkmcnt(15)\n"
"v_mfma_f32_16x16x32_bf16 v[192:195], a[192:195], a[160:163], v[192:195]\n"
"s_waitcnt lgkmcnt(15)\n"
"v_mfma_f32_16x16x32_bf16 v[192:195], a[196:199], a[164:167], v[192:195]\n"
"ds_read_b64_tr_b16 a[248:249], v13 offset:49152\n"
"v_mfma_f32_16x16x32_bf16 v[196:199], a[204:207], a[164:167], v[196:199]\n"
"v_mfma_f32_16x16x32_bf16 v[196:199], a[200:203], a[160:163], v[196:199]\n"
"v_mfma_f32_16x16x32_bf16 v[200:203], a[208:211], a[160:163], v[200:203]\n"
"ds_read_b64_tr_b16 a[250:251], v13 offset:50176\n"
"v_mfma_f32_16x16x32_bf16 v[200:203], a[212:215], a[164:167], v[200:203]\n"
"v_mfma_f32_16x16x32_bf16 v[204:207], a[220:223], a[164:167], v[204:207]\n"
"v_mfma_f32_16x16x32_bf16 v[204:207], a[216:219], a[160:163], v[204:207]\n"
"s_waitcnt lgkmcnt(15)\n"
"ds_read_b64_tr_b16 a[252:253], v13 offset:57344\n"
"v_mfma_f32_16x16x32_bf16 v[220:223], a[216:219], a[168:171], v[220:223]\n"
"s_waitcnt lgkmcnt(15)\n"
"v_mfma_f32_16x16x32_bf16 v[220:223], a[220:223], a[172:175], v[220:223]\n"
"v_mfma_f32_16x16x32_bf16 v[216:219], a[212:215], a[172:175], v[216:219]\n"
"ds_read_b64_tr_b16 a[254:255], v13 offset:58368\n"
"v_mfma_f32_16x16x32_bf16 v[216:219], a[208:211], a[168:171], v[216:219]\n"
"v_mfma_f32_16x16x32_bf16 v[212:215], a[200:203], a[168:171], v[212:215]\n"
"v_mfma_f32_16x16x32_bf16 v[212:215], a[204:207], a[172:175], v[212:215]\n"
"v_mfma_f32_16x16x32_bf16 v[208:211], a[196:199], a[172:175], v[208:211]\n"
"v_mfma_f32_16x16x32_bf16 v[208:211], a[192:195], a[168:171], v[208:211]\n"
"s_waitcnt lgkmcnt(15)\n"
"v_mfma_f32_16x16x32_bf16 v[224:227], a[192:195], a[176:179], v[224:227]\n"
"s_waitcnt lgkmcnt(15)\n"
"v_mfma_f32_16x16x32_bf16 v[224:227], a[196:199], a[180:183], v[224:227]\n"
"v_mfma_f32_16x16x32_bf16 v[228:231], a[204:207], a[180:183], v[228:231]\n"
"v_mfma_f32_16x16x32_bf16 v[228:231], a[200:203], a[176:179], v[228:231]\n"
"v_mfma_f32_16x16x32_bf16 v[232:235], a[208:211], a[176:179], v[232:235]\n"
"v_mfma_f32_16x16x32_bf16 v[232:235], a[212:215], a[180:183], v[232:235]\n"
"v_mfma_f32_16x16x32_bf16 v[236:239], a[220:223], a[180:183], v[236:239]\n"
"v_mfma_f32_16x16x32_bf16 v[236:239], a[216:219], a[176:179], v[236:239]\n"
"s_waitcnt lgkmcnt(15)\n"
"v_mfma_f32_16x16x32_bf16 v[252:255], a[216:219], a[184:187], v[252:255]\n"
"s_waitcnt lgkmcnt(15)\n"
"v_mfma_f32_16x16x32_bf16 v[252:255], a[220:223], a[188:191], v[252:255]\n"
"v_mfma_f32_16x16x32_bf16 v[248:251], a[212:215], a[188:191], v[248:251]\n"
"v_mfma_f32_16x16x32_bf16 v[248:251], a[208:211], a[184:187], v[248:251]\n"
"v_mfma_f32_16x16x32_bf16 v[244:247], a[200:203], a[184:187], v[244:247]\n"
"v_mfma_f32_16x16x32_bf16 v[244:247], a[204:207], a[188:191], v[244:247]\n"
"v_mfma_f32_16x16x32_bf16 v[240:243], a[196:199], a[188:191], v[240:243]\n"
"v_mfma_f32_16x16x32_bf16 v[240:243], a[192:195], a[184:187], v[240:243]\n"
"s_waitcnt vmcnt(2) lgkmcnt(0)\n"
"s_barrier\n"
"v_mfma_f32_16x16x32_bf16 a[0:3], a[128:131], a[224:227], a[0:3]\n"
"s_add_u32 m0, s74, s78\n"
"ds_read_b128 v[32:35], v60\n"
"ds_read_b128 v[36:39], v60 offset:16\n"
"v_mfma_f32_16x16x32_bf16 a[0:3], a[132:135], a[228:231], a[0:3]\n"
"ds_read_b64_tr_b16 a[192:193], v10 offset:0\n"
"ds_read_b64_tr_b16 a[194:195], v10 offset:1024\n"
"v_mfma_f32_16x16x32_bf16 a[32:35], a[132:135], a[236:239], a[32:35]\n"
"buffer_load_dwordx4 v14, s[16:19], s72 offen offset:0 lds\n"
"v_mfma_f32_16x16x32_bf16 a[32:35], a[128:131], a[232:235], a[32:35]\n"
"ds_read_b64_tr_b16 a[196:197], v10 offset:8192\n"
"v_mfma_f32_16x16x32_bf16 a[64:67], a[128:131], a[240:243], a[64:67]\n"
"v_mfma_f32_16x16x32_bf16 a[64:67], a[132:135], a[244:247], a[64:67]\n"
"ds_read_b64_tr_b16 a[198:199], v10 offset:9216\n"
"v_mfma_f32_16x16x32_bf16 a[96:99], a[132:135], a[252:255], a[96:99]\n"
"buffer_load_dwordx4 v15, s[16:19], s72 offen offset:1024 lds\n"
"v_mfma_f32_16x16x32_bf16 a[96:99], a[128:131], a[248:251], a[96:99]\n"
"ds_read_b64_tr_b16 a[200:201], v11 offset:0\n"
"s_waitcnt lgkmcnt(6)\n"
"v_add_f32 v32, v32, v34\n"
"v_mfma_f32_16x16x32_bf16 a[100:103], a[136:139], a[248:251], a[100:103]\n"
"ds_read_b64_tr_b16 a[202:203], v11 offset:1024\n"
"v_add_f32 v33, v33, v35\n"
"v_mfma_f32_16x16x32_bf16 a[100:103], a[140:143], a[252:255], a[100:103]\n"
"s_waitcnt lgkmcnt(6)\n"
"v_add_f32 v36, v36, v38\n"
"v_add_f32 v37, v37, v39\n"
"v_mfma_f32_16x16x32_bf16 a[68:71], a[140:143], a[244:247], a[68:71]\n"
"ds_read_b64_tr_b16 a[204:205], v11 offset:8192\n"
"v_add_f32 v32, v32, v36\n"
"v_mfma_f32_16x16x32_bf16 a[68:71], a[136:139], a[240:243], a[68:71]\n"
"buffer_load_dwordx4 v16, s[16:19], s72 offen offset:2048 lds\n"
"v_mfma_f32_16x16x32_bf16 a[36:39], a[136:139], a[232:235], a[36:39]\n"
"ds_read_b64_tr_b16 a[206:207], v11 offset:9216\n"
"v_add_f32 v33, v33, v37\n"
"v_mfma_f32_16x16x32_bf16 a[36:39], a[140:143], a[236:239], a[36:39]\n"
"v_mul_f32 v32, 0x3b000000, v32\n"
"v_mul_f32 v33, 0x3b000000, v33\n"
"v_mfma_f32_16x16x32_bf16 a[4:7], a[140:143], a[228:231], a[4:7]\n"
"ds_read_b64_tr_b16 a[208:209], v12 offset:0\n"
"v_fma_f32 v33, -v32, v32, v33\n"
"v_mfma_f32_16x16x32_bf16 a[4:7], a[136:139], a[224:227], a[4:7]\n"
"v_max_f32 v33, 0, v33\n"
"v_add_f32 v33, 0x3727c5ac, v33\n"
"v_mfma_f32_16x16x32_bf16 a[8:11], a[144:147], a[224:227], a[8:11]\n"
"ds_read_b64_tr_b16 a[210:211], v12 offset:1024\n"
"v_mfma_f32_16x16x32_bf16 a[8:11], a[148:151], a[228:231], a[8:11]\n"
"ds_read_b64_tr_b16 a[212:213], v12 offset:8192\n"
"v_mfma_f32_16x16x32_bf16 a[40:43], a[148:151], a[236:239], a[40:43]\n"
"buffer_load_dwordx4 v17, s[16:19], s72 offen offset:3072 lds\n"
"v_mfma_f32_16x16x32_bf16 a[40:43], a[144:147], a[232:235], a[40:43]\n"
"s_add_u32 m0, s74, s79\n"
"ds_read_b64_tr_b16 a[214:215], v12 offset:9216\n"
"v_mfma_f32_16x16x32_bf16 a[72:75], a[144:147], a[240:243], a[72:75]\n"
"v_rsq_f32 v33, v33\n"
"v_mfma_f32_16x16x32_bf16 a[72:75], a[148:151], a[244:247], a[72:75]\n"
"ds_read_b64_tr_b16 a[216:217], v13 offset:0\n"
"v_mul_f32 v33, 0x3e0293ee, v33\n"
"v_mfma_f32_16x16x32_bf16 a[104:107], a[148:151], a[252:255], a[104:107]\n"
"buffer_load_dwordx4 v18, s[16:19], s72 offen offset:0 lds\n"
"v_mfma_f32_16x16x32_bf16 a[104:107], a[144:147], a[248:251], a[104:107]\n"
"ds_read_b64_tr_b16 a[218:219], v13 offset:1024\n"
"v_mul_f32 v32, v32, v33\n"
"v_mfma_f32_16x16x32_bf16 a[108:111], a[152:155], a[248:251], a[108:111]\n"
"ds_read_b64_tr_b16 a[220:221], v13 offset:8192\n"
"v_fma_f32 v40, v40, v33, -v32\n"
"v_mfma_f32_16x16x32_bf16 a[108:111], a[156:159], a[252:255], a[108:111]\n"
"v_fma_f32 v41, v41, v33, -v32\n"
"v_fma_f32 v42, v42, v33, -v32\n"
"v_mfma_f32_16x16x32_bf16 a[76:79], a[156:159], a[244:247], a[76:79]\n"
"ds_read_b64_tr_b16 a[222:223], v13 offset:9216\n"
"v_fma_f32 v43, v43, v33, -v32\n"
"v_mfma_f32_16x16x32_bf16 a[76:79], a[152:155], a[240:243], a[76:79]\n"
"v_fma_f32 v44, v44, v33, -v32\n"
"v_fma_f32 v45, v45, v33, -v32\n"
"v_mfma_f32_16x16x32_bf16 a[44:47], a[152:155], a[232:235], a[44:47]\n"
"ds_read_b128 v[64:67], v8 offset:0\n"
"v_fma_f32 v46, v46, v33, -v32\n"
"v_mfma_f32_16x16x32_bf16 a[44:47], a[156:159], a[236:239], a[44:47]\n"
"buffer_load_dwordx4 v19, s[16:19], s72 offen offset:1024 lds\n"
"v_mfma_f32_16x16x32_bf16 a[12:15], a[156:159], a[228:231], a[12:15]\n"
"ds_read_b128 v[68:71], v9 offset:0\n"
"v_fma_f32 v47, v47, v33, -v32\n"
"v_mfma_f32_16x16x32_bf16 a[12:15], a[152:155], a[224:227], a[12:15]\n"
"v_cvt_pk_bf16_f32 v40, v40, v41\n"
"v_cvt_pk_bf16_f32 v41, v42, v43\n"
"v_mfma_f32_16x16x32_bf16 a[16:19], a[160:163], a[224:227], a[16:19]\n"
"ds_read_b128 v[72:75], v8 offset:2048\n"
"v_cvt_pk_bf16_f32 v42, v44, v45\n"
"v_mfma_f32_16x16x32_bf16 a[16:19], a[164:167], a[228:231], a[16:19]\n"
"ds_read_b128 v[76:79], v9 offset:2048\n"
"v_cvt_pk_bf16_f32 v43, v46, v47\n"
"v_mfma_f32_16x16x32_bf16 a[48:51], a[164:167], a[236:239], a[48:51]\n"
"buffer_load_dwordx4 v20, s[16:19], s72 offen offset:2048 lds\n"
"v_mfma_f32_16x16x32_bf16 a[48:51], a[160:163], a[232:235], a[48:51]\n"
"ds_read_b128 v[80:83], v8 offset:8192\n"
"v_fma_f32 v48, v48, v33, -v32\n"
"v_mfma_f32_16x16x32_bf16 a[80:83], a[160:163], a[240:243], a[80:83]\n"
"v_fma_f32 v49, v49, v33, -v32\n"
"v_fma_f32 v50, v50, v33, -v32\n"
"v_mfma_f32_16x16x32_bf16 a[80:83], a[164:167], a[244:247], a[80:83]\n"
"ds_read_b128 v[84:87], v9 offset:8192\n"
"v_fma_f32 v51, v51, v33, -v32\n"
"v_mfma_f32_16x16x32_bf16 a[112:115], a[164:167], a[252:255], a[112:115]\n"
"v_fma_f32 v52, v52, v33, -v32\n"
"v_fma_f32 v53, v53, v33, -v32\n"
"v_mfma_f32_16x16x32_bf16 a[112:115], a[160:163], a[248:251], a[112:115]\n"
"ds_read_b128 v[88:91], v8 offset:10240\n"
"v_fma_f32 v54, v54, v33, -v32\n"
"v_mfma_f32_16x16x32_bf16 a[116:119], a[168:171], a[248:251], a[116:119]\n"
"ds_read_b128 v[92:95], v9 offset:10240\n"
"v_fma_f32 v55, v55, v33, -v32\n"
"v_mfma_f32_16x16x32_bf16 a[116:119], a[172:175], a[252:255], a[116:119]\n"
"buffer_load_dwordx4 v21, s[16:19], s72 offen offset:3072 lds\n"
"v_mfma_f32_16x16x32_bf16 a[84:87], a[172:175], a[244:247], a[84:87]\n"
"s_add_u32 m0, s74, s80\n"
"ds_read_b128 v[96:99], v8 offset:16384\n"
"v_cvt_pk_bf16_f32 v44, v48, v49\n"
"v_mfma_f32_16x16x32_bf16 a[84:87], a[168:171], a[240:243], a[84:87]\n"
"v_cvt_pk_bf16_f32 v45, v50, v51\n"
"v_cvt_pk_bf16_f32 v46, v52, v53\n"
"v_mfma_f32_16x16x32_bf16 a[52:55], a[168:171], a[232:235], a[52:55]\n"
"ds_read_b128 v[100:103], v9 offset:16384\n"
"v_cvt_pk_bf16_f32 v47, v54, v55\n"
"v_mfma_f32_16x16x32_bf16 a[52:55], a[172:175], a[236:239], a[52:55]\n"
"PG2_%=:\n"
"s_cbranch_execz PADRET_%=\n"
"buffer_store_dwordx4 v[40:43], v58, s[44:47], s88 offen offset:0 sc1\n"
"v_mfma_f32_16x16x32_bf16 a[20:23], a[172:175], a[228:231], a[20:23]\n"
"buffer_store_dwordx4 v[44:47], v58, s[44:47], s88 offen offset:2048 sc1\n"
"v_mfma_f32_16x16x32_bf16 a[20:23], a[168:171], a[224:227], a[20:23]\n"
"buffer_load_dwordx4 v22, s[24:27], s73 offen offset:0 lds\n"
"v_mfma_f32_16x16x32_bf16 a[24:27], a[176:179], a[224:227], a[24:27]\n"
"ds_read_b128 v[104:107], v8 offset:18432\n"
"ds_read_b128 v[108:111], v9 offset:18432\n"
"v_mfma_f32_16x16x32_bf16 a[24:27], a[180:183], a[228:231], a[24:27]\n"
"ds_read_b128 v[112:115], v8 offset:24576\n"
"v_mfma_f32_16x16x32_bf16 a[56:59], a[180:183], a[236:239], a[56:59]\n"
"v_mfma_f32_16x16x32_bf16 a[56:59], a[176:179], a[232:235], a[56:59]\n"
"ds_read_b128 v[116:119], v9 offset:24576\n"
"v_mfma_f32_16x16x32_bf16 a[88:91], a[176:179], a[240:243], a[88:91]\n"
"buffer_load_dwordx4 v23, s[24:27], s73 offen offset:1024 lds\n"
"v_mfma_f32_16x16x32_bf16 a[88:91], a[180:183], a[244:247], a[88:91]\n"
"ds_read_b128 v[120:123], v8 offset:26624\n"
"v_mfma_f32_16x16x32_bf16 a[120:123], a[180:183], a[252:255], a[120:123]\n"
"v_mfma_f32_16x16x32_bf16 a[120:123], a[176:179], a[248:251], a[120:123]\n"
"ds_read_b128 v[124:127], v9 offset:26624\n"
"v_mfma_f32_16x16x32_bf16 a[124:127], a[184:187], a[248:251], a[124:127]\n"
"v_mfma_f32_16x16x32_bf16 a[124:127], a[188:191], a[252:255], a[124:127]\n"
"buffer_load_dwordx4 v30, s[24:27], s73 offen offset:2048 lds\n"
"v_mfma_f32_16x16x32_bf16 a[92:95], a[188:191], a[244:247], a[92:95]\n"
"v_mfma_f32_16x16x32_bf16 a[92:95], a[184:187], a[240:243], a[92:95]\n"
"v_mfma_f32_16x16x32_bf16 a[60:63], a[184:187], a[232:235], a[60:63]\n"
"v_mfma_f32_16x16x32_bf16 a[60:63], a[188:191], a[236:239], a[60:63]\n"
"v_mfma_f32_16x16x32_bf16 a[28:31], a[188:191], a[228:231], a[28:31]\n"
"v_mfma_f32_16x16x32_bf16 a[28:31], a[184:187], a[224:227], a[28:31]\n"
"s_waitcnt lgkmcnt(15)\n"
"v_mfma_f32_16x16x32_bf16 v[128:131], a[192:195], v[64:67], v[128:131]\n"
"s_add_u32 s72, s72, 128\n"
"ds_read_b64_tr_b16 a[224:225], v10 offset:16384\n"
"s_waitcnt lgkmcnt(15)\n"
"v_mfma_f32_16x16x32_bf16 v[128:131], a[196:199], v[68:71], v[128:131]\n"
"buffer_load_dwordx4 v31, s[24:27], s73 offen offset:3072 lds\n"
"v_mfma_f32_16x16x32_bf16 v[132:135], a[204:207], v[68:71], v[132:135]\n"
"s_add_u32 m0, s74, s81\n"
"s_xor_b32 s74, s74, 0x8000\n"
"ds_read_b64_tr_b16 a[226:227], v10 offset:17408\n"
"v_mfma_f32_16x16x32_bf16 v[132:135], a[200:203], v[64:67], v[132:135]\n"
"v_mfma_f32_16x16x32_bf16 v[136:139], a[208:211], v[64:67], v[136:139]\n"
"v_mfma_f32_16x16x32_bf16 v[136:139], a[212:215], v[68:71], v[136:139]\n"
"ds_read_b64_tr_b16 a[228:229], v10 offset:24576\n"
"v_mfma_f32_16x16x32_bf16 v[140:143], a[220:223], v[68:71], v[140:143]\n"
"buffer_load_dwordx4 v22, s[28:31], s73 offen offset:0 lds\n"
"v_mfma_f32_16x16x32_bf16 v[140:143], a[216:219], v[64:67], v[140:143]\n"
"s_waitcnt lgkmcnt(15)\n"
"v_mfma_f32_16x16x32_bf16 v[156:159], a[216:219], v[72:75], v[156:159]\n"
"s_waitcnt lgkmcnt(15)\n"
"ds_read_b64_tr_b16 a[230:231], v10 offset:25600\n"
"v_mfma_f32_16x16x32_bf16 v[156:159], a[220:223], v[76:79], v[156:159]\n"
"v_mfma_f32_16x16x32_bf16 v[152:155], a[212:215], v[76:79], v[152:155]\n"
"v_mfma_f32_16x16x32_bf16 v[152:155], a[208:211], v[72:75], v[152:155]\n"
"buffer_load_dwordx4 v23, s[28:31], s73 offen offset:1024 lds\n"
"v_mfma_f32_16x16x32_bf16 v[148:151], a[200:203], v[72:75], v[148:151]\n"
"ds_read_b64_tr_b16 a[232:233], v11 offset:16384\n"
"v_mfma_f32_16x16x32_bf16 v[148:151], a[204:207], v[76:79], v[148:151]\n"
"v_mfma_f32_16x16x32_bf16 v[144:147], a[196:199], v[76:79], v[144:147]\n"
"ds_read_b64_tr_b16 a[234:235], v11 offset:17408\n"
"v_mfma_f32_16x16x32_bf16 v[144:147], a[192:195], v[72:75], v[144:147]\n"
"s_waitcnt lgkmcnt(15)\n"
"v_mfma_f32_16x16x32_bf16 v[160:163], a[192:195], v[80:83], v[160:163]\n"
"buffer_load_dwordx4 v30, s[28:31], s73 offen offset:2048 lds\n"
"s_waitcnt lgkmcnt(15)\n"
"v_mfma_f32_16x16x32_bf16 v[160:163], a[196:199], v[84:87], v[160:163]\n"
"ds_read_b64_tr_b16 a[236:237], v11 offset:24576\n"
"v_mfma_f32_16x16x32_bf16 v[164:167], a[204:207], v[84:87], v[164:167]\n"
"v_mfma_f32_16x16x32_bf16 v[164:167], a[200:203], v[80:83], v[164:167]\n"
"ds_read_b64_tr_b16 a[238:239], v11 offset:25600\n"
"v_mfma_f32_16x16x32_bf16 v[168:171], a[208:211], v[80:83], v[168:171]\n"
"v_mfma_f32_16x16x32_bf16 v[168:171], a[212:215], v[84:87], v[168:171]\n"
"buffer_load_dwordx4 v31, s[28:31], s73 offen offset:3072 lds\n"
"v_mfma_f32_16x16x32_bf16 v[172:175], a[220:223], v[84:87], v[172:175]\n"
"s_add_u32 s73, s73, 524288\n"
"ds_read_b64_tr_b16 a[240:241], v12 offset:16384\n"
"v_mfma_f32_16x16x32_bf16 v[172:175], a[216:219], v[80:83], v[172:175]\n"
"s_waitcnt lgkmcnt(15)\n"
"v_mfma_f32_16x16x32_bf16 v[188:191], a[216:219], v[88:91], v[188:191]\n"
"s_waitcnt lgkmcnt(15)\n"
"v_mfma_f32_16x16x32_bf16 v[188:191], a[220:223], v[92:95], v[188:191]\n"
"ds_read_b64_tr_b16 a[242:243], v12 offset:17408\n"
"v_mfma_f32_16x16x32_bf16 v[184:187], a[212:215], v[92:95], v[184:187]\n"
"buffer_load_dword v61, v27, s[56:59], s89 offen\n"
"v_mfma_f32_16x16x32_bf16 v[184:187], a[208:211], v[88:91], v[184:187]\n"
"s_add_u32 s89, s89, s90\n"
"v_mfma_f32_16x16x32_bf16 v[180:183], a[200:203], v[88:91], v[180:183]\n"
"ds_read_b64_tr_b16 a[244:245], v12 offset:24576\n"
"v_mfma_f32_16x16x32_bf16 v[180:183], a[204:207], v[92:95], v[180:183]\n"
"v_mfma_f32_16x16x32_bf16 v[176:179], a[196:199], v[92:95], v[176:179]\n"
"ds_read_b64_tr_b16 a[246:247], v12 offset:25600\n"
"v_mfma_f32_16x16x32_bf16 v[176:179], a[192:195], v[88:91], v[176:179]\n"
"s_waitcnt lgkmcnt(15)\n"
"v_mfma_f32_16x16x32_bf16 v[192:195], a[192:195], v[96:99], v[192:195]\n"
"s_waitcnt lgkmcnt(15)\n"
"v_mfma_f32_16x16x32_bf16 v[192:195], a[196:199], v[100:103], v[192:195]\n"
"ds_read_b64_tr_b16 a[248:249], v13 offset:16384\n"
"v_mfma_f32_16x16x32_bf16 v[196:199], a[204:207], v[100:103], v[196:199]\n"
"v_mfma_f32_16x16x32_bf16 v[196:199], a[200:203], v[96:99], v[196:199]\n"
"v_mfma_f32_16x16x32_bf16 v[200:203], a[208:211], v[96:99], v[200:203]\n"
"ds_read_b64_tr_b16 a[250:251], v13 offset:17408\n"
"v_mfma_f32_16x16x32_bf16 v[200:203], a[212:215], v[100:103], v[200:203]\n"
"v_mfma_f32_16x16x32_bf16 v[204:207], a[220:223], v[100:103], v[204:207]\n"
"v_mfma_f32_16x16x32_bf16 v[204:207], a[216:219], v[96:99], v[204:207]\n"
"s_waitcnt lgkmcnt(15)\n"
"ds_read_b64_tr_b16 a[252:253], v13 offset:24576\n"
"v_mfma_f32_16x16x32_bf16 v[220:223], a[216:219], v[104:107], v[220:223]\n"
"s_waitcnt lgkmcnt(15)\n"
"v_mfma_f32_16x16x32_bf16 v[220:223], a[220:223], v[108:111], v[220:223]\n"
"v_mfma_f32_16x16x32_bf16 v[216:219], a[212:215], v[108:111], v[216:219]\n"
"ds_read_b64_tr_b16 a[254:255], v13 offset:25600\n"
"v_mfma_f32_16x16x32_bf16 v[216:219], a[208:211], v[104:107], v[216:219]\n"
"v_mfma_f32_16x16x32_bf16 v[212:215], a[200:203], v[104:107], v[212:215]\n"
"v_mfma_f32_16x16x32_bf16 v[212:215], a[204:207], v[108:111], v[212:215]\n"
"v_mfma_f32_16x16x32_bf16 v[208:211], a[196:199], v[108:111], v[208:211]\n"
"v_mfma_f32_16x16x32_bf16 v[208:211], a[192:195], v[104:107], v[208:211]\n"
"s_waitcnt lgkmcnt(15)\n"
"v_mfma_f32_16x16x32_bf16 v[224:227], a[192:195], v[112:115], v[224:227]\n"
"s_waitcnt lgkmcnt(15)\n"
"v_mfma_f32_16x16x32_bf16 v[224:227], a[196:199], v[116:119], v[224:227]\n"
"v_mfma_f32_16x16x32_bf16 v[228:231], a[204:207], v[116:119], v[228:231]\n"
"v_mfma_f32_16x16x32_bf16 v[228:231], a[200:203], v[112:115], v[228:231]\n"
"v_mfma_f32_16x16x32_bf16 v[232:235], a[208:211], v[112:115], v[232:235]\n"
"v_mfma_f32_16x16x32_bf16 v[232:235], a[212:215], v[116:119], v[232:235]\n"
"v_mfma_f32_16x16x32_bf16 v[236:239], a[220:223], v[116:119], v[236:239]\n"
"v_mfma_f32_16x16x32_bf16 v[236:239], a[216:219], v[112:115], v[236:239]\n"
"s_waitcnt lgkmcnt(15)\n"
"v_mfma_f32_16x16x32_bf16 v[252:255], a[216:219], v[120:123], v[252:255]\n"
"s_waitcnt lgkmcnt(15)\n"
"v_mfma_f32_16x16x32_bf16 v[252:255], a[220:223], v[124:127], v[252:255]\n"
"v_mfma_f32_16x16x32_bf16 v[248:251], a[212:215], v[124:127], v[248:251]\n"
"v_mfma_f32_16x16x32_bf16 v[248:251], a[208:211], v[120:123], v[248:251]\n"
"v_mfma_f32_16x16x32_bf16 v[244:247], a[200:203], v[120:123], v[244:247]\n"
"v_mfma_f32_16x16x32_bf16 v[244:247], a[204:207], v[124:127], v[244:247]\n"
"v_mfma_f32_16x16x32_bf16 v[240:243], a[196:199], v[124:127], v[240:243]\n"
"v_mfma_f32_16x16x32_bf16 v[240:243], a[192:195], v[120:123], v[240:243]\n"
"s_waitcnt vmcnt(1) lgkmcnt(0)\n"
"s_barrier\n"
"s_lshl_b32 s99, s60, 7\n"
"s_add_u32 s99, s99, 0\n"
"v_mov_b32 v28, s64\n"
"v_mov_b32 v29, s65\n"
"s_cmp_eq_u32 s61, 0\n"
"s_cselect_b64 exec, 1, 0\n"
"buffer_store_dwordx2 v[28:29], off, s[44:47], s99 sc1\n"
"s_mov_b64 exec, -1\n"
"v_mfma_f32_16x16x32_bf16 a[0:3], v[64:67], a[224:227], a[0:3]\n"
"s_add_u32 m0, s74, s78\n"
"ds_read_b64_tr_b16 a[192:193], v10 offset:32768\n"
"v_mfma_f32_16x16x32_bf16 a[0:3], v[68:71], a[228:231], a[0:3]\n"
"buffer_load_dwordx4 v14, s[16:19], s72 offen offset:0 lds\n"
"v_mfma_f32_16x16x32_bf16 a[32:35], v[68:71], a[236:239], a[32:35]\n"
"ds_read_b64_tr_b16 a[194:195], v10 offset:33792\n"
"v_mfma_f32_16x16x32_bf16 a[32:35], v[64:67], a[232:235], a[32:35]\n"
"ds_read_b64_tr_b16 a[196:197], v10 offset:40960\n"
"v_mfma_f32_16x16x32_bf16 a[64:67], v[64:67], a[240:243], a[64:67]\n"
"v_mfma_f32_16x16x32_bf16 a[64:67], v[68:71], a[244:247], a[64:67]\n"
"ds_read_b64_tr_b16 a[198:199], v10 offset:41984\n"
"v_mfma_f32_16x16x32_bf16 a[96:99], v[68:71], a[252:255], a[96:99]\n"
"buffer_load_dwordx4 v15, s[16:19], s72 offen offset:1024 lds\n"
"v_mfma_f32_16x16x32_bf16 a[96:99], v[64:67], a[248:251], a[96:99]\n"
"ds_read_b64_tr_b16 a[200:201], v11 offset:32768\n"
"v_mfma_f32_16x16x32_bf16 a[100:103], v[72:75], a[248:251], a[100:103]\n"
"ds_read_b64_tr_b16 a[202:203], v11 offset:33792\n"
"v_mfma_f32_16x16x32_bf16 a[100:103], v[76:79], a[252:255], a[100:103]\n"
"v_mfma_f32_16x16x32_bf16 a[68:71], v[76:79], a[244:247], a[68:71]\n"
"ds_read_b64_tr_b16 a[204:205], v11 offset:40960\n"
"v_mfma_f32_16x16x32_bf16 a[68:71], v[72:75], a[240:243], a[68:71]\n"
"buffer_load_dwordx4 v16, s[16:19], s72 offen offset:2048 lds\n"
"v_mfma_f32_16x16x32_bf16 a[36:39], v[72:75], a[232:235], a[36:39]\n"
"ds_read_b64_tr_b16 a[206:207], v11 offset:41984\n"
"v_mfma_f32_16x16x32_bf16 a[36:39], v[76:79], a[236:239], a[36:39]\n"
"v_mfma_f32_16x16x32_bf16 a[4:7], v[76:79], a[228:231], a[4:7]\n"
"ds_read_b64_tr_b16 a[208:209], v12 offset:32768\n"
"v_mfma_f32_16x16x32_bf16 a[4:7], v[72:75], a[224:227], a[4:7]\n"
"v_mfma_f32_16x16x32_bf16 a[8:11], v[80:83], a[224:227], a[8:11]\n"
"ds_read_b64_tr_b16 a[210:211], v12 offset:33792\n"
"v_mfma_f32_16x16x32_bf16 a[8:11], v[84:87], a[228:231], a[8:11]\n"
"ds_read_b64_tr_b16 a[212:213], v12 offset:40960\n"
"v_mfma_f32_16x16x32_bf16 a[40:43], v[84:87], a[236:239], a[40:43]\n"
"buffer_load_dwordx4 v17, s[16:19], s72 offen offset:3072 lds\n"
"v_mfma_f32_16x16x32_bf16 a[40:43], v[80:83], a[232:235], a[40:43]\n"
"s_add_u32 m0, s74, s79\n"
"ds_read_b64_tr_b16 a[214:215], v12 offset:41984\n"
"v_mfma_f32_16x16x32_bf16 a[72:75], v[80:83], a[240:243], a[72:75]\n"
"v_mfma_f32_16x16x32_bf16 a[72:75], v[84:87], a[244:247], a[72:75]\n"
"ds_read_b64_tr_b16 a[216:217], v13 offset:32768\n"
"v_mfma_f32_16x16x32_bf16 a[104:107], v[84:87], a[252:255], a[104:107]\n"
"buffer_load_dwordx4 v18, s[16:19], s72 offen offset:0 lds\n"
"v_mfma_f32_16x16x32_bf16 a[104:107], v[80:83], a[248:251], a[104:107]\n"
"ds_read_b64_tr_b16 a[218:219], v13 offset:33792\n"
"v_mfma_f32_16x16x32_bf16 a[108:111], v[88:91], a[248:251], a[108:111]\n"
"ds_read_b64_tr_b16 a[220:221], v13 offset:40960\n"
"v_mfma_f32_16x16x32_bf16 a[108:111], v[92:95], a[252:255], a[108:111]\n"
"v_mfma_f32_16x16x32_bf16 a[76:79], v[92:95], a[244:247], a[76:79]\n"
"ds_read_b64_tr_b16 a[222:223], v13 offset:41984\n"
"v_mfma_f32_16x16x32_bf16 a[76:79], v[88:91], a[240:243], a[76:79]\n"
"v_mfma_f32_16x16x32_bf16 a[44:47], v[88:91], a[232:235], a[44:47]\n"
"ds_read_b128 a[128:131], v8 offset:32768\n"
"v_mfma_f32_16x16x32_bf16 a[44:47], v[92:95], a[236:239], a[44:47]\n"
"buffer_load_dwordx4 v19, s[16:19], s72 offen offset:1024 lds\n"
"v_mfma_f32_16x16x32_bf16 a[12:15], v[92:95], a[228:231], a[12:15]\n"
"ds_read_b128 a[132:135], v9 offset:32768\n"
"v_mfma_f32_16x16x32_bf16 a[12:15], v[88:91], a[224:227], a[12:15]\n"
"v_mfma_f32_16x16x32_bf16 a[16:19], v[96:99], a[224:227], a[16:19]\n"
"ds_read_b128 a[136:139], v8 offset:34816\n"
"v_mfma_f32_16x16x32_bf16 a[16:19], v[100:103], a[228:231], a[16:19]\n"
"ds_read_b128 a[140:143], v9 offset:34816\n"
"v_mfma_f32_16x16x32_bf16 a[48:51], v[100:103], a[236:239], a[48:51]\n"
"buffer_load_dwordx4 v20, s[16:19], s72 offen offset:2048 lds\n"
"v_mfma_f32_16x16x32_bf16 a[48:51], v[96:99], a[232:235], a[48:51]\n"
"ds_read_b128 a[144:147], v8 offset:40960\n"
"v_mfma_f32_16x16x32_bf16 a[80:83], v[96:99], a[240:243], a[80:83]\n"
"v_mfma_f32_16x16x32_bf16 a[80:83], v[100:103], a[244:247], a[80:83]\n"
"ds_read_b128 a[148:151], v9 offset:40960\n"
"v_mfma_f32_16x16x32_bf16 a[112:115], v[100:103], a[252:255], a[112:115]\n"
"v_mfma_f32_16x16x32_bf16 a[112:115], v[96:99], a[248:251], a[112:115]\n"
"ds_read_b128 a[152:155], v8 offset:43008\n"
"v_mfma_f32_16x16x32_bf16 a[116:119], v[104:107], a[248:251], a[116:119]\n"
"ds_read_b128 a[156:159], v9 offset:43008\n"
"v_mfma_f32_16x16x32_bf16 a[116:119], v[108:111], a[252:255], a[116:119]\n"
"buffer_load_dwordx4 v21, s[16:19], s72 offen offset:3072 lds\n"
"v_mfma_f32_16x16x32_bf16 a[84:87], v[108:111], a[244:247], a[84:87]\n"
"s_add_u32 m0, s74, s80\n"
"ds_read_b128 a[160:163], v8 offset:49152\n"
"v_mfma_f32_16x16x32_bf16 a[84:87], v[104:107], a[240:243], a[84:87]\n"
"v_mfma_f32_16x16x32_bf16 a[52:55], v[104:107], a[232:235], a[52:55]\n"
"ds_read_b128 a[164:167], v9 offset:49152\n"
"v_mfma_f32_16x16x32_bf16 a[52:55], v[108:111], a[236:239], a[52:55]\n"
"buffer_load_dwordx4 v22, s[24:27], s73 offen offset:0 lds\n"
"v_mfma_f32_16x16x32_bf16 a[20:23], v[108:111], a[228:231], a[20:23]\n"
"ds_read_b128 a[168:171], v8 offset:51200\n"
"v_mfma_f32_16x16x32_bf16 a[20:23], v[104:107], a[224:227], a[20:23]\n"
"v_mfma_f32_16x16x32_bf16 a[24:27], v[112:115], a[224:227], a[24:27]\n"
"ds_read_b128 a[172:175], v9 offset:51200\n"
"v_mfma_f32_16x16x32_bf16 a[24:27], v[116:119], a[228:231], a[24:27]\n"
"ds_read_b128 a[176:179], v8 offset:57344\n"
"v_mfma_f32_16x16x32_bf16 a[56:59], v[116:119], a[236:239], a[56:59]\n"
"v_mfma_f32_16x16x32_bf16 a[56:59], v[112:115], a[232:235], a[56:59]\n"
"ds_read_b128 a[180:183], v9 offset:57344\n"
"v_mfma_f32_16x16x32_bf16 a[88:91], v[112:115], a[240:243], a[88:91]\n"
"buffer_load_dwordx4 v23, s[24:27], s73 offen offset:1024 lds\n"
"v_mfma_f32_16x16x32_bf16 a[88:91], v[116:119], a[244:247], a[88:91]\n"
"ds_read_b128 a[184:187], v8 offset:59392\n"
"v_mfma_f32_16x16x32_bf16 a[120:123], v[116:119], a[252:255], a[120:123]\n"
"v_mfma_f32_16x16x32_bf16 a[120:123], v[112:115], a[248:251], a[120:123]\n"
"ds_read_b128 a[188:191], v9 offset:59392\n"
"v_mfma_f32_16x16x32_bf16 a[124:127], v[120:123], a[248:251], a[124:127]\n"
"v_mfma_f32_16x16x32_bf16 a[124:127], v[124:127], a[252:255], a[124:127]\n"
"buffer_load_dwordx4 v30, s[24:27], s73 offen offset:2048 lds\n"
"v_mfma_f32_16x16x32_bf16 a[92:95], v[124:127], a[244:247], a[92:95]\n"
"v_mfma_f32_16x16x32_bf16 a[92:95], v[120:123], a[240:243], a[92:95]\n"
"v_mfma_f32_16x16x32_bf16 a[60:63], v[120:123], a[232:235], a[60:63]\n"
"v_mfma_f32_16x16x32_bf16 a[60:63], v[124:127], a[236:239], a[60:63]\n"
"v_mfma_f32_16x16x32_bf16 a[28:31], v[124:127], a[228:231], a[28:31]\n"
"v_mfma_f32_16x16x32_bf16 a[28:31], v[120:123], a[224:227], a[28:31]\n"
"s_waitcnt lgkmcnt(15)\n"
"v_mfma_f32_16x16x32_bf16 v[128:131], a[192:195], a[128:131], v[128:131]\n"
"s_add_u32 s72, s72, 128\n"
"s_add_u32 s77, s76, 2097152\n"
"ds_read_b64_tr_b16 a[224:225], v10 offset:49152\n"
"s_waitcnt lgkmcnt(15)\n"
"v_mfma_f32_16x16x32_bf16 v[128:131], a[196:199], a[132:135], v[128:131]\n"
"buffer_load_dwordx4 v31, s[24:27], s73 offen offset:3072 lds\n"
"v_mfma_f32_16x16x32_bf16 v[132:135], a[204:207], a[132:135], v[132:135]\n"
"s_add_u32 m0, s74, s81\n"
"s_xor_b32 s74, s74, 0x8000\n"
"ds_read_b64_tr_b16 a[226:227], v10 offset:50176\n"
"v_mfma_f32_16x16x32_bf16 v[132:135], a[200:203], a[128:131], v[132:135]\n"
"v_mfma_f32_16x16x32_bf16 v[136:139], a[208:211], a[128:131], v[136:139]\n"
"v_mfma_f32_16x16x32_bf16 v[136:139], a[212:215], a[132:135], v[136:139]\n"
"ds_read_b64_tr_b16 a[228:229], v10 offset:57344\n"
"v_mfma_f32_16x16x32_bf16 v[140:143], a[220:223], a[132:135], v[140:143]\n"
"v_mfma_f32_16x16x32_bf16 v[140:143], a[216:219], a[128:131], v[140:143]\n"
"buffer_load_dwordx4 v22, s[28:31], s73 offen offset:0 lds\n"
"s_waitcnt lgkmcnt(15)\n"
"v_mfma_f32_16x16x32_bf16 v[156:159], a[216:219], a[136:139], v[156:159]\n"
"s_waitcnt lgkmcnt(15)\n"
"ds_read_b64_tr_b16 a[230:231], v10 offset:58368\n"
"v_mfma_f32_16x16x32_bf16 v[156:159], a[220:223], a[140:143], v[156:159]\n"
"v_mfma_f32_16x16x32_bf16 v[152:155], a[212:215], a[140:143], v[152:155]\n"
"v_mfma_f32_16x16x32_bf16 v[152:155], a[208:211], a[136:139], v[152:155]\n"
"ds_read_b64_tr_b16 a[232:233], v11 offset:49152\n"
"v_mfma_f32_16x16x32_bf16 v[148:151], a[200:203], a[136:139], v[148:151]\n"
"v_mfma_f32_16x16x32_bf16 v[148:151], a[204:207], a[140:143], v[148:151]\n"
"buffer_load_dwordx4 v23, s[28:31], s73 offen offset:1024 lds\n"
"v_mfma_f32_16x16x32_bf16 v[144:147], a[196:199], a[140:143], v[144:147]\n"
"ds_read_b64_tr_b16 a[234:235], v11 offset:50176\n"
"v_mfma_f32_16x16x32_bf16 v[144:147], a[192:195], a[136:139], v[144:147]\n"
"s_waitcnt lgkmcnt(15)\n"
"v_mfma_f32_16x16x32_bf16 v[160:163], a[192:195], a[144:147], v[160:163]\n"
"s_waitcnt lgkmcnt(15)\n"
"ds_read_b64_tr_b16 a[236:237], v11 offset:57344\n"
"v_mfma_f32_16x16x32_bf16 v[160:163], a[196:199], a[148:151], v[160:163]\n"
"v_mfma_f32_16x16x32_bf16 v[164:167], a[204:207], a[148:151], v[164:167]\n"
"v_mfma_f32_16x16x32_bf16 v[164:167], a[200:203], a[144:147], v[164:167]\n"
"buffer_load_dwordx4 v30, s[28:31], s73 offen offset:2048 lds\n"
"v_mfma_f32_16x16x32_bf16 v[168:171], a[208:211], a[144:147], v[168:171]\n"
"ds_read_b64_tr_b16 a[238:239], v11 offset:58368\n"
"v_mfma_f32_16x16x32_bf16 v[168:171], a[212:215], a[148:151], v[168:171]\n"
"v_mfma_f32_16x16x32_bf16 v[172:175], a[220:223], a[148:151], v[172:175]\n"
"ds_read_b64_tr_b16 a[240:241], v12 offset:49152\n"
"v_mfma_f32_16x16x32_bf16 v[172:175], a[216:219], a[144:147], v[172:175]\n"
"s_waitcnt lgkmcnt(15)\n"
"v_mfma_f32_16x16x32_bf16 v[188:191], a[216:219], a[152:155], v[188:191]\n"
"s_waitcnt lgkmcnt(15)\n"
"v_mfma_f32_16x16x32_bf16 v[188:191], a[220:223], a[156:159], v[188:191]\n"
"buffer_load_dwordx4 v31, s[28:31], s73 offen offset:3072 lds\n"
"v_mfma_f32_16x16x32_bf16 v[184:187], a[212:215], a[156:159], v[184:187]\n"
"s_add_u32 s73, s73, 524288\n"
"s_add_u32 m0, s78, 131072\n"
"ds_read_b64_tr_b16 a[242:243], v12 offset:50176\n"
"v_mfma_f32_16x16x32_bf16 v[184:187], a[208:211], a[152:155], v[184:187]\n"
"v_mfma_f32_16x16x32_bf16 v[180:183], a[200:203], a[152:155], v[180:183]\n"
"ds_read_b64_tr_b16 a[244:245], v12 offset:57344\n"
"v_mfma_f32_16x16x32_bf16 v[180:183], a[204:207], a[156:159], v[180:183]\n"
"v_mfma_f32_16x16x32_bf16 v[176:179], a[196:199], a[156:159], v[176:179]\n"
"ds_read_b64_tr_b16 a[246:247], v12 offset:58368\n"
"v_mfma_f32_16x16x32_bf16 v[176:179], a[192:195], a[152:155], v[176:179]\n"
"s_waitcnt lgkmcnt(15)\n"
"v_mfma_f32_16x16x32_bf16 v[192:195], a[192:195], a[160:163], v[192:195]\n"
"s_waitcnt lgkmcnt(15)\n"
"v_mfma_f32_16x16x32_bf16 v[192:195], a[196:199], a[164:167], v[192:195]\n"
"ds_read_b64_tr_b16 a[248:249], v13 offset:49152\n"
"v_mfma_f32_16x16x32_bf16 v[196:199], a[204:207], a[164:167], v[196:199]\n"
"v_mfma_f32_16x16x32_bf16 v[196:199], a[200:203], a[160:163], v[196:199]\n"
"v_mfma_f32_16x16x32_bf16 v[200:203], a[208:211], a[160:163], v[200:203]\n"
"buffer_load_dwordx4 v22, s[20:23], s77 offen offset:0 lds\n"
"v_mfma_f32_16x16x32_bf16 v[200:203], a[212:215], a[164:167], v[200:203]\n"
"ds_read_b64_tr_b16 a[250:251], v13 offset:50176\n"
"v_mfma_f32_16x16x32_bf16 v[204:207], a[220:223], a[164:167], v[204:207]\n"
"v_mfma_f32_16x16x32_bf16 v[204:207], a[216:219], a[160:163], v[204:207]\n"
"s_waitcnt lgkmcnt(15)\n"
"ds_read_b64_tr_b16 a[252:253], v13 offset:57344\n"
"v_mfma_f32_16x16x32_bf16 v[220:223], a[216:219], a[168:171], v[220:223]\n"
"s_waitcnt lgkmcnt(15)\n"
"v_mfma_f32_16x16x32_bf16 v[220:223], a[220:223], a[172:175], v[220:223]\n"
"v_mfma_f32_16x16x32_bf16 v[216:219], a[212:215], a[172:175], v[216:219]\n"
"buffer_load_dwordx4 v23, s[20:23], s77 offen offset:1024 lds\n"
"v_mfma_f32_16x16x32_bf16 v[216:219], a[208:211], a[168:171], v[216:219]\n"
"ds_read_b64_tr_b16 a[254:255], v13 offset:58368\n"
"v_mfma_f32_16x16x32_bf16 v[212:215], a[200:203], a[168:171], v[212:215]\n"
"v_mfma_f32_16x16x32_bf16 v[212:215], a[204:207], a[172:175], v[212:215]\n"
"v_mfma_f32_16x16x32_bf16 v[208:211], a[196:199], a[172:175], v[208:211]\n"
"v_mfma_f32_16x16x32_bf16 v[208:211], a[192:195], a[168:171], v[208:211]\n"
"s_waitcnt lgkmcnt(15)\n"
"v_mfma_f32_16x16x32_bf16 v[224:227], a[192:195], a[176:179], v[224:227]\n"
"buffer_load_dwordx4 v30, s[20:23], s77 offen offset:2048 lds\n"
"s_waitcnt lgkmcnt(15)\n"
"v_mfma_f32_16x16x32_bf16 v[224:227], a[196:199], a[180:183], v[224:227]\n"
"v_mfma_f32_16x16x32_bf16 v[228:231], a[204:207], a[180:183], v[228:231]\n"
"v_mfma_f32_16x16x32_bf16 v[228:231], a[200:203], a[176:179], v[228:231]\n"
"v_mfma_f32_16x16x32_bf16 v[232:235], a[208:211], a[176:179], v[232:235]\n"
"v_mfma_f32_16x16x32_bf16 v[232:235], a[212:215], a[180:183], v[232:235]\n"
"v_mfma_f32_16x16x32_bf16 v[236:239], a[220:223], a[180:183], v[236:239]\n"
"buffer_load_dwordx4 v31, s[20:23], s77 offen offset:3072 lds\n"
"v_mfma_f32_16x16x32_bf16 v[236:239], a[216:219], a[176:179], v[236:239]\n"
"s_waitcnt lgkmcnt(15)\n"
"v_mfma_f32_16x16x32_bf16 v[252:255], a[216:219], a[184:187], v[252:255]\n"
"s_waitcnt lgkmcnt(15)\n"
"v_mfma_f32_16x16x32_bf16 v[252:255], a[220:223], a[188:191], v[252:255]\n"
"v_mfma_f32_16x16x32_bf16 v[248:251], a[212:215], a[188:191], v[248:251]\n"
"v_mfma_f32_16x16x32_bf16 v[248:251], a[208:211], a[184:187], v[248:251]\n"
"v_mfma_f32_16x16x32_bf16 v[244:247], a[200:203], a[184:187], v[244:247]\n"
"v_mfma_f32_16x16x32_bf16 v[244:247], a[204:207], a[188:191], v[244:247]\n"
"v_mfma_f32_16x16x32_bf16 v[240:243], a[196:199], a[188:191], v[240:243]\n"
"v_mfma_f32_16x16x32_bf16 v[240:243], a[192:195], a[184:187], v[240:243]\n"
"s_waitcnt vmcnt(4) lgkmcnt(0)\n"
"s_barrier\n"
"s_getpc_b64 s[8:9]\n"
"PC_7_%=:\n"
"s_add_u32 s8, s8, ATT0_%=-PC_7_%=\n"
"s_addc_u32 s9, s9, 0\n"
"s_mov_b32 s10, TAIL0_%=-ATT0_%=\n"
"s_mov_b32 s11, 0x20000\n"
"s_lshr_b32 s98, s60, 3\n"
"s_lshl_b32 s98, s98, 2\n"
"s_add_u32 s98, s98, s61\n"
"s_lshl_b32 s98, s98, 7\n"
"v_and_b32 v60, 7, v0\n"
"v_lshlrev_b32 v60, 14, v60\n"
"v_mfma_f32_16x16x32_bf16 a[0:3], a[128:131], a[224:227], a[0:3]\n"
"s_add_u32 m0, s74, s78\n"
"ds_read_b64_tr_b16 a[192:193], v10 offset:0\n"
"v_mfma_f32_16x16x32_bf16 a[0:3], a[132:135], a[228:231], a[0:3]\n"
"buffer_load_dwordx4 v14, s[16:19], s72 offen offset:0 lds\n"
"v_mfma_f32_16x16x32_bf16 a[32:35], a[132:135], a[236:239], a[32:35]\n"
"ds_read_b64_tr_b16 a[194:195], v10 offset:1024\n"
"v_mfma_f32_16x16x32_bf16 a[32:35], a[128:131], a[232:235], a[32:35]\n"
"ds_read_b64_tr_b16 a[196:197], v10 offset:8192\n"
"v_mfma_f32_16x16x32_bf16 a[64:67], a[128:131], a[240:243], a[64:67]\n"
"v_mfma_f32_16x16x32_bf16 a[64:67], a[132:135], a[244:247], a[64:67]\n"
"ds_read_b64_tr_b16 a[198:199], v10 offset:9216\n"
"v_mfma_f32_16x16x32_bf16 a[96:99], a[132:135], a[252:255], a[96:99]\n"
"buffer_load_dwordx4 v15, s[16:19], s72 offen offset:1024 lds\n"
"v_mfma_f32_16x16x32_bf16 a[96:99], a[128:131], a[248:251], a[96:99]\n"
"ds_read_b64_tr_b16 a[200:201], v11 offset:0\n"
"v_mfma_f32_16x16x32_bf16 a[100:103], a[136:139], a[248:251], a[100:103]\n"
"ds_read_b64_tr_b16 a[202:203], v11 offset:1024\n"
"v_mfma_f32_16x16x32_bf16 a[100:103], a[140:143], a[252:255], a[100:103]\n"
"v_mfma_f32_16x16x32_bf16 a[68:71], a[140:143], a[244:247], a[68:71]\n"
"ds_read_b64_tr_b16 a[204:205], v11 offset:8192\n"
"v_mfma_f32_16x16x32_bf16 a[68:71], a[136:139], a[240:243], a[68:71]\n"
"buffer_load_dwordx4 v16, s[16:19], s72 offen offset:2048 lds\n"
"v_mfma_f32_16x16x32_bf16 a[36:39], a[136:139], a[232:235], a[36:39]\n"
"ds_read_b64_tr_b16 a[206:207], v11 offset:9216\n"
"v_mfma_f32_16x16x32_bf16 a[36:39], a[140:143], a[236:239], a[36:39]\n"
"v_mfma_f32_16x16x32_bf16 a[4:7], a[140:143], a[228:231], a[4:7]\n"
"ds_read_b64_tr_b16 a[208:209], v12 offset:0\n"
"v_mfma_f32_16x16x32_bf16 a[4:7], a[136:139], a[224:227], a[4:7]\n"
"v_mfma_f32_16x16x32_bf16 a[8:11], a[144:147], a[224:227], a[8:11]\n"
"ds_read_b64_tr_b16 a[210:211], v12 offset:1024\n"
"v_mfma_f32_16x16x32_bf16 a[8:11], a[148:151], a[228:231], a[8:11]\n"
"ds_read_b64_tr_b16 a[212:213], v12 offset:8192\n"
"v_mfma_f32_16x16x32_bf16 a[40:43], a[148:151], a[236:239], a[40:43]\n"
"buffer_load_dwordx4 v17, s[16:19], s72 offen offset:3072 lds\n"
"v_mfma_f32_16x16x32_bf16 a[40:43], a[144:147], a[232:235], a[40:43]\n"
"s_add_u32 m0, s74, s79\n"
"ds_read_b64_tr_b16 a[214:215], v12 offset:9216\n"
"v_mfma_f32_16x16x32_bf16 a[72:75], a[144:147], a[240:243], a[72:75]\n"
"v_mfma_f32_16x16x32_bf16 a[72:75], a[148:151], a[244:247], a[72:75]\n"
"ds_read_b64_tr_b16 a[216:217], v13 offset:0\n"
"v_mfma_f32_16x16x32_bf16 a[104:107], a[148:151], a[252:255], a[104:107]\n"
"buffer_load_dwordx4 v18, s[16:19], s72 offen offset:0 lds\n"
"v_mfma_f32_16x16x32_bf16 a[104:107], a[144:147], a[248:251], a[104:107]\n"
"ds_read_b64_tr_b16 a[218:219], v13 offset:1024\n"
"v_mfma_f32_16x16x32_bf16 a[108:111], a[152:155], a[248:251], a[108:111]\n"
"ds_read_b64_tr_b16 a[220:221], v13 offset:8192\n"
"v_mfma_f32_16x16x32_bf16 a[108:111], a[156:159], a[252:255], a[108:111]\n"
"v_mfma_f32_16x16x32_bf16 a[76:79], a[156:159], a[244:247], a[76:79]\n"
"ds_read_b64_tr_b16 a[222:223], v13 offset:9216\n"
"v_mfma_f32_16x16x32_bf16 a[76:79], a[152:155], a[240:243], a[76:79]\n"
"v_mfma_f32_16x16x32_bf16 a[44:47], a[152:155], a[232:235], a[44:47]\n"
"ds_read_b128 v[64:67], v8 offset:0\n"
"v_mfma_f32_16x16x32_bf16 a[44:47], a[156:159], a[236:239], a[44:47]\n"
"buffer_load_dwordx4 v19, s[16:19], s72 offen offset:1024 lds\n"
"v_mfma_f32_16x16x32_bf16 a[12:15], a[156:159], a[228:231], a[12:15]\n"
"ds_read_b128 v[68:71], v9 offset:0\n"
"v_mfma_f32_16x16x32_bf16 a[12:15], a[152:155], a[224:227], a[12:15]\n"
"v_mfma_f32_16x16x32_bf16 a[16:19], a[160:163], a[224:227], a[16:19]\n"
"ds_read_b128 v[72:75], v8 offset:2048\n"
"v_mfma_f32_16x16x32_bf16 a[16:19], a[164:167], a[228:231], a[16:19]\n"
"ds_read_b128 v[76:79], v9 offset:2048\n"
"v_mfma_f32_16x16x32_bf16 a[48:51], a[164:167], a[236:239], a[48:51]\n"
"buffer_load_dwordx4 v20, s[16:19], s72 offen offset:2048 lds\n"
"v_mfma_f32_16x16x32_bf16 a[48:51], a[160:163], a[232:235], a[48:51]\n"
"ds_read_b128 v[80:83], v8 offset:8192\n"
"v_mfma_f32_16x16x32_bf16 a[80:83], a[160:163], a[240:243], a[80:83]\n"
"v_mfma_f32_16x16x32_bf16 a[80:83], a[164:167], a[244:247], a[80:83]\n"
"ds_read_b128 v[84:87], v9 offset:8192\n"
"v_mfma_f32_16x16x32_bf16 a[112:115], a[164:167], a[252:255], a[112:115]\n"
"v_mfma_f32_16x16x32_bf16 a[112:115], a[160:163], a[248:251], a[112:115]\n"
"ds_read_b128 v[88:91], v8 offset:10240\n"
"v_mfma_f32_16x16x32_bf16 a[116:119], a[168:171], a[248:251], a[116:119]\n"
"ds_read_b128 v[92:95], v9 offset:10240\n"
"v_mfma_f32_16x16x32_bf16 a[116:119], a[172:175], a[252:255], a[116:119]\n"
"buffer_load_dwordx4 v21, s[16:19], s72 offen offset:3072 lds\n"
"v_mfma_f32_16x16x32_bf16 a[84:87], a[172:175], a[244:247], a[84:87]\n"
"s_add_u32 m0, s74, s80\n"
"ds_read_b128 v[96:99], v8 offset:16384\n"
"v_mfma_f32_16x16x32_bf16 a[84:87], a[168:171], a[240:243], a[84:87]\n"
"v_mfma_f32_16x16x32_bf16 a[52:55], a[168:171], a[232:235], a[52:55]\n"
"ds_read_b128 v[100:103], v9 offset:16384\n"
"v_mfma_f32_16x16x32_bf16 a[52:55], a[172:175], a[236:239], a[52:55]\n"
"buffer_load_dwordx4 v22, s[24:27], s73 offen offset:0 lds\n"
"v_mfma_f32_16x16x32_bf16 a[20:23], a[172:175], a[228:231], a[20:23]\n"
"ds_read_b128 v[104:107], v8 offset:18432\n"
"v_mfma_f32_16x16x32_bf16 a[20:23], a[168:171], a[224:227], a[20:23]\n"
"v_mfma_f32_16x16x32_bf16 a[24:27], a[176:179], a[224:227], a[24:27]\n"
"ds_read_b128 v[108:111], v9 offset:18432\n"
"v_mfma_f32_16x16x32_bf16 a[24:27], a[180:183], a[228:231], a[24:27]\n"
"ds_read_b128 v[112:115], v8 offset:24576\n"
"v_mfma_f32_16x16x32_bf16 a[56:59], a[180:183], a[236:239], a[56:59]\n"
"v_mfma_f32_16x16x32_bf16 a[56:59], a[176:179], a[232:235], a[56:59]\n"
"ds_read_b128 v[116:119], v9 offset:24576\n"
"v_mfma_f32_16x16x32_bf16 a[88:91], a[176:179], a[240:243], a[88:91]\n"
"buffer_load_dwordx4 v23, s[24:27], s73 offen offset:1024 lds\n"
"v_mfma_f32_16x16x32_bf16 a[88:91], a[180:183], a[244:247], a[88:91]\n"
"ds_read_b128 v[120:123], v8 offset:26624\n"
"v_mfma_f32_16x16x32_bf16 a[120:123], a[180:183], a[252:255], a[120:123]\n"
"v_mfma_f32_16x16x32_bf16 a[120:123], a[176:179], a[248:251], a[120:123]\n"
"ds_read_b128 v[124:127], v9 offset:26624\n"
"v_mfma_f32_16x16x32_bf16 a[124:127], a[184:187], a[248:251], a[124:127]\n"
"v_mfma_f32_16x16x32_bf16 a[124:127], a[188:191], a[252:255], a[124:127]\n"
"buffer_load_dwordx4 v30, s[24:27], s73 offen offset:2048 lds\n"
"v_mfma_f32_16x16x32_bf16 a[92:95], a[188:191], a[244:247], a[92:95]\n"
"v_mfma_f32_16x16x32_bf16 a[92:95], a[184:187], a[240:243], a[92:95]\n"
"v_mfma_f32_16x16x32_bf16 a[60:63], a[184:187], a[232:235], a[60:63]\n"
"v_mfma_f32_16x16x32_bf16 a[60:63], a[188:191], a[236:239], a[60:63]\n"
"v_mfma_f32_16x16x32_bf16 a[28:31], a[188:191], a[228:231], a[28:31]\n"
"v_mfma_f32_16x16x32_bf16 a[28:31], a[184:187], a[224:227], a[28:31]\n"
"s_waitcnt lgkmcnt(15)\n"
"v_mfma_f32_16x16x32_bf16 v[128:131], a[192:195], v[64:67], v[128:131]\n"
"s_add_u32 s72, s72, 128\n"
"s_add_u32 s77, s76, 2228224\n"
"ds_read_b64_tr_b16 a[224:225], v10 offset:16384\n"
"s_waitcnt lgkmcnt(15)\n"
"v_mfma_f32_16x16x32_bf16 v[128:131], a[196:199], v[68:71], v[128:131]\n"
"buffer_load_dwordx4 v31, s[24:27], s73 offen offset:3072 lds\n"
"v_mfma_f32_16x16x32_bf16 v[132:135], a[204:207], v[68:71], v[132:135]\n"
"s_add_u32 m0, s74, s81\n"
"s_xor_b32 s74, s74, 0x8000\n"
"ds_read_b64_tr_b16 a[226:227], v10 offset:17408\n"
"v_mfma_f32_16x16x32_bf16 v[132:135], a[200:203], v[64:67], v[132:135]\n"
"v_lshlrev_b32 v24, 1, v3\n"
"v_xor_b32 v24, v24, v2\n"
"v_mfma_f32_16x16x32_bf16 v[136:139], a[208:211], v[64:67], v[136:139]\n"
"v_lshlrev_b32 v24, 4, v24\n"
"v_lshl_add_u32 v24, v3, 10, v24\n"
"v_mfma_f32_16x16x32_bf16 v[136:139], a[212:215], v[68:71], v[136:139]\n"
"buffer_load_dwordx4 v22, s[28:31], s73 offen offset:0 lds\n"
"v_mfma_f32_16x16x32_bf16 v[140:143], a[220:223], v[68:71], v[140:143]\n"
"ds_read_b64_tr_b16 a[228:229], v10 offset:24576\n"
"v_xor_b32 v25, 128, v24\n"
"v_mfma_f32_16x16x32_bf16 v[140:143], a[216:219], v[64:67], v[140:143]\n"
"s_waitcnt lgkmcnt(15)\n"
"v_add_u32 v26, 6144, v24\n"
"v_add_u32 v27, 9216, v25\n"
"v_mfma_f32_16x16x32_bf16 v[156:159], a[216:219], v[72:75], v[156:159]\n"
"s_waitcnt lgkmcnt(15)\n"
"ds_read_b64_tr_b16 a[230:231], v10 offset:25600\n"
"v_add_u32 v25, 3072, v25\n"
"v_mfma_f32_16x16x32_bf16 v[156:159], a[220:223], v[76:79], v[156:159]\n"
"buffer_load_dwordx4 v23, s[28:31], s73 offen offset:1024 lds\n"
"v_mfma_f32_16x16x32_bf16 v[152:155], a[212:215], v[76:79], v[152:155]\n"
"v_mfma_f32_16x16x32_bf16 v[152:155], a[208:211], v[72:75], v[152:155]\n"
"ds_read_b64_tr_b16 a[232:233], v11 offset:16384\n"
"v_mfma_f32_16x16x32_bf16 v[148:151], a[200:203], v[72:75], v[148:151]\n"
"v_mfma_f32_16x16x32_bf16 v[148:151], a[204:207], v[76:79], v[148:151]\n"
"buffer_load_dwordx4 v30, s[28:31], s73 offen offset:2048 lds\n"
"v_mfma_f32_16x16x32_bf16 v[144:147], a[196:199], v[76:79], v[144:147]\n"
"ds_read_b64_tr_b16 a[234:235], v11 offset:17408\n"
"v_mfma_f32_16x16x32_bf16 v[144:147], a[192:195], v[72:75], v[144:147]\n"
"s_waitcnt lgkmcnt(15)\n"
"v_mfma_f32_16x16x32_bf16 v[160:163], a[192:195], v[80:83], v[160:163]\n"
"s_waitcnt lgkmcnt(15)\n"
"ds_read_b64_tr_b16 a[236:237], v11 offset:24576\n"
"v_mfma_f32_16x16x32_bf16 v[160:163], a[196:199], v[84:87], v[160:163]\n"
"v_mfma_f32_16x16x32_bf16 v[164:167], a[204:207], v[84:87], v[164:167]\n"
"buffer_load_dwordx4 v31, s[28:31], s73 offen offset:3072 lds\n"
"v_mfma_f32_16x16x32_bf16 v[164:167], a[200:203], v[80:83], v[164:167]\n"
"s_add_u32 s73, s73, 524288\n"
"s_add_u32 m0, s79, 131072\n"
"ds_read_b64_tr_b16 a[238:239], v11 offset:25600\n"
"v_mfma_f32_16x16x32_bf16 v[168:171], a[208:211], v[80:83], v[168:171]\n"
"v_mfma_f32_16x16x32_bf16 v[168:171], a[212:215], v[84:87], v[168:171]\n"
"v_mfma_f32_16x16x32_bf16 v[172:175], a[220:223], v[84:87], v[172:175]\n"
"s_nop 0\n"
"PG3_%=:\n"
"s_cbranch_execz PADRET_%=\n"
"buffer_load_dwordx2 v[28:29], v7, s[44:47], 0 offen sc1\n"
"v_mfma_f32_16x16x32_bf16 v[172:175], a[216:219], v[80:83], v[172:175]\n"
"s_waitcnt lgkmcnt(15)\n"
"ds_read_b64_tr_b16 a[240:241], v12 offset:16384\n"
"v_mfma_f32_16x16x32_bf16 v[188:191], a[216:219], v[88:91], v[188:191]\n"
"s_waitcnt lgkmcnt(15)\n"
"v_mfma_f32_16x16x32_bf16 v[188:191], a[220:223], v[92:95], v[188:191]\n"
"ds_read_b64_tr_b16 a[242:243], v12 offset:17408\n"
"v_mfma_f32_16x16x32_bf16 v[184:187], a[212:215], v[92:95], v[184:187]\n"
"buffer_load_dword v61, v60, s[8:11], s98 offen\n"
"v_mfma_f32_16x16x32_bf16 v[184:187], a[208:211], v[88:91], v[184:187]\n"
"v_mfma_f32_16x16x32_bf16 v[180:183], a[200:203], v[88:91], v[180:183]\n"
"ds_read_b64_tr_b16 a[244:245], v12 offset:24576\n"
"v_mfma_f32_16x16x32_bf16 v[180:183], a[204:207], v[92:95], v[180:183]\n"
"v_mfma_f32_16x16x32_bf16 v[176:179], a[196:199], v[92:95], v[176:179]\n"
"ds_read_b64_tr_b16 a[246:247], v12 offset:25600\n"
"v_mfma_f32_16x16x32_bf16 v[176:179], a[192:195], v[88:91], v[176:179]\n"
"s_waitcnt lgkmcnt(15)\n"
"v_mfma_f32_16x16x32_bf16 v[192:195], a[192:195], v[96:99], v[192:195]\n"
"s_waitcnt lgkmcnt(15)\n"
"v_mfma_f32_16x16x32_bf16 v[192:195], a[196:199], v[100:103], v[192:195]\n"
"ds_read_b64_tr_b16 a[248:249], v13 offset:16384\n"
"v_mfma_f32_16x16x32_bf16 v[196:199], a[204:207], v[100:103], v[196:199]\n"
"v_mfma_f32_16x16x32_bf16 v[196:199], a[200:203], v[96:99], v[196:199]\n"
"v_mfma_f32_16x16x32_bf16 v[200:203], a[208:211], v[96:99], v[200:203]\n"
"buffer_load_dwordx4 v22, s[20:23], s77 offen offset:0 lds\n"
"v_mfma_f32_16x16x32_bf16 v[200:203], a[212:215], v[100:103], v[200:203]\n"
"ds_read_b64_tr_b16 a[250:251], v13 offset:17408\n"
"v_mfma_f32_16x16x32_bf16 v[204:207], a[220:223], v[100:103], v[204:207]\n"
"v_mfma_f32_16x16x32_bf16 v[204:207], a[216:219], v[96:99], v[204:207]\n"
"s_waitcnt lgkmcnt(15)\n"
"ds_read_b64_tr_b16 a[252:253], v13 offset:24576\n"
"v_mfma_f32_16x16x32_bf16 v[220:223], a[216:219], v[104:107], v[220:223]\n"
"s_waitcnt lgkmcnt(15)\n"
"v_mfma_f32_16x16x32_bf16 v[220:223], a[220:223], v[108:111], v[220:223]\n"
"v_mfma_f32_16x16x32_bf16 v[216:219], a[212:215], v[108:111], v[216:219]\n"
"buffer_load_dwordx4 v23, s[20:23], s77 offen offset:1024 lds\n"
"v_mfma_f32_16x16x32_bf16 v[216:219], a[208:211], v[104:107], v[216:219]\n"
"ds_read_b64_tr_b16 a[254:255], v13 offset:25600\n"
"v_mfma_f32_16x16x32_bf16 v[212:215], a[200:203], v[104:107], v[212:215]\n"
"v_mfma_f32_16x16x32_bf16 v[212:215], a[204:207], v[108:111], v[212:215]\n"
"v_mfma_f32_16x16x32_bf16 v[208:211], a[196:199], v[108:111], v[208:211]\n"
"v_mfma_f32_16x16x32_bf16 v[208:211], a[192:195], v[104:107], v[208:211]\n"
"s_waitcnt lgkmcnt(15)\n"
"v_mfma_f32_16x16x32_bf16 v[224:227], a[192:195], v[112:115], v[224:227]\n"
"buffer_load_dwordx4 v30, s[20:23], s77 offen offset:2048 lds\n"
"s_waitcnt lgkmcnt(15)\n"
"v_mfma_f32_16x16x32_bf16 v[224:227], a[196:199], v[116:119], v[224:227]\n"
"v_mfma_f32_16x16x32_bf16 v[228:231], a[204:207], v[116:119], v[228:231]\n"
"v_mfma_f32_16x16x32_bf16 v[228:231], a[200:203], v[112:115], v[228:231]\n"
"v_mfma_f32_16x16x32_bf16 v[232:235], a[208:211], v[112:115], v[232:235]\n"
"v_mfma_f32_16x16x32_bf16 v[232:235], a[212:215], v[116:119], v[232:235]\n"
"v_mfma_f32_16x16x32_bf16 v[236:239], a[220:223], v[116:119], v[236:239]\n"
"buffer_load_dwordx4 v31, s[20:23], s77 offen offset:3072 lds\n"
"v_mfma_f32_16x16x32_bf16 v[236:239], a[216:219], v[112:115], v[236:239]\n"
"s_waitcnt lgkmcnt(15)\n"
"v_mfma_f32_16x16x32_bf16 v[252:255], a[216:219], v[120:123], v[252:255]\n"
"s_waitcnt lgkmcnt(15)\n"
"v_mfma_f32_16x16x32_bf16 v[252:255], a[220:223], v[124:127], v[252:255]\n"
"v_mfma_f32_16x16x32_bf16 v[248:251], a[212:215], v[124:127], v[248:251]\n"
"v_mfma_f32_16x16x32_bf16 v[248:251], a[208:211], v[120:123], v[248:251]\n"
"v_mfma_f32_16x16x32_bf16 v[244:247], a[200:203], v[120:123], v[244:247]\n"
"v_mfma_f32_16x16x32_bf16 v[244:247], a[204:207], v[124:127], v[244:247]\n"
"v_mfma_f32_16x16x32_bf16 v[240:243], a[196:199], v[124:127], v[240:243]\n"
"v_mfma_f32_16x16x32_bf16 v[240:243], a[192:195], v[120:123], v[240:243]\n"
"s_waitcnt vmcnt(5) lgkmcnt(0)\n"
"s_barrier\n"
"v_cmp_ne_u64 vcc, s[64:65], v[28:29]\n"
"s_nop 1\n"
"s_cbranch_vccz FOK_8_%=\n"
"FSP_9_%=:\n"
"s_cmp_eq_u32 s69, 0\n"
"s_cbranch_scc1 FOK_8_%=\n"
"s_sub_u32 s69, s69, 1\n"
"s_sleep 2\n"
"s_mov_b64 exec, vcc\n"
"buffer_load_dwordx2 v[28:29], v7, s[44:47], 0 offen sc1\n"
"s_mov_b64 exec, -1\n"
"s_waitcnt vmcnt(0)\n"
"v_cmp_ne_u64 vcc, s[64:65], v[28:29]\n"
"s_nop 1\n"
"s_cbranch_vccnz FSP_9_%=\n"
"FOK_8_%=:\n"
"v_mfma_f32_16x16x32_bf16 a[0:3], v[64:67], a[224:227], a[0:3]\n"
"s_add_u32 m0, s78, 0\n"
"s_add_u32 s77, s76, 0\n"
"ds_read_b64_tr_b16 a[192:193], v10 offset:32768\n"
"v_mfma_f32_16x16x32_bf16 a[0:3], v[68:71], a[228:231], a[0:3]\n"
"buffer_load_dwordx4 v22, s[20:23], s77 offen offset:0 lds\n"
"v_mfma_f32_16x16x32_bf16 a[32:35], v[68:71], a[236:239], a[32:35]\n"
"ds_read_b64_tr_b16 a[194:195], v10 offset:33792\n"
"v_mfma_f32_16x16x32_bf16 a[32:35], v[64:67], a[232:235], a[32:35]\n"
"ds_read_b64_tr_b16 a[196:197], v10 offset:40960\n"
"v_mfma_f32_16x16x32_bf16 a[64:67], v[64:67], a[240:243], a[64:67]\n"
"v_mfma_f32_16x16x32_bf16 a[64:67], v[68:71], a[244:247], a[64:67]\n"
"ds_read_b64_tr_b16 a[198:199], v10 offset:41984\n"
"v_mfma_f32_16x16x32_bf16 a[96:99], v[68:71], a[252:255], a[96:99]\n"
"buffer_load_dwordx4 v23, s[20:23], s77 offen offset:1024 lds\n"
"v_mfma_f32_16x16x32_bf16 a[96:99], v[64:67], a[248:251], a[96:99]\n"
"ds_read_b64_tr_b16 a[200:201], v11 offset:32768\n"
"v_mfma_f32_16x16x32_bf16 a[100:103], v[72:75], a[248:251], a[100:103]\n"
"ds_read_b64_tr_b16 a[202:203], v11 offset:33792\n"
"v_mfma_f32_16x16x32_bf16 a[100:103], v[76:79], a[252:255], a[100:103]\n"
"v_mfma_f32_16x16x32_bf16 a[68:71], v[76:79], a[244:247], a[68:71]\n"
"ds_read_b64_tr_b16 a[204:205], v11 offset:40960\n"
"v_mfma_f32_16x16x32_bf16 a[68:71], v[72:75], a[240:243], a[68:71]\n"
"buffer_load_dwordx4 v30, s[20:23], s77 offen offset:2048 lds\n"
"v_mfma_f32_16x16x32_bf16 a[36:39], v[72:75], a[232:235], a[36:39]\n"
"ds_read_b64_tr_b16 a[206:207], v11 offset:41984\n"
"v_mfma_f32_16x16x32_bf16 a[36:39], v[76:79], a[236:239], a[36:39]\n"
"v_mfma_f32_16x16x32_bf16 a[4:7], v[76:79], a[228:231], a[4:7]\n"
"ds_read_b64_tr_b16 a[208:209], v12 offset:32768\n"
"v_mfma_f32_16x16x32_bf16 a[4:7], v[72:75], a[224:227], a[4:7]\n"
"v_mfma_f32_16x16x32_bf16 a[8:11], v[80:83], a[224:227], a[8:11]\n"
"ds_read_b64_tr_b16 a[210:211], v12 offset:33792\n"
"v_mfma_f32_16x16x32_bf16 a[8:11], v[84:87], a[228:231], a[8:11]\n"
"ds_read_b64_tr_b16 a[212:213], v12 offset:40960\n"
"v_mfma_f32_16x16x32_bf16 a[40:43], v[84:87], a[236:239], a[40:43]\n"
"buffer_load_dwordx4 v31, s[20:23], s77 offen offset:3072 lds\n"
"v_mfma_f32_16x16x32_bf16 a[40:43], v[80:83], a[232:235], a[40:43]\n"
"s_add_u32 m0, s79, 0\n"
"s_add_u32 s77, s76, 131072\n"
"ds_read_b64_tr_b16 a[214:215], v12 offset:41984\n"
"v_mfma_f32_16x16x32_bf16 a[72:75], v[80:83], a[240:243], a[72:75]\n"
"v_mfma_f32_16x16x32_bf16 a[72:75], v[84:87], a[244:247], a[72:75]\n"
"ds_read_b64_tr_b16 a[216:217], v13 offset:32768\n"
"v_mfma_f32_16x16x32_bf16 a[104:107], v[84:87], a[252:255], a[104:107]\n"
"buffer_load_dwordx4 v22, s[20:23], s77 offen offset:0 lds\n"
"v_mfma_f32_16x16x32_bf16 a[104:107], v[80:83], a[248:251], a[104:107]\n"
"ds_read_b64_tr_b16 a[218:219], v13 offset:33792\n"
"v_mfma_f32_16x16x32_bf16 a[108:111], v[88:91], a[248:251], a[108:111]\n"
"ds_read_b64_tr_b16 a[220:221], v13 offset:40960\n"
"v_mfma_f32_16x16x32_bf16 a[108:111], v[92:95], a[252:255], a[108:111]\n"
"v_mfma_f32_16x16x32_bf16 a[76:79], v[92:95], a[244:247], a[76:79]\n"
"ds_read_b64_tr_b16 a[222:223], v13 offset:41984\n"
"v_mfma_f32_16x16x32_bf16 a[76:79], v[88:91], a[240:243], a[76:79]\n"
"v_mfma_f32_16x16x32_bf16 a[44:47], v[88:91], a[232:235], a[44:47]\n"
"ds_read_b128 a[128:131], v8 offset:32768\n"
"v_mfma_f32_16x16x32_bf16 a[44:47], v[92:95], a[236:239], a[44:47]\n"
"buffer_load_dwordx4 v23, s[20:23], s77 offen offset:1024 lds\n"
"v_mfma_f32_16x16x32_bf16 a[12:15], v[92:95], a[228:231], a[12:15]\n"
"ds_read_b128 a[132:135], v9 offset:32768\n"
"v_mfma_f32_16x16x32_bf16 a[12:15], v[88:91], a[224:227], a[12:15]\n"
"v_mfma_f32_16x16x32_bf16 a[16:19], v[96:99], a[224:227], a[16:19]\n"
"ds_read_b128 a[136:139], v8 offset:34816\n"
"v_mfma_f32_16x16x32_bf16 a[16:19], v[100:103], a[228:231], a[16:19]\n"
"ds_read_b128 a[140:143], v9 offset:34816\n"
"v_mfma_f32_16x16x32_bf16 a[48:51], v[100:103], a[236:239], a[48:51]\n"
"buffer_load_dwordx4 v30, s[20:23], s77 offen offset:2048 lds\n"
"v_mfma_f32_16x16x32_bf16 a[48:51], v[96:99], a[232:235], a[48:51]\n"
"ds_read_b128 a[144:147], v8 offset:40960\n"
"v_mfma_f32_16x16x32_bf16 a[80:83], v[96:99], a[240:243], a[80:83]\n"
"v_mfma_f32_16x16x32_bf16 a[80:83], v[100:103], a[244:247], a[80:83]\n"
"ds_read_b128 a[148:151], v9 offset:40960\n"
"v_mfma_f32_16x16x32_bf16 a[112:115], v[100:103], a[252:255], a[112:115]\n"
"v_mfma_f32_16x16x32_bf16 a[112:115], v[96:99], a[248:251], a[112:115]\n"
"ds_read_b128 a[152:155], v8 offset:43008\n"
"v_mfma_f32_16x16x32_bf16 a[116:119], v[104:107], a[248:251], a[116:119]\n"
"ds_read_b128 a[156:159], v9 offset:43008\n"
"v_mfma_f32_16x16x32_bf16 a[116:119], v[108:111], a[252:255], a[116:119]\n"
"buffer_load_dwordx4 v31, s[20:23], s77 offen offset:3072 lds\n"
"v_mfma_f32_16x16x32_bf16 a[84:87], v[108:111], a[244:247], a[84:87]\n"
"s_add_u32 m0, s78, 65536\n"
"s_add_u32 s77, s76, 1048576\n"
"ds_read_b128 a[160:163], v8 offset:49152\n"
"v_mfma_f32_16x16x32_bf16 a[84:87], v[104:107], a[240:243], a[84:87]\n"
"v_mfma_f32_16x16x32_bf16 a[52:55], v[104:107], a[232:235], a[52:55]\n"
"ds_read_b128 a[164:167], v9 offset:49152\n"
"v_mfma_f32_16x16x32_bf16 a[52:55], v[108:111], a[236:239], a[52:55]\n"
"buffer_load_dwordx4 v22, s[20:23], s77 offen offset:0 lds\n"
"v_mfma_f32_16x16x32_bf16 a[20:23], v[108:111], a[228:231], a[20:23]\n"
"ds_read_b128 a[168:171], v8 offset:51200\n"
"v_mfma_f32_16x16x32_bf16 a[20:23], v[104:107], a[224:227], a[20:23]\n"
"v_mfma_f32_16x16x32_bf16 a[24:27], v[112:115], a[224:227], a[24:27]\n"
"ds_read_b128 a[172:175], v9 offset:51200\n"
"v_mfma_f32_16x16x32_bf16 a[24:27], v[116:119], a[228:231], a[24:27]\n"
"ds_read_b128 a[176:179], v8 offset:57344\n"
"v_mfma_f32_16x16x32_bf16 a[56:59], v[116:119], a[236:239], a[56:59]\n"
"v_mfma_f32_16x16x32_bf16 a[56:59], v[112:115], a[232:235], a[56:59]\n"
"ds_read_b128 a[180:183], v9 offset:57344\n"
"v_mfma_f32_16x16x32_bf16 a[88:91], v[112:115], a[240:243], a[88:91]\n"
"buffer_load_dwordx4 v23, s[20:23], s77 offen offset:1024 lds\n"
"v_mfma_f32_16x16x32_bf16 a[88:91], v[116:119], a[244:247], a[88:91]\n"
"ds_read_b128 a[184:187], v8 offset:59392\n"
"v_mfma_f32_16x16x32_bf16 a[120:123], v[116:119], a[252:255], a[120:123]\n"
"v_mfma_f32_16x16x32_bf16 a[120:123], v[112:115], a[248:251], a[120:123]\n"
"ds_read_b128 a[188:191], v9 offset:59392\n"
"v_mfma_f32_16x16x32_bf16 a[124:127], v[120:123], a[248:251], a[124:127]\n"
"v_mfma_f32_16x16x32_bf16 a[124:127], v[124:127], a[252:255], a[124:127]\n"
"buffer_load_dwordx4 v30, s[20:23], s77 offen offset:2048 lds\n"
"v_mfma_f32_16x16x32_bf16 a[92:95], v[124:127], a[244:247], a[92:95]\n"
"v_mfma_f32_16x16x32_bf16 a[92:95], v[120:123], a[240:243], a[92:95]\n"
"v_mfma_f32_16x16x32_bf16 a[60:63], v[120:123], a[232:235], a[60:63]\n"
"v_mfma_f32_16x16x32_bf16 a[60:63], v[124:127], a[236:239], a[60:63]\n"
"v_mfma_f32_16x16x32_bf16 a[28:31], v[124:127], a[228:231], a[28:31]\n"
"v_mfma_f32_16x16x32_bf16 a[28:31], v[120:123], a[224:227], a[28:31]\n"
"s_waitcnt lgkmcnt(15)\n"
"v_mfma_f32_16x16x32_bf16 v[128:131], a[192:195], a[128:131], v[128:131]\n"
"s_xor_b32 s74, s74, 0x8000\n"
"s_add_u32 s85, s82, 0\n"
"ds_read_b64_tr_b16 a[224:225], v10 offset:49152\n"
"s_waitcnt lgkmcnt(15)\n"
"v_mfma_f32_16x16x32_bf16 v[128:131], a[196:199], a[132:135], v[128:131]\n"
"buffer_load_dwordx4 v31, s[20:23], s77 offen offset:3072 lds\n"
"v_mfma_f32_16x16x32_bf16 v[132:135], a[204:207], a[132:135], v[132:135]\n"
"s_add_u32 m0, s79, 65536\n"
"s_add_u32 s77, s76, 1179648\n"
"ds_read_b64_tr_b16 a[226:227], v10 offset:50176\n"
"v_mfma_f32_16x16x32_bf16 v[132:135], a[200:203], a[128:131], v[132:135]\n"
"v_xor_b32 v8, 0, v4\n"
"v_xor_b32 v9, 32, v4\n"
"v_mfma_f32_16x16x32_bf16 v[136:139], a[208:211], a[128:131], v[136:139]\n"
"v_xor_b32 v14, 192, v4\n"
"v_xor_b32 v15, 224, v4\n"
"v_mfma_f32_16x16x32_bf16 v[136:139], a[212:215], a[132:135], v[136:139]\n"
"ds_read_b64_tr_b16 a[228:229], v10 offset:57344\n"
"v_mfma_f32_16x16x32_bf16 v[140:143], a[220:223], a[132:135], v[140:143]\n"
"v_mfma_f32_16x16x32_bf16 v[140:143], a[216:219], a[128:131], v[140:143]\n"
"buffer_load_dwordx4 v22, s[20:23], s77 offen offset:0 lds\n"
"s_waitcnt lgkmcnt(15)\n"
"v_mfma_f32_16x16x32_bf16 v[156:159], a[216:219], a[136:139], v[156:159]\n"
"s_waitcnt lgkmcnt(15)\n"
"ds_read_b64_tr_b16 a[230:231], v10 offset:58368\n"
"v_xor_b32 v10, 64, v4\n"
"v_mfma_f32_16x16x32_bf16 v[156:159], a[220:223], a[140:143], v[156:159]\n"
"v_mfma_f32_16x16x32_bf16 v[152:155], a[212:215], a[140:143], v[152:155]\n"
"v_mfma_f32_16x16x32_bf16 v[152:155], a[208:211], a[136:139], v[152:155]\n"
"ds_read_b64_tr_b16 a[232:233], v11 offset:49152\n"
"v_mfma_f32_16x16x32_bf16 v[148:151], a[200:203], a[136:139], v[148:151]\n"
"v_mfma_f32_16x16x32_bf16 v[148:151], a[204:207], a[140:143], v[148:151]\n"
"buffer_load_dwordx4 v23, s[20:23], s77 offen offset:1024 lds\n"
"v_mfma_f32_16x16x32_bf16 v[144:147], a[196:199], a[140:143], v[144:147]\n"
"ds_read_b64_tr_b16 a[234:235], v11 offset:50176\n"
"v_mfma_f32_16x16x32_bf16 v[144:147], a[192:195], a[136:139], v[144:147]\n"
"s_waitcnt lgkmcnt(15)\n"
"v_mfma_f32_16x16x32_bf16 v[160:163], a[192:195], a[144:147], v[160:163]\n"
"s_waitcnt lgkmcnt(15)\n"
"ds_read_b64_tr_b16 a[236:237], v11 offset:57344\n"
"v_mfma_f32_16x16x32_bf16 v[160:163], a[196:199], a[148:151], v[160:163]\n"
"v_mfma_f32_16x16x32_bf16 v[164:167], a[204:207], a[148:151], v[164:167]\n"
"v_mfma_f32_16x16x32_bf16 v[164:167], a[200:203], a[144:147], v[164:167]\n"
"buffer_load_dwordx4 v30, s[20:23], s77 offen offset:2048 lds\n"
"v_mfma_f32_16x16x32_bf16 v[168:171], a[208:211], a[144:147], v[168:171]\n"
"ds_read_b64_tr_b16 a[238:239], v11 offset:58368\n"
"v_xor_b32 v11, 96, v4\n"
"v_mfma_f32_16x16x32_bf16 v[168:171], a[212:215], a[148:151], v[168:171]\n"
"v_mfma_f32_16x16x32_bf16 v[172:175], a[220:223], a[148:151], v[172:175]\n"
"ds_read_b64_tr_b16 a[240:241], v12 offset:49152\n"
"v_mfma_f32_16x16x32_bf16 v[172:175], a[216:219], a[144:147], v[172:175]\n"
"s_waitcnt lgkmcnt(15)\n"
"v_mfma_f32_16x16x32_bf16 v[188:191], a[216:219], a[152:155], v[188:191]\n"
"s_waitcnt lgkmcnt(15)\n"
"v_mfma_f32_16x16x32_bf16 v[188:191], a[220:223], a[156:159], v[188:191]\n"
"buffer_load_dwordx4 v31, s[20:23], s77 offen offset:3072 lds\n"
"v_mfma_f32_16x16x32_bf16 v[184:187], a[212:215], a[156:159], v[184:187]\n"
"ds_read_b64_tr_b16 a[242:243], v12 offset:50176\n"
"v_mfma_f32_16x16x32_bf16 v[184:187], a[208:211], a[152:155], v[184:187]\n"
"v_mfma_f32_16x16x32_bf16 v[180:183], a[200:203], a[152:155], v[180:183]\n"
"ds_read_b64_tr_b16 a[244:245], v12 offset:57344\n"
"v_mfma_f32_16x16x32_bf16 v[180:183], a[204:207], a[156:159], v[180:183]\n"
"v_mfma_f32_16x16x32_bf16 v[176:179], a[196:199], a[156:159], v[176:179]\n"
"ds_read_b64_tr_b16 a[246:247], v12 offset:58368\n"
"v_xor_b32 v12, 128, v4\n"
"v_mfma_f32_16x16x32_bf16 v[176:179], a[192:195], a[152:155], v[176:179]\n"
"s_waitcnt lgkmcnt(15)\n"
"v_mfma_f32_16x16x32_bf16 v[192:195], a[192:195], a[160:163], v[192:195]\n"
"s_waitcnt lgkmcnt(15)\n"
"v_mfma_f32_16x16x32_bf16 v[192:195], a[196:199], a[164:167], v[192:195]\n"
"ds_read_b64_tr_b16 a[248:249], v13 offset:49152\n"
"v_mfma_f32_16x16x32_bf16 v[196:199], a[204:207], a[164:167], v[196:199]\n"
"v_mfma_f32_16x16x32_bf16 v[196:199], a[200:203], a[160:163], v[196:199]\n"
"v_mfma_f32_16x16x32_bf16 v[200:203], a[208:211], a[160:163], v[200:203]\n"
"buffer_load_dwordx4 v[32:35], v1, s[44:47], s85 offen offset:0 sc1\n"
"v_mfma_f32_16x16x32_bf16 v[200:203], a[212:215], a[164:167], v[200:203]\n"
"s_add_u32 s85, s82, 32768\n"
"ds_read_b64_tr_b16 a[250:251], v13 offset:50176\n"
"v_mfma_f32_16x16x32_bf16 v[204:207], a[220:223], a[164:167], v[204:207]\n"
"v_mfma_f32_16x16x32_bf16 v[204:207], a[216:219], a[160:163], v[204:207]\n"
"buffer_load_dwordx4 v[36:39], v1, s[44:47], s85 offen offset:0 sc1\n"
"s_waitcnt lgkmcnt(15)\n"
"v_mfma_f32_16x16x32_bf16 v[220:223], a[216:219], a[168:171], v[220:223]\n"
"s_add_u32 s85, s82, 65536\n"
"ds_read_b64_tr_b16 a[252:253], v13 offset:57344\n"
"s_waitcnt lgkmcnt(15)\n"
"v_mfma_f32_16x16x32_bf16 v[220:223], a[220:223], a[172:175], v[220:223]\n"
"v_mfma_f32_16x16x32_bf16 v[216:219], a[212:215], a[172:175], v[216:219]\n"
"buffer_load_dwordx4 v[40:43], v1, s[44:47], s85 offen offset:0 sc1\n"
"v_mfma_f32_16x16x32_bf16 v[216:219], a[208:211], a[168:171], v[216:219]\n"
"s_add_u32 s85, s82, 98304\n"
"ds_read_b64_tr_b16 a[254:255], v13 offset:58368\n"
"v_xor_b32 v13, 160, v4\n"
"v_mfma_f32_16x16x32_bf16 v[212:215], a[200:203], a[168:171], v[212:215]\n"
"v_mfma_f32_16x16x32_bf16 v[212:215], a[204:207], a[172:175], v[212:215]\n"
"buffer_load_dwordx4 v[44:47], v1, s[44:47], s85 offen offset:0 sc1\n"
"v_mfma_f32_16x16x32_bf16 v[208:211], a[196:199], a[172:175], v[208:211]\n"
"s_add_u32 s85, s82, 0\n"
"v_mfma_f32_16x16x32_bf16 v[208:211], a[192:195], a[168:171], v[208:211]\n"
"s_waitcnt lgkmcnt(15)\n"
"v_mfma_f32_16x16x32_bf16 v[224:227], a[192:195], a[176:179], v[224:227]\n"
"buffer_load_dwordx4 v[48:51], v1, s[44:47], s85 offen offset:1024 sc1\n"
"s_waitcnt lgkmcnt(15)\n"
"v_mfma_f32_16x16x32_bf16 v[224:227], a[196:199], a[180:183], v[224:227]\n"
"s_add_u32 s85, s82, 32768\n"
"v_mfma_f32_16x16x32_bf16 v[228:231], a[204:207], a[180:183], v[228:231]\n"
"v_mfma_f32_16x16x32_bf16 v[228:231], a[200:203], a[176:179], v[228:231]\n"
"buffer_load_dwordx4 v[52:55], v1, s[44:47], s85 offen offset:1024 sc1\n"
"v_mfma_f32_16x16x32_bf16 v[232:235], a[208:211], a[176:179], v[232:235]\n"
"s_add_u32 s85, s82, 65536\n"
"v_mfma_f32_16x16x32_bf16 v[232:235], a[212:215], a[180:183], v[232:235]\n"
"v_mfma_f32_16x16x32_bf16 v[236:239], a[220:223], a[180:183], v[236:239]\n"
"buffer_load_dwordx4 v[56:59], v1, s[44:47], s85 offen offset:1024 sc1\n"
"v_mfma_f32_16x16x32_bf16 v[236:239], a[216:219], a[176:179], v[236:239]\n"
"s_add_u32 s85, s82, 98304\n"
"s_waitcnt lgkmcnt(15)\n"
"v_mfma_f32_16x16x32_bf16 v[252:255], a[216:219], a[184:187], v[252:255]\n"
"s_waitcnt lgkmcnt(15)\n"
"v_mfma_f32_16x16x32_bf16 v[252:255], a[220:223], a[188:191], v[252:255]\n"
"buffer_load_dwordx4 v[60:63], v1, s[44:47], s85 offen offset:1024 sc1\n"
"v_mfma_f32_16x16x32_bf16 v[248:251], a[212:215], a[188:191], v[248:251]\n"
"v_mfma_f32_16x16x32_bf16 v[248:251], a[208:211], a[184:187], v[248:251]\n"
"v_mfma_f32_16x16x32_bf16 v[244:247], a[200:203], a[184:187], v[244:247]\n"
"v_mfma_f32_16x16x32_bf16 v[244:247], a[204:207], a[188:191], v[244:247]\n"
"v_mfma_f32_16x16x32_bf16 v[240:243], a[196:199], a[188:191], v[240:243]\n"
"v_mfma_f32_16x16x32_bf16 v[240:243], a[192:195], a[184:187], v[240:243]\n"
"s_waitcnt vmcnt(8) lgkmcnt(0)\n"
"s_barrier\n"
"v_mfma_f32_16x16x32_bf16 a[0:3], a[128:131], a[224:227], a[0:3]\n"
"s_add_u32 s88, s83, 0\n"
"v_cvt_pk_bf16_f32 v64, v128, v129\n"
"v_cvt_pk_bf16_f32 v65, v130, v131\n"
"v_mfma_f32_16x16x32_bf16 a[0:3], a[132:135], a[228:231], a[0:3]\n"
"s_add_u32 s85, s82, 0\n"
"v_cvt_pk_bf16_f32 v66, v132, v133\n"
"v_cvt_pk_bf16_f32 v67, v134, v135\n"
"v_mfma_f32_16x16x32_bf16 a[32:35], a[132:135], a[236:239], a[32:35]\n"
"s_add_u32 m0, s78, 32768\n"
"v_cvt_pk_bf16_f32 v68, v136, v137\n"
"v_cvt_pk_bf16_f32 v69, v138, v139\n"
"v_mfma_f32_16x16x32_bf16 a[32:35], a[128:131], a[232:235], a[32:35]\n"
"s_add_u32 s77, s76, 3145728\n"
"v_cvt_pk_bf16_f32 v70, v140, v141\n"
"v_cvt_pk_bf16_f32 v71, v142, v143\n"
"v_mfma_f32_16x16x32_bf16 a[64:67], a[128:131], a[240:243], a[64:67]\n"
"buffer_store_dwordx4 v[64:67], v24, s[44:47], s88 offen offset:0 sc1\n"
"v_mfma_f32_16x16x32_bf16 a[64:67], a[132:135], a[244:247], a[64:67]\n"
"v_cvt_pk_bf16_f32 v72, v144, v145\n"
"v_cvt_pk_bf16_f32 v73, v146, v147\n"
"v_mfma_f32_16x16x32_bf16 a[96:99], a[132:135], a[252:255], a[96:99]\n"
"v_cvt_pk_bf16_f32 v74, v148, v149\n"
"v_cvt_pk_bf16_f32 v75, v150, v151\n"
"v_mfma_f32_16x16x32_bf16 a[96:99], a[128:131], a[248:251], a[96:99]\n"
"buffer_store_dwordx4 v[68:71], v25, s[44:47], s88 offen offset:1024 sc1\n"
"v_mfma_f32_16x16x32_bf16 a[100:103], a[136:139], a[248:251], a[100:103]\n"
"s_add_u32 s88, s83, 256\n"
"v_cvt_pk_bf16_f32 v76, v152, v153\n"
"v_cvt_pk_bf16_f32 v77, v154, v155\n"
"v_mfma_f32_16x16x32_bf16 a[100:103], a[140:143], a[252:255], a[100:103]\n"
"v_cvt_pk_bf16_f32 v78, v156, v157\n"
"v_cvt_pk_bf16_f32 v79, v158, v159\n"
"v_mfma_f32_16x16x32_bf16 a[68:71], a[140:143], a[244:247], a[68:71]\n"
"v_cvt_pk_bf16_f32 v80, v160, v161\n"
"v_cvt_pk_bf16_f32 v81, v162, v163\n"
"v_mfma_f32_16x16x32_bf16 a[68:71], a[136:139], a[240:243], a[68:71]\n"
"buffer_load_dwordx4 a[128:131], v1, s[44:47], s85 offen offset:2048 sc1\n"
"v_mfma_f32_16x16x32_bf16 a[36:39], a[136:139], a[232:235], a[36:39]\n"
"s_add_u32 s85, s82, 32768\n"
"v_cvt_pk_bf16_f32 v82, v164, v165\n"
"v_cvt_pk_bf16_f32 v83, v166, v167\n"
"v_mfma_f32_16x16x32_bf16 a[36:39], a[140:143], a[236:239], a[36:39]\n"
"v_cvt_pk_bf16_f32 v84, v168, v169\n"
"v_cvt_pk_bf16_f32 v85, v170, v171\n"
"v_mfma_f32_16x16x32_bf16 a[4:7], a[140:143], a[228:231], a[4:7]\n"
"v_cvt_pk_bf16_f32 v86, v172, v173\n"
"v_cvt_pk_bf16_f32 v87, v174, v175\n"
"v_mfma_f32_16x16x32_bf16 a[4:7], a[136:139], a[224:227], a[4:7]\n"
"buffer_load_dwordx4 a[132:135], v1, s[44:47], s85 offen offset:2048 sc1\n"
"v_mfma_f32_16x16x32_bf16 a[8:11], a[144:147], a[224:227], a[8:11]\n"
"s_add_u32 s85, s82, 65536\n"
"v_cvt_pk_bf16_f32 v88, v176, v177\n"
"v_cvt_pk_bf16_f32 v89, v178, v179\n"
"v_mfma_f32_16x16x32_bf16 a[8:11], a[148:151], a[228:231], a[8:11]\n"
"v_cvt_pk_bf16_f32 v90, v180, v181\n"
"v_cvt_pk_bf16_f32 v91, v182, v183\n"
"v_mfma_f32_16x16x32_bf16 a[40:43], a[148:151], a[236:239], a[40:43]\n"
"buffer_load_dwordx4 a[136:139], v1, s[44:47], s85 offen offset:2048 sc1\n"
"v_mfma_f32_16x16x32_bf16 a[40:43], a[144:147], a[232:235], a[40:43]\n"
"s_add_u32 s85, s82, 98304\n"
"v_cvt_pk_bf16_f32 v92, v184, v185\n"
"v_cvt_pk_bf16_f32 v93, v186, v187\n"
"v_mfma_f32_16x16x32_bf16 a[72:75], a[144:147], a[240:243], a[72:75]\n"
"v_cvt_pk_bf16_f32 v94, v188, v189\n"
"v_cvt_pk_bf16_f32 v95, v190, v191\n"
"v_mfma_f32_16x16x32_bf16 a[72:75], a[148:151], a[244:247], a[72:75]\n"
"v_cvt_pk_bf16_f32 v96, v192, v193\n"
"v_cvt_pk_bf16_f32 v97, v194, v195\n"
"v_mfma_f32_16x16x32_bf16 a[104:107], a[148:151], a[252:255], a[104:107]\n"
"buffer_load_dwordx4 a[140:143], v1, s[44:47], s85 offen offset:2048 sc1\n"
"v_mfma_f32_16x16x32_bf16 a[104:107], a[144:147], a[248:251], a[104:107]\n"
"s_add_u32 s85, s82, 0\n"
"v_cvt_pk_bf16_f32 v98, v196, v197\n"
"v_cvt_pk_bf16_f32 v99, v198, v199\n"
"v_mfma_f32_16x16x32_bf16 a[108:111], a[152:155], a[248:251], a[108:111]\n"
"v_cvt_pk_bf16_f32 v100, v200, v201\n"
"v_cvt_pk_bf16_f32 v101, v202, v203\n"
"v_mfma_f32_16x16x32_bf16 a[108:111], a[156:159], a[252:255], a[108:111]\n"
"v_cvt_pk_bf16_f32 v102, v204, v205\n"
"v_cvt_pk_bf16_f32 v103, v206, v207\n"
"v_mfma_f32_16x16x32_bf16 a[76:79], a[156:159], a[244:247], a[76:79]\n"
"buffer_store_dwordx4 v[72:75], v24, s[44:47], s88 offen offset:0 sc1\n"
"v_mfma_f32_16x16x32_bf16 a[76:79], a[152:155], a[240:243], a[76:79]\n"
"v_cvt_pk_bf16_f32 v104, v208, v209\n"
"v_cvt_pk_bf16_f32 v105, v210, v211\n"
"v_mfma_f32_16x16x32_bf16 a[44:47], a[152:155], a[232:235], a[44:47]\n"
"v_cvt_pk_bf16_f32 v106, v212, v213\n"
"v_cvt_pk_bf16_f32 v107, v214, v215\n"
"v_mfma_f32_16x16x32_bf16 a[44:47], a[156:159], a[236:239], a[44:47]\n"
"buffer_load_dwordx4 a[144:147], v1, s[44:47], s85 offen offset:3072 sc1\n"
"v_mfma_f32_16x16x32_bf16 a[12:15], a[156:159], a[228:231], a[12:15]\n"
"s_add_u32 s85, s82, 32768\n"
"ds_read_b64_tr_b16 a[192:193], v8 offset:0\n"
"v_cvt_pk_bf16_f32 v108, v216, v217\n"
"v_mfma_f32_16x16x32_bf16 a[12:15], a[152:155], a[224:227], a[12:15]\n"
"ds_read_b64_tr_b16 a[194:195], v8 offset:1024\n"
"v_cvt_pk_bf16_f32 v109, v218, v219\n"
"v_mfma_f32_16x16x32_bf16 a[16:19], a[160:163], a[224:227], a[16:19]\n"
"v_cvt_pk_bf16_f32 v110, v220, v221\n"
"v_cvt_pk_bf16_f32 v111, v222, v223\n"
"v_mfma_f32_16x16x32_bf16 a[16:19], a[164:167], a[228:231], a[16:19]\n"
"ds_read_b64_tr_b16 a[196:197], v8 offset:8192\n"
"v_cvt_pk_bf16_f32 v112, v224, v225\n"
"v_mfma_f32_16x16x32_bf16 a[48:51], a[164:167], a[236:239], a[48:51]\n"
"buffer_load_dwordx4 a[148:151], v1, s[44:47], s85 offen offset:3072 sc1\n"
"v_mfma_f32_16x16x32_bf16 a[48:51], a[160:163], a[232:235], a[48:51]\n"
"s_add_u32 s85, s82, 65536\n"
"ds_read_b64_tr_b16 a[198:199], v8 offset:9216\n"
"v_cvt_pk_bf16_f32 v113, v226, v227\n"
"v_mfma_f32_16x16x32_bf16 a[80:83], a[160:163], a[240:243], a[80:83]\n"
"ds_read_b64_tr_b16 a[200:201], v9 offset:0\n"
"v_cvt_pk_bf16_f32 v114, v228, v229\n"
"v_mfma_f32_16x16x32_bf16 a[80:83], a[164:167], a[244:247], a[80:83]\n"
"buffer_load_dwordx4 a[152:155], v1, s[44:47], s85 offen offset:3072 sc1\n"
"v_mfma_f32_16x16x32_bf16 a[112:115], a[164:167], a[252:255], a[112:115]\n"
"s_add_u32 s85, s82, 98304\n"
"ds_read_b64_tr_b16 a[202:203], v9 offset:1024\n"
"v_cvt_pk_bf16_f32 v115, v230, v231\n"
"v_mfma_f32_16x16x32_bf16 a[112:115], a[160:163], a[248:251], a[112:115]\n"
"v_cvt_pk_bf16_f32 v116, v232, v233\n"
"v_cvt_pk_bf16_f32 v117, v234, v235\n"
"v_mfma_f32_16x16x32_bf16 a[116:119], a[168:171], a[248:251], a[116:119]\n"
"ds_read_b64_tr_b16 a[204:205], v9 offset:8192\n"
"v_cvt_pk_bf16_f32 v118, v236, v237\n"
"v_mfma_f32_16x16x32_bf16 a[116:119], a[172:175], a[252:255], a[116:119]\n"
"buffer_load_dwordx4 a[156:159], v1, s[44:47], s85 offen offset:3072 sc1\n"
"v_mfma_f32_16x16x32_bf16 a[84:87], a[172:175], a[244:247], a[84:87]\n"
"ds_read_b64_tr_b16 a[206:207], v9 offset:9216\n"
"v_cvt_pk_bf16_f32 v119, v238, v239\n"
"v_mfma_f32_16x16x32_bf16 a[84:87], a[168:171], a[240:243], a[84:87]\n"
"ds_read_b64_tr_b16 a[208:209], v10 offset:0\n"
"v_cvt_pk_bf16_f32 v120, v240, v241\n"
"v_mfma_f32_16x16x32_bf16 a[52:55], a[168:171], a[232:235], a[52:55]\n"
"buffer_store_dwordx4 v[76:79], v25, s[44:47], s88 offen offset:1024 sc1\n"
"v_mfma_f32_16x16x32_bf16 a[52:55], a[172:175], a[236:239], a[52:55]\n"
"ds_read_b64_tr_b16 a[210:211], v10 offset:1024\n"
"v_cvt_pk_bf16_f32 v121, v242, v243\n"
"v_mfma_f32_16x16x32_bf16 a[20:23], a[172:175], a[228:231], a[20:23]\n"
"ds_read_b64_tr_b16 a[212:213], v10 offset:8192\n"
"v_cvt_pk_bf16_f32 v122, v244, v245\n"
"v_mfma_f32_16x16x32_bf16 a[20:23], a[168:171], a[224:227], a[20:23]\n"
"v_cvt_pk_bf16_f32 v123, v246, v247\n"
"v_cvt_pk_bf16_f32 v124, v248, v249\n"
"v_mfma_f32_16x16x32_bf16 a[24:27], a[176:179], a[224:227], a[24:27]\n"
"ds_read_b64_tr_b16 a[214:215], v10 offset:9216\n"
"v_cvt_pk_bf16_f32 v125, v250, v251\n"
"v_mfma_f32_16x16x32_bf16 a[24:27], a[180:183], a[228:231], a[24:27]\n"
"buffer_load_dwordx4 v22, s[20:23], s77 offen offset:0 lds\n"
"v_mfma_f32_16x16x32_bf16 a[56:59], a[180:183], a[236:239], a[56:59]\n"
"ds_read_b64_tr_b16 a[216:217], v11 offset:0\n"
"v_cvt_pk_bf16_f32 v126, v252, v253\n"
"v_mfma_f32_16x16x32_bf16 a[56:59], a[176:179], a[232:235], a[56:59]\n"
"ds_read_b64_tr_b16 a[218:219], v11 offset:1024\n"
"v_cvt_pk_bf16_f32 v127, v254, v255\n"
"v_mfma_f32_16x16x32_bf16 a[88:91], a[176:179], a[240:243], a[88:91]\n"
"buffer_load_dwordx4 v23, s[20:23], s77 offen offset:1024 lds\n"
"v_mfma_f32_16x16x32_bf16 a[88:91], a[180:183], a[244:247], a[88:91]\n"
"ds_read_b64_tr_b16 a[220:221], v11 offset:8192\n"
"v_mfma_f32_16x16x32_bf16 a[120:123], a[180:183], a[252:255], a[120:123]\n"
"v_mfma_f32_16x16x32_bf16 a[120:123], a[176:179], a[248:251], a[120:123]\n"
"ds_read_b64_tr_b16 a[222:223], v11 offset:9216\n"
"v_mfma_f32_16x16x32_bf16 a[124:127], a[184:187], a[248:251], a[124:127]\n"
"buffer_load_dwordx4 v30, s[20:23], s77 offen offset:2048 lds\n"
"v_mfma_f32_16x16x32_bf16 a[124:127], a[188:191], a[252:255], a[124:127]\n"
"v_mfma_f32_16x16x32_bf16 a[92:95], a[188:191], a[244:247], a[92:95]\n"
"v_mfma_f32_16x16x32_bf16 a[92:95], a[184:187], a[240:243], a[92:95]\n"
"buffer_load_dwordx4 v31, s[20:23], s77 offen offset:3072 lds\n"
"v_mfma_f32_16x16x32_bf16 a[60:63], a[184:187], a[232:235], a[60:63]\n"
"v_mfma_f32_16x16x32_bf16 a[60:63], a[188:191], a[236:239], a[60:63]\n"
"v_mfma_f32_16x16x32_bf16 a[28:31], a[188:191], a[228:231], a[28:31]\n"
"v_mfma_f32_16x16x32_bf16 a[28:31], a[184:187], a[224:227], a[28:31]\n"
"Q0_%=:\n"
"s_waitcnt vmcnt(16)\n"
"s_waitcnt lgkmcnt(14)\n"
"v_mfma_f32_16x16x32_bf16 v[128:131], a[192:195], v[32:35], 0\n"
"s_add_u32 s85, s82, 4096\n"
"ds_read_b64_tr_b16 a[224:225], v12 offset:0\n"
"ds_read_b64_tr_b16 a[226:227], v12 offset:1024\n"
"s_waitcnt lgkmcnt(14)\n"
"v_mfma_f32_16x16x32_bf16 v[128:131], a[196:199], v[48:51], v[128:131]\n"
"s_add_u32 s88, s84, 0\n"
"ds_read_b64_tr_b16 a[228:229], v12 offset:8192\n"
"ds_read_b64_tr_b16 a[230:231], v12 offset:9216\n"
"v_mfma_f32_16x16x32_bf16 v[160:163], a[196:199], v[52:55], 0\n"
"ds_read_b64_tr_b16 a[232:233], v13 offset:0\n"
"ds_read_b64_tr_b16 a[234:235], v13 offset:1024\n"
"v_mfma_f32_16x16x32_bf16 v[160:163], a[192:195], v[36:39], v[160:163]\n"
"ds_read_b64_tr_b16 a[236:237], v13 offset:8192\n"
"ds_read_b64_tr_b16 a[238:239], v13 offset:9216\n"
"v_mfma_f32_16x16x32_bf16 v[192:195], a[192:195], v[40:43], 0\n"
"ds_read_b64_tr_b16 a[240:241], v14 offset:0\n"
"ds_read_b64_tr_b16 a[242:243], v14 offset:1024\n"
"v_mfma_f32_16x16x32_bf16 v[192:195], a[196:199], v[56:59], v[192:195]\n"
"ds_read_b64_tr_b16 a[244:245], v14 offset:8192\n"
"ds_read_b64_tr_b16 a[246:247], v14 offset:9216\n"
"v_mfma_f32_16x16x32_bf16 v[224:227], a[196:199], v[60:63], 0\n"
"ds_read_b64_tr_b16 a[248:249], v15 offset:0\n"
"ds_read_b64_tr_b16 a[250:251], v15 offset:1024\n"
"v_mfma_f32_16x16x32_bf16 v[224:227], a[192:195], v[44:47], v[224:227]\n"
"s_waitcnt lgkmcnt(15)\n"
"ds_read_b64_tr_b16 a[252:253], v15 offset:8192\n"
"ds_read_b64_tr_b16 a[254:255], v15 offset:9216\n"
"v_mfma_f32_16x16x32_bf16 v[228:231], a[200:203], v[44:47], 0\n"
"buffer_load_dwordx4 a[160:163], v1, s[44:47], s85 offen offset:0 sc1\n"
"s_waitcnt lgkmcnt(15)\n"
"v_mfma_f32_16x16x32_bf16 v[228:231], a[204:207], v[60:63], v[228:231]\n"
"s_add_u32 s85, s82, 36864\n"
"v_accvgpr_read_b32 v64, a0\n"
"v_accvgpr_read_b32 v65, a1\n"
"v_mfma_f32_16x16x32_bf16 v[196:199], a[204:207], v[56:59], 0\n"
"buffer_load_dwordx4 a[164:167], v1, s[44:47], s85 offen offset:0 sc1\n"
"v_mfma_f32_16x16x32_bf16 v[196:199], a[200:203], v[40:43], v[196:199]\n"
"s_add_u32 s85, s82, 69632\n"
"v_accvgpr_read_b32 v66, a2\n"
"v_accvgpr_read_b32 v67, a3\n"
"v_mfma_f32_16x16x32_bf16 v[164:167], a[200:203], v[36:39], 0\n"
"v_accvgpr_read_b32 v68, a4\n"
"s_nop 0\n"
"PG4_%=:\n"
"s_cbranch_execz PADRET_%=\n"
"v_accvgpr_read_b32 v69, a5\n"
"v_mfma_f32_16x16x32_bf16 v[164:167], a[204:207], v[52:55], v[164:167]\n"
"buffer_load_dwordx4 a[168:171], v1, s[44:47], s85 offen offset:0 sc1\n"
"v_mfma_f32_16x16x32_bf16 v[132:135], a[204:207], v[48:51], 0\n"
"s_add_u32 s85, s82, 102400\n"
"v_accvgpr_read_b32 v70, a6\n"
"v_accvgpr_read_b32 v71, a7\n"
"v_mfma_f32_16x16x32_bf16 v[132:135], a[200:203], v[32:35], v[132:135]\n"
"s_waitcnt lgkmcnt(15)\n"
"v_cvt_pk_bf16_f32 v64, v64, v65\n"
"v_cvt_pk_bf16_f32 v65, v66, v67\n"
"v_mfma_f32_16x16x32_bf16 v[136:139], a[208:211], v[32:35], 0\n"
"s_waitcnt lgkmcnt(15)\n"
"v_cvt_pk_bf16_f32 v66, v68, v69\n"
"v_cvt_pk_bf16_f32 v67, v70, v71\n"
"v_mfma_f32_16x16x32_bf16 v[136:139], a[212:215], v[48:51], v[136:139]\n"
"v_accvgpr_read_b32 v72, a32\n"
"v_accvgpr_read_b32 v73, a33\n"
"v_mfma_f32_16x16x32_bf16 v[168:171], a[212:215], v[52:55], 0\n"
"v_accvgpr_read_b32 v74, a34\n"
"v_accvgpr_read_b32 v75, a35\n"
"v_mfma_f32_16x16x32_bf16 v[168:171], a[208:211], v[36:39], v[168:171]\n"
"buffer_load_dwordx4 a[172:175], v1, s[44:47], s85 offen offset:0 sc1\n"
"v_mfma_f32_16x16x32_bf16 v[200:203], a[208:211], v[40:43], 0\n"
"s_add_u32 s85, s82, 4096\n"
"v_accvgpr_read_b32 v76, a36\n"
"v_accvgpr_read_b32 v77, a37\n"
"v_mfma_f32_16x16x32_bf16 v[200:203], a[212:215], v[56:59], v[200:203]\n"
"v_accvgpr_read_b32 v78, a38\n"
"v_accvgpr_read_b32 v79, a39\n"
"v_mfma_f32_16x16x32_bf16 v[232:235], a[212:215], v[60:63], 0\n"
"v_cvt_pk_bf16_f32 v72, v72, v73\n"
"v_cvt_pk_bf16_f32 v73, v74, v75\n"
"v_mfma_f32_16x16x32_bf16 v[232:235], a[208:211], v[44:47], v[232:235]\n"
"s_waitcnt lgkmcnt(15)\n"
"v_cvt_pk_bf16_f32 v74, v76, v77\n"
"v_cvt_pk_bf16_f32 v75, v78, v79\n"
"v_mfma_f32_16x16x32_bf16 v[236:239], a[216:219], v[44:47], 0\n"
"s_waitcnt lgkmcnt(15)\n"
"ds_read_b64_tr_b16 a[192:193], v8 offset:16384\n"
"ds_read_b64_tr_b16 a[194:195], v8 offset:17408\n"
"v_mfma_f32_16x16x32_bf16 v[236:239], a[220:223], v[60:63], v[236:239]\n"
"buffer_store_dwordx4 v[64:67], v24, s[44:47], s88 offen offset:0 sc1\n"
"v_mfma_f32_16x16x32_bf16 v[204:207], a[220:223], v[56:59], 0\n"
"ds_read_b64_tr_b16 a[196:197], v8 offset:24576\n"
"ds_read_b64_tr_b16 a[198:199], v8 offset:25600\n"
"v_mfma_f32_16x16x32_bf16 v[204:207], a[216:219], v[40:43], v[204:207]\n"
"ds_read_b64_tr_b16 a[200:201], v9 offset:16384\n"
"ds_read_b64_tr_b16 a[202:203], v9 offset:17408\n"
"v_mfma_f32_16x16x32_bf16 v[172:175], a[216:219], v[36:39], 0\n"
"ds_read_b64_tr_b16 a[204:205], v9 offset:24576\n"
"ds_read_b64_tr_b16 a[206:207], v9 offset:25600\n"
"v_mfma_f32_16x16x32_bf16 v[172:175], a[220:223], v[52:55], v[172:175]\n"
"ds_read_b64_tr_b16 a[208:209], v10 offset:16384\n"
"ds_read_b64_tr_b16 a[210:211], v10 offset:17408\n"
"v_mfma_f32_16x16x32_bf16 v[140:143], a[220:223], v[48:51], 0\n"
"ds_read_b64_tr_b16 a[212:213], v10 offset:24576\n"
"ds_read_b64_tr_b16 a[214:215], v10 offset:25600\n"
"v_mfma_f32_16x16x32_bf16 v[140:143], a[216:219], v[32:35], v[140:143]\n"
"s_waitcnt lgkmcnt(15)\n"
"ds_read_b64_tr_b16 a[216:217], v11 offset:16384\n"
"ds_read_b64_tr_b16 a[218:219], v11 offset:17408\n"
"v_mfma_f32_16x16x32_bf16 v[144:147], a[224:227], v[32:35], 0\n"
"buffer_load_dwordx4 a[176:179], v1, s[44:47], s85 offen offset:1024 sc1\n"
"s_waitcnt lgkmcnt(15)\n"
"v_mfma_f32_16x16x32_bf16 v[144:147], a[228:231], v[48:51], v[144:147]\n"
"s_add_u32 s85, s82, 36864\n"
"ds_read_b64_tr_b16 a[220:221], v11 offset:24576\n"
"ds_read_b64_tr_b16 a[222:223], v11 offset:25600\n"
"v_mfma_f32_16x16x32_bf16 v[176:179], a[228:231], v[52:55], 0\n"
"v_mfma_f32_16x16x32_bf16 v[176:179], a[224:227], v[36:39], v[176:179]\n"
"v_mfma_f32_16x16x32_bf16 v[208:211], a[224:227], v[40:43], 0\n"
"v_mfma_f32_16x16x32_bf16 v[208:211], a[228:231], v[56:59], v[208:211]\n"
"v_mfma_f32_16x16x32_bf16 v[240:243], a[228:231], v[60:63], 0\n"
"buffer_load_dwordx4 a[180:183], v1, s[44:47], s85 offen offset:1024 sc1\n"
"v_mfma_f32_16x16x32_bf16 v[240:243], a[224:227], v[44:47], v[240:243]\n"
"s_add_u32 s85, s82, 69632\n"
"ds_read_b64_tr_b16 a[224:225], v12 offset:16384\n"
"ds_read_b64_tr_b16 a[226:227], v12 offset:17408\n"
"s_waitcnt lgkmcnt(15)\n"
"v_mfma_f32_16x16x32_bf16 v[244:247], a[232:235], v[44:47], 0\n"
"s_waitcnt lgkmcnt(15)\n"
"ds_read_b64_tr_b16 a[228:229], v12 offset:24576\n"
"ds_read_b64_tr_b16 a[230:231], v12 offset:25600\n"
"v_mfma_f32_16x16x32_bf16 v[244:247], a[236:239], v[60:63], v[244:247]\n"
"v_mfma_f32_16x16x32_bf16 v[212:215], a[236:239], v[56:59], 0\n"
"v_mfma_f32_16x16x32_bf16 v[212:215], a[232:235], v[40:43], v[212:215]\n"
"v_mfma_f32_16x16x32_bf16 v[180:183], a[232:235], v[36:39], 0\n"
"buffer_load_dwordx4 a[184:187], v1, s[44:47], s85 offen offset:1024 sc1\n"
"v_mfma_f32_16x16x32_bf16 v[180:183], a[236:239], v[52:55], v[180:183]\n"
"s_add_u32 s85, s82, 102400\n"
"v_mfma_f32_16x16x32_bf16 v[148:151], a[236:239], v[48:51], 0\n"
"v_mfma_f32_16x16x32_bf16 v[148:151], a[232:235], v[32:35], v[148:151]\n"
"s_waitcnt lgkmcnt(15)\n"
"ds_read_b64_tr_b16 a[232:233], v13 offset:16384\n"
"ds_read_b64_tr_b16 a[234:235], v13 offset:17408\n"
"v_mfma_f32_16x16x32_bf16 v[152:155], a[240:243], v[32:35], 0\n"
"s_waitcnt lgkmcnt(15)\n"
"ds_read_b64_tr_b16 a[236:237], v13 offset:24576\n"
"ds_read_b64_tr_b16 a[238:239], v13 offset:25600\n"
"v_mfma_f32_16x16x32_bf16 v[152:155], a[244:247], v[48:51], v[152:155]\n"
"v_mfma_f32_16x16x32_bf16 v[184:187], a[244:247], v[52:55], 0\n"
"buffer_load_dwordx4 a[188:191], v1, s[44:47], s85 offen offset:1024 sc1\n"
"v_mfma_f32_16x16x32_bf16 v[184:187], a[240:243], v[36:39], v[184:187]\n"
"v_mfma_f32_16x16x32_bf16 v[216:219], a[240:243], v[40:43], 0\n"
"v_mfma_f32_16x16x32_bf16 v[216:219], a[244:247], v[56:59], v[216:219]\n"
"v_mfma_f32_16x16x32_bf16 v[248:251], a[244:247], v[60:63], 0\n"
"v_mfma_f32_16x16x32_bf16 v[248:251], a[240:243], v[44:47], v[248:251]\n"
"s_waitcnt lgkmcnt(15)\n"
"ds_read_b64_tr_b16 a[240:241], v14 offset:16384\n"
"ds_read_b64_tr_b16 a[242:243], v14 offset:17408\n"
"v_mfma_f32_16x16x32_bf16 v[252:255], a[248:251], v[44:47], 0\n"
"buffer_store_dwordx4 v[72:75], v25, s[44:47], s88 offen offset:1024 sc1\n"
"s_waitcnt lgkmcnt(15)\n"
"v_mfma_f32_16x16x32_bf16 v[252:255], a[252:255], v[60:63], v[252:255]\n"
"ds_read_b64_tr_b16 a[244:245], v14 offset:24576\n"
"ds_read_b64_tr_b16 a[246:247], v14 offset:25600\n"
"v_mfma_f32_16x16x32_bf16 v[220:223], a[252:255], v[56:59], 0\n"
"v_mfma_f32_16x16x32_bf16 v[220:223], a[248:251], v[40:43], v[220:223]\n"
"v_mfma_f32_16x16x32_bf16 v[188:191], a[248:251], v[36:39], 0\n"
"v_mfma_f32_16x16x32_bf16 v[188:191], a[252:255], v[52:55], v[188:191]\n"
"v_mfma_f32_16x16x32_bf16 v[156:159], a[252:255], v[48:51], 0\n"
"v_mfma_f32_16x16x32_bf16 v[156:159], a[248:251], v[32:35], v[156:159]\n"
"s_waitcnt vmcnt(15)\n"
"s_waitcnt lgkmcnt(15)\n"
"v_mfma_f32_16x16x32_bf16 v[128:131], a[192:195], a[128:131], v[128:131]\n"
"s_add_u32 s85, s82, 4096\n"
"ds_read_b64_tr_b16 a[248:249], v15 offset:16384\n"
"ds_read_b64_tr_b16 a[250:251], v15 offset:17408\n"
"s_waitcnt lgkmcnt(15)\n"
"v_mfma_f32_16x16x32_bf16 v[128:131], a[196:199], a[144:147], v[128:131]\n"
"s_add_u32 m0, s79, 32768\n"
"ds_read_b64_tr_b16 a[252:253], v15 offset:24576\n"
"ds_read_b64_tr_b16 a[254:255], v15 offset:25600\n"
"v_mfma_f32_16x16x32_bf16 v[160:163], a[196:199], a[148:151], v[160:163]\n"
"buffer_load_dwordx4 v[32:35], v1, s[44:47], s85 offen offset:2048 sc1\n"
"v_mfma_f32_16x16x32_bf16 v[160:163], a[192:195], a[132:135], v[160:163]\n"
"s_add_u32 s85, s82, 36864\n"
"v_accvgpr_read_b32 v64, a64\n"
"v_accvgpr_read_b32 v65, a65\n"
"v_mfma_f32_16x16x32_bf16 v[192:195], a[192:195], a[136:139], v[192:195]\n"
"s_add_u32 s77, s76, 3276800\n"
"v_accvgpr_read_b32 v66, a66\n"
"v_accvgpr_read_b32 v67, a67\n"
"v_mfma_f32_16x16x32_bf16 v[192:195], a[196:199], a[152:155], v[192:195]\n"
"v_accvgpr_read_b32 v68, a68\n"
"v_accvgpr_read_b32 v69, a69\n"
"v_mfma_f32_16x16x32_bf16 v[224:227], a[196:199], a[156:159], v[224:227]\n"
"buffer_load_dwordx4 v[36:39], v1, s[44:47], s85 offen offset:2048 sc1\n"
"v_mfma_f32_16x16x32_bf16 v[224:227], a[192:195], a[140:143], v[224:227]\n"
"s_add_u32 s85, s82, 69632\n"
"v_accvgpr_read_b32 v70, a70\n"
"v_accvgpr_read_b32 v71, a71\n"
"s_waitcnt lgkmcnt(15)\n"
"v_mfma_f32_16x16x32_bf16 v[228:231], a[200:203], a[140:143], v[228:231]\n"
"s_waitcnt lgkmcnt(15)\n"
"v_cvt_pk_bf16_f32 v64, v64, v65\n"
"v_cvt_pk_bf16_f32 v65, v66, v67\n"
"v_mfma_f32_16x16x32_bf16 v[228:231], a[204:207], a[156:159], v[228:231]\n"
"v_cvt_pk_bf16_f32 v66, v68, v69\n"
"v_cvt_pk_bf16_f32 v67, v70, v71\n"
"v_mfma_f32_16x16x32_bf16 v[196:199], a[204:207], a[152:155], v[196:199]\n"
"v_accvgpr_read_b32 v72, a96\n"
"v_accvgpr_read_b32 v73, a97\n"
"v_mfma_f32_16x16x32_bf16 v[196:199], a[200:203], a[136:139], v[196:199]\n"
"buffer_load_dwordx4 v[40:43], v1, s[44:47], s85 offen offset:2048 sc1\n"
"v_mfma_f32_16x16x32_bf16 v[164:167], a[200:203], a[132:135], v[164:167]\n"
"s_add_u32 s85, s82, 102400\n"
"v_accvgpr_read_b32 v74, a98\n"
"v_accvgpr_read_b32 v75, a99\n"
"v_mfma_f32_16x16x32_bf16 v[164:167], a[204:207], a[148:151], v[164:167]\n"
"v_accvgpr_read_b32 v76, a100\n"
"v_accvgpr_read_b32 v77, a101\n"
"v_mfma_f32_16x16x32_bf16 v[132:135], a[204:207], a[144:147], v[132:135]\n"
"v_accvgpr_read_b32 v78, a102\n"
"v_accvgpr_read_b32 v79, a103\n"
"v_mfma_f32_16x16x32_bf16 v[132:135], a[200:203], a[128:131], v[132:135]\n"
"s_waitcnt lgkmcnt(15)\n"
"v_cvt_pk_bf16_f32 v72, v72, v73\n"
"v_cvt_pk_bf16_f32 v73, v74, v75\n"
"v_mfma_f32_16x16x32_bf16 v[136:139], a[208:211], a[128:131], v[136:139]\n"
"buffer_load_dwordx4 v[44:47], v1, s[44:47], s85 offen offset:2048 sc1\n"
"s_waitcnt lgkmcnt(15)\n"
"v_mfma_f32_16x16x32_bf16 v[136:139], a[212:215], a[144:147], v[136:139]\n"
"s_add_u32 s85, s82, 4096\n"
"v_cvt_pk_bf16_f32 v74, v76, v77\n"
"v_cvt_pk_bf16_f32 v75, v78, v79\n"
"v_mfma_f32_16x16x32_bf16 v[168:171], a[212:215], a[148:151], v[168:171]\n"
"v_add_u32 v8, 65536, v8\n"
"v_add_u32 v9, 65536, v9\n"
"v_mfma_f32_16x16x32_bf16 v[168:171], a[208:211], a[132:135], v[168:171]\n"
"v_add_u32 v10, 65536, v10\n"
"v_add_u32 v11, 65536, v11\n"
"v_mfma_f32_16x16x32_bf16 v[200:203], a[208:211], a[136:139], v[200:203]\n"
"v_add_u32 v12, 65536, v12\n"
"v_add_u32 v13, 65536, v13\n"
"v_mfma_f32_16x16x32_bf16 v[200:203], a[212:215], a[152:155], v[200:203]\n"
"buffer_load_dwordx4 v[48:51], v1, s[44:47], s85 offen offset:3072 sc1\n"
"v_mfma_f32_16x16x32_bf16 v[232:235], a[212:215], a[156:159], v[232:235]\n"
"s_add_u32 s85, s82, 36864\n"
"ds_read_b64_tr_b16 a[192:193], v8 offset:0\n"
"v_add_u32 v14, 65536, v14\n"
"v_mfma_f32_16x16x32_bf16 v[232:235], a[208:211], a[140:143], v[232:235]\n"
"s_waitcnt lgkmcnt(15)\n"
"ds_read_b64_tr_b16 a[194:195], v8 offset:1024\n"
"ds_read_b64_tr_b16 a[196:197], v8 offset:8192\n"
"v_mfma_f32_16x16x32_bf16 v[236:239], a[216:219], a[140:143], v[236:239]\n"
"s_waitcnt lgkmcnt(15)\n"
"ds_read_b64_tr_b16 a[198:199], v8 offset:9216\n"
"ds_read_b64_tr_b16 a[200:201], v9 offset:0\n"
"v_mfma_f32_16x16x32_bf16 v[236:239], a[220:223], a[156:159], v[236:239]\n"
"ds_read_b64_tr_b16 a[202:203], v9 offset:1024\n"
"ds_read_b64_tr_b16 a[204:205], v9 offset:8192\n"
"v_mfma_f32_16x16x32_bf16 v[204:207], a[220:223], a[152:155], v[204:207]\n"
"buffer_store_dwordx4 v[64:67], v26, s[44:47], s88 offen offset:2048 sc1\n"
"v_mfma_f32_16x16x32_bf16 v[204:207], a[216:219], a[136:139], v[204:207]\n"
"ds_read_b64_tr_b16 a[206:207], v9 offset:9216\n"
"ds_read_b64_tr_b16 a[208:209], v10 offset:0\n"
"v_mfma_f32_16x16x32_bf16 v[172:175], a[216:219], a[132:135], v[172:175]\n"
"ds_read_b64_tr_b16 a[210:211], v10 offset:1024\n"
"ds_read_b64_tr_b16 a[212:213], v10 offset:8192\n"
"v_mfma_f32_16x16x32_bf16 v[172:175], a[220:223], a[148:151], v[172:175]\n"
"ds_read_b64_tr_b16 a[214:215], v10 offset:9216\n"
"v_mfma_f32_16x16x32_bf16 v[140:143], a[220:223], a[144:147], v[140:143]\n"
"v_mfma_f32_16x16x32_bf16 v[140:143], a[216:219], a[128:131], v[140:143]\n"
"s_waitcnt lgkmcnt(15)\n"
"ds_read_b64_tr_b16 a[216:217], v11 offset:0\n"
"ds_read_b64_tr_b16 a[218:219], v11 offset:1024\n"
"v_mfma_f32_16x16x32_bf16 v[144:147], a[224:227], a[128:131], v[144:147]\n"
"buffer_load_dwordx4 v[52:55], v1, s[44:47], s85 offen offset:3072 sc1\n"
"s_waitcnt lgkmcnt(15)\n"
"v_mfma_f32_16x16x32_bf16 v[144:147], a[228:231], a[144:147], v[144:147]\n"
"s_add_u32 s85, s82, 69632\n"
"ds_read_b64_tr_b16 a[220:221], v11 offset:8192\n"
"ds_read_b64_tr_b16 a[222:223], v11 offset:9216\n"
"v_mfma_f32_16x16x32_bf16 v[176:179], a[228:231], a[148:151], v[176:179]\n"
"v_mfma_f32_16x16x32_bf16 v[176:179], a[224:227], a[132:135], v[176:179]\n"
"v_mfma_f32_16x16x32_bf16 v[208:211], a[224:227], a[136:139], v[208:211]\n"
"v_mfma_f32_16x16x32_bf16 v[208:211], a[228:231], a[152:155], v[208:211]\n"
"buffer_load_dwordx4 v[56:59], v1, s[44:47], s85 offen offset:3072 sc1\n"
"v_mfma_f32_16x16x32_bf16 v[240:243], a[228:231], a[156:159], v[240:243]\n"
"s_add_u32 s85, s82, 102400\n"
"v_mfma_f32_16x16x32_bf16 v[240:243], a[224:227], a[140:143], v[240:243]\n"
"s_waitcnt lgkmcnt(15)\n"
"ds_read_b64_tr_b16 a[224:225], v12 offset:0\n"
"ds_read_b64_tr_b16 a[226:227], v12 offset:1024\n"
"v_mfma_f32_16x16x32_bf16 v[244:247], a[232:235], a[140:143], v[244:247]\n"
"s_waitcnt lgkmcnt(15)\n"
"ds_read_b64_tr_b16 a[228:229], v12 offset:8192\n"
"ds_read_b64_tr_b16 a[230:231], v12 offset:9216\n"
"v_mfma_f32_16x16x32_bf16 v[244:247], a[236:239], a[156:159], v[244:247]\n"
"v_mfma_f32_16x16x32_bf16 v[212:215], a[236:239], a[152:155], v[212:215]\n"
"buffer_load_dwordx4 v[60:63], v1, s[44:47], s85 offen offset:3072 sc1\n"
"v_mfma_f32_16x16x32_bf16 v[212:215], a[232:235], a[136:139], v[212:215]\n"
"v_mfma_f32_16x16x32_bf16 v[180:183], a[232:235], a[132:135], v[180:183]\n"
"v_mfma_f32_16x16x32_bf16 v[180:183], a[236:239], a[148:151], v[180:183]\n"
"v_mfma_f32_16x16x32_bf16 v[148:151], a[236:239], a[144:147], v[148:151]\n"
"v_mfma_f32_16x16x32_bf16 v[148:151], a[232:235], a[128:131], v[148:151]\n"
"buffer_load_dwordx4 v22, s[20:23], s77 offen offset:0 lds\n"
"s_waitcnt lgkmcnt(15)\n"
"v_mfma_f32_16x16x32_bf16 v[152:155], a[240:243], a[128:131], v[152:155]\n"
"s_waitcnt lgkmcnt(15)\n"
"ds_read_b64_tr_b16 a[232:233], v13 offset:0\n"
"ds_read_b64_tr_b16 a[234:235], v13 offset:1024\n"
"v_mfma_f32_16x16x32_bf16 v[152:155], a[244:247], a[144:147], v[152:155]\n"
"ds_read_b64_tr_b16 a[236:237], v13 offset:8192\n"
"ds_read_b64_tr_b16 a[238:239], v13 offset:9216\n"
"v_mfma_f32_16x16x32_bf16 v[184:187], a[244:247], a[148:151], v[184:187]\n"
"v_mfma_f32_16x16x32_bf16 v[184:187], a[240:243], a[132:135], v[184:187]\n"
"v_mfma_f32_16x16x32_bf16 v[216:219], a[240:243], a[136:139], v[216:219]\n"
"buffer_load_dwordx4 v23, s[20:23], s77 offen offset:1024 lds\n"
"v_mfma_f32_16x16x32_bf16 v[216:219], a[244:247], a[152:155], v[216:219]\n"
"v_mfma_f32_16x16x32_bf16 v[248:251], a[244:247], a[156:159], v[248:251]\n"
"v_mfma_f32_16x16x32_bf16 v[248:251], a[240:243], a[140:143], v[248:251]\n"
"s_waitcnt lgkmcnt(15)\n"
"ds_read_b64_tr_b16 a[240:241], v14 offset:0\n"
"ds_read_b64_tr_b16 a[242:243], v14 offset:1024\n"
"v_mfma_f32_16x16x32_bf16 v[252:255], a[248:251], a[140:143], v[252:255]\n"
"s_waitcnt lgkmcnt(15)\n"
"ds_read_b64_tr_b16 a[244:245], v14 offset:8192\n"
"ds_read_b64_tr_b16 a[246:247], v14 offset:9216\n"
"v_mfma_f32_16x16x32_bf16 v[252:255], a[252:255], a[156:159], v[252:255]\n"
"buffer_store_dwordx4 v[72:75], v27, s[44:47], s88 offen offset:3072 sc1\n"
"v_mfma_f32_16x16x32_bf16 v[220:223], a[252:255], a[152:155], v[220:223]\n"
"v_mfma_f32_16x16x32_bf16 v[220:223], a[248:251], a[136:139], v[220:223]\n"
"v_mfma_f32_16x16x32_bf16 v[188:191], a[248:251], a[132:135], v[188:191]\n"
"v_mfma_f32_16x16x32_bf16 v[188:191], a[252:255], a[148:151], v[188:191]\n"
"v_mfma_f32_16x16x32_bf16 v[156:159], a[252:255], a[144:147], v[156:159]\n"
"v_mfma_f32_16x16x32_bf16 v[156:159], a[248:251], a[128:131], v[156:159]\n"
"s_waitcnt vmcnt(13)\n"
"s_waitcnt lgkmcnt(15)\n"
"v_mfma_f32_16x16x32_bf16 v[128:131], a[192:195], a[160:163], v[128:131]\n"
"s_add_u32 s85, s82, 8192\n"
"s_add_u32 s88, s83, 262144\n"
"v_add_u32 v15, 65536, v15\n"
"s_waitcnt lgkmcnt(15)\n"
"v_mfma_f32_16x16x32_bf16 v[128:131], a[196:199], a[176:179], v[128:131]\n"
"ds_read_b64_tr_b16 a[248:249], v15 offset:0\n"
"ds_read_b64_tr_b16 a[250:251], v15 offset:1024\n"
"v_mfma_f32_16x16x32_bf16 v[160:163], a[196:199], a[180:183], v[160:163]\n"
"ds_read_b64_tr_b16 a[252:253], v15 offset:8192\n"
"ds_read_b64_tr_b16 a[254:255], v15 offset:9216\n"
"v_mfma_f32_16x16x32_bf16 v[160:163], a[192:195], a[164:167], v[160:163]\n"
"buffer_load_dwordx4 a[128:131], v1, s[44:47], s85 offen offset:0 sc1\n"
"v_mfma_f32_16x16x32_bf16 v[192:195], a[192:195], a[168:171], v[192:195]\n"
"s_add_u32 s85, s82, 40960\n"
"v_mfma_f32_16x16x32_bf16 v[192:195], a[196:199], a[184:187], v[192:195]\n"
"v_mfma_f32_16x16x32_bf16 v[224:227], a[196:199], a[188:191], v[224:227]\n"
"buffer_load_dwordx4 a[132:135], v1, s[44:47], s85 offen offset:0 sc1\n"
"v_mfma_f32_16x16x32_bf16 v[224:227], a[192:195], a[172:175], v[224:227]\n"
"s_add_u32 s85, s82, 73728\n"
"ds_read_b64_tr_b16 a[192:193], v8 offset:16384\n"
"ds_read_b64_tr_b16 a[194:195], v8 offset:17408\n"
"s_waitcnt lgkmcnt(15)\n"
"v_mfma_f32_16x16x32_bf16 v[228:231], a[200:203], a[172:175], v[228:231]\n"
"s_waitcnt lgkmcnt(15)\n"
"ds_read_b64_tr_b16 a[196:197], v8 offset:24576\n"
"ds_read_b64_tr_b16 a[198:199], v8 offset:25600\n"
"v_mfma_f32_16x16x32_bf16 v[228:231], a[204:207], a[188:191], v[228:231]\n"
"v_mfma_f32_16x16x32_bf16 v[196:199], a[204:207], a[184:187], v[196:199]\n"
"v_mfma_f32_16x16x32_bf16 v[196:199], a[200:203], a[168:171], v[196:199]\n"
"buffer_load_dwordx4 a[136:139], v1, s[44:47], s85 offen offset:0 sc1\n"
"v_mfma_f32_16x16x32_bf16 v[164:167], a[200:203], a[164:167], v[164:167]\n"
"s_add_u32 s85, s82, 106496\n"
"v_mfma_f32_16x16x32_bf16 v[164:167], a[204:207], a[180:183], v[164:167]\n"
"v_mfma_f32_16x16x32_bf16 v[132:135], a[204:207], a[176:179], v[132:135]\n"
"v_mfma_f32_16x16x32_bf16 v[132:135], a[200:203], a[160:163], v[132:135]\n"
"s_waitcnt lgkmcnt(15)\n"
"ds_read_b64_tr_b16 a[200:201], v9 offset:16384\n"
"ds_read_b64_tr_b16 a[202:203], v9 offset:17408\n"
"v_mfma_f32_16x16x32_bf16 v[136:139], a[208:211], a[160:163], v[136:139]\n"
"buffer_load_dwordx4 a[140:143], v1, s[44:47], s85 offen offset:0 sc1\n"
"s_waitcnt lgkmcnt(15)\n"
"v_mfma_f32_16x16x32_bf16 v[136:139], a[212:215], a[176:179], v[136:139]\n"
"s_add_u32 s85, s82, 8192\n"
"ds_read_b64_tr_b16 a[204:205], v9 offset:24576\n"
"ds_read_b64_tr_b16 a[206:207], v9 offset:25600\n"
"v_mfma_f32_16x16x32_bf16 v[168:171], a[212:215], a[180:183], v[168:171]\n"
"v_mfma_f32_16x16x32_bf16 v[168:171], a[208:211], a[164:167], v[168:171]\n"
"v_mfma_f32_16x16x32_bf16 v[200:203], a[208:211], a[168:171], v[200:203]\n"
"v_mfma_f32_16x16x32_bf16 v[200:203], a[212:215], a[184:187], v[200:203]\n"
"buffer_load_dwordx4 a[144:147], v1, s[44:47], s85 offen offset:1024 sc1\n"
"v_mfma_f32_16x16x32_bf16 v[232:235], a[212:215], a[188:191], v[232:235]\n"
"s_add_u32 s85, s82, 40960\n"
"v_mfma_f32_16x16x32_bf16 v[232:235], a[208:211], a[172:175], v[232:235]\n"
"s_waitcnt lgkmcnt(15)\n"
"ds_read_b64_tr_b16 a[208:209], v10 offset:16384\n"
"ds_read_b64_tr_b16 a[210:211], v10 offset:17408\n"
"v_mfma_f32_16x16x32_bf16 v[236:239], a[216:219], a[172:175], v[236:239]\n"
"s_waitcnt lgkmcnt(15)\n"
"ds_read_b64_tr_b16 a[212:213], v10 offset:24576\n"
"ds_read_b64_tr_b16 a[214:215], v10 offset:25600\n"
"v_mfma_f32_16x16x32_bf16 v[236:239], a[220:223], a[188:191], v[236:239]\n"
"v_mfma_f32_16x16x32_bf16 v[204:207], a[220:223], a[184:187], v[204:207]\n"
"buffer_store_dwordx4 v[80:83], v24, s[44:47], s88 offen offset:0 sc1\n"
"v_mfma_f32_16x16x32_bf16 v[204:207], a[216:219], a[168:171], v[204:207]\n"
"v_mfma_f32_16x16x32_bf16 v[172:175], a[216:219], a[164:167], v[172:175]\n"
"v_mfma_f32_16x16x32_bf16 v[172:175], a[220:223], a[180:183], v[172:175]\n"
"v_mfma_f32_16x16x32_bf16 v[140:143], a[220:223], a[176:179], v[140:143]\n"
"v_mfma_f32_16x16x32_bf16 v[140:143], a[216:219], a[160:163], v[140:143]\n"
"s_waitcnt lgkmcnt(15)\n"
"ds_read_b64_tr_b16 a[216:217], v11 offset:16384\n"
"ds_read_b64_tr_b16 a[218:219], v11 offset:17408\n"
"v_mfma_f32_16x16x32_bf16 v[144:147], a[224:227], a[160:163], v[144:147]\n"
"buffer_load_dwordx4 a[148:151], v1, s[44:47], s85 offen offset:1024 sc1\n"
"s_waitcnt lgkmcnt(15)\n"
"v_mfma_f32_16x16x32_bf16 v[144:147], a[228:231], a[176:179], v[144:147]\n"
"s_add_u32 s85, s82, 73728\n"
"ds_read_b64_tr_b16 a[220:221], v11 offset:24576\n"
"ds_read_b64_tr_b16 a[222:223], v11 offset:25600\n"
"v_mfma_f32_16x16x32_bf16 v[176:179], a[228:231], a[180:183], v[176:179]\n"
"v_mfma_f32_16x16x32_bf16 v[176:179], a[224:227], a[164:167], v[176:179]\n"
"v_mfma_f32_16x16x32_bf16 v[208:211], a[224:227], a[168:171], v[208:211]\n"
"v_mfma_f32_16x16x32_bf16 v[208:211], a[228:231], a[184:187], v[208:211]\n"
"buffer_load_dwordx4 a[152:155], v1, s[44:47], s85 offen offset:1024 sc1\n"
"v_mfma_f32_16x16x32_bf16 v[240:243], a[228:231], a[188:191], v[240:243]\n"
"s_add_u32 s85, s82, 106496\n"
"v_mfma_f32_16x16x32_bf16 v[240:243], a[224:227], a[172:175], v[240:243]\n"
"s_waitcnt lgkmcnt(15)\n"
"ds_read_b64_tr_b16 a[224:225], v12 offset:16384\n"
"ds_read_b64_tr_b16 a[226:227], v12 offset:17408\n"
"v_mfma_f32_16x16x32_bf16 v[244:247], a[232:235], a[172:175], v[244:247]\n"
"s_waitcnt lgkmcnt(15)\n"
"ds_read_b64_tr_b16 a[228:229], v12 offset:24576\n"
"ds_read_b64_tr_b16 a[230:231], v12 offset:25600\n"
"v_mfma_f32_16x16x32_bf16 v[244:247], a[236:239], a[188:191], v[244:247]\n"
"v_mfma_f32_16x16x32_bf16 v[212:215], a[236:239], a[184:187], v[212:215]\n"
"buffer_load_dwordx4 a[156:159], v1, s[44:47], s85 offen offset:1024 sc1\n"
"v_mfma_f32_16x16x32_bf16 v[212:215], a[232:235], a[168:171], v[212:215]\n"
"v_mfma_f32_16x16x32_bf16 v[180:183], a[232:235], a[164:167], v[180:183]\n"
"v_mfma_f32_16x16x32_bf16 v[180:183], a[236:239], a[180:183], v[180:183]\n"
"v_mfma_f32_16x16x32_bf16 v[148:151], a[236:239], a[176:179], v[148:151]\n"
"v_mfma_f32_16x16x32_bf16 v[148:151], a[232:235], a[160:163], v[148:151]\n"
"buffer_load_dwordx4 v30, s[20:23], s77 offen offset:2048 lds\n"
"s_waitcnt lgkmcnt(15)\n"
"v_mfma_f32_16x16x32_bf16 v[152:155], a[240:243], a[160:163], v[152:155]\n"
"s_waitcnt lgkmcnt(15)\n"
"ds_read_b64_tr_b16 a[232:233], v13 offset:16384\n"
"ds_read_b64_tr_b16 a[234:235], v13 offset:17408\n"
"v_mfma_f32_16x16x32_bf16 v[152:155], a[244:247], a[176:179], v[152:155]\n"
"ds_read_b64_tr_b16 a[236:237], v13 offset:24576\n"
"ds_read_b64_tr_b16 a[238:239], v13 offset:25600\n"
"v_mfma_f32_16x16x32_bf16 v[184:187], a[244:247], a[180:183], v[184:187]\n"
"v_mfma_f32_16x16x32_bf16 v[184:187], a[240:243], a[164:167], v[184:187]\n"
"v_mfma_f32_16x16x32_bf16 v[216:219], a[240:243], a[168:171], v[216:219]\n"
"buffer_load_dwordx4 v31, s[20:23], s77 offen offset:3072 lds\n"
"v_mfma_f32_16x16x32_bf16 v[216:219], a[244:247], a[184:187], v[216:219]\n"
"v_mfma_f32_16x16x32_bf16 v[248:251], a[244:247], a[188:191], v[248:251]\n"
"v_mfma_f32_16x16x32_bf16 v[248:251], a[240:243], a[172:175], v[248:251]\n"
"s_waitcnt lgkmcnt(15)\n"
"ds_read_b64_tr_b16 a[240:241], v14 offset:16384\n"
"ds_read_b64_tr_b16 a[242:243], v14 offset:17408\n"
"v_mfma_f32_16x16x32_bf16 v[252:255], a[248:251], a[172:175], v[252:255]\n"
"s_waitcnt lgkmcnt(15)\n"
"ds_read_b64_tr_b16 a[244:245], v14 offset:24576\n"
"ds_read_b64_tr_b16 a[246:247], v14 offset:25600\n"
"v_mfma_f32_16x16x32_bf16 v[252:255], a[252:255], a[188:191], v[252:255]\n"
"buffer_store_dwordx4 v[84:87], v25, s[44:47], s88 offen offset:1024 sc1\n"
"v_mfma_f32_16x16x32_bf16 v[220:223], a[252:255], a[184:187], v[220:223]\n"
"v_mfma_f32_16x16x32_bf16 v[220:223], a[248:251], a[168:171], v[220:223]\n"
"v_mfma_f32_16x16x32_bf16 v[188:191], a[248:251], a[164:167], v[188:191]\n"
"v_mfma_f32_16x16x32_bf16 v[188:191], a[252:255], a[180:183], v[188:191]\n"
"v_mfma_f32_16x16x32_bf16 v[156:159], a[252:255], a[176:179], v[156:159]\n"
"v_mfma_f32_16x16x32_bf16 v[156:159], a[248:251], a[160:163], v[156:159]\n"
"s_waitcnt vmcnt(12)\n"
"s_barrier\n"
"s_lshl_b32 s99, s60, 7\n"
"s_add_u32 s99, s99, 32768\n"
"v_mov_b32 v28, s64\n"
"v_mov_b32 v29, s65\n"
"s_cmp_eq_u32 s61, 0\n"
"s_cselect_b64 exec, 1, 0\n"
"buffer_store_dwordx2 v[28:29], off, s[44:47], s99 sc1\n"
"s_mov_b64 exec, -1\n"
"s_waitcnt lgkmcnt(15)\n"
"v_mfma_f32_16x16x32_bf16 v[128:131], a[192:195], v[32:35], v[128:131]\n"
"s_add_u32 s85, s82, 8192\n"
"ds_read_b64_tr_b16 a[248:249], v15 offset:16384\n"
"ds_read_b64_tr_b16 a[250:251], v15 offset:17408\n"
"s_waitcnt lgkmcnt(15)\n"
"v_mfma_f32_16x16x32_bf16 v[128:131], a[196:199], v[48:51], v[128:131]\n"
"s_add_u32 s88, s83, 262400\n"
"ds_read_b64_tr_b16 a[252:253], v15 offset:24576\n"
"ds_read_b64_tr_b16 a[254:255], v15 offset:25600\n"
"v_mfma_f32_16x16x32_bf16 v[160:163], a[196:199], v[52:55], v[160:163]\n"
"buffer_load_dwordx4 a[160:163], v1, s[44:47], s85 offen offset:2048 sc1\n"
"v_mfma_f32_16x16x32_bf16 v[160:163], a[192:195], v[36:39], v[160:163]\n"
"s_add_u32 s85, s82, 40960\n"
"v_add_u32 v8, 65536, v8\n"
"v_add_u32 v9, 65536, v9\n"
"v_mfma_f32_16x16x32_bf16 v[192:195], a[192:195], v[40:43], v[192:195]\n"
"s_lshl_b32 s94, s62, 20\n"
"v_add_u32 v10, 65536, v10\n"
"v_add_u32 v11, 65536, v11\n"
"v_mfma_f32_16x16x32_bf16 v[192:195], a[196:199], v[56:59], v[192:195]\n"
"s_add_u32 s94, s94, 4194304\n"
"v_add_u32 v12, 65536, v12\n"
"v_add_u32 v13, 65536, v13\n"
"v_mfma_f32_16x16x32_bf16 v[224:227], a[196:199], v[60:63], v[224:227]\n"
"s_mov_b32 s93, 0\n"
"s_lshl_b32 s78, s67, 8\n"
"v_add_u32 v14, 65536, v14\n"
"v_mfma_f32_16x16x32_bf16 v[224:227], a[192:195], v[44:47], v[224:227]\n"
"buffer_load_dwordx4 a[164:167], v1, s[44:47], s85 offen offset:2048 sc1\n"
"s_waitcnt lgkmcnt(15)\n"
"v_mfma_f32_16x16x32_bf16 v[228:231], a[200:203], v[44:47], v[228:231]\n"
"s_add_u32 s85, s82, 73728\n"
"s_add_u32 s78, s78, s94\n"
"s_lshl_b32 s87, s66, 14\n"
"s_waitcnt lgkmcnt(15)\n"
"v_mfma_f32_16x16x32_bf16 v[228:231], a[204:207], v[60:63], v[228:231]\n"
"s_sub_u32 s79, s78, s87\n"
"s_add_u32 s79, s79, 49152\n"
"s_add_u32 s78, s78, s87\n"
"v_mfma_f32_16x16x32_bf16 v[196:199], a[204:207], v[56:59], v[196:199]\n"
"s_add_u32 s80, s78, 512\n"
"s_add_u32 s81, s79, 512\n"
"s_mov_b32 s92, 65536\n"
"v_mfma_f32_16x16x32_bf16 v[196:199], a[200:203], v[40:43], v[196:199]\n"
"s_movk_i32 s5, 512\n"
"s_lshl_b32 s76, s61, 12\n"
"s_add_u32 s77, s76, 65536\n"
"v_mfma_f32_16x16x32_bf16 v[164:167], a[200:203], v[36:39], v[164:167]\n"
"s_mov_b64 s[20:21], s[44:45]\n"
"s_mov_b32 s22, s46\n"
"s_mov_b64 s[24:25], s[44:45]\n"
"v_mfma_f32_16x16x32_bf16 v[164:167], a[204:207], v[52:55], v[164:167]\n"
"buffer_load_dwordx4 a[168:171], v1, s[44:47], s85 offen offset:2048 sc1\n"
"v_mfma_f32_16x16x32_bf16 v[132:135], a[204:207], v[48:51], v[132:135]\n"
"s_add_u32 s85, s82, 106496\n"
"s_mov_b32 s26, s46\n"
"s_mov_b32 s74, s70\n"
"v_mfma_f32_16x16x32_bf16 v[132:135], a[200:203], v[32:35], v[132:135]\n"
"s_mov_b32 s75, s71\n"
"s_mov_b64 s[72:73], s[78:79]\n"
"s_and_b32 s95, s60, 0xc7\n"
"s_waitcnt lgkmcnt(15)\n"
"v_mfma_f32_16x16x32_bf16 v[136:139], a[208:211], v[32:35], v[136:139]\n"
"s_lshl_b32 s95, s95, 7\n"
"s_add_u32 s95, s95, 32768\n"
"v_and_b32 v7, 7, v0\n"
"s_waitcnt lgkmcnt(15)\n"
"v_mfma_f32_16x16x32_bf16 v[136:139], a[212:215], v[48:51], v[136:139]\n"
"s_bfe_u32 s19, s60, 0x20001\n"
"s_and_b32 s87, s60, 1\n"
"v_lshl_add_u32 v7, v7, 10, s95\n"
"v_mfma_f32_16x16x32_bf16 v[168:171], a[212:215], v[52:55], v[168:171]\n"
"s_lshl_b32 s18, s87, 3\n"
"s_lshr_b32 s16, s60, 5\n"
"s_add_u32 s18, s18, s16\n"
"v_mfma_f32_16x16x32_bf16 v[168:171], a[208:211], v[36:39], v[168:171]\n"
"buffer_load_dwordx4 a[172:175], v1, s[44:47], s85 offen offset:2048 sc1\n"
"v_mfma_f32_16x16x32_bf16 v[200:203], a[208:211], v[40:43], v[200:203]\n"
"s_add_u32 s85, s82, 8192\n"
"s_lshl_b32 s16, s19, 3\n"
"s_add_u32 s87, s87, s16\n"
"v_mfma_f32_16x16x32_bf16 v[200:203], a[212:215], v[56:59], v[200:203]\n"
"s_lshl_b32 s16, s87, 17\n"
"s_lshl_b32 s91, s87, 12\n"
"s_bfe_u32 s87, s60, 0x20003\n"
"v_mfma_f32_16x16x32_bf16 v[232:235], a[212:215], v[60:63], v[232:235]\n"
"s_lshl_b32 s87, s87, 8\n"
"s_add_u32 s16, s16, s87\n"
"s_lshl_b32 s87, s61, 14\n"
"v_mfma_f32_16x16x32_bf16 v[232:235], a[208:211], v[44:47], v[232:235]\n"
"s_add_u32 s16, s16, s87\n"
"s_lshl_b32 s87, s18, 20\n"
"s_add_u32 s91, s91, s87\n"
"s_waitcnt lgkmcnt(15)\n"
"v_mfma_f32_16x16x32_bf16 v[236:239], a[216:219], v[44:47], v[236:239]\n"
"s_lshl_b32 s87, s61, 18\n"
"s_add_u32 s91, s91, s87\n"
"s_add_u32 s91, s91, 37748736\n"
"s_waitcnt lgkmcnt(15)\n"
"v_mfma_f32_16x16x32_bf16 v[236:239], a[220:223], v[60:63], v[236:239]\n"
"buffer_store_dwordx4 v[88:91], v24, s[44:47], s88 offen offset:0 sc1\n"
"v_mfma_f32_16x16x32_bf16 v[204:207], a[220:223], v[56:59], v[204:207]\n"
"s_add_u32 s17, s16, 65536\n"
"ds_read_b64_tr_b16 a[192:193], v8 offset:0\n"
"ds_read_b64_tr_b16 a[194:195], v8 offset:1024\n"
"v_mfma_f32_16x16x32_bf16 v[204:207], a[216:219], v[40:43], v[204:207]\n"
"ds_read_b64_tr_b16 a[196:197], v8 offset:8192\n"
"ds_read_b64_tr_b16 a[198:199], v8 offset:9216\n"
"v_mfma_f32_16x16x32_bf16 v[172:175], a[216:219], v[36:39], v[172:175]\n"
"ds_read_b64_tr_b16 a[200:201], v9 offset:0\n"
"ds_read_b64_tr_b16 a[202:203], v9 offset:1024\n"
"v_mfma_f32_16x16x32_bf16 v[172:175], a[220:223], v[52:55], v[172:175]\n"
"ds_read_b64_tr_b16 a[204:205], v9 offset:8192\n"
"ds_read_b64_tr_b16 a[206:207], v9 offset:9216\n"
"v_mfma_f32_16x16x32_bf16 v[140:143], a[220:223], v[48:51], v[140:143]\n"
"ds_read_b64_tr_b16 a[208:209], v10 offset:0\n"
"ds_read_b64_tr_b16 a[210:211], v10 offset:1024\n"
"v_mfma_f32_16x16x32_bf16 v[140:143], a[216:219], v[32:35], v[140:143]\n"
"s_waitcnt lgkmcnt(15)\n"
"ds_read_b64_tr_b16 a[212:213], v10 offset:8192\n"
"ds_read_b64_tr_b16 a[214:215], v10 offset:9216\n"
"v_mfma_f32_16x16x32_bf16 v[144:147], a[224:227], v[32:35], v[144:147]\n"
"buffer_load_dwordx4 a[176:179], v1, s[44:47], s85 offen offset:3072 sc1\n"
"s_waitcnt lgkmcnt(15)\n"
"v_mfma_f32_16x16x32_bf16 v[144:147], a[228:231], v[48:51], v[144:147]\n"
"s_nop 0\n"
"PG5_%=:\n"
"s_cbranch_execz PADRET_%=\n"
"s_add_u32 s85, s82, 40960\n"
"ds_read_b64_tr_b16 a[216:217], v11 offset:0\n"
"ds_read_b64_tr_b16 a[218:219], v11 offset:1024\n"
"v_mfma_f32_16x16x32_bf16 v[176:179], a[228:231], v[52:55], v[176:179]\n"
"ds_read_b64_tr_b16 a[220:221], v11 offset:8192\n"
"ds_read_b64_tr_b16 a[222:223], v11 offset:9216\n"
"v_mfma_f32_16x16x32_bf16 v[176:179], a[224:227], v[36:39], v[176:179]\n"
"v_mfma_f32_16x16x32_bf16 v[208:211], a[224:227], v[40:43], v[208:211]\n"
"v_mfma_f32_16x16x32_bf16 v[208:211], a[228:231], v[56:59], v[208:211]\n"
"v_mfma_f32_16x16x32_bf16 v[240:243], a[228:231], v[60:63], v[240:243]\n"
"buffer_load_dwordx4 a[180:183], v1, s[44:47], s85 offen offset:3072 sc1\n"
"v_mfma_f32_16x16x32_bf16 v[240:243], a[224:227], v[44:47], v[240:243]\n"
"s_add_u32 s85, s82, 73728\n"
"ds_read_b64_tr_b16 a[224:225], v12 offset:0\n"
"ds_read_b64_tr_b16 a[226:227], v12 offset:1024\n"
"s_waitcnt lgkmcnt(15)\n"
"v_mfma_f32_16x16x32_bf16 v[244:247], a[232:235], v[44:47], v[244:247]\n"
"s_waitcnt lgkmcnt(15)\n"
"ds_read_b64_tr_b16 a[228:229], v12 offset:8192\n"
"ds_read_b64_tr_b16 a[230:231], v12 offset:9216\n"
"v_mfma_f32_16x16x32_bf16 v[244:247], a[236:239], v[60:63], v[244:247]\n"
"v_mfma_f32_16x16x32_bf16 v[212:215], a[236:239], v[56:59], v[212:215]\n"
"v_mfma_f32_16x16x32_bf16 v[212:215], a[232:235], v[40:43], v[212:215]\n"
"v_mfma_f32_16x16x32_bf16 v[180:183], a[232:235], v[36:39], v[180:183]\n"
"buffer_load_dwordx4 a[184:187], v1, s[44:47], s85 offen offset:3072 sc1\n"
"v_mfma_f32_16x16x32_bf16 v[180:183], a[236:239], v[52:55], v[180:183]\n"
"s_add_u32 s85, s82, 106496\n"
"v_mfma_f32_16x16x32_bf16 v[148:151], a[236:239], v[48:51], v[148:151]\n"
"v_mfma_f32_16x16x32_bf16 v[148:151], a[232:235], v[32:35], v[148:151]\n"
"s_waitcnt lgkmcnt(15)\n"
"ds_read_b64_tr_b16 a[232:233], v13 offset:0\n"
"ds_read_b64_tr_b16 a[234:235], v13 offset:1024\n"
"v_mfma_f32_16x16x32_bf16 v[152:155], a[240:243], v[32:35], v[152:155]\n"
"s_waitcnt lgkmcnt(15)\n"
"ds_read_b64_tr_b16 a[236:237], v13 offset:8192\n"
"ds_read_b64_tr_b16 a[238:239], v13 offset:9216\n"
"v_mfma_f32_16x16x32_bf16 v[152:155], a[244:247], v[48:51], v[152:155]\n"
"v_mfma_f32_16x16x32_bf16 v[184:187], a[244:247], v[52:55], v[184:187]\n"
"buffer_load_dwordx4 a[188:191], v1, s[44:47], s85 offen offset:3072 sc1\n"
"v_mfma_f32_16x16x32_bf16 v[184:187], a[240:243], v[36:39], v[184:187]\n"
"v_mfma_f32_16x16x32_bf16 v[216:219], a[240:243], v[40:43], v[216:219]\n"
"v_mfma_f32_16x16x32_bf16 v[216:219], a[244:247], v[56:59], v[216:219]\n"
"v_mfma_f32_16x16x32_bf16 v[248:251], a[244:247], v[60:63], v[248:251]\n"
"v_mfma_f32_16x16x32_bf16 v[248:251], a[240:243], v[44:47], v[248:251]\n"
"s_waitcnt lgkmcnt(15)\n"
"ds_read_b64_tr_b16 a[240:241], v14 offset:0\n"
"ds_read_b64_tr_b16 a[242:243], v14 offset:1024\n"
"v_mfma_f32_16x16x32_bf16 v[252:255], a[248:251], v[44:47], v[252:255]\n"
"buffer_store_dwordx4 v[92:95], v25, s[44:47], s88 offen offset:1024 sc1\n"
"s_waitcnt lgkmcnt(15)\n"
"v_mfma_f32_16x16x32_bf16 v[252:255], a[252:255], v[60:63], v[252:255]\n"
"ds_read_b64_tr_b16 a[244:245], v14 offset:8192\n"
"ds_read_b64_tr_b16 a[246:247], v14 offset:9216\n"
"v_mfma_f32_16x16x32_bf16 v[220:223], a[252:255], v[56:59], v[220:223]\n"
"v_mfma_f32_16x16x32_bf16 v[220:223], a[248:251], v[40:43], v[220:223]\n"
"v_mfma_f32_16x16x32_bf16 v[188:191], a[248:251], v[36:39], v[188:191]\n"
"v_mfma_f32_16x16x32_bf16 v[188:191], a[252:255], v[52:55], v[188:191]\n"
"v_mfma_f32_16x16x32_bf16 v[156:159], a[252:255], v[48:51], v[156:159]\n"
"v_mfma_f32_16x16x32_bf16 v[156:159], a[248:251], v[32:35], v[156:159]\n"
"s_waitcnt vmcnt(14)\n"
"s_waitcnt lgkmcnt(15)\n"
"v_mfma_f32_16x16x32_bf16 v[128:131], a[192:195], a[128:131], v[128:131]\n"
"s_lshl_b32 s97, s61, 13\n"
"s_add_u32 s97, s97, 131072\n"
"v_add_u32 v22, s97, v1\n"
"s_waitcnt lgkmcnt(15)\n"
"v_mfma_f32_16x16x32_bf16 v[128:131], a[196:199], a[144:147], v[128:131]\n"
"s_lshl_b32 s97, s61, 8\n"
"s_add_u32 s97, s97, 139264\n"
"v_lshl_add_u32 v19, v2, 4, s97\n"
"v_mfma_f32_16x16x32_bf16 v[160:163], a[196:199], a[148:151], v[160:163]\n"
"s_cmp_eq_u32 s61, 0\n"
"s_cselect_b32 s58, 1, 0\n"
"s_lshl_b32 s97, s67, 15\n"
"v_mfma_f32_16x16x32_bf16 v[160:163], a[192:195], a[132:135], v[160:163]\n"
"s_add_u32 s97, s97, 98304\n"
"v_add_u32 v18, s97, v7\n"
"v_and_b32 v20, 1, v0\n"
"v_mfma_f32_16x16x32_bf16 v[192:195], a[192:195], a[136:139], v[192:195]\n"
"s_lshl_b32 s97, 262144, s67\n"
"s_add_u32 s97, s97, s78\n"
"s_cmp_eq_u32 s66, 1\n"
"v_mfma_f32_16x16x32_bf16 v[192:195], a[196:199], a[152:155], v[192:195]\n"
"s_cselect_b32 s95, s97, -1\n"
"s_sub_u32 s97, 15, s18\n"
"s_min_u32 s97, s97, s18\n"
"v_mfma_f32_16x16x32_bf16 v[224:227], a[196:199], a[156:159], v[224:227]\n"
"s_lshl_b32 s97, s97, 3\n"
"s_lshl_b32 s96, s19, 1\n"
"s_add_u32 s97, s97, s96\n"
"v_mfma_f32_16x16x32_bf16 v[224:227], a[192:195], a[140:143], v[224:227]\n"
"s_and_b32 s96, s60, 1\n"
"s_lshl_b32 s96, s96, 6\n"
"s_add_u32 s97, s97, s96\n"
"s_waitcnt lgkmcnt(15)\n"
"v_mfma_f32_16x16x32_bf16 v[228:231], a[200:203], a[140:143], v[228:231]\n"
"s_lshl_b32 s97, s97, 7\n"
"s_add_u32 s97, s97, 65536\n"
"v_lshlrev_b32 v20, 14, v20\n"
"s_waitcnt lgkmcnt(15)\n"
"v_mfma_f32_16x16x32_bf16 v[228:231], a[204:207], a[156:159], v[228:231]\n"
"s_mov_b32 s56, 0xffff0000\n"
"v_bfe_u32 v21, v0, 1, 1\n"
"v_lshl_add_u32 v20, v21, 7, v20\n"
"v_mfma_f32_16x16x32_bf16 v[196:199], a[204:207], a[152:155], v[196:199]\n"
"s_mov_b32 s57, 0xffff0000\n"
"v_add_u32 v30, s97, v20\n"
"v_add_u32 v15, 65536, v15\n"
"v_mfma_f32_16x16x32_bf16 v[196:199], a[200:203], a[136:139], v[196:199]\n"
"s_lshl_b32 s97, s66, 15\n"
"s_lshl_b32 s96, s67, 13\n"
"s_add_u32 s97, s97, s96\n"
"v_mfma_f32_16x16x32_bf16 v[164:167], a[200:203], a[132:135], v[164:167]\n"
"s_add_u32 s97, s97, 98304\n"
"s_and_b32 s96, s62, 1\n"
"s_lshl_b32 s96, s96, 14\n"
"v_mfma_f32_16x16x32_bf16 v[164:167], a[204:207], a[148:151], v[164:167]\n"
"s_add_u32 s97, s97, s96\n"
"ds_read_b64_tr_b16 a[248:249], v15 offset:0\n"
"ds_read_b64_tr_b16 a[250:251], v15 offset:1024\n"
"v_mfma_f32_16x16x32_bf16 v[132:135], a[204:207], a[144:147], v[132:135]\n"
"s_add_u32 s85, s82, 12288\n"
"ds_read_b64_tr_b16 a[252:253], v15 offset:8192\n"
"ds_read_b64_tr_b16 a[254:255], v15 offset:9216\n"
"v_mfma_f32_16x16x32_bf16 v[132:135], a[200:203], a[128:131], v[132:135]\n"
"buffer_load_dwordx4 a[0:3], v1, s[44:47], s85 offen offset:0 sc1\n"
"s_waitcnt lgkmcnt(15)\n"
"v_mfma_f32_16x16x32_bf16 v[136:139], a[208:211], a[128:131], v[136:139]\n"
"s_add_u32 s85, s82, 45056\n"
"v_accvgpr_read_b32 v64, a8\n"
"v_accvgpr_read_b32 v65, a9\n"
"s_waitcnt lgkmcnt(15)\n"
"v_mfma_f32_16x16x32_bf16 v[136:139], a[212:215], a[144:147], v[136:139]\n"
"buffer_load_dwordx4 a[32:35], v1, s[44:47], s85 offen offset:0 sc1\n"
"v_mfma_f32_16x16x32_bf16 v[168:171], a[212:215], a[148:151], v[168:171]\n"
"s_add_u32 s85, s82, 77824\n"
"v_accvgpr_read_b32 v66, a10\n"
"v_accvgpr_read_b32 v67, a11\n"
"v_mfma_f32_16x16x32_bf16 v[168:171], a[208:211], a[132:135], v[168:171]\n"
"buffer_load_dwordx4 a[64:67], v1, s[44:47], s85 offen offset:0 sc1\n"
"v_mfma_f32_16x16x32_bf16 v[200:203], a[208:211], a[136:139], v[200:203]\n"
"s_add_u32 s85, s82, 110592\n"
"v_accvgpr_read_b32 v68, a12\n"
"v_accvgpr_read_b32 v69, a13\n"
"v_mfma_f32_16x16x32_bf16 v[200:203], a[212:215], a[152:155], v[200:203]\n"
"buffer_load_dwordx4 a[96:99], v1, s[44:47], s85 offen offset:0 sc1\n"
"v_mfma_f32_16x16x32_bf16 v[232:235], a[212:215], a[156:159], v[232:235]\n"
"s_add_u32 s88, s84, 262144\n"
"v_accvgpr_read_b32 v70, a14\n"
"v_accvgpr_read_b32 v71, a15\n"
"v_mfma_f32_16x16x32_bf16 v[232:235], a[208:211], a[140:143], v[232:235]\n"
"s_add_u32 s85, s82, 12288\n"
"v_cvt_pk_bf16_f32 v64, v64, v65\n"
"v_cvt_pk_bf16_f32 v65, v66, v67\n"
"s_waitcnt lgkmcnt(15)\n"
"v_mfma_f32_16x16x32_bf16 v[236:239], a[216:219], a[140:143], v[236:239]\n"
"s_mov_b32 s100, 0\n"
"v_cvt_pk_bf16_f32 v66, v68, v69\n"
"v_cvt_pk_bf16_f32 v67, v70, v71\n"
"s_waitcnt lgkmcnt(15)\n"
"v_mfma_f32_16x16x32_bf16 v[236:239], a[220:223], a[156:159], v[236:239]\n"
"buffer_store_dwordx4 v[64:67], v24, s[44:47], s88 offen offset:0 sc1\n"
"v_mfma_f32_16x16x32_bf16 v[204:207], a[220:223], a[152:155], v[204:207]\n"
"s_mov_b32 s101, -1\n"
"v_accvgpr_read_b32 v72, a40\n"
"v_accvgpr_read_b32 v73, a41\n"
"v_mfma_f32_16x16x32_bf16 v[204:207], a[216:219], a[136:139], v[204:207]\n"
"v_accvgpr_read_b32 v74, a42\n"
"v_accvgpr_read_b32 v75, a43\n"
"v_mfma_f32_16x16x32_bf16 v[172:175], a[216:219], a[132:135], v[172:175]\n"
"v_accvgpr_read_b32 v76, a44\n"
"v_accvgpr_read_b32 v77, a45\n"
"v_mfma_f32_16x16x32_bf16 v[172:175], a[220:223], a[148:151], v[172:175]\n"
"v_accvgpr_read_b32 v78, a46\n"
"v_accvgpr_read_b32 v79, a47\n"
"v_mfma_f32_16x16x32_bf16 v[140:143], a[220:223], a[144:147], v[140:143]\n"
"v_cvt_pk_bf16_f32 v72, v72, v73\n"
"v_cvt_pk_bf16_f32 v73, v74, v75\n"
"v_mfma_f32_16x16x32_bf16 v[140:143], a[216:219], a[128:131], v[140:143]\n"
"s_waitcnt lgkmcnt(14)\n"
"v_cvt_pk_bf16_f32 v74, v76, v77\n"
"v_cvt_pk_bf16_f32 v75, v78, v79\n"
"v_mfma_f32_16x16x32_bf16 v[144:147], a[224:227], a[128:131], v[144:147]\n"
"buffer_load_dwordx4 a[4:7], v1, s[44:47], s85 offen offset:1024 sc1\n"
"s_waitcnt lgkmcnt(12)\n"
"v_mfma_f32_16x16x32_bf16 v[144:147], a[228:231], a[144:147], v[144:147]\n"
"s_add_u32 s85, s82, 45056\n"
"v_lshl_add_u32 v19, v3, 2, v19\n"
"v_add_u32 v31, 131072, v1\n"
"v_mfma_f32_16x16x32_bf16 v[176:179], a[228:231], a[148:151], v[176:179]\n"
"v_add_u32 v33, s97, v1\n"
"v_xor_b32 v34, 0x2000, v33\n"
"v_mfma_f32_16x16x32_bf16 v[176:179], a[224:227], a[132:135], v[176:179]\n"
"v_xor_b32 v35, 0x4000, v33\n"
"v_xor_b32 v42, 0x6000, v33\n"
"v_mfma_f32_16x16x32_bf16 v[208:211], a[224:227], a[136:139], v[208:211]\n"
"v_mov_b32 v60, 0x3f803f80\n"
"v_mov_b32 v61, 0x3f803f80\n"
"v_mfma_f32_16x16x32_bf16 v[208:211], a[228:231], a[152:155], v[208:211]\n"
"v_mov_b32 v62, 0x3f803f80\n"
"v_mov_b32 v63, 0x3f803f80\n"
"v_mfma_f32_16x16x32_bf16 v[240:243], a[228:231], a[156:159], v[240:243]\n"
"buffer_load_dwordx4 a[36:39], v1, s[44:47], s85 offen offset:1024 sc1\n"
"v_mfma_f32_16x16x32_bf16 v[240:243], a[224:227], a[140:143], v[240:243]\n"
"s_add_u32 s85, s82, 77824\n"
"v_xor_b32 v37, 0x2000, v22\n"
"v_and_b32 v39, 0xfffffff0, v19\n"
"s_waitcnt lgkmcnt(10)\n"
"v_mfma_f32_16x16x32_bf16 v[244:247], a[232:235], a[140:143], v[244:247]\n"
"s_waitcnt lgkmcnt(8)\n"
"v_xor_b32 v40, 0x200, v39\n"
"v_xor_b32 v41, 0x300, v39\n"
"v_mfma_f32_16x16x32_bf16 v[244:247], a[236:239], a[156:159], v[244:247]\n"
"ds_read_b64_tr_b16 a[192:193], v8 offset:16384\n"
"v_xor_b32 v39, 0x100, v39\n"
"v_mfma_f32_16x16x32_bf16 v[212:215], a[236:239], a[152:155], v[212:215]\n"
"ds_read_b64_tr_b16 a[194:195], v8 offset:17408\n"
"ds_read_b64_tr_b16 a[196:197], v8 offset:24576\n"
"v_mfma_f32_16x16x32_bf16 v[212:215], a[232:235], a[136:139], v[212:215]\n"
"ds_read_b64_tr_b16 a[198:199], v8 offset:25600\n"
"ds_read_b64_tr_b16 a[200:201], v9 offset:16384\n"
"v_mfma_f32_16x16x32_bf16 v[180:183], a[232:235], a[132:135], v[180:183]\n"
"buffer_load_dwordx4 a[68:71], v1, s[44:47], s85 offen offset:1024 sc1\n"
"v_mfma_f32_16x16x32_bf16 v[180:183], a[236:239], a[148:151], v[180:183]\n"
"s_add_u32 s85, s82, 110592\n"
"ds_read_b64_tr_b16 a[202:203], v9 offset:17408\n"
"ds_read_b64_tr_b16 a[204:205], v9 offset:24576\n"
"v_mfma_f32_16x16x32_bf16 v[148:151], a[236:239], a[144:147], v[148:151]\n"
"ds_read_b64_tr_b16 a[206:207], v9 offset:25600\n"
"ds_read_b64_tr_b16 a[208:209], v10 offset:16384\n"
"v_mfma_f32_16x16x32_bf16 v[148:151], a[232:235], a[128:131], v[148:151]\n"
"s_waitcnt lgkmcnt(15)\n"
"ds_read_b64_tr_b16 a[210:211], v10 offset:17408\n"
"ds_read_b64_tr_b16 a[212:213], v10 offset:24576\n"
"v_mfma_f32_16x16x32_bf16 v[152:155], a[240:243], a[128:131], v[152:155]\n"
"s_waitcnt lgkmcnt(15)\n"
"ds_read_b64_tr_b16 a[214:215], v10 offset:25600\n"
"ds_read_b64_tr_b16 a[216:217], v11 offset:16384\n"
"v_mfma_f32_16x16x32_bf16 v[152:155], a[244:247], a[144:147], v[152:155]\n"
"ds_read_b64_tr_b16 a[218:219], v11 offset:17408\n"
"ds_read_b64_tr_b16 a[220:221], v11 offset:24576\n"
"v_mfma_f32_16x16x32_bf16 v[184:187], a[244:247], a[148:151], v[184:187]\n"
"buffer_load_dwordx4 a[100:103], v1, s[44:47], s85 offen offset:1024 sc1\n"
"v_mfma_f32_16x16x32_bf16 v[184:187], a[240:243], a[132:135], v[184:187]\n"
"ds_read_b64_tr_b16 a[222:223], v11 offset:25600\n"
"ds_read_b64_tr_b16 a[224:225], v12 offset:16384\n"
"v_mfma_f32_16x16x32_bf16 v[216:219], a[240:243], a[136:139], v[216:219]\n"
"ds_read_b64_tr_b16 a[226:227], v12 offset:17408\n"
"ds_read_b64_tr_b16 a[228:229], v12 offset:24576\n"
"v_mfma_f32_16x16x32_bf16 v[216:219], a[244:247], a[152:155], v[216:219]\n"
"ds_read_b64_tr_b16 a[230:231], v12 offset:25600\n"
"ds_read_b64_tr_b16 a[232:233], v13 offset:16384\n"
"v_mfma_f32_16x16x32_bf16 v[248:251], a[244:247], a[156:159], v[248:251]\n"
"ds_read_b64_tr_b16 a[234:235], v13 offset:17408\n"
"ds_read_b64_tr_b16 a[236:237], v13 offset:24576\n"
"v_mfma_f32_16x16x32_bf16 v[248:251], a[240:243], a[140:143], v[248:251]\n"
"s_waitcnt lgkmcnt(15)\n"
"ds_read_b64_tr_b16 a[238:239], v13 offset:25600\n"
"ds_read_b64_tr_b16 a[240:241], v14 offset:16384\n"
"v_mfma_f32_16x16x32_bf16 v[252:255], a[248:251], a[140:143], v[252:255]\n"
"buffer_store_dwordx4 v[72:75], v25, s[44:47], s88 offen offset:1024 sc1\n"
"s_waitcnt lgkmcnt(15)\n"
"v_mfma_f32_16x16x32_bf16 v[252:255], a[252:255], a[156:159], v[252:255]\n"
"ds_read_b64_tr_b16 a[242:243], v14 offset:17408\n"
"ds_read_b64_tr_b16 a[244:245], v14 offset:24576\n"
"v_mfma_f32_16x16x32_bf16 v[220:223], a[252:255], a[152:155], v[220:223]\n"
"ds_read_b64_tr_b16 a[246:247], v14 offset:25600\n"
"v_mfma_f32_16x16x32_bf16 v[220:223], a[248:251], a[136:139], v[220:223]\n"
"v_mfma_f32_16x16x32_bf16 v[188:191], a[248:251], a[132:135], v[188:191]\n"
"v_mfma_f32_16x16x32_bf16 v[188:191], a[252:255], a[148:151], v[188:191]\n"
"v_mfma_f32_16x16x32_bf16 v[156:159], a[252:255], a[144:147], v[156:159]\n"
"v_mfma_f32_16x16x32_bf16 v[156:159], a[248:251], a[128:131], v[156:159]\n"
"s_waitcnt vmcnt(11)\n"
"s_waitcnt lgkmcnt(15)\n"
"v_mfma_f32_16x16x32_bf16 v[128:131], a[192:195], a[160:163], v[128:131]\n"
"s_add_u32 s85, s82, 12288\n"
"ds_read_b64_tr_b16 a[248:249], v15 offset:16384\n"
"ds_read_b64_tr_b16 a[250:251], v15 offset:17408\n"
"s_waitcnt lgkmcnt(15)\n"
"v_mfma_f32_16x16x32_bf16 v[128:131], a[196:199], a[176:179], v[128:131]\n"
"s_lshl_b32 s97, s66, 4\n"
"ds_read_b64_tr_b16 a[252:253], v15 offset:24576\n"
"ds_read_b64_tr_b16 a[254:255], v15 offset:25600\n"
"v_mfma_f32_16x16x32_bf16 v[160:163], a[196:199], a[180:183], v[160:163]\n"
"v_accvgpr_read_b32 v64, a72\n"
"v_accvgpr_read_b32 v65, a73\n"
"v_mfma_f32_16x16x32_bf16 v[160:163], a[192:195], a[164:167], v[160:163]\n"
"v_accvgpr_read_b32 v66, a74\n"
"v_accvgpr_read_b32 v67, a75\n"
"v_mfma_f32_16x16x32_bf16 v[192:195], a[192:195], a[168:171], v[192:195]\n"
"v_accvgpr_read_b32 v68, a76\n"
"v_accvgpr_read_b32 v69, a77\n"
"v_mfma_f32_16x16x32_bf16 v[192:195], a[196:199], a[184:187], v[192:195]\n"
"v_accvgpr_read_b32 v70, a78\n"
"v_accvgpr_read_b32 v71, a79\n"
"v_mfma_f32_16x16x32_bf16 v[224:227], a[196:199], a[188:191], v[224:227]\n"
"v_cvt_pk_bf16_f32 v64, v64, v65\n"
"v_cvt_pk_bf16_f32 v65, v66, v67\n"
"v_mfma_f32_16x16x32_bf16 v[224:227], a[192:195], a[172:175], v[224:227]\n"
"s_waitcnt lgkmcnt(0)\n"
"s_barrier\n"
"v_cvt_pk_bf16_f32 v66, v68, v69\n"
"v_cvt_pk_bf16_f32 v67, v70, v71\n"
"v_mfma_f32_16x16x32_bf16 v[228:231], a[200:203], a[172:175], v[228:231]\n"
"v_accvgpr_read_b32 v72, a104\n"
"v_accvgpr_read_b32 v73, a105\n"
"v_mfma_f32_16x16x32_bf16 v[228:231], a[204:207], a[188:191], v[228:231]\n"
"buffer_load_dwordx2 v[28:29], v7, s[44:47], 0 offen sc1\n"
"v_mfma_f32_16x16x32_bf16 v[196:199], a[204:207], a[184:187], v[196:199]\n"
"v_accvgpr_read_b32 v74, a106\n"
"v_accvgpr_read_b32 v75, a107\n"
"v_mfma_f32_16x16x32_bf16 v[196:199], a[200:203], a[168:171], v[196:199]\n"
"v_accvgpr_read_b32 v76, a108\n"
"v_accvgpr_read_b32 v77, a109\n"
"v_mfma_f32_16x16x32_bf16 v[164:167], a[200:203], a[164:167], v[164:167]\n"
"v_accvgpr_read_b32 v78, a110\n"
"v_accvgpr_read_b32 v79, a111\n"
"v_mfma_f32_16x16x32_bf16 v[164:167], a[204:207], a[180:183], v[164:167]\n"
"buffer_load_dwordx4 a[128:131], v1, s[44:47], s85 offen offset:2048 sc1\n"
"v_mfma_f32_16x16x32_bf16 v[132:135], a[204:207], a[176:179], v[132:135]\n"
"s_add_u32 s85, s82, 45056\n"
"v_cvt_pk_bf16_f32 v72, v72, v73\n"
"v_cvt_pk_bf16_f32 v73, v74, v75\n"
"v_mfma_f32_16x16x32_bf16 v[132:135], a[200:203], a[160:163], v[132:135]\n"
"v_cvt_pk_bf16_f32 v74, v76, v77\n"
"v_cvt_pk_bf16_f32 v75, v78, v79\n"
"v_mfma_f32_16x16x32_bf16 v[136:139], a[208:211], a[160:163], v[136:139]\n"
"v_add_u32 v8, 4294836224, v8\n"
"v_add_u32 v9, 4294836224, v9\n"
"v_mfma_f32_16x16x32_bf16 v[136:139], a[212:215], a[176:179], v[136:139]\n"
"v_add_u32 v10, 4294836224, v10\n"
"v_add_u32 v11, 4294836224, v11\n"
"v_mfma_f32_16x16x32_bf16 v[168:171], a[212:215], a[180:183], v[168:171]\n"
"buffer_load_dwordx4 a[132:135], v1, s[44:47], s85 offen offset:2048 sc1\n"
"v_mfma_f32_16x16x32_bf16 v[168:171], a[208:211], a[164:167], v[168:171]\n"
"s_add_u32 s85, s82, 77824\n"
"v_add_u32 v12, 4294836224, v12\n"
"v_add_u32 v13, 4294836224, v13\n"
"v_mfma_f32_16x16x32_bf16 v[200:203], a[208:211], a[168:171], v[200:203]\n"
"v_lshlrev_b32 v36, 2, v3\n"
"v_add_u32 v14, 4294836224, v14\n"
"v_mfma_f32_16x16x32_bf16 v[200:203], a[212:215], a[184:187], v[200:203]\n"
"v_sub_u32 v36, v2, v36\n"
"v_add_u32 v36, s97, v36\n"
"v_mfma_f32_16x16x32_bf16 v[232:235], a[212:215], a[188:191], v[232:235]\n"
"v_cvt_f32_i32 v36, v36\n"
"v_add_f32 v36, 0.5, v36\n"
"v_mfma_f32_16x16x32_bf16 v[232:235], a[208:211], a[172:175], v[232:235]\n"
"buffer_load_dwordx4 a[136:139], v1, s[44:47], s85 offen offset:2048 sc1\n"
"v_mfma_f32_16x16x32_bf16 v[236:239], a[216:219], a[172:175], v[236:239]\n"
"s_add_u32 s85, s82, 110592\n"
"v_mul_f32 v36, 0x71800000, v36\n"
"v_add_f32 v52, 0x00000000, v36\n"
"v_mfma_f32_16x16x32_bf16 v[236:239], a[220:223], a[188:191], v[236:239]\n"
"v_min_f32 v44, 0, v52\n"
"v_min_f32 v52, 0, v52\n"
"v_mfma_f32_16x16x32_bf16 v[204:207], a[220:223], a[184:187], v[204:207]\n"
"v_add_f32 v53, 0xf1800000, v36\n"
"v_min_f32 v45, 0, v53\n"
"v_mfma_f32_16x16x32_bf16 v[204:207], a[216:219], a[168:171], v[204:207]\n"
"v_min_f32 v53, 0, v53\n"
"v_add_f32 v54, 0xf2000000, v36\n"
"v_mfma_f32_16x16x32_bf16 v[172:175], a[216:219], a[164:167], v[172:175]\n"
"buffer_load_dwordx4 a[140:143], v1, s[44:47], s85 offen offset:2048 sc1\n"
"v_mfma_f32_16x16x32_bf16 v[172:175], a[220:223], a[180:183], v[172:175]\n"
"s_add_u32 s85, s82, 12288\n"
"v_min_f32 v46, 0, v54\n"
"v_min_f32 v54, 0, v54\n"
"v_mfma_f32_16x16x32_bf16 v[140:143], a[220:223], a[176:179], v[140:143]\n"
"v_add_f32 v55, 0xf2400000, v36\n"
"v_min_f32 v47, 0, v55\n"
"v_mfma_f32_16x16x32_bf16 v[140:143], a[216:219], a[160:163], v[140:143]\n"
"v_min_f32 v55, 0, v55\n"
"v_add_f32 v56, 0xf3800000, v36\n"
"v_mfma_f32_16x16x32_bf16 v[144:147], a[224:227], a[160:163], v[144:147]\n"
"v_min_f32 v48, 0, v56\n"
"v_min_f32 v56, 0, v56\n"
"v_mfma_f32_16x16x32_bf16 v[144:147], a[228:231], a[176:179], v[144:147]\n"
"buffer_store_dwordx4 v[64:67], v26, s[44:47], s88 offen offset:2048 sc1\n"
"v_mfma_f32_16x16x32_bf16 v[176:179], a[228:231], a[180:183], v[176:179]\n"
"v_add_f32 v57, 0xf3880000, v36\n"
"v_min_f32 v49, 0, v57\n"
"v_mfma_f32_16x16x32_bf16 v[176:179], a[224:227], a[164:167], v[176:179]\n"
"v_min_f32 v57, 0, v57\n"
"v_add_f32 v58, 0xf3900000, v36\n"
"v_mfma_f32_16x16x32_bf16 v[208:211], a[224:227], a[168:171], v[208:211]\n"
"v_min_f32 v50, 0, v58\n"
"v_min_f32 v58, 0, v58\n"
"v_mfma_f32_16x16x32_bf16 v[208:211], a[228:231], a[184:187], v[208:211]\n"
"v_add_f32 v59, 0xf3980000, v36\n"
"v_min_f32 v51, 0, v59\n"
"v_mfma_f32_16x16x32_bf16 v[240:243], a[228:231], a[188:191], v[240:243]\n"
"buffer_load_dwordx4 a[144:147], v1, s[44:47], s85 offen offset:3072 sc1\n"
"v_mfma_f32_16x16x32_bf16 v[240:243], a[224:227], a[172:175], v[240:243]\n"
"s_add_u32 s85, s82, 45056\n"
"ds_read_b64_tr_b16 a[192:193], v8 offset:32768\n"
"v_min_f32 v59, 0, v59\n"
"v_mfma_f32_16x16x32_bf16 v[244:247], a[232:235], a[172:175], v[244:247]\n"
"ds_read_b64_tr_b16 a[194:195], v8 offset:33792\n"
"ds_read_b64_tr_b16 a[196:197], v8 offset:40960\n"
"v_mfma_f32_16x16x32_bf16 v[244:247], a[236:239], a[188:191], v[244:247]\n"
"ds_read_b64_tr_b16 a[198:199], v8 offset:41984\n"
"ds_read_b64_tr_b16 a[200:201], v9 offset:32768\n"
"v_mfma_f32_16x16x32_bf16 v[212:215], a[236:239], a[184:187], v[212:215]\n"
"ds_read_b64_tr_b16 a[202:203], v9 offset:33792\n"
"ds_read_b64_tr_b16 a[204:205], v9 offset:40960\n"
"v_mfma_f32_16x16x32_bf16 v[212:215], a[232:235], a[168:171], v[212:215]\n"
"buffer_load_dwordx4 a[148:151], v1, s[44:47], s85 offen offset:3072 sc1\n"
"v_mfma_f32_16x16x32_bf16 v[180:183], a[232:235], a[164:167], v[180:183]\n"
"s_add_u32 s85, s82, 77824\n"
"ds_read_b64_tr_b16 a[206:207], v9 offset:41984\n"
"ds_read_b64_tr_b16 a[208:209], v10 offset:32768\n"
"v_mfma_f32_16x16x32_bf16 v[180:183], a[236:239], a[180:183], v[180:183]\n"
"ds_read_b64_tr_b16 a[210:211], v10 offset:33792\n"
"ds_read_b64_tr_b16 a[212:213], v10 offset:40960\n"
"v_mfma_f32_16x16x32_bf16 v[148:151], a[236:239], a[176:179], v[148:151]\n"
"ds_read_b64_tr_b16 a[214:215], v10 offset:41984\n"
"ds_read_b64_tr_b16 a[216:217], v11 offset:32768\n"
"v_mfma_f32_16x16x32_bf16 v[148:151], a[232:235], a[160:163], v[148:151]\n"
"ds_read_b64_tr_b16 a[218:219], v11 offset:33792\n"
"ds_read_b64_tr_b16 a[220:221], v11 offset:40960\n"
"v_mfma_f32_16x16x32_bf16 v[152:155], a[240:243], a[160:163], v[152:155]\n"
"buffer_load_dwordx4 a[152:155], v1, s[44:47], s85 offen offset:3072 sc1\n"
"v_mfma_f32_16x16x32_bf16 v[152:155], a[244:247], a[176:179], v[152:155]\n"
"s_add_u32 s85, s82, 110592\n"
"ds_read_b64_tr_b16 a[222:223], v11 offset:41984\n"
"ds_read_b64_tr_b16 a[224:225], v12 offset:32768\n"
"v_mfma_f32_16x16x32_bf16 v[184:187], a[244:247], a[180:183], v[184:187]\n"
"ds_read_b64_tr_b16 a[226:227], v12 offset:33792\n"
"ds_read_b64_tr_b16 a[228:229], v12 offset:40960\n"
"v_mfma_f32_16x16x32_bf16 v[184:187], a[240:243], a[164:167], v[184:187]\n"
"ds_read_b64_tr_b16 a[230:231], v12 offset:41984\n"
"ds_read_b64_tr_b16 a[232:233], v13 offset:32768\n"
"v_mfma_f32_16x16x32_bf16 v[216:219], a[240:243], a[168:171], v[216:219]\n"
"ds_read_b64_tr_b16 a[234:235], v13 offset:33792\n"
"ds_read_b64_tr_b16 a[236:237], v13 offset:40960\n"
"v_mfma_f32_16x16x32_bf16 v[216:219], a[244:247], a[184:187], v[216:219]\n"
"buffer_load_dwordx4 a[156:159], v1, s[44:47], s85 offen offset:3072 sc1\n"
"v_mfma_f32_16x16x32_bf16 v[248:251], a[244:247], a[188:191], v[248:251]\n"
"ds_read_b64_tr_b16 a[238:239], v13 offset:41984\n"
"v_mfma_f32_16x16x32_bf16 v[248:251], a[240:243], a[172:175], v[248:251]\n"
"ds_read_b64_tr_b16 a[240:241], v14 offset:32768\n"
"ds_read_b64_tr_b16 a[242:243], v14 offset:33792\n"
"v_mfma_f32_16x16x32_bf16 v[252:255], a[248:251], a[172:175], v[252:255]\n"
"ds_read_b64_tr_b16 a[244:245], v14 offset:40960\n"
"ds_read_b64_tr_b16 a[246:247], v14 offset:41984\n"
"v_mfma_f32_16x16x32_bf16 v[252:255], a[252:255], a[188:191], v[252:255]\n"
"v_mfma_f32_16x16x32_bf16 v[220:223], a[252:255], a[184:187], v[220:223]\n"
"buffer_store_dwordx4 v[72:75], v27, s[44:47], s88 offen offset:3072 sc1\n"
"v_mfma_f32_16x16x32_bf16 v[220:223], a[248:251], a[168:171], v[220:223]\n"
"v_mfma_f32_16x16x32_bf16 v[188:191], a[248:251], a[164:167], v[188:191]\n"
"v_mfma_f32_16x16x32_bf16 v[188:191], a[252:255], a[180:183], v[188:191]\n"
"v_mfma_f32_16x16x32_bf16 v[156:159], a[252:255], a[176:179], v[156:159]\n"
"v_mfma_f32_16x16x32_bf16 v[156:159], a[248:251], a[160:163], v[156:159]\n"
"s_waitcnt vmcnt(10)\n"
"v_cmp_ne_u64 vcc, s[64:65], v[28:29]\n"
"s_nop 1\n"
"s_cbranch_vccz FOK_10_%=\n"
"FSP_11_%=:\n"
"s_cmp_eq_u32 s69, 0\n"
"s_cbranch_scc1 FOK_10_%=\n"
"s_sub_u32 s69, s69, 1\n"
"s_sleep 2\n"
"s_mov_b64 exec, vcc\n"
"buffer_load_dwordx2 v[28:29], v7, s[44:47], 0 offen sc1\n"
"s_mov_b64 exec, -1\n"
"s_waitcnt vmcnt(0)\n"
"v_cmp_ne_u64 vcc, s[64:65], v[28:29]\n"
"s_nop 1\n"
"s_cbranch_vccnz FSP_11_%=\n"
"FOK_10_%=:\n"
"s_waitcnt lgkmcnt(15)\n"
"v_mfma_f32_16x16x32_bf16 v[128:131], a[192:195], a[0:3], v[128:131]\n"
"s_add_u32 m0, s76, 0\n"
"s_add_u32 s88, s83, 524288\n"
"v_add_u32 v15, 4294836224, v15\n"
"s_waitcnt lgkmcnt(15)\n"
"v_mfma_f32_16x16x32_bf16 v[128:131], a[196:199], a[4:7], v[128:131]\n"
"s_add_u32 s86, s84, 786432\n"
"ds_read_b64_tr_b16 a[248:249], v15 offset:32768\n"
"ds_read_b64_tr_b16 a[250:251], v15 offset:33792\n"
"v_mfma_f32_16x16x32_bf16 v[160:163], a[196:199], a[36:39], v[160:163]\n"
"s_add_u32 s84, s84, 524288\n"
"ds_read_b64_tr_b16 a[252:253], v15 offset:40960\n"
"ds_read_b64_tr_b16 a[254:255], v15 offset:41984\n"
"v_mfma_f32_16x16x32_bf16 v[160:163], a[192:195], a[32:35], v[160:163]\n"
"buffer_load_dwordx4 v24, s[20:23], s78 offen offset:0 sc1 lds\n"
"v_mfma_f32_16x16x32_bf16 v[192:195], a[192:195], a[64:67], v[192:195]\n"
"v_accvgpr_read_b32 v4, a16\n"
"v_accvgpr_read_b32 v5, a17\n"
"v_mfma_f32_16x16x32_bf16 v[192:195], a[196:199], a[68:71], v[192:195]\n"
"v_cvt_pk_bf16_f32 v64, v4, v5\n"
"v_accvgpr_read_b32 v16, a18\n"
"v_mfma_f32_16x16x32_bf16 v[224:227], a[196:199], a[100:103], v[224:227]\n"
"buffer_load_dwordx4 v25, s[20:23], s78 offen offset:1024 sc1 lds\n"
"v_mfma_f32_16x16x32_bf16 v[224:227], a[192:195], a[96:99], v[224:227]\n"
"s_waitcnt lgkmcnt(15)\n"
"v_accvgpr_read_b32 v17, a19\n"
"v_cvt_pk_bf16_f32 v65, v16, v17\n"
"v_mfma_f32_16x16x32_bf16 v[228:231], a[200:203], a[96:99], v[228:231]\n"
"s_waitcnt lgkmcnt(15)\n"
"v_accvgpr_read_b32 v4, a20\n"
"v_accvgpr_read_b32 v5, a21\n"
"v_mfma_f32_16x16x32_bf16 v[228:231], a[204:207], a[100:103], v[228:231]\n"
"v_cvt_pk_bf16_f32 v66, v4, v5\n"
"v_accvgpr_read_b32 v16, a22\n"
"v_mfma_f32_16x16x32_bf16 v[196:199], a[204:207], a[68:71], v[196:199]\n"
"v_accvgpr_read_b32 v17, a23\n"
"v_cvt_pk_bf16_f32 v67, v16, v17\n"
"v_mfma_f32_16x16x32_bf16 v[196:199], a[200:203], a[64:67], v[196:199]\n"
"v_accvgpr_read_b32 v4, a48\n"
"v_accvgpr_read_b32 v5, a49\n"
"v_mfma_f32_16x16x32_bf16 v[164:167], a[200:203], a[32:35], v[164:167]\n"
"buffer_load_dwordx4 v26, s[20:23], s78 offen offset:2048 sc1 lds\n"
"v_mfma_f32_16x16x32_bf16 v[164:167], a[204:207], a[36:39], v[164:167]\n"
"v_cvt_pk_bf16_f32 v68, v4, v5\n"
"v_accvgpr_read_b32 v16, a50\n"
"v_mfma_f32_16x16x32_bf16 v[132:135], a[204:207], a[4:7], v[132:135]\n"
"v_accvgpr_read_b32 v17, a51\n"
"v_cvt_pk_bf16_f32 v69, v16, v17\n"
"v_mfma_f32_16x16x32_bf16 v[132:135], a[200:203], a[0:3], v[132:135]\n"
"s_waitcnt lgkmcnt(15)\n"
"v_accvgpr_read_b32 v4, a52\n"
"v_accvgpr_read_b32 v5, a53\n"
"v_mfma_f32_16x16x32_bf16 v[136:139], a[208:211], a[0:3], v[136:139]\n"
"s_waitcnt lgkmcnt(15)\n"
"v_cvt_pk_bf16_f32 v70, v4, v5\n"
"v_accvgpr_read_b32 v16, a54\n"
"v_mfma_f32_16x16x32_bf16 v[136:139], a[212:215], a[4:7], v[136:139]\n"
"buffer_store_dwordx4 v[96:99], v24, s[44:47], s88 offen offset:0 sc1\n"
"v_mfma_f32_16x16x32_bf16 v[168:171], a[212:215], a[36:39], v[168:171]\n"
"v_accvgpr_read_b32 v17, a55\n"
"v_cvt_pk_bf16_f32 v71, v16, v17\n"
"v_mfma_f32_16x16x32_bf16 v[168:171], a[208:211], a[32:35], v[168:171]\n"
"v_accvgpr_read_b32 v4, a80\n"
"v_accvgpr_read_b32 v5, a81\n"
"v_mfma_f32_16x16x32_bf16 v[200:203], a[208:211], a[64:67], v[200:203]\n"
"v_cvt_pk_bf16_f32 v72, v4, v5\n"
"v_accvgpr_read_b32 v16, a82\n"
"v_mfma_f32_16x16x32_bf16 v[200:203], a[212:215], a[68:71], v[200:203]\n"
"v_accvgpr_read_b32 v17, a83\n"
"v_cvt_pk_bf16_f32 v73, v16, v17\n"
"v_mfma_f32_16x16x32_bf16 v[232:235], a[212:215], a[100:103], v[232:235]\n"
"v_accvgpr_read_b32 v4, a84\n"
"v_accvgpr_read_b32 v5, a85\n"
"v_mfma_f32_16x16x32_bf16 v[232:235], a[208:211], a[96:99], v[232:235]\n"
"buffer_load_dwordx4 v27, s[20:23], s78 offen offset:3072 sc1 lds\n"
"s_waitcnt lgkmcnt(15)\n"
"v_mfma_f32_16x16x32_bf16 v[236:239], a[216:219], a[96:99], v[236:239]\n"
"s_add_u32 m0, s76, 16384\n"
"v_cvt_pk_bf16_f32 v74, v4, v5\n"
"v_accvgpr_read_b32 v16, a86\n"
"s_waitcnt lgkmcnt(15)\n"
"v_mfma_f32_16x16x32_bf16 v[236:239], a[220:223], a[100:103], v[236:239]\n"
"v_accvgpr_read_b32 v17, a87\n"
"v_cvt_pk_bf16_f32 v75, v16, v17\n"
"v_mfma_f32_16x16x32_bf16 v[204:207], a[220:223], a[68:71], v[204:207]\n"
"v_accvgpr_read_b32 v4, a112\n"
"v_accvgpr_read_b32 v5, a113\n"
"v_mfma_f32_16x16x32_bf16 v[204:207], a[216:219], a[64:67], v[204:207]\n"
"v_cvt_pk_bf16_f32 v76, v4, v5\n"
"v_accvgpr_read_b32 v16, a114\n"
"v_mfma_f32_16x16x32_bf16 v[172:175], a[216:219], a[32:35], v[172:175]\n"
"v_accvgpr_read_b32 v17, a115\n"
"v_cvt_pk_bf16_f32 v77, v16, v17\n"
"v_mfma_f32_16x16x32_bf16 v[172:175], a[220:223], a[36:39], v[172:175]\n"
"s_nop 0\n"
"PG6_%=:\n"
"s_cbranch_execz PADRET_%=\n"
"buffer_load_dwordx4 v24, s[20:23], s79 offen offset:0 sc1 lds\n"
"v_mfma_f32_16x16x32_bf16 v[140:143], a[220:223], a[4:7], v[140:143]\n"
"v_accvgpr_read_b32 v4, a116\n"
"v_accvgpr_read_b32 v5, a117\n"
"v_mfma_f32_16x16x32_bf16 v[140:143], a[216:219], a[0:3], v[140:143]\n"
"s_waitcnt lgkmcnt(14)\n"
"v_cvt_pk_bf16_f32 v78, v4, v5\n"
"v_accvgpr_read_b32 v16, a118\n"
"v_mfma_f32_16x16x32_bf16 v[144:147], a[224:227], a[0:3], v[144:147]\n"
"s_waitcnt lgkmcnt(12)\n"
"v_accvgpr_read_b32 v17, a119\n"
"v_cvt_pk_bf16_f32 v79, v16, v17\n"
"v_mfma_f32_16x16x32_bf16 v[144:147], a[228:231], a[4:7], v[144:147]\n"
"v_accvgpr_read_b32 v4, a24\n"
"v_accvgpr_read_b32 v5, a25\n"
"v_mfma_f32_16x16x32_bf16 v[176:179], a[228:231], a[36:39], v[176:179]\n"
"buffer_store_dwordx4 v[100:103], v25, s[44:47], s88 offen offset:1024 sc1\n"
"v_mfma_f32_16x16x32_bf16 v[176:179], a[224:227], a[32:35], v[176:179]\n"
"s_add_u32 s88, s83, 524544\n"
"v_cvt_pk_bf16_f32 v80, v4, v5\n"
"v_accvgpr_read_b32 v16, a26\n"
"v_mfma_f32_16x16x32_bf16 v[208:211], a[224:227], a[64:67], v[208:211]\n"
"v_accvgpr_read_b32 v17, a27\n"
"v_cvt_pk_bf16_f32 v81, v16, v17\n"
"v_mfma_f32_16x16x32_bf16 v[208:211], a[228:231], a[68:71], v[208:211]\n"
"v_accvgpr_read_b32 v4, a28\n"
"v_accvgpr_read_b32 v5, a29\n"
"v_mfma_f32_16x16x32_bf16 v[240:243], a[228:231], a[100:103], v[240:243]\n"
"v_cvt_pk_bf16_f32 v82, v4, v5\n"
"v_accvgpr_read_b32 v16, a30\n"
"v_mfma_f32_16x16x32_bf16 v[240:243], a[224:227], a[96:99], v[240:243]\n"
"s_waitcnt lgkmcnt(10)\n"
"v_accvgpr_read_b32 v17, a31\n"
"v_cvt_pk_bf16_f32 v83, v16, v17\n"
"v_mfma_f32_16x16x32_bf16 v[244:247], a[232:235], a[96:99], v[244:247]\n"
"buffer_load_dwordx4 v25, s[20:23], s79 offen offset:1024 sc1 lds\n"
"s_waitcnt lgkmcnt(8)\n"
"v_mfma_f32_16x16x32_bf16 v[244:247], a[236:239], a[100:103], v[244:247]\n"
"v_accvgpr_read_b32 v4, a56\n"
"v_accvgpr_read_b32 v5, a57\n"
"v_mfma_f32_16x16x32_bf16 v[212:215], a[236:239], a[68:71], v[212:215]\n"
"v_cvt_pk_bf16_f32 v84, v4, v5\n"
"v_accvgpr_read_b32 v16, a58\n"
"v_mfma_f32_16x16x32_bf16 v[212:215], a[232:235], a[64:67], v[212:215]\n"
"v_accvgpr_read_b32 v17, a59\n"
"v_cvt_pk_bf16_f32 v85, v16, v17\n"
"v_mfma_f32_16x16x32_bf16 v[180:183], a[232:235], a[32:35], v[180:183]\n"
"v_accvgpr_read_b32 v4, a60\n"
"v_accvgpr_read_b32 v5, a61\n"
"v_mfma_f32_16x16x32_bf16 v[180:183], a[236:239], a[36:39], v[180:183]\n"
"v_cvt_pk_bf16_f32 v86, v4, v5\n"
"v_accvgpr_read_b32 v16, a62\n"
"v_mfma_f32_16x16x32_bf16 v[148:151], a[236:239], a[4:7], v[148:151]\n"
"buffer_load_dwordx4 v26, s[20:23], s79 offen offset:2048 sc1 lds\n"
"v_mfma_f32_16x16x32_bf16 v[148:151], a[232:235], a[0:3], v[148:151]\n"
"s_waitcnt lgkmcnt(6)\n"
"v_accvgpr_read_b32 v17, a63\n"
"v_cvt_pk_bf16_f32 v87, v16, v17\n"
"v_mfma_f32_16x16x32_bf16 v[152:155], a[240:243], a[0:3], v[152:155]\n"
"s_waitcnt lgkmcnt(4)\n"
"ds_read_b64_tr_b16 a[192:193], v8 offset:49152\n"
"ds_read_b64_tr_b16 a[194:195], v8 offset:50176\n"
"v_mfma_f32_16x16x32_bf16 v[152:155], a[244:247], a[4:7], v[152:155]\n"
"ds_read_b64_tr_b16 a[196:197], v8 offset:57344\n"
"ds_read_b64_tr_b16 a[198:199], v8 offset:58368\n"
"v_mfma_f32_16x16x32_bf16 v[184:187], a[244:247], a[36:39], v[184:187]\n"
"ds_read_b64_tr_b16 a[200:201], v9 offset:49152\n"
"ds_read_b64_tr_b16 a[202:203], v9 offset:50176\n"
"v_mfma_f32_16x16x32_bf16 v[184:187], a[240:243], a[32:35], v[184:187]\n"
"buffer_load_dwordx4 v27, s[20:23], s79 offen offset:3072 sc1 lds\n"
"v_mfma_f32_16x16x32_bf16 v[216:219], a[240:243], a[64:67], v[216:219]\n"
"ds_read_b64_tr_b16 a[204:205], v9 offset:57344\n"
"ds_read_b64_tr_b16 a[206:207], v9 offset:58368\n"
"v_mfma_f32_16x16x32_bf16 v[216:219], a[244:247], a[68:71], v[216:219]\n"
"ds_read_b64_tr_b16 a[208:209], v10 offset:49152\n"
"ds_read_b64_tr_b16 a[210:211], v10 offset:50176\n"
"v_mfma_f32_16x16x32_bf16 v[248:251], a[244:247], a[100:103], v[248:251]\n"
"ds_read_b64_tr_b16 a[212:213], v10 offset:57344\n"
"ds_read_b64_tr_b16 a[214:215], v10 offset:58368\n"
"v_mfma_f32_16x16x32_bf16 v[248:251], a[240:243], a[96:99], v[248:251]\n"
"s_waitcnt lgkmcnt(14)\n"
"ds_read_b64_tr_b16 a[216:217], v11 offset:49152\n"
"ds_read_b64_tr_b16 a[218:219], v11 offset:50176\n"
"v_mfma_f32_16x16x32_bf16 v[252:255], a[248:251], a[96:99], v[252:255]\n"
"s_waitcnt lgkmcnt(14)\n"
"ds_read_b64_tr_b16 a[220:221], v11 offset:57344\n"
"ds_read_b64_tr_b16 a[222:223], v11 offset:58368\n"
"v_mfma_f32_16x16x32_bf16 v[252:255], a[252:255], a[100:103], v[252:255]\n"
"buffer_store_dwordx4 v[104:107], v24, s[44:47], s88 offen offset:0 sc1\n"
"v_mfma_f32_16x16x32_bf16 v[220:223], a[252:255], a[68:71], v[220:223]\n"
"ds_read_b64_tr_b16 a[224:225], v12 offset:49152\n"
"ds_read_b64_tr_b16 a[226:227], v12 offset:50176\n"
"v_mfma_f32_16x16x32_bf16 v[220:223], a[248:251], a[64:67], v[220:223]\n"
"ds_read_b64_tr_b16 a[228:229], v12 offset:57344\n"
"ds_read_b64_tr_b16 a[230:231], v12 offset:58368\n"
"v_mfma_f32_16x16x32_bf16 v[188:191], a[248:251], a[32:35], v[188:191]\n"
"ds_read_b64_tr_b16 a[232:233], v13 offset:49152\n"
"ds_read_b64_tr_b16 a[234:235], v13 offset:50176\n"
"v_mfma_f32_16x16x32_bf16 v[188:191], a[252:255], a[36:39], v[188:191]\n"
"ds_read_b64_tr_b16 a[236:237], v13 offset:57344\n"
"ds_read_b64_tr_b16 a[238:239], v13 offset:58368\n"
"v_mfma_f32_16x16x32_bf16 v[156:159], a[252:255], a[4:7], v[156:159]\n"
"ds_read_b64_tr_b16 a[240:241], v14 offset:49152\n"
"ds_read_b64_tr_b16 a[242:243], v14 offset:50176\n"
"v_mfma_f32_16x16x32_bf16 v[156:159], a[248:251], a[0:3], v[156:159]\n"
"ds_read_b64_tr_b16 a[244:245], v14 offset:57344\n"
"ds_read_b64_tr_b16 a[246:247], v14 offset:58368\n"
"s_waitcnt vmcnt(12)\n"
"s_waitcnt lgkmcnt(15)\n"
"v_mfma_f32_16x16x32_bf16 v[128:131], a[192:195], a[128:131], v[128:131]\n"
"s_lshl_b32 s97, s70, 4\n"
"s_lshr_b32 s96, s62, 1\n"
"s_add_u32 s97, s97, s96\n"
"s_waitcnt lgkmcnt(15)\n"
"v_mfma_f32_16x16x32_bf16 v[128:131], a[196:199], a[144:147], v[128:131]\n"
"s_lshl_b32 s97, s97, 2\n"
"s_add_u32 s97, s97, s61\n"
"s_cmp_lt_u32 s97, 203\n"
"v_mfma_f32_16x16x32_bf16 v[160:163], a[196:199], a[148:151], v[160:163]\n"
"s_cselect_b64 s[28:29], s[32:33], s[36:37]\n"
"s_cselect_b32 s30, s34, s38\n"
"s_cselect_b32 s96, 0, 203\n"
"v_mfma_f32_16x16x32_bf16 v[160:163], a[192:195], a[132:135], v[160:163]\n"
"s_cmp_lt_u32 s97, 557\n"
"s_cselect_b64 s[28:29], s[28:29], s[40:41]\n"
"s_cselect_b32 s96, s96, 557\n"
"v_mfma_f32_16x16x32_bf16 v[192:195], a[192:195], a[136:139], v[192:195]\n"
"s_sub_u32 s97, s97, s96\n"
"s_mul_i32 s89, s97, 20736\n"
"s_sub_u32 s97, s30, 20736\n"
"v_mfma_f32_16x16x32_bf16 v[192:195], a[196:199], a[152:155], v[192:195]\n"
"s_min_u32 s89, s89, s97\n"
"s_add_u32 s90, s89, 20736\n"
"ds_read_b64_tr_b16 a[248:249], v15 offset:49152\n"
"v_mfma_f32_16x16x32_bf16 v[224:227], a[196:199], a[156:159], v[224:227]\n"
"s_add_u32 m0, s77, 0\n"
"ds_read_b64_tr_b16 a[250:251], v15 offset:50176\n"
"ds_read_b64_tr_b16 a[252:253], v15 offset:57344\n"
"v_mfma_f32_16x16x32_bf16 v[224:227], a[192:195], a[140:143], v[224:227]\n"
"s_waitcnt lgkmcnt(15)\n"
"ds_read_b64_tr_b16 a[254:255], v15 offset:58368\n"
"v_cvt_pk_bf16_f32 v128, v128, v129\n"
"v_mfma_f32_16x16x32_bf16 v[228:231], a[200:203], a[140:143], v[228:231]\n"
"buffer_load_dwordx4 v24, s[24:27], s80 offen offset:0 sc1 lds\n"
"s_waitcnt lgkmcnt(15)\n"
"v_mfma_f32_16x16x32_bf16 v[228:231], a[204:207], a[156:159], v[228:231]\n"
"buffer_load_dwordx4 v25, s[24:27], s80 offen offset:1024 sc1 lds\n"
"v_mfma_f32_16x16x32_bf16 v[196:199], a[204:207], a[152:155], v[196:199]\n"
"v_cvt_pk_bf16_f32 v129, v130, v131\n"
"v_cvt_pk_bf16_f32 v160, v160, v161\n"
"v_mfma_f32_16x16x32_bf16 v[196:199], a[200:203], a[136:139], v[196:199]\n"
"buffer_store_dwordx4 v[108:111], v25, s[44:47], s88 offen offset:1024 sc1\n"
"v_mfma_f32_16x16x32_bf16 v[164:167], a[200:203], a[132:135], v[164:167]\n"
"s_add_u32 s88, s83, 786432\n"
"v_cvt_pk_bf16_f32 v161, v162, v163\n"
"v_cvt_pk_bf16_f32 v192, v192, v193\n"
"v_mfma_f32_16x16x32_bf16 v[164:167], a[204:207], a[148:151], v[164:167]\n"
"v_cvt_pk_bf16_f32 v193, v194, v195\n"
"v_cvt_pk_bf16_f32 v224, v224, v225\n"
"v_mfma_f32_16x16x32_bf16 v[132:135], a[204:207], a[144:147], v[132:135]\n"
"v_cvt_pk_bf16_f32 v225, v226, v227\n"
"v_cvt_pk_bf16_f32 v226, v228, v229\n"
"v_mfma_f32_16x16x32_bf16 v[132:135], a[200:203], a[128:131], v[132:135]\n"
"s_waitcnt lgkmcnt(15)\n"
"v_cvt_pk_bf16_f32 v194, v196, v197\n"
"v_cvt_pk_bf16_f32 v195, v198, v199\n"
"v_mfma_f32_16x16x32_bf16 v[136:139], a[208:211], a[128:131], v[136:139]\n"
"buffer_load_dwordx4 v26, s[24:27], s80 offen offset:2048 sc1 lds\n"
"s_waitcnt lgkmcnt(15)\n"
"v_mfma_f32_16x16x32_bf16 v[136:139], a[212:215], a[144:147], v[136:139]\n"
"v_cvt_pk_bf16_f32 v162, v164, v165\n"
"v_cvt_pk_bf16_f32 v163, v166, v167\n"
"v_mfma_f32_16x16x32_bf16 v[168:171], a[212:215], a[148:151], v[168:171]\n"
"v_accvgpr_read_b32 v4, a88\n"
"v_cvt_pk_bf16_f32 v227, v230, v231\n"
"v_mfma_f32_16x16x32_bf16 v[168:171], a[208:211], a[132:135], v[168:171]\n"
"v_cvt_pk_bf16_f32 v130, v132, v133\n"
"v_cvt_pk_bf16_f32 v131, v134, v135\n"
"v_mfma_f32_16x16x32_bf16 v[200:203], a[208:211], a[136:139], v[200:203]\n"
"v_accvgpr_read_b32 v5, a89\n"
"v_cvt_pk_bf16_f32 v88, v4, v5\n"
"v_mfma_f32_16x16x32_bf16 v[200:203], a[212:215], a[152:155], v[200:203]\n"
"buffer_load_dwordx4 v27, s[24:27], s80 offen offset:3072 sc1 lds\n"
"v_mfma_f32_16x16x32_bf16 v[232:235], a[212:215], a[156:159], v[232:235]\n"
"s_add_u32 m0, s77, 16384\n"
"v_cvt_pk_bf16_f32 v136, v136, v137\n"
"v_cvt_pk_bf16_f32 v137, v138, v139\n"
"v_mfma_f32_16x16x32_bf16 v[232:235], a[208:211], a[140:143], v[232:235]\n"
"s_waitcnt lgkmcnt(15)\n"
"v_cvt_pk_bf16_f32 v168, v168, v169\n"
"v_cvt_pk_bf16_f32 v169, v170, v171\n"
"v_mfma_f32_16x16x32_bf16 v[236:239], a[216:219], a[140:143], v[236:239]\n"
"s_waitcnt lgkmcnt(15)\n"
"v_accvgpr_read_b32 v16, a90\n"
"v_accvgpr_read_b32 v17, a91\n"
"v_mfma_f32_16x16x32_bf16 v[236:239], a[220:223], a[156:159], v[236:239]\n"
"v_cvt_pk_bf16_f32 v200, v200, v201\n"
"v_cvt_pk_bf16_f32 v201, v202, v203\n"
"v_mfma_f32_16x16x32_bf16 v[204:207], a[220:223], a[152:155], v[204:207]\n"
"buffer_store_dwordx4 v[112:115], v24, s[44:47], s88 offen offset:0 sc1\n"
"v_mfma_f32_16x16x32_bf16 v[204:207], a[216:219], a[136:139], v[204:207]\n"
"v_cvt_pk_bf16_f32 v232, v232, v233\n"
"v_cvt_pk_bf16_f32 v233, v234, v235\n"
"v_mfma_f32_16x16x32_bf16 v[172:175], a[216:219], a[132:135], v[172:175]\n"
"v_cvt_pk_bf16_f32 v89, v16, v17\n"
"v_accvgpr_read_b32 v4, a92\n"
"v_mfma_f32_16x16x32_bf16 v[172:175], a[220:223], a[148:151], v[172:175]\n"
"v_cvt_pk_bf16_f32 v234, v236, v237\n"
"v_cvt_pk_bf16_f32 v235, v238, v239\n"
"v_mfma_f32_16x16x32_bf16 v[140:143], a[220:223], a[144:147], v[140:143]\n"
"v_accvgpr_read_b32 v5, a93\n"
"v_cvt_pk_bf16_f32 v90, v4, v5\n"
"v_mfma_f32_16x16x32_bf16 v[140:143], a[216:219], a[128:131], v[140:143]\n"
"s_waitcnt lgkmcnt(14)\n"
"v_cvt_pk_bf16_f32 v202, v204, v205\n"
"v_cvt_pk_bf16_f32 v203, v206, v207\n"
"v_mfma_f32_16x16x32_bf16 v[144:147], a[224:227], a[128:131], v[144:147]\n"
"buffer_load_dwordx4 v24, s[24:27], s81 offen offset:0 sc1 lds\n"
"s_waitcnt lgkmcnt(12)\n"
"v_mfma_f32_16x16x32_bf16 v[144:147], a[228:231], a[144:147], v[144:147]\n"
"v_cvt_pk_bf16_f32 v170, v172, v173\n"
"v_cvt_pk_bf16_f32 v171, v174, v175\n"
"v_mfma_f32_16x16x32_bf16 v[176:179], a[228:231], a[148:151], v[176:179]\n"
"v_accvgpr_read_b32 v16, a94\n"
"v_accvgpr_read_b32 v17, a95\n"
"v_mfma_f32_16x16x32_bf16 v[176:179], a[224:227], a[132:135], v[176:179]\n"
"v_cvt_pk_bf16_f32 v138, v140, v141\n"
"v_cvt_pk_bf16_f32 v139, v142, v143\n"
"v_mfma_f32_16x16x32_bf16 v[208:211], a[224:227], a[136:139], v[208:211]\n"
"v_cvt_pk_bf16_f32 v91, v16, v17\n"
"v_accvgpr_read_b32 v4, a120\n"
"v_mfma_f32_16x16x32_bf16 v[208:211], a[228:231], a[152:155], v[208:211]\n"
"buffer_load_dwordx4 v25, s[24:27], s81 offen offset:1024 sc1 lds\n"
"v_mfma_f32_16x16x32_bf16 v[240:243], a[228:231], a[156:159], v[240:243]\n"
"v_cvt_pk_bf16_f32 v144, v144, v145\n"
"v_cvt_pk_bf16_f32 v145, v146, v147\n"
"v_mfma_f32_16x16x32_bf16 v[240:243], a[224:227], a[140:143], v[240:243]\n"
"s_waitcnt lgkmcnt(10)\n"
"v_cvt_pk_bf16_f32 v176, v176, v177\n"
"v_cvt_pk_bf16_f32 v177, v178, v179\n"
"v_mfma_f32_16x16x32_bf16 v[244:247], a[232:235], a[140:143], v[244:247]\n"
"s_waitcnt lgkmcnt(8)\n"
"v_accvgpr_read_b32 v5, a121\n"
"v_cvt_pk_bf16_f32 v92, v4, v5\n"
"v_mfma_f32_16x16x32_bf16 v[244:247], a[236:239], a[156:159], v[244:247]\n"
"v_cvt_pk_bf16_f32 v208, v208, v209\n"
"v_cvt_pk_bf16_f32 v209, v210, v211\n"
"v_mfma_f32_16x16x32_bf16 v[212:215], a[236:239], a[152:155], v[212:215]\n"
"buffer_store_dwordx4 v[116:119], v25, s[44:47], s88 offen offset:1024 sc1\n"
"v_mfma_f32_16x16x32_bf16 v[212:215], a[232:235], a[136:139], v[212:215]\n"
"s_add_u32 s88, s83, 786688\n"
"s_lshl_b32 s85, s61, 14\n"
"s_add_u32 s85, s85, 98304\n"
"v_mfma_f32_16x16x32_bf16 v[180:183], a[232:235], a[132:135], v[180:183]\n"
"v_add_u32 v32, s85, v1\n"
"v_cvt_pk_bf16_f32 v240, v240, v241\n"
"v_mfma_f32_16x16x32_bf16 v[180:183], a[236:239], a[148:151], v[180:183]\n"
"ds_write_b128 v32, v[128:131] offset:0\n"
"v_mfma_f32_16x16x32_bf16 v[148:151], a[236:239], a[144:147], v[148:151]\n"
"ds_write_b128 v32, v[160:163] offset:4096\n"
"v_mfma_f32_16x16x32_bf16 v[148:151], a[232:235], a[128:131], v[148:151]\n"
"ds_write_b128 v32, v[192:195] offset:8192\n"
"s_waitcnt lgkmcnt(9)\n"
"v_mfma_f32_16x16x32_bf16 v[152:155], a[240:243], a[128:131], v[152:155]\n"
"ds_write_b128 v32, v[224:227] offset:12288\n"
"s_waitcnt lgkmcnt(8)\n"
"v_mfma_f32_16x16x32_bf16 v[152:155], a[244:247], a[144:147], v[152:155]\n"
"ds_write_b128 v32, v[136:139] offset:1024\n"
"v_mfma_f32_16x16x32_bf16 v[184:187], a[244:247], a[148:151], v[184:187]\n"
"ds_write_b128 v32, v[168:171] offset:5120\n"
"v_mfma_f32_16x16x32_bf16 v[184:187], a[240:243], a[132:135], v[184:187]\n"
"ds_write_b128 v32, v[200:203] offset:9216\n"
"v_mfma_f32_16x16x32_bf16 v[216:219], a[240:243], a[136:139], v[216:219]\n"
"buffer_load_dwordx4 v26, s[24:27], s81 offen offset:2048 sc1 lds\n"
"v_mfma_f32_16x16x32_bf16 v[216:219], a[244:247], a[152:155], v[216:219]\n"
"ds_write_b128 v32, v[232:235] offset:13312\n"
"v_mfma_f32_16x16x32_bf16 v[248:251], a[244:247], a[156:159], v[248:251]\n"
"v_cvt_pk_bf16_f32 v146, v148, v149\n"
"v_cvt_pk_bf16_f32 v147, v150, v151\n"
"v_mfma_f32_16x16x32_bf16 v[248:251], a[240:243], a[140:143], v[248:251]\n"
"buffer_load_dwordx4 v27, s[24:27], s81 offen offset:3072 sc1 lds\n"
"s_waitcnt lgkmcnt(10)\n"
"v_mfma_f32_16x16x32_bf16 v[252:255], a[248:251], a[140:143], v[252:255]\n"
"ds_write_b128 v32, v[144:147] offset:2048\n"
"s_waitcnt lgkmcnt(9)\n"
"v_mfma_f32_16x16x32_bf16 v[252:255], a[252:255], a[156:159], v[252:255]\n"
"v_cvt_pk_bf16_f32 v178, v180, v181\n"
"v_cvt_pk_bf16_f32 v179, v182, v183\n"
"v_mfma_f32_16x16x32_bf16 v[220:223], a[252:255], a[152:155], v[220:223]\n"
"ds_write_b128 v32, v[176:179] offset:6144\n"
"v_mfma_f32_16x16x32_bf16 v[220:223], a[248:251], a[136:139], v[220:223]\n"
"buffer_store_dwordx4 v[120:123], v24, s[44:47], s88 offen offset:0 sc1\n"
"v_mfma_f32_16x16x32_bf16 v[188:191], a[248:251], a[132:135], v[188:191]\n"
"v_cvt_pk_bf16_f32 v210, v212, v213\n"
"v_cvt_pk_bf16_f32 v211, v214, v215\n"
"v_mfma_f32_16x16x32_bf16 v[188:191], a[252:255], a[148:151], v[188:191]\n"
"ds_write_b128 v32, v[208:211] offset:10240\n"
"v_mfma_f32_16x16x32_bf16 v[156:159], a[252:255], a[144:147], v[156:159]\n"
"v_cvt_pk_bf16_f32 v241, v242, v243\n"
"v_cvt_pk_bf16_f32 v242, v244, v245\n"
"v_mfma_f32_16x16x32_bf16 v[156:159], a[248:251], a[128:131], v[156:159]\n"
"v_accvgpr_read_b32 v16, a122\n"
"v_cvt_pk_bf16_f32 v243, v246, v247\n"
"v_accvgpr_read_b32 v17, a123\n"
"v_cvt_pk_bf16_f32 v93, v16, v17\n"
"v_accvgpr_read_b32 v4, a124\n"
"v_accvgpr_read_b32 v5, a125\n"
"v_cvt_pk_bf16_f32 v94, v4, v5\n"
"v_accvgpr_read_b32 v16, a126\n"
"v_accvgpr_read_b32 v17, a127\n"
"v_cvt_pk_bf16_f32 v95, v16, v17\n"
"ds_write_b128 v32, v[240:243] offset:14336\n"
"s_lshl_b32 s96, s67, 13\n"
"v_add_u32 v8, s96, v1\n"
"v_cvt_pk_bf16_f32 v152, v152, v153\n"
"v_cvt_pk_bf16_f32 v153, v154, v155\n"
"v_cvt_pk_bf16_f32 v154, v156, v157\n"
"v_cvt_pk_bf16_f32 v155, v158, v159\n"
"ds_write_b128 v32, v[152:155] offset:3072\n"
"v_add_u32 v9, 0x10000, v8\n"
"v_mov_b32 v10, s64\n"
"v_cvt_pk_bf16_f32 v184, v184, v185\n"
"v_cvt_pk_bf16_f32 v185, v186, v187\n"
"v_cvt_pk_bf16_f32 v186, v188, v189\n"
"v_cvt_pk_bf16_f32 v187, v190, v191\n"
"ds_write_b128 v32, v[184:187] offset:7168\n"
"v_mov_b32 v11, s65\n"
"s_lshl_b32 s85, s60, 7\n"
"v_cvt_pk_bf16_f32 v216, v216, v217\n"
"v_cvt_pk_bf16_f32 v217, v218, v219\n"
"v_cvt_pk_bf16_f32 v218, v220, v221\n"
"v_cvt_pk_bf16_f32 v219, v222, v223\n"
"ds_write_b128 v32, v[216:219] offset:11264\n"
"s_add_u32 s85, s85, 163840\n"
"v_cvt_pk_bf16_f32 v248, v248, v249\n"
"v_cvt_pk_bf16_f32 v249, v250, v251\n"
"v_cvt_pk_bf16_f32 v250, v252, v253\n"
"v_cvt_pk_bf16_f32 v251, v254, v255\n"
"ds_write_b128 v32, v[248:251] offset:15360\n"
"s_waitcnt vmcnt(13) lgkmcnt(0)\n"
"s_barrier\n"
"s_lshl_b32 s99, s60, 7\n"
"s_add_u32 s99, s99, 131072\n"
"v_mov_b32 v28, s64\n"
"v_mov_b32 v29, s65\n"
"s_cmp_eq_u32 s61, 0\n"
"s_cselect_b64 exec, 1, 0\n"
"buffer_store_dwordx2 v[28:29], off, s[44:47], s99 sc1\n"
"s_mov_b64 exec, -1\n"
"ATT0_%=:\n"
"ds_read_b128 a[224:227], v8 offset:0\n"
"ds_read_b128 a[228:231], v8 offset:1024\n"
"ds_read_b128 a[232:235], v8 offset:2048\n"
"ds_read_b128 a[236:239], v8 offset:3072\n"
"ds_read_b128 a[240:243], v8 offset:4096\n"
"ds_read_b128 a[244:247], v8 offset:5120\n"
"ds_read_b128 a[248:251], v8 offset:6144\n"
"ds_read_b128 a[252:255], v8 offset:7168\n"
"ds_read_b128 a[64:67], v33 offset:0\n"
"ds_read_b128 a[68:71], v33 offset:1024\n"
"ds_read_b128 a[72:75], v33 offset:2048\n"
"ds_read_b128 a[76:79], v33 offset:3072\n"
"ds_read_b128 a[80:83], v33 offset:4096\n"
"s_add_u32 s78, s78, s92\n"
"s_add_u32 s79, s79, s92\n"
"s_xor_b32 s76, s76, 0x8000\n"
"s_sub_u32 s74, s74, 1\n"
"s_cselect_b64 s[78:79], s[72:73], s[78:79]\n"
"s_cselect_b64 s[72:73], s[16:17], s[72:73]\n"
"s_cselect_b32 s74, s75, s74\n"
"ds_read_b128 a[84:87], v33 offset:5120\n"
"s_cselect_b32 s75, 64, s75\n"
"s_cmp_gt_u32 s74, 32\n"
"s_cselect_b64 s[20:21], s[32:33], s[20:21]\n"
"s_cselect_b32 s22, s34, s22\n"
"s_cselect_b32 s92, 262144, s92\n"
"s_cselect_b32 s5, 524288, s5\n"
"ds_read_b128 a[88:91], v33 offset:6144\n"
"ds_read_b128 a[92:95], v33 offset:7168\n"
"s_add_u32 m0, s76, 0\n"
"ds_read_b128 a[96:99], v34 offset:0\n"
"buffer_load_dwordx4 v24, s[20:23], s78 offen offset:0 sc1 lds\n"
"ds_read_b128 a[100:103], v34 offset:1024\n"
"ds_read_b128 a[104:107], v34 offset:2048\n"
"buffer_load_dwordx4 v25, s[20:23], s78 offen offset:1024 sc1 lds\n"
"ds_read_b128 a[108:111], v34 offset:3072\n"
"buffer_load_dwordx4 v26, s[20:23], s78 offen offset:2048 sc1 lds\n"
"ds_read_b128 a[112:115], v34 offset:4096\n"
"ds_read_b128 a[116:119], v34 offset:5120\n"
"buffer_load_dwordx4 v27, s[20:23], s78 offen offset:3072 sc1 lds\n"
"ds_read_b128 a[120:123], v34 offset:6144\n"
"s_add_u32 m0, s76, 16384\n"
"ds_read_b128 a[124:127], v34 offset:7168\n"
"ds_read_b128 a[128:131], v35 offset:0\n"
"buffer_load_dwordx4 v24, s[20:23], s79 offen offset:0 sc1 lds\n"
"ds_read_b128 a[132:135], v35 offset:1024\n"
"ds_read_b128 a[136:139], v35 offset:2048\n"
"buffer_load_dwordx4 v25, s[20:23], s79 offen offset:1024 sc1 lds\n"
"ds_read_b128 a[140:143], v35 offset:3072\n"
"buffer_load_dwordx4 v26, s[20:23], s79 offen offset:2048 sc1 lds\n"
"ds_read_b128 a[144:147], v35 offset:4096\n"
"ds_read_b128 a[148:151], v35 offset:5120\n"
"buffer_load_dwordx4 v27, s[20:23], s79 offen offset:3072 sc1 lds\n"
"ds_read_b128 a[152:155], v35 offset:6144\n"
"s_add_u32 s80, s78, s5\n"
"s_add_u32 s81, s79, s5\n"
"s_mov_b64 s[24:25], s[20:21]\n"
"s_mov_b32 s26, s22\n"
"s_xor_b32 s77, s77, 0x8000\n"
"ds_read_b128 a[156:159], v35 offset:7168\n"
"ds_read_b128 a[160:163], v42 offset:0\n"
"s_add_u32 s78, s78, s92\n"
"s_add_u32 s79, s79, s92\n"
"s_xor_b32 s76, s76, 0x8000\n"
"s_sub_u32 s74, s74, 1\n"
"s_cselect_b64 s[78:79], s[72:73], s[78:79]\n"
"s_cselect_b64 s[72:73], s[16:17], s[72:73]\n"
"s_cselect_b32 s74, s75, s74\n"
"ds_read_b128 a[164:167], v42 offset:1024\n"
"s_cselect_b32 s75, 64, s75\n"
"s_cmp_gt_u32 s74, 32\n"
"s_cselect_b64 s[20:21], s[32:33], s[20:21]\n"
"s_cselect_b32 s22, s34, s22\n"
"s_cselect_b32 s92, 262144, s92\n"
"s_cselect_b32 s5, 524288, s5\n"
"ds_read_b128 a[168:171], v42 offset:2048\n"
"ds_read_b128 a[172:175], v42 offset:3072\n"
"buffer_store_dwordx4 v[124:127], v25, s[44:47], s88 offen offset:1024 sc1\n"
"ds_read_b128 a[176:179], v42 offset:4096\n"
"s_mov_b32 s88, s91\n"
"ds_read_b128 a[180:183], v42 offset:5120\n"
"ds_read_b128 a[184:187], v42 offset:6144\n"
"v_pk_mul_f32 v[176:177], 0, 0\n"
"v_pk_mul_f32 v[178:179], 0, 0\n"
"v_pk_mul_f32 v[180:181], 0, 0\n"
"v_pk_mul_f32 v[182:183], 0, 0\n"
"v_pk_mul_f32 v[184:185], 0, 0\n"
"v_pk_mul_f32 v[186:187], 0, 0\n"
"v_pk_mul_f32 v[188:189], 0, 0\n"
"v_pk_mul_f32 v[190:191], 0, 0\n"
"ds_read_b128 a[188:191], v42 offset:7168\n"
"s_waitcnt lgkmcnt(0)\n"
"s_barrier\n"
"s_add_u32 m0, s76, 0\n"
"s_nop 0\n"
"buffer_load_dwordx4 v24, s[20:23], s78 offen offset:0 sc1 lds\n"
"buffer_load_dwordx4 v25, s[20:23], s78 offen offset:1024 sc1 lds\n"
"buffer_load_dwordx4 v26, s[20:23], s78 offen offset:2048 sc1 lds\n"
"buffer_load_dwordx4 v27, s[20:23], s78 offen offset:3072 sc1 lds\n"
"BLKS_%=:\n"
"s_sub_u32 s91, s70, 1\n"
"s_cbranch_scc1 N1_18_%=\n"
"v_mfma_f32_16x16x32_bf16 v[128:131], a[224:227], a[64:67], v[176:179]\n"
"ds_read_b128 a[192:195], v8 offset:16384\n"
"v_mfma_f32_16x16x32_bf16 v[128:131], a[228:231], a[68:71], v[128:131]\n"
"ds_read_b128 a[196:199], v8 offset:17408\n"
"v_mfma_f32_16x16x32_bf16 v[128:131], a[232:235], a[72:75], v[128:131]\n"
"buffer_store_dwordx4 v[64:67], v24, s[44:47], s84 offen offset:0 sc1\n"
"v_mfma_f32_16x16x32_bf16 v[128:131], a[236:239], a[76:79], v[128:131]\n"
"ds_read_b128 a[200:203], v8 offset:18432\n"
"ds_read_b128 a[204:207], v8 offset:19456\n"
"v_mfma_f32_16x16x32_bf16 v[132:135], a[252:255], a[76:79], v[176:179]\n"
"buffer_store_dwordx4 v[68:71], v25, s[44:47], s84 offen offset:1024 sc1\n"
"v_mfma_f32_16x16x32_bf16 v[132:135], a[248:251], a[72:75], v[132:135]\n"
"ds_read_b128 a[208:211], v8 offset:20480\n"
"ds_read_b128 a[212:215], v8 offset:21504\n"
"v_mfma_f32_16x16x32_bf16 v[132:135], a[244:247], a[68:71], v[132:135]\n"
"buffer_store_dwordx4 v[72:75], v26, s[44:47], s84 offen offset:2048 sc1\n"
"v_mfma_f32_16x16x32_bf16 v[132:135], a[240:243], a[64:67], v[132:135]\n"
"ds_read_b128 a[216:219], v8 offset:22528\n"
"ds_read_b128 a[220:223], v8 offset:23552\n"
"v_mfma_f32_16x16x32_bf16 v[140:143], a[240:243], a[80:83], v[180:183]\n"
"buffer_store_dwordx4 v[76:79], v27, s[44:47], s84 offen offset:3072 sc1\n"
"v_mfma_f32_16x16x32_bf16 v[140:143], a[244:247], a[84:87], v[140:143]\n"
"v_exp_f32 v128, v128\n"
"v_mfma_f32_16x16x32_bf16 v[140:143], a[248:251], a[88:91], v[140:143]\n"
"v_exp_f32 v129, v129\n"
"v_mfma_f32_16x16x32_bf16 v[140:143], a[252:255], a[92:95], v[140:143]\n"
"v_exp_f32 v130, v130\n"
"v_mfma_f32_16x16x32_bf16 v[136:139], a[236:239], a[92:95], v[180:183]\n"
"v_exp_f32 v131, v131\n"
"v_mfma_f32_16x16x32_bf16 v[136:139], a[232:235], a[88:91], v[136:139]\n"
"v_exp_f32 v132, v132\n"
"v_mfma_f32_16x16x32_bf16 v[136:139], a[228:231], a[84:87], v[136:139]\n"
"v_exp_f32 v133, v133\n"
"v_mfma_f32_16x16x32_bf16 v[136:139], a[224:227], a[80:83], v[136:139]\n"
"v_exp_f32 v134, v134\n"
"v_mfma_f32_16x16x32_bf16 v[144:147], a[224:227], a[96:99], v[184:187]\n"
"v_exp_f32 v135, v135\n"
"v_mfma_f32_16x16x32_bf16 v[144:147], a[228:231], a[100:103], v[144:147]\n"
"v_exp_f32 v140, v140\n"
"v_mfma_f32_16x16x32_bf16 v[144:147], a[232:235], a[104:107], v[144:147]\n"
"v_exp_f32 v141, v141\n"
"v_mfma_f32_16x16x32_bf16 v[144:147], a[236:239], a[108:111], v[144:147]\n"
"v_exp_f32 v136, v136\n"
"v_mfma_f32_16x16x32_bf16 v[148:151], a[252:255], a[108:111], v[184:187]\n"
"v_exp_f32 v137, v137\n"
"v_mfma_f32_16x16x32_bf16 v[148:151], a[248:251], a[104:107], v[148:151]\n"
"v_exp_f32 v138, v138\n"
"v_mfma_f32_16x16x32_bf16 v[148:151], a[244:247], a[100:103], v[148:151]\n"
"v_exp_f32 v139, v139\n"
"v_mfma_f32_16x16x32_bf16 v[148:151], a[240:243], a[96:99], v[148:151]\n"
"v_exp_f32 v142, v142\n"
"v_mfma_f32_16x16x32_bf16 v[156:159], a[240:243], a[112:115], v[188:191]\n"
"v_exp_f32 v143, v143\n"
"v_mfma_f32_16x16x32_bf16 v[156:159], a[244:247], a[116:119], v[156:159]\n"
"v_exp_f32 v144, v144\n"
"v_mfma_f32_16x16x32_bf16 v[156:159], a[248:251], a[120:123], v[156:159]\n"
"v_exp_f32 v145, v145\n"
"v_mfma_f32_16x16x32_bf16 v[156:159], a[252:255], a[124:127], v[156:159]\n"
"v_exp_f32 v146, v146\n"
"v_mfma_f32_16x16x32_bf16 v[152:155], a[236:239], a[124:127], v[188:191]\n"
"v_exp_f32 v147, v147\n"
"v_mfma_f32_16x16x32_bf16 v[152:155], a[232:235], a[120:123], v[152:155]\n"
"v_xor_b32 v8, 0x8000, v8\n"
"v_cvt_pk_bf16_f32 v128, v128, v129\n"
"v_mfma_f32_16x16x32_bf16 v[152:155], a[228:231], a[116:119], v[152:155]\n"
"s_waitcnt vmcnt(13)\n"
"v_cvt_pk_bf16_f32 v129, v130, v131\n"
"v_cvt_pk_bf16_f32 v130, v132, v133\n"
"v_mfma_f32_16x16x32_bf16 v[152:155], a[224:227], a[112:115], v[152:155]\n"
"s_waitcnt lgkmcnt(0)\n"
"s_barrier\n"
"v_cvt_pk_bf16_f32 v131, v134, v135\n"
"v_cvt_pk_bf16_f32 v136, v136, v137\n"
"v_cvt_pk_bf16_f32 v137, v138, v139\n"
"v_cvt_pk_bf16_f32 v138, v140, v141\n"
"v_cvt_pk_bf16_f32 v139, v142, v143\n"
"v_cvt_pk_bf16_f32 v144, v144, v145\n"
"v_cvt_pk_bf16_f32 v145, v146, v147\n"
"v_mfma_f32_16x16x32_bf16 v[96:99], a[192:195], a[64:67], v[176:179]\n"
"s_add_u32 m0, s77, 0\n"
"ds_read_b128 a[224:227], v9 offset:0\n"
"v_mfma_f32_16x16x32_bf16 v[96:99], a[196:199], a[68:71], v[96:99]\n"
"v_exp_f32 v148, v148\n"
"v_mfma_f32_16x16x32_bf16 v[96:99], a[200:203], a[72:75], v[96:99]\n"
"ds_read_b128 a[228:231], v9 offset:1024\n"
"v_mfma_f32_16x16x32_bf16 v[96:99], a[204:207], a[76:79], v[96:99]\n"
"buffer_load_dwordx4 v24, s[24:27], s80 offen offset:0 sc1 lds\n"
"v_mfma_f32_16x16x32_bf16 v[100:103], a[220:223], a[76:79], v[176:179]\n"
"ds_read_b128 a[232:235], v9 offset:2048\n"
"v_mfma_f32_16x16x32_bf16 v[100:103], a[216:219], a[72:75], v[100:103]\n"
"buffer_load_dwordx4 v25, s[24:27], s80 offen offset:1024 sc1 lds\n"
"v_mfma_f32_16x16x32_bf16 v[100:103], a[212:215], a[68:71], v[100:103]\n"
"v_exp_f32 v149, v149\n"
"v_mfma_f32_16x16x32_bf16 v[100:103], a[208:211], a[64:67], v[100:103]\n"
"ds_read_b128 a[236:239], v9 offset:3072\n"
"v_cvt_pk_bf16_f32 v146, v148, v149\n"
"v_mfma_f32_16x16x32_bf16 v[108:111], a[208:211], a[80:83], v[180:183]\n"
"buffer_load_dwordx4 v26, s[24:27], s80 offen offset:2048 sc1 lds\n"
"v_mfma_f32_16x16x32_bf16 v[108:111], a[212:215], a[84:87], v[108:111]\n"
"ds_read_b128 a[240:243], v9 offset:4096\n"
"v_mfma_f32_16x16x32_bf16 v[108:111], a[216:219], a[88:91], v[108:111]\n"
"v_exp_f32 v150, v150\n"
"v_mfma_f32_16x16x32_bf16 v[108:111], a[220:223], a[92:95], v[108:111]\n"
"ds_read_b128 a[244:247], v9 offset:5120\n"
"v_mfma_f32_16x16x32_bf16 v[104:107], a[204:207], a[92:95], v[180:183]\n"
"buffer_load_dwordx4 v27, s[24:27], s80 offen offset:3072 sc1 lds\n"
"v_mfma_f32_16x16x32_bf16 v[104:107], a[200:203], a[88:91], v[104:107]\n"
"s_add_u32 m0, s77, 16384\n"
"s_add_u32 s80, s78, s5\n"
"v_mfma_f32_16x16x32_bf16 v[104:107], a[196:199], a[84:87], v[104:107]\n"
"ds_read_b128 a[248:251], v9 offset:6144\n"
"v_mfma_f32_16x16x32_bf16 v[104:107], a[192:195], a[80:83], v[104:107]\n"
"buffer_load_dwordx4 v24, s[24:27], s81 offen offset:0 sc1 lds\n"
"v_mfma_f32_16x16x32_bf16 v[112:115], a[192:195], a[96:99], v[184:187]\n"
"ds_read_b128 a[252:255], v9 offset:7168\n"
"v_mfma_f32_16x16x32_bf16 v[112:115], a[196:199], a[100:103], v[112:115]\n"
"buffer_load_dwordx4 v25, s[24:27], s81 offen offset:1024 sc1 lds\n"
"PG7_%=:\n"
"s_cbranch_execz PADRET_%=\n"
"v_mfma_f32_16x16x32_bf16 v[112:115], a[200:203], a[104:107], v[112:115]\n"
"v_exp_f32 v151, v151\n"
"v_mfma_f32_16x16x32_bf16 v[112:115], a[204:207], a[108:111], v[112:115]\n"
"v_exp_f32 v152, v152\n"
"v_mfma_f32_16x16x32_bf16 v[116:119], a[220:223], a[108:111], v[184:187]\n"
"buffer_load_dwordx4 v26, s[24:27], s81 offen offset:2048 sc1 lds\n"
"v_mfma_f32_16x16x32_bf16 v[116:119], a[216:219], a[104:107], v[116:119]\n"
"v_exp_f32 v153, v153\n"
"v_mfma_f32_16x16x32_bf16 v[116:119], a[212:215], a[100:103], v[116:119]\n"
"v_cvt_pk_bf16_f32 v147, v150, v151\n"
"v_cvt_pk_bf16_f32 v152, v152, v153\n"
"v_mfma_f32_16x16x32_bf16 v[116:119], a[208:211], a[96:99], v[116:119]\n"
"buffer_load_dwordx4 v27, s[24:27], s81 offen offset:3072 sc1 lds\n"
"v_mfma_f32_16x16x32_bf16 v[124:127], a[208:211], a[112:115], v[188:191]\n"
"s_add_u32 s81, s79, s5\n"
"s_xor_b32 s77, s77, 0x8000\n"
"s_add_u32 m0, s76, 16384\n"
"v_mfma_f32_16x16x32_bf16 v[124:127], a[212:215], a[116:119], v[124:127]\n"
"s_mov_b64 s[24:25], s[20:21]\n"
"v_exp_f32 v154, v154\n"
"v_mfma_f32_16x16x32_bf16 v[124:127], a[216:219], a[120:123], v[124:127]\n"
"buffer_load_dwordx4 v24, s[20:23], s79 offen offset:0 sc1 lds\n"
"v_mfma_f32_16x16x32_bf16 v[124:127], a[220:223], a[124:127], v[124:127]\n"
"s_mov_b32 s26, s22\n"
"v_exp_f32 v155, v155\n"
"v_mfma_f32_16x16x32_bf16 v[120:123], a[204:207], a[124:127], v[188:191]\n"
"s_add_u32 s78, s78, s92\n"
"v_exp_f32 v156, v156\n"
"v_mfma_f32_16x16x32_bf16 v[120:123], a[200:203], a[120:123], v[120:123]\n"
"buffer_load_dwordx4 v25, s[20:23], s79 offen offset:1024 sc1 lds\n"
"v_mfma_f32_16x16x32_bf16 v[120:123], a[196:199], a[116:119], v[120:123]\n"
"v_exp_f32 v157, v157\n"
"v_mfma_f32_16x16x32_bf16 v[120:123], a[192:195], a[112:115], v[120:123]\n"
"s_waitcnt lgkmcnt(7)\n"
"v_exp_f32 v158, v158\n"
"v_mfma_f32_16x16x32_bf16 v[192:195], a[224:227], v[128:131], 0\n"
"s_waitcnt lgkmcnt(6)\n"
"ds_read_b128 a[192:195], v8 offset:0\n"
"v_cvt_pk_bf16_f32 v153, v154, v155\n"
"v_mfma_f32_16x16x32_bf16 v[196:199], a[228:231], v[128:131], 0\n"
"buffer_load_dwordx4 v26, s[20:23], s79 offen offset:2048 sc1 lds\n"
"s_waitcnt lgkmcnt(6)\n"
"v_mfma_f32_16x16x32_bf16 v[200:203], a[232:235], v[128:131], 0\n"
"s_waitcnt lgkmcnt(5)\n"
"ds_read_b128 a[196:199], v8 offset:1024\n"
"v_cvt_pk_bf16_f32 v154, v156, v157\n"
"v_mfma_f32_16x16x32_bf16 v[204:207], a[236:239], v[128:131], 0\n"
"buffer_load_dwordx4 v27, s[20:23], s79 offen offset:3072 sc1 lds\n"
"v_mfma_f32_16x16x32_bf16 v[160:163], v[60:63], v[128:131], 0\n"
"s_add_u32 s79, s79, s92\n"
"v_exp_f32 v159, v159\n"
"s_waitcnt lgkmcnt(5)\n"
"v_mfma_f32_16x16x32_bf16 v[208:211], a[240:243], v[128:131], 0\n"
"s_xor_b32 s76, s76, 0x8000\n"
"ds_read_b128 a[200:203], v8 offset:2048\n"
"v_cvt_pk_bf16_f32 v155, v158, v159\n"
"s_waitcnt lgkmcnt(5)\n"
"v_mfma_f32_16x16x32_bf16 v[212:215], a[244:247], v[128:131], 0\n"
"buffer_store_dwordx4 v[80:83], v24, s[44:47], s86 offen offset:0 sc1\n"
"s_waitcnt lgkmcnt(4)\n"
"v_mfma_f32_16x16x32_bf16 v[216:219], a[248:251], v[128:131], 0\n"
"s_sub_u32 s74, s74, 1\n"
"s_cselect_b64 s[78:79], s[72:73], s[78:79]\n"
"ds_read_b128 a[204:207], v8 offset:3072\n"
"s_waitcnt lgkmcnt(4)\n"
"v_mfma_f32_16x16x32_bf16 v[220:223], a[252:255], v[128:131], 0\n"
"s_cselect_b64 s[72:73], s[16:17], s[72:73]\n"
"v_exp_f32 v96, v96\n"
"v_mfma_f32_16x16x32_bf16 v[252:255], a[252:255], v[136:139], 0\n"
"buffer_store_dwordx4 v[84:87], v25, s[44:47], s86 offen offset:1024 sc1\n"
"v_mfma_f32_16x16x32_bf16 v[248:251], a[248:251], v[136:139], 0\n"
"s_cselect_b32 s74, s75, s74\n"
"s_cselect_b32 s75, 64, s75\n"
"ds_read_b128 a[208:211], v8 offset:4096\n"
"v_mfma_f32_16x16x32_bf16 v[244:247], a[244:247], v[136:139], 0\n"
"s_cmp_gt_u32 s74, 32\n"
"v_exp_f32 v97, v97\n"
"v_mfma_f32_16x16x32_bf16 v[240:243], a[240:243], v[136:139], 0\n"
"buffer_store_dwordx4 v[88:91], v26, s[44:47], s86 offen offset:2048 sc1\n"
"v_mfma_f32_16x16x32_bf16 v[164:167], v[60:63], v[136:139], 0\n"
"s_cselect_b64 s[20:21], s[32:33], s[20:21]\n"
"ds_read_b128 a[212:215], v8 offset:5120\n"
"v_cvt_pk_bf16_f32 v96, v96, v97\n"
"v_mfma_f32_16x16x32_bf16 v[236:239], a[236:239], v[136:139], 0\n"
"s_cselect_b32 s22, s34, s22\n"
"v_exp_f32 v98, v98\n"
"v_mfma_f32_16x16x32_bf16 v[232:235], a[232:235], v[136:139], 0\n"
"s_cselect_b32 s92, 262144, s92\n"
"s_cselect_b32 s5, 524288, s5\n"
"ds_read_b128 a[216:219], v8 offset:6144\n"
"v_mfma_f32_16x16x32_bf16 v[228:231], a[228:231], v[136:139], 0\n"
"buffer_store_dwordx4 v[92:95], v27, s[44:47], s86 offen offset:3072 sc1\n"
"v_mfma_f32_16x16x32_bf16 v[224:227], a[224:227], v[136:139], 0\n"
"v_exp_f32 v99, v99\n"
"v_mfma_f32_16x16x32_bf16 a[0:3], a[224:227], v[144:147], 0\n"
"ds_read_b128 a[220:223], v8 offset:7168\n"
"v_cvt_pk_bf16_f32 v97, v98, v99\n"
"v_mfma_f32_16x16x32_bf16 a[4:7], a[228:231], v[144:147], 0\n"
"v_exp_f32 v100, v100\n"
"v_mfma_f32_16x16x32_bf16 a[8:11], a[232:235], v[144:147], 0\n"
"v_exp_f32 v101, v101\n"
"v_mfma_f32_16x16x32_bf16 a[12:15], a[236:239], v[144:147], 0\n"
"v_exp_f32 v102, v102\n"
"v_mfma_f32_16x16x32_bf16 v[168:171], v[60:63], v[144:147], 0\n"
"v_exp_f32 v103, v103\n"
"v_mfma_f32_16x16x32_bf16 a[16:19], a[240:243], v[144:147], 0\n"
"v_exp_f32 v104, v104\n"
"v_mfma_f32_16x16x32_bf16 a[20:23], a[244:247], v[144:147], 0\n"
"v_exp_f32 v105, v105\n"
"v_mfma_f32_16x16x32_bf16 a[24:27], a[248:251], v[144:147], 0\n"
"v_exp_f32 v106, v106\n"
"v_mfma_f32_16x16x32_bf16 a[28:31], a[252:255], v[144:147], 0\n"
"v_exp_f32 v107, v107\n"
"v_mfma_f32_16x16x32_bf16 a[60:63], a[252:255], v[152:155], 0\n"
"v_exp_f32 v108, v108\n"
"v_mfma_f32_16x16x32_bf16 a[56:59], a[248:251], v[152:155], 0\n"
"v_exp_f32 v109, v109\n"
"v_mfma_f32_16x16x32_bf16 a[52:55], a[244:247], v[152:155], 0\n"
"v_exp_f32 v110, v110\n"
"v_mfma_f32_16x16x32_bf16 a[48:51], a[240:243], v[152:155], 0\n"
"v_exp_f32 v111, v111\n"
"v_mfma_f32_16x16x32_bf16 v[172:175], v[60:63], v[152:155], 0\n"
"v_exp_f32 v112, v112\n"
"v_mfma_f32_16x16x32_bf16 a[44:47], a[236:239], v[152:155], 0\n"
"v_exp_f32 v113, v113\n"
"v_mfma_f32_16x16x32_bf16 a[40:43], a[232:235], v[152:155], 0\n"
"v_exp_f32 v114, v114\n"
"v_mfma_f32_16x16x32_bf16 a[36:39], a[228:231], v[152:155], 0\n"
"s_waitcnt vmcnt(25)\n"
"v_exp_f32 v115, v115\n"
"v_mfma_f32_16x16x32_bf16 a[32:35], a[224:227], v[152:155], 0\n"
"s_waitcnt lgkmcnt(0)\n"
"s_barrier\n"
"v_exp_f32 v116, v116\n"
"v_cvt_pk_bf16_f32 v98, v100, v101\n"
"v_cvt_pk_bf16_f32 v99, v102, v103\n"
"v_cvt_pk_bf16_f32 v104, v104, v105\n"
"v_cvt_pk_bf16_f32 v105, v106, v107\n"
"v_cvt_pk_bf16_f32 v106, v108, v109\n"
"v_cvt_pk_bf16_f32 v107, v110, v111\n"
"v_cvt_pk_bf16_f32 v112, v112, v113\n"
"v_cvt_pk_bf16_f32 v113, v114, v115\n"
"v_exp_f32 v117, v117\n"
"s_nop 0\n"
"v_cvt_pk_bf16_f32 v114, v116, v117\n"
"s_sub_u32 s91, s91, 1\n"
"s_cbranch_scc1 DG_14_%=\n"
"s_nop 0\n"
"ATL_13_%=:\n"
"v_mfma_f32_16x16x32_bf16 v[128:131], a[192:195], a[64:67], v[176:179]\n"
"s_add_u32 m0, s77, 0\n"
"ds_read_b128 a[224:227], v9 offset:16384\n"
"v_mfma_f32_16x16x32_bf16 v[128:131], a[196:199], a[68:71], v[128:131]\n"
"v_exp_f32 v118, v118\n"
"v_mfma_f32_16x16x32_bf16 v[128:131], a[200:203], a[72:75], v[128:131]\n"
"ds_read_b128 a[228:231], v9 offset:17408\n"
"v_mfma_f32_16x16x32_bf16 v[128:131], a[204:207], a[76:79], v[128:131]\n"
"buffer_load_dwordx2 v[20:21], v18, s[44:47], 0 offen sc1\n"
"v_mfma_f32_16x16x32_bf16 v[132:135], a[220:223], a[76:79], v[176:179]\n"
"ds_read_b128 a[232:235], v9 offset:18432\n"
"v_mfma_f32_16x16x32_bf16 v[132:135], a[216:219], a[72:75], v[132:135]\n"
"v_exp_f32 v119, v119\n"
"v_mfma_f32_16x16x32_bf16 v[132:135], a[212:215], a[68:71], v[132:135]\n"
"v_exp_f32 v120, v120\n"
"v_mfma_f32_16x16x32_bf16 v[132:135], a[208:211], a[64:67], v[132:135]\n"
"ds_read_b128 a[236:239], v9 offset:19456\n"
"v_cvt_pk_bf16_f32 v115, v118, v119\n"
"v_mfma_f32_16x16x32_bf16 v[140:143], a[208:211], a[80:83], v[180:183]\n"
"buffer_load_dwordx4 v24, s[24:27], s80 offen offset:0 sc1 lds\n"
"v_mfma_f32_16x16x32_bf16 v[140:143], a[212:215], a[84:87], v[140:143]\n"
"ds_read_b128 a[240:243], v9 offset:20480\n"
"v_mfma_f32_16x16x32_bf16 v[140:143], a[216:219], a[88:91], v[140:143]\n"
"v_exp_f32 v121, v121\n"
"v_mfma_f32_16x16x32_bf16 v[140:143], a[220:223], a[92:95], v[140:143]\n"
"ds_read_b128 a[244:247], v9 offset:21504\n"
"v_cvt_pk_bf16_f32 v120, v120, v121\n"
"v_mfma_f32_16x16x32_bf16 v[136:139], a[204:207], a[92:95], v[180:183]\n"
"v_exp_f32 v122, v122\n"
"v_mfma_f32_16x16x32_bf16 v[136:139], a[200:203], a[88:91], v[136:139]\n"
"v_exp_f32 v123, v123\n"
"v_mfma_f32_16x16x32_bf16 v[136:139], a[196:199], a[84:87], v[136:139]\n"
"ds_read_b128 a[248:251], v9 offset:22528\n"
"v_cvt_pk_bf16_f32 v121, v122, v123\n"
"v_mfma_f32_16x16x32_bf16 v[136:139], a[192:195], a[80:83], v[136:139]\n"
"buffer_load_dwordx4 v25, s[24:27], s80 offen offset:1024 sc1 lds\n"
"v_mfma_f32_16x16x32_bf16 v[144:147], a[192:195], a[96:99], v[184:187]\n"
"ds_read_b128 a[252:255], v9 offset:23552\n"
"v_xor_b32 v9, 0x8000, v9\n"
"v_mfma_f32_16x16x32_bf16 v[144:147], a[196:199], a[100:103], v[144:147]\n"
"v_exp_f32 v124, v124\n"
"v_mfma_f32_16x16x32_bf16 v[144:147], a[200:203], a[104:107], v[144:147]\n"
"v_exp_f32 v125, v125\n"
"v_mfma_f32_16x16x32_bf16 v[144:147], a[204:207], a[108:111], v[144:147]\n"
"v_exp_f32 v126, v126\n"
"v_mfma_f32_16x16x32_bf16 v[148:151], a[220:223], a[108:111], v[184:187]\n"
"v_exp_f32 v127, v127\n"
"v_mfma_f32_16x16x32_bf16 v[148:151], a[216:219], a[104:107], v[148:151]\n"
"buffer_load_dwordx4 v26, s[24:27], s80 offen offset:2048 sc1 lds\n"
"v_mfma_f32_16x16x32_bf16 v[148:151], a[212:215], a[100:103], v[148:151]\n"
"v_cvt_pk_bf16_f32 v122, v124, v125\n"
"v_cvt_pk_bf16_f32 v123, v126, v127\n"
"v_mfma_f32_16x16x32_bf16 v[148:151], a[208:211], a[96:99], v[148:151]\n"
"v_exp_f32 v128, v128\n"
"v_mfma_f32_16x16x32_bf16 v[156:159], a[208:211], a[112:115], v[188:191]\n"
"v_exp_f32 v129, v129\n"
"v_mfma_f32_16x16x32_bf16 v[156:159], a[212:215], a[116:119], v[156:159]\n"
"v_exp_f32 v130, v130\n"
"v_mfma_f32_16x16x32_bf16 v[156:159], a[216:219], a[120:123], v[156:159]\n"
"v_exp_f32 v131, v131\n"
"v_mfma_f32_16x16x32_bf16 v[156:159], a[220:223], a[124:127], v[156:159]\n"
"v_exp_f32 v132, v132\n"
"v_mfma_f32_16x16x32_bf16 v[152:155], a[204:207], a[124:127], v[188:191]\n"
"buffer_load_dwordx4 v27, s[24:27], s80 offen offset:3072 sc1 lds\n"
"v_mfma_f32_16x16x32_bf16 v[152:155], a[200:203], a[120:123], v[152:155]\n"
"s_add_u32 m0, s76, 0\n"
"v_exp_f32 v133, v133\n"
"v_mfma_f32_16x16x32_bf16 v[152:155], a[196:199], a[116:119], v[152:155]\n"
"v_exp_f32 v134, v134\n"
"v_mfma_f32_16x16x32_bf16 v[152:155], a[192:195], a[112:115], v[152:155]\n"
"s_waitcnt lgkmcnt(7)\n"
"v_exp_f32 v135, v135\n"
"v_mfma_f32_16x16x32_bf16 v[192:195], a[224:227], v[96:99], v[192:195]\n"
"s_waitcnt lgkmcnt(6)\n"
"ds_read_b128 a[192:195], v8 offset:16384\n"
"v_cvt_pk_bf16_f32 v128, v128, v129\n"
"v_mfma_f32_16x16x32_bf16 v[196:199], a[228:231], v[96:99], v[196:199]\n"
"s_waitcnt lgkmcnt(6)\n"
"v_exp_f32 v136, v136\n"
"v_mfma_f32_16x16x32_bf16 v[200:203], a[232:235], v[96:99], v[200:203]\n"
"s_waitcnt lgkmcnt(5)\n"
"ds_read_b128 a[196:199], v8 offset:17408\n"
"v_cvt_pk_bf16_f32 v129, v130, v131\n"
"v_mfma_f32_16x16x32_bf16 v[204:207], a[236:239], v[96:99], v[204:207]\n"
"buffer_load_dwordx4 v24, s[20:23], s78 offen offset:0 sc1 lds\n"
"v_mfma_f32_16x16x32_bf16 v[160:163], v[60:63], v[96:99], v[160:163]\n"
"s_waitcnt lgkmcnt(5)\n"
"v_exp_f32 v137, v137\n"
"v_mfma_f32_16x16x32_bf16 v[208:211], a[240:243], v[96:99], v[208:211]\n"
"s_waitcnt lgkmcnt(4)\n"
"ds_read_b128 a[200:203], v8 offset:18432\n"
"v_cvt_pk_bf16_f32 v130, v132, v133\n"
"v_mfma_f32_16x16x32_bf16 v[212:215], a[244:247], v[96:99], v[212:215]\n"
"s_waitcnt lgkmcnt(4)\n"
"v_exp_f32 v138, v138\n"
"v_mfma_f32_16x16x32_bf16 v[216:219], a[248:251], v[96:99], v[216:219]\n"
"s_waitcnt lgkmcnt(3)\n"
"ds_read_b128 a[204:207], v8 offset:19456\n"
"v_cvt_pk_bf16_f32 v131, v134, v135\n"
"v_mfma_f32_16x16x32_bf16 v[220:223], a[252:255], v[96:99], v[220:223]\n"
"v_exp_f32 v139, v139\n"
"v_mfma_f32_16x16x32_bf16 v[252:255], a[252:255], v[104:107], v[252:255]\n"
"buffer_load_dwordx4 v25, s[20:23], s78 offen offset:1024 sc1 lds\n"
"v_mfma_f32_16x16x32_bf16 v[248:251], a[248:251], v[104:107], v[248:251]\n"
"ds_read_b128 a[208:211], v8 offset:20480\n"
"v_cvt_pk_bf16_f32 v136, v136, v137\n"
"v_mfma_f32_16x16x32_bf16 v[244:247], a[244:247], v[104:107], v[244:247]\n"
"v_exp_f32 v140, v140\n"
"v_mfma_f32_16x16x32_bf16 v[240:243], a[240:243], v[104:107], v[240:243]\n"
"v_exp_f32 v141, v141\n"
"v_mfma_f32_16x16x32_bf16 v[164:167], v[60:63], v[104:107], v[164:167]\n"
"ds_read_b128 a[212:215], v8 offset:21504\n"
"v_cvt_pk_bf16_f32 v137, v138, v139\n"
"v_mfma_f32_16x16x32_bf16 v[236:239], a[236:239], v[104:107], v[236:239]\n"
"v_exp_f32 v142, v142\n"
"v_mfma_f32_16x16x32_bf16 v[232:235], a[232:235], v[104:107], v[232:235]\n"
"ds_read_b128 a[216:219], v8 offset:22528\n"
"v_cvt_pk_bf16_f32 v138, v140, v141\n"
"v_mfma_f32_16x16x32_bf16 v[228:231], a[228:231], v[104:107], v[228:231]\n"
"buffer_load_dwordx4 v26, s[20:23], s78 offen offset:2048 sc1 lds\n"
"v_mfma_f32_16x16x32_bf16 v[224:227], a[224:227], v[104:107], v[224:227]\n"
"v_exp_f32 v143, v143\n"
"v_mfma_f32_16x16x32_bf16 a[0:3], a[224:227], v[112:115], a[0:3]\n"
"ds_read_b128 a[220:223], v8 offset:23552\n"
"v_xor_b32 v8, 0x8000, v8\n"
"v_mfma_f32_16x16x32_bf16 a[4:7], a[228:231], v[112:115], a[4:7]\n"
"v_exp_f32 v144, v144\n"
"v_mfma_f32_16x16x32_bf16 a[8:11], a[232:235], v[112:115], a[8:11]\n"
"v_exp_f32 v145, v145\n"
"v_mfma_f32_16x16x32_bf16 a[12:15], a[236:239], v[112:115], a[12:15]\n"
"v_exp_f32 v146, v146\n"
"v_mfma_f32_16x16x32_bf16 v[168:171], v[60:63], v[112:115], v[168:171]\n"
"buffer_load_dwordx4 v27, s[20:23], s78 offen offset:3072 sc1 lds\n"
"v_mfma_f32_16x16x32_bf16 a[16:19], a[240:243], v[112:115], a[16:19]\n"
"v_exp_f32 v147, v147\n"
"v_mfma_f32_16x16x32_bf16 a[20:23], a[244:247], v[112:115], a[20:23]\n"
"v_exp_f32 v148, v148\n"
"v_mfma_f32_16x16x32_bf16 a[24:27], a[248:251], v[112:115], a[24:27]\n"
"v_exp_f32 v149, v149\n"
"v_mfma_f32_16x16x32_bf16 a[28:31], a[252:255], v[112:115], a[28:31]\n"
"v_cvt_pk_bf16_f32 v139, v142, v143\n"
"v_cvt_pk_bf16_f32 v144, v144, v145\n"
"v_mfma_f32_16x16x32_bf16 a[60:63], a[252:255], v[120:123], a[60:63]\n"
"v_cvt_pk_bf16_f32 v145, v146, v147\n"
"v_cvt_pk_bf16_f32 v146, v148, v149\n"
"v_mfma_f32_16x16x32_bf16 a[56:59], a[248:251], v[120:123], a[56:59]\n"
"v_mfma_f32_16x16x32_bf16 a[52:55], a[244:247], v[120:123], a[52:55]\n"
"v_mfma_f32_16x16x32_bf16 a[48:51], a[240:243], v[120:123], a[48:51]\n"
"v_mfma_f32_16x16x32_bf16 v[172:175], v[60:63], v[120:123], v[172:175]\n"
"v_mfma_f32_16x16x32_bf16 a[44:47], a[236:239], v[120:123], a[44:47]\n"
"v_mfma_f32_16x16x32_bf16 a[40:43], a[232:235], v[120:123], a[40:43]\n"
"v_mfma_f32_16x16x32_bf16 a[36:39], a[228:231], v[120:123], a[36:39]\n"
"s_waitcnt vmcnt(17) lgkmcnt(0)\n"
"s_barrier\n"
"v_mfma_f32_16x16x32_bf16 a[32:35], a[224:227], v[120:123], a[32:35]\n"
"v_mfma_f32_16x16x32_bf16 v[96:99], a[192:195], a[64:67], v[176:179]\n"
"s_add_u32 m0, s77, 16384\n"
"s_add_u32 s80, s78, s5\n"
"ds_read_b128 a[224:227], v9 offset:0\n"
"v_mfma_f32_16x16x32_bf16 v[96:99], a[196:199], a[68:71], v[96:99]\n"
"v_exp_f32 v150, v150\n"
"v_mfma_f32_16x16x32_bf16 v[96:99], a[200:203], a[72:75], v[96:99]\n"
"ds_read_b128 a[228:231], v9 offset:1024\n"
"v_mfma_f32_16x16x32_bf16 v[96:99], a[204:207], a[76:79], v[96:99]\n"
"buffer_load_dwordx4 v24, s[24:27], s81 offen offset:0 sc1 lds\n"
"v_mfma_f32_16x16x32_bf16 v[100:103], a[220:223], a[76:79], v[176:179]\n"
"ds_read_b128 a[232:235], v9 offset:2048\n"
"v_mfma_f32_16x16x32_bf16 v[100:103], a[216:219], a[72:75], v[100:103]\n"
"v_exp_f32 v151, v151\n"
"v_mfma_f32_16x16x32_bf16 v[100:103], a[212:215], a[68:71], v[100:103]\n"
"v_exp_f32 v152, v152\n"
"v_mfma_f32_16x16x32_bf16 v[100:103], a[208:211], a[64:67], v[100:103]\n"
"ds_read_b128 a[236:239], v9 offset:3072\n"
"v_cvt_pk_bf16_f32 v147, v150, v151\n"
"v_mfma_f32_16x16x32_bf16 v[108:111], a[208:211], a[80:83], v[180:183]\n"
"buffer_load_dwordx4 v25, s[24:27], s81 offen offset:1024 sc1 lds\n"
"v_mfma_f32_16x16x32_bf16 v[108:111], a[212:215], a[84:87], v[108:111]\n"
"ds_read_b128 a[240:243], v9 offset:4096\n"
"v_mfma_f32_16x16x32_bf16 v[108:111], a[216:219], a[88:91], v[108:111]\n"
"v_exp_f32 v153, v153\n"
"v_mfma_f32_16x16x32_bf16 v[108:111], a[220:223], a[92:95], v[108:111]\n"
"ds_read_b128 a[244:247], v9 offset:5120\n"
"v_cvt_pk_bf16_f32 v152, v152, v153\n"
"v_mfma_f32_16x16x32_bf16 v[104:107], a[204:207], a[92:95], v[180:183]\n"
"v_exp_f32 v154, v154\n"
"v_mfma_f32_16x16x32_bf16 v[104:107], a[200:203], a[88:91], v[104:107]\n"
"v_exp_f32 v155, v155\n"
"v_mfma_f32_16x16x32_bf16 v[104:107], a[196:199], a[84:87], v[104:107]\n"
"ds_read_b128 a[248:251], v9 offset:6144\n"
"v_cvt_pk_bf16_f32 v153, v154, v155\n"
"v_mfma_f32_16x16x32_bf16 v[104:107], a[192:195], a[80:83], v[104:107]\n"
"buffer_load_dwordx4 v26, s[24:27], s81 offen offset:2048 sc1 lds\n"
"v_mfma_f32_16x16x32_bf16 v[112:115], a[192:195], a[96:99], v[184:187]\n"
"ds_read_b128 a[252:255], v9 offset:7168\n"
"v_mfma_f32_16x16x32_bf16 v[112:115], a[196:199], a[100:103], v[112:115]\n"
"v_exp_f32 v156, v156\n"
"v_mfma_f32_16x16x32_bf16 v[112:115], a[200:203], a[104:107], v[112:115]\n"
"v_exp_f32 v157, v157\n"
"v_mfma_f32_16x16x32_bf16 v[112:115], a[204:207], a[108:111], v[112:115]\n"
"v_exp_f32 v158, v158\n"
"v_mfma_f32_16x16x32_bf16 v[116:119], a[220:223], a[108:111], v[184:187]\n"
"buffer_load_dwordx4 v27, s[24:27], s81 offen offset:3072 sc1 lds\n"
"v_mfma_f32_16x16x32_bf16 v[116:119], a[216:219], a[104:107], v[116:119]\n"
"s_add_u32 s81, s79, s5\n"
"s_xor_b32 s77, s77, 0x8000\n"
"s_add_u32 m0, s76, 16384\n"
"v_mfma_f32_16x16x32_bf16 v[116:119], a[212:215], a[100:103], v[116:119]\n"
"s_mov_b64 s[24:25], s[20:21]\n"
"v_exp_f32 v159, v159\n"
"v_mfma_f32_16x16x32_bf16 v[116:119], a[208:211], a[96:99], v[116:119]\n"
"s_mov_b32 s26, s22\n"
"v_cvt_pk_bf16_f32 v154, v156, v157\n"
"v_cvt_pk_bf16_f32 v155, v158, v159\n"
"v_mfma_f32_16x16x32_bf16 v[124:127], a[208:211], a[112:115], v[188:191]\n"
"s_add_u32 s78, s78, s92\n"
"v_exp_f32 v96, v96\n"
"v_mfma_f32_16x16x32_bf16 v[124:127], a[212:215], a[116:119], v[124:127]\n"
"v_exp_f32 v97, v97\n"
"v_mfma_f32_16x16x32_bf16 v[124:127], a[216:219], a[120:123], v[124:127]\n"
"buffer_load_dwordx4 v24, s[20:23], s79 offen offset:0 sc1 lds\n"
"v_mfma_f32_16x16x32_bf16 v[124:127], a[220:223], a[124:127], v[124:127]\n"
"v_exp_f32 v98, v98\n"
"v_mfma_f32_16x16x32_bf16 v[120:123], a[204:207], a[124:127], v[188:191]\n"
"v_exp_f32 v99, v99\n"
"v_mfma_f32_16x16x32_bf16 v[120:123], a[200:203], a[120:123], v[120:123]\n"
"v_exp_f32 v100, v100\n"
"v_mfma_f32_16x16x32_bf16 v[120:123], a[196:199], a[116:119], v[120:123]\n"
"v_exp_f32 v101, v101\n"
"v_mfma_f32_16x16x32_bf16 v[120:123], a[192:195], a[112:115], v[120:123]\n"
"s_waitcnt lgkmcnt(7)\n"
"v_exp_f32 v102, v102\n"
"v_mfma_f32_16x16x32_bf16 v[192:195], a[224:227], v[128:131], v[192:195]\n"
"s_waitcnt lgkmcnt(6)\n"
"ds_read_b128 a[192:195], v8 offset:0\n"
"v_cvt_pk_bf16_f32 v96, v96, v97\n"
"v_mfma_f32_16x16x32_bf16 v[196:199], a[228:231], v[128:131], v[196:199]\n"
"buffer_load_dwordx4 v25, s[20:23], s79 offen offset:1024 sc1 lds\n"
"s_waitcnt lgkmcnt(6)\n"
"v_mfma_f32_16x16x32_bf16 v[200:203], a[232:235], v[128:131], v[200:203]\n"
"s_waitcnt lgkmcnt(5)\n"
"ds_read_b128 a[196:199], v8 offset:1024\n"
"v_cvt_pk_bf16_f32 v97, v98, v99\n"
"v_mfma_f32_16x16x32_bf16 v[204:207], a[236:239], v[128:131], v[204:207]\n"
"v_exp_f32 v103, v103\n"
"v_mfma_f32_16x16x32_bf16 v[160:163], v[60:63], v[128:131], v[160:163]\n"
"s_waitcnt lgkmcnt(5)\n"
"v_exp_f32 v104, v104\n"
"v_mfma_f32_16x16x32_bf16 v[208:211], a[240:243], v[128:131], v[208:211]\n"
"s_waitcnt lgkmcnt(4)\n"
"ds_read_b128 a[200:203], v8 offset:2048\n"
"v_cvt_pk_bf16_f32 v98, v100, v101\n"
"v_mfma_f32_16x16x32_bf16 v[212:215], a[244:247], v[128:131], v[212:215]\n"
"buffer_load_dwordx4 v26, s[20:23], s79 offen offset:2048 sc1 lds\n"
"s_waitcnt lgkmcnt(4)\n"
"v_mfma_f32_16x16x32_bf16 v[216:219], a[248:251], v[128:131], v[216:219]\n"
"s_waitcnt lgkmcnt(3)\n"
"ds_read_b128 a[204:207], v8 offset:3072\n"
"v_cvt_pk_bf16_f32 v99, v102, v103\n"
"v_mfma_f32_16x16x32_bf16 v[220:223], a[252:255], v[128:131], v[220:223]\n"
"v_exp_f32 v105, v105\n"
"v_mfma_f32_16x16x32_bf16 v[252:255], a[252:255], v[136:139], v[252:255]\n"
"v_exp_f32 v106, v106\n"
"v_mfma_f32_16x16x32_bf16 v[248:251], a[248:251], v[136:139], v[248:251]\n"
"ds_read_b128 a[208:211], v8 offset:4096\n"
"v_cvt_pk_bf16_f32 v104, v104, v105\n"
"v_mfma_f32_16x16x32_bf16 v[244:247], a[244:247], v[136:139], v[244:247]\n"
"v_exp_f32 v107, v107\n"
"v_mfma_f32_16x16x32_bf16 v[240:243], a[240:243], v[136:139], v[240:243]\n"
"buffer_load_dwordx4 v27, s[20:23], s79 offen offset:3072 sc1 lds\n"
"v_mfma_f32_16x16x32_bf16 v[164:167], v[60:63], v[136:139], v[164:167]\n"
"s_add_u32 s79, s79, s92\n"
"ds_read_b128 a[212:215], v8 offset:5120\n"
"v_cvt_pk_bf16_f32 v105, v106, v107\n"
"v_mfma_f32_16x16x32_bf16 v[236:239], a[236:239], v[136:139], v[236:239]\n"
"s_xor_b32 s76, s76, 0x8000\n"
"v_exp_f32 v108, v108\n"
"v_mfma_f32_16x16x32_bf16 v[232:235], a[232:235], v[136:139], v[232:235]\n"
"s_sub_u32 s74, s74, 1\n"
"s_cselect_b64 s[78:79], s[72:73], s[78:79]\n"
"ds_read_b128 a[216:219], v8 offset:6144\n"
"v_mfma_f32_16x16x32_bf16 v[228:231], a[228:231], v[136:139], v[228:231]\n"
"s_cselect_b64 s[72:73], s[16:17], s[72:73]\n"
"v_exp_f32 v109, v109\n"
"v_mfma_f32_16x16x32_bf16 v[224:227], a[224:227], v[136:139], v[224:227]\n"
"s_cselect_b32 s74, s75, s74\n"
"v_exp_f32 v110, v110\n"
"v_mfma_f32_16x16x32_bf16 a[0:3], a[224:227], v[144:147], a[0:3]\n"
"s_cselect_b32 s75, 64, s75\n"
"ds_read_b128 a[220:223], v8 offset:7168\n"
"v_cvt_pk_bf16_f32 v106, v108, v109\n"
"v_mfma_f32_16x16x32_bf16 a[4:7], a[228:231], v[144:147], a[4:7]\n"
"s_cmp_gt_u32 s74, 32\n"
"v_exp_f32 v111, v111\n"
"v_mfma_f32_16x16x32_bf16 a[8:11], a[232:235], v[144:147], a[8:11]\n"
"s_cselect_b64 s[20:21], s[32:33], s[20:21]\n"
"v_exp_f32 v112, v112\n"
"v_mfma_f32_16x16x32_bf16 a[12:15], a[236:239], v[144:147], a[12:15]\n"
"s_cselect_b32 s22, s34, s22\n"
"v_exp_f32 v113, v113\n"
"v_mfma_f32_16x16x32_bf16 v[168:171], v[60:63], v[144:147], v[168:171]\n"
"s_cselect_b32 s92, 262144, s92\n"
"v_exp_f32 v114, v114\n"
"v_mfma_f32_16x16x32_bf16 a[16:19], a[240:243], v[144:147], a[16:19]\n"
"s_cselect_b32 s5, 524288, s5\n"
"v_exp_f32 v115, v115\n"
"v_mfma_f32_16x16x32_bf16 a[20:23], a[244:247], v[144:147], a[20:23]\n"
"s_cmp_eq_u32 s78, s95\n"
"v_exp_f32 v116, v116\n"
"v_mfma_f32_16x16x32_bf16 a[24:27], a[248:251], v[144:147], a[24:27]\n"
"v_exp_f32 v117, v117\n"
"v_mfma_f32_16x16x32_bf16 a[28:31], a[252:255], v[144:147], a[28:31]\n"
"v_cvt_pk_bf16_f32 v107, v110, v111\n"
"v_cvt_pk_bf16_f32 v112, v112, v113\n"
"v_mfma_f32_16x16x32_bf16 a[60:63], a[252:255], v[152:155], a[60:63]\n"
"v_cvt_pk_bf16_f32 v113, v114, v115\n"
"v_cvt_pk_bf16_f32 v114, v116, v117\n"
"v_mfma_f32_16x16x32_bf16 a[56:59], a[248:251], v[152:155], a[56:59]\n"
"v_mfma_f32_16x16x32_bf16 a[52:55], a[244:247], v[152:155], a[52:55]\n"
"s_waitcnt vmcnt(16)\n"
"v_cmp_ne_u64 vcc, s[64:65], v[20:21]\n"
"v_mfma_f32_16x16x32_bf16 a[48:51], a[240:243], v[152:155], a[48:51]\n"
"s_cselect_b64 vcc, vcc, 0\n"
"s_cbranch_vccnz RA_22_%=a\n"
"RA_22_%=:\n"
"s_sub_u32 s91, s91, 1\n"
"v_mfma_f32_16x16x32_bf16 v[172:175], v[60:63], v[152:155], v[172:175]\n"
"v_mfma_f32_16x16x32_bf16 a[44:47], a[236:239], v[152:155], a[44:47]\n"
"v_mfma_f32_16x16x32_bf16 a[40:43], a[232:235], v[152:155], a[40:43]\n"
"s_waitcnt lgkmcnt(0)\n"
"s_barrier\n"
"v_mfma_f32_16x16x32_bf16 a[36:39], a[228:231], v[152:155], a[36:39]\n"
"v_mfma_f32_16x16x32_bf16 a[32:35], a[224:227], v[152:155], a[32:35]\n"
"s_mov_b32 exec_lo, s58\n"
"s_mov_b32 exec_hi, 0\n"
"buffer_store_dwordx2 v[10:11], off, s[44:47], s85 sc1\n"
"s_mov_b64 exec, -1\n"
"s_mov_b32 s58, 0\n"
"s_cbranch_scc0 ATL_13_%=\n"
"DG_14_%=:\n"
"s_cmp_lg_u32 s67, 0\n"
"s_cbranch_scc1 B1_15_%=\n"
"v_mfma_f32_16x16x32_bf16 v[144:147], a[192:195], a[96:99], v[184:187]\n"
"s_add_u32 m0, s77, 0\n"
"ds_read_b128 a[224:227], v9 offset:16384\n"
"v_mfma_f32_16x16x32_bf16 v[144:147], a[196:199], a[100:103], v[144:147]\n"
"ds_read_b128 a[228:231], v9 offset:17408\n"
"v_mfma_f32_16x16x32_bf16 v[144:147], a[200:203], a[104:107], v[144:147]\n"
"buffer_load_dwordx4 v24, s[24:27], s80 offen offset:0 sc1 lds\n"
"v_mfma_f32_16x16x32_bf16 v[144:147], a[204:207], a[108:111], v[144:147]\n"
"ds_read_b128 a[232:235], v9 offset:18432\n"
"v_mfma_f32_16x16x32_bf16 v[148:151], a[220:223], a[108:111], v[184:187]\n"
"v_exp_f32 v118, v118\n"
"v_mfma_f32_16x16x32_bf16 v[148:151], a[216:219], a[104:107], v[148:151]\n"
"ds_read_b128 a[236:239], v9 offset:19456\n"
"v_mfma_f32_16x16x32_bf16 v[148:151], a[212:215], a[100:103], v[148:151]\n"
"v_exp_f32 v119, v119\n"
"v_mfma_f32_16x16x32_bf16 v[148:151], a[208:211], a[96:99], v[148:151]\n"
"ds_read_b128 a[240:243], v9 offset:20480\n"
"v_cvt_pk_bf16_f32 v115, v118, v119\n"
"v_mfma_f32_16x16x32_bf16 v[156:159], a[208:211], a[112:115], v[188:191]\n"
"v_exp_f32 v120, v120\n"
"v_mfma_f32_16x16x32_bf16 v[156:159], a[212:215], a[116:119], v[156:159]\n"
"ds_read_b128 a[244:247], v9 offset:21504\n"
"v_mfma_f32_16x16x32_bf16 v[156:159], a[216:219], a[120:123], v[156:159]\n"
"buffer_load_dwordx4 v25, s[24:27], s80 offen offset:1024 sc1 lds\n"
"v_mfma_f32_16x16x32_bf16 v[156:159], a[220:223], a[124:127], v[156:159]\n"
"ds_read_b128 a[248:251], v9 offset:22528\n"
"v_mfma_f32_16x16x32_bf16 v[152:155], a[204:207], a[124:127], v[188:191]\n"
"v_exp_f32 v121, v121\n"
"v_mfma_f32_16x16x32_bf16 v[152:155], a[200:203], a[120:123], v[152:155]\n"
"ds_read_b128 a[252:255], v9 offset:23552\n"
"v_cvt_pk_bf16_f32 v120, v120, v121\n"
"v_mfma_f32_16x16x32_bf16 v[152:155], a[196:199], a[116:119], v[152:155]\n"
"v_exp_f32 v122, v122\n"
"v_mfma_f32_16x16x32_bf16 v[152:155], a[192:195], a[112:115], v[152:155]\n"
"v_exp_f32 v123, v123\n"
"v_mfma_f32_16x16x32_bf16 v[136:139], a[192:195], a[80:83], v[180:183]\n"
"buffer_load_dwordx4 v26, s[24:27], s80 offen offset:2048 sc1 lds\n"
"v_mfma_f32_16x16x32_bf16 v[136:139], a[196:199], a[84:87], v[136:139]\n"
"v_exp_f32 v124, v124\n"
"v_mfma_f32_16x16x32_bf16 v[136:139], a[200:203], a[88:91], v[136:139]\n"
"v_exp_f32 v125, v125\n"
"v_mfma_f32_16x16x32_bf16 v[136:139], a[204:207], a[92:95], v[136:139]\n"
"v_exp_f32 v126, v126\n"
"v_mfma_f32_16x16x32_bf16 v[140:143], a[220:223], a[92:95], v[180:183]\n"
"v_exp_f32 v127, v127\n"
"v_mfma_f32_16x16x32_bf16 v[140:143], a[216:219], a[88:91], v[140:143]\n"
"v_cvt_pk_bf16_f32 v121, v122, v123\n"
"v_cvt_pk_bf16_f32 v122, v124, v125\n"
"v_mfma_f32_16x16x32_bf16 v[140:143], a[212:215], a[84:87], v[140:143]\n"
"v_cvt_pk_bf16_f32 v123, v126, v127\n"
"v_xor_b32 v9, 0x8000, v9\n"
"v_mfma_f32_16x16x32_bf16 v[140:143], a[208:211], a[80:83], v[140:143]\n"
"v_exp_f32 v144, v144\n"
"v_mfma_f32_16x16x32_bf16 v[132:135], a[208:211], a[64:67], v[48:51]\n"
"buffer_load_dwordx4 v27, s[24:27], s80 offen offset:3072 sc1 lds\n"
"v_mfma_f32_16x16x32_bf16 v[132:135], a[212:215], a[68:71], v[132:135]\n"
"s_add_u32 m0, s76, 0\n"
"v_exp_f32 v145, v145\n"
"v_mfma_f32_16x16x32_bf16 v[132:135], a[216:219], a[72:75], v[132:135]\n"
"v_exp_f32 v146, v146\n"
"v_mfma_f32_16x16x32_bf16 v[132:135], a[220:223], a[76:79], v[132:135]\n"
"v_exp_f32 v147, v147\n"
"v_mfma_f32_16x16x32_bf16 v[128:131], a[204:207], a[76:79], v[44:47]\n"
"v_exp_f32 v148, v148\n"
"v_mfma_f32_16x16x32_bf16 v[128:131], a[200:203], a[72:75], v[128:131]\n"
"v_exp_f32 v149, v149\n"
"v_mfma_f32_16x16x32_bf16 v[128:131], a[196:199], a[68:71], v[128:131]\n"
"v_exp_f32 v150, v150\n"
"v_mfma_f32_16x16x32_bf16 v[128:131], a[192:195], a[64:67], v[128:131]\n"
"buffer_load_dwordx4 v24, s[20:23], s78 offen offset:0 sc1 lds\n"
"s_waitcnt lgkmcnt(7)\n"
"v_mfma_f32_16x16x32_bf16 v[192:195], a[224:227], v[96:99], v[192:195]\n"
"s_waitcnt lgkmcnt(6)\n"
"ds_read_b128 a[192:195], v8 offset:16384\n"
"v_cvt_pk_bf16_f32 v144, v144, v145\n"
"v_mfma_f32_16x16x32_bf16 v[196:199], a[228:231], v[96:99], v[196:199]\n"
"s_waitcnt lgkmcnt(6)\n"
"v_exp_f32 v151, v151\n"
"v_mfma_f32_16x16x32_bf16 v[200:203], a[232:235], v[96:99], v[200:203]\n"
"s_waitcnt lgkmcnt(5)\n"
"ds_read_b128 a[196:199], v8 offset:17408\n"
"v_cvt_pk_bf16_f32 v145, v146, v147\n"
"v_mfma_f32_16x16x32_bf16 v[204:207], a[236:239], v[96:99], v[204:207]\n"
"s_waitcnt lgkmcnt(5)\n"
"v_exp_f32 v152, v152\n"
"v_mfma_f32_16x16x32_bf16 v[208:211], a[240:243], v[96:99], v[208:211]\n"
"s_waitcnt lgkmcnt(4)\n"
"v_exp_f32 v153, v153\n"
"v_mfma_f32_16x16x32_bf16 v[212:215], a[244:247], v[96:99], v[212:215]\n"
"s_waitcnt lgkmcnt(3)\n"
"ds_read_b128 a[200:203], v8 offset:18432\n"
"v_cvt_pk_bf16_f32 v146, v148, v149\n"
"v_mfma_f32_16x16x32_bf16 v[216:219], a[248:251], v[96:99], v[216:219]\n"
"buffer_load_dwordx4 v25, s[20:23], s78 offen offset:1024 sc1 lds\n"
"s_waitcnt lgkmcnt(3)\n"
"v_mfma_f32_16x16x32_bf16 v[220:223], a[252:255], v[96:99], v[220:223]\n"
"ds_read_b128 a[204:207], v8 offset:19456\n"
"v_cvt_pk_bf16_f32 v147, v150, v151\n"
"v_mfma_f32_16x16x32_bf16 v[160:163], v[60:63], v[96:99], v[160:163]\n"
"v_exp_f32 v154, v154\n"
"v_mfma_f32_16x16x32_bf16 v[224:227], a[224:227], v[104:107], v[224:227]\n"
"v_exp_f32 v155, v155\n"
"v_mfma_f32_16x16x32_bf16 v[228:231], a[228:231], v[104:107], v[228:231]\n"
"ds_read_b128 a[208:211], v8 offset:20480\n"
"v_cvt_pk_bf16_f32 v152, v152, v153\n"
"v_mfma_f32_16x16x32_bf16 v[232:235], a[232:235], v[104:107], v[232:235]\n"
"v_exp_f32 v156, v156\n"
"v_mfma_f32_16x16x32_bf16 v[236:239], a[236:239], v[104:107], v[236:239]\n"
"v_exp_f32 v157, v157\n"
"v_mfma_f32_16x16x32_bf16 v[240:243], a[240:243], v[104:107], v[240:243]\n"
"ds_read_b128 a[212:215], v8 offset:21504\n"
"v_cvt_pk_bf16_f32 v153, v154, v155\n"
"v_mfma_f32_16x16x32_bf16 v[244:247], a[244:247], v[104:107], v[244:247]\n"
"buffer_load_dwordx4 v26, s[20:23], s78 offen offset:2048 sc1 lds\n"
"v_mfma_f32_16x16x32_bf16 v[248:251], a[248:251], v[104:107], v[248:251]\n"
"ds_read_b128 a[216:219], v8 offset:22528\n"
"v_cvt_pk_bf16_f32 v154, v156, v157\n"
"v_mfma_f32_16x16x32_bf16 v[252:255], a[252:255], v[104:107], v[252:255]\n"
"v_exp_f32 v158, v158\n"
"v_mfma_f32_16x16x32_bf16 v[164:167], v[60:63], v[104:107], v[164:167]\n"
"v_exp_f32 v159, v159\n"
"v_mfma_f32_16x16x32_bf16 a[0:3], a[224:227], v[112:115], a[0:3]\n"
"ds_read_b128 a[220:223], v8 offset:23552\n"
"v_xor_b32 v8, 0x8000, v8\n"
"v_mfma_f32_16x16x32_bf16 a[4:7], a[228:231], v[112:115], a[4:7]\n"
"v_exp_f32 v136, v136\n"
"v_mfma_f32_16x16x32_bf16 a[8:11], a[232:235], v[112:115], a[8:11]\n"
"v_exp_f32 v137, v137\n"
"v_mfma_f32_16x16x32_bf16 a[12:15], a[236:239], v[112:115], a[12:15]\n"
"s_nop 0\n"
"PG8_%=:\n"
"s_cbranch_execz PADRET_%=\n"
"buffer_load_dwordx4 v27, s[20:23], s78 offen offset:3072 sc1 lds\n"
"v_mfma_f32_16x16x32_bf16 a[16:19], a[240:243], v[112:115], a[16:19]\n"
"v_exp_f32 v138, v138\n"
"v_mfma_f32_16x16x32_bf16 a[20:23], a[244:247], v[112:115], a[20:23]\n"
"v_exp_f32 v139, v139\n"
"v_mfma_f32_16x16x32_bf16 a[24:27], a[248:251], v[112:115], a[24:27]\n"
"v_exp_f32 v140, v140\n"
"v_mfma_f32_16x16x32_bf16 a[28:31], a[252:255], v[112:115], a[28:31]\n"
"v_exp_f32 v141, v141\n"
"v_mfma_f32_16x16x32_bf16 v[168:171], v[60:63], v[112:115], v[168:171]\n"
"v_exp_f32 v142, v142\n"
"v_mfma_f32_16x16x32_bf16 a[32:35], a[224:227], v[120:123], a[32:35]\n"
"v_exp_f32 v143, v143\n"
"v_mfma_f32_16x16x32_bf16 a[36:39], a[228:231], v[120:123], a[36:39]\n"
"v_exp_f32 v128, v128\n"
"v_mfma_f32_16x16x32_bf16 a[40:43], a[232:235], v[120:123], a[40:43]\n"
"v_exp_f32 v129, v129\n"
"v_mfma_f32_16x16x32_bf16 a[44:47], a[236:239], v[120:123], a[44:47]\n"
"v_exp_f32 v130, v130\n"
"v_mfma_f32_16x16x32_bf16 a[48:51], a[240:243], v[120:123], a[48:51]\n"
"v_exp_f32 v131, v131\n"
"v_mfma_f32_16x16x32_bf16 a[52:55], a[244:247], v[120:123], a[52:55]\n"
"v_cvt_pk_bf16_f32 v155, v158, v159\n"
"v_cvt_pk_bf16_f32 v136, v136, v137\n"
"v_mfma_f32_16x16x32_bf16 a[56:59], a[248:251], v[120:123], a[56:59]\n"
"v_cvt_pk_bf16_f32 v137, v138, v139\n"
"v_cvt_pk_bf16_f32 v138, v140, v141\n"
"v_mfma_f32_16x16x32_bf16 a[60:63], a[252:255], v[120:123], a[60:63]\n"
"s_waitcnt vmcnt(8)\n"
"v_cvt_pk_bf16_f32 v139, v142, v143\n"
"v_cvt_pk_bf16_f32 v128, v128, v129\n"
"v_mfma_f32_16x16x32_bf16 v[172:175], v[60:63], v[120:123], v[172:175]\n"
"s_waitcnt lgkmcnt(0)\n"
"s_barrier\n"
"v_cvt_pk_bf16_f32 v129, v130, v131\n"
"s_mov_b32 exec_lo, s58\n"
"s_mov_b32 exec_hi, 0\n"
"buffer_store_dwordx2 v[10:11], off, s[44:47], s85 sc1\n"
"s_mov_b64 exec, -1\n"
"s_mov_b32 s58, 0\n"
"F0_16_%=:\n"
"v_mfma_f32_16x16x32_bf16 v[120:123], a[192:195], a[112:115], v[52:55]\n"
"s_lshl_b32 s82, s61, 13\n"
"s_and_b32 s83, s77, 0x18000\n"
"s_xor_b32 s83, s83, 0x8000\n"
"v_mfma_f32_16x16x32_bf16 v[120:123], a[196:199], a[116:119], v[120:123]\n"
"s_add_u32 s82, s82, s83\n"
"ds_read_b128 v[64:67], v9 offset:0\n"
"v_add_u32 v23, s82, v1\n"
"v_mfma_f32_16x16x32_bf16 v[120:123], a[200:203], a[120:123], v[120:123]\n"
"s_lshl_b32 s82, s70, 3\n"
"s_lshl_b32 s83, s67, 2\n"
"s_add_u32 s82, s82, s83\n"
"v_mfma_f32_16x16x32_bf16 v[120:123], a[204:207], a[124:127], v[120:123]\n"
"s_add_u32 s82, s82, s66\n"
"s_lshl_b32 s82, s82, 17\n"
"s_lshl_b32 s83, s62, 12\n"
"v_mfma_f32_16x16x32_bf16 v[124:127], a[220:223], a[124:127], v[56:59]\n"
"s_add_u32 s82, s82, s83\n"
"s_add_u32 s82, s82, 37748736\n"
"s_add_u32 s83, s82, 262144\n"
"v_mfma_f32_16x16x32_bf16 v[124:127], a[216:219], a[120:123], v[124:127]\n"
"s_add_u32 m0, s77, 16384\n"
"ds_read_b128 v[68:71], v9 offset:1024\n"
"ds_read_b128 v[72:75], v9 offset:2048\n"
"v_mfma_f32_16x16x32_bf16 v[124:127], a[212:215], a[116:119], v[124:127]\n"
"ds_read_b128 v[76:79], v9 offset:3072\n"
"ds_read_b128 v[80:83], v9 offset:4096\n"
"v_mfma_f32_16x16x32_bf16 v[124:127], a[208:211], a[112:115], v[124:127]\n"
"buffer_load_dwordx4 v24, s[24:27], s81 offen offset:0 sc1 lds\n"
"v_mfma_f32_16x16x32_bf16 v[168:171], v[60:63], v[144:147], v[168:171]\n"
"buffer_load_dwordx4 v25, s[24:27], s81 offen offset:1024 sc1 lds\n"
"v_mfma_f32_16x16x32_bf16 v[172:175], v[60:63], v[152:155], v[172:175]\n"
"s_waitcnt lgkmcnt(4)\n"
"ds_read_b128 v[84:87], v9 offset:5120\n"
"ds_read_b128 v[88:91], v9 offset:6144\n"
"v_mfma_f32_16x16x32_bf16 a[0:3], v[64:67], v[144:147], a[0:3]\n"
"buffer_load_dwordx4 v26, s[24:27], s81 offen offset:2048 sc1 lds\n"
"s_waitcnt lgkmcnt(5)\n"
"v_mfma_f32_16x16x32_bf16 a[4:7], v[68:71], v[144:147], a[4:7]\n"
"s_waitcnt lgkmcnt(4)\n"
"ds_read_b128 v[92:95], v9 offset:7168\n"
"ds_read_b128 a[192:195], v9 offset:16384\n"
"v_mfma_f32_16x16x32_bf16 a[8:11], v[72:75], v[144:147], a[8:11]\n"
"buffer_load_dwordx4 v27, s[24:27], s81 offen offset:3072 sc1 lds\n"
"s_waitcnt lgkmcnt(5)\n"
"v_mfma_f32_16x16x32_bf16 a[12:15], v[76:79], v[144:147], a[12:15]\n"
"s_add_u32 m0, s76, 16384\n"
"v_min_u32 v34, 53, v0\n"
"v_lshlrev_b32 v34, 7, v34\n"
"s_waitcnt lgkmcnt(4)\n"
"v_mfma_f32_16x16x32_bf16 a[16:19], v[80:83], v[144:147], a[16:19]\n"
"buffer_load_dwordx4 v24, s[20:23], s79 offen offset:0 sc1 lds\n"
"s_waitcnt lgkmcnt(3)\n"
"v_mfma_f32_16x16x32_bf16 a[20:23], v[84:87], v[144:147], a[20:23]\n"
"buffer_load_dwordx4 v25, s[20:23], s79 offen offset:1024 sc1 lds\n"
"s_waitcnt lgkmcnt(2)\n"
"v_mfma_f32_16x16x32_bf16 a[24:27], v[88:91], v[144:147], a[24:27]\n"
"buffer_load_dwordx4 v26, s[20:23], s79 offen offset:2048 sc1 lds\n"
"s_waitcnt lgkmcnt(1)\n"
"v_mfma_f32_16x16x32_bf16 a[28:31], v[92:95], v[144:147], a[28:31]\n"
"buffer_load_dwordx4 v27, s[20:23], s79 offen offset:3072 sc1 lds\n"
"v_mfma_f32_16x16x32_bf16 a[32:35], v[64:67], v[152:155], a[32:35]\n"
"s_add_u32 s90, s89, 0\n"
"ds_read_b128 a[196:199], v9 offset:17408\n"
"ds_read_b128 a[200:203], v9 offset:18432\n"
"v_mfma_f32_16x16x32_bf16 a[36:39], v[68:71], v[152:155], a[36:39]\n"
"buffer_load_dword v42, v34, s[28:31], s90 offen\n"
"v_mfma_f32_16x16x32_bf16 a[40:43], v[72:75], v[152:155], a[40:43]\n"
"s_add_u32 s90, s89, 6912\n"
"ds_read_b128 a[204:207], v9 offset:19456\n"
"ds_read_b128 a[208:211], v9 offset:20480\n"
"v_mfma_f32_16x16x32_bf16 a[44:47], v[76:79], v[152:155], a[44:47]\n"
"buffer_load_dword v42, v34, s[28:31], s90 offen\n"
"v_mfma_f32_16x16x32_bf16 a[48:51], v[80:83], v[152:155], a[48:51]\n"
"s_add_u32 s90, s89, 13824\n"
"ds_read_b128 a[212:215], v9 offset:21504\n"
"ds_read_b128 a[216:219], v9 offset:22528\n"
"v_mfma_f32_16x16x32_bf16 a[52:55], v[84:87], v[152:155], a[52:55]\n"
"buffer_load_dword v42, v34, s[28:31], s90 offen\n"
"v_mfma_f32_16x16x32_bf16 a[56:59], v[88:91], v[152:155], a[56:59]\n"
"s_add_u32 s80, s78, s5\n"
"ds_read_b128 a[220:223], v9 offset:23552\n"
"ds_read_b128 v[96:99], v22 offset:8192\n"
"v_mfma_f32_16x16x32_bf16 a[60:63], v[92:95], v[152:155], a[60:63]\n"
"ds_write_b128 v22, a[0:3] offset:0\n"
"v_mfma_f32_16x16x32_bf16 v[164:167], v[60:63], v[136:139], v[164:167]\n"
"ds_write_b128 v22, a[4:7] offset:1024\n"
"v_exp_f32 v120, v120\n"
"v_exp_f32 v121, v121\n"
"s_nop 0\n"
"v_cvt_pk_bf16_f32 v120, v120, v121\n"
"v_exp_f32 v122, v122\n"
"v_exp_f32 v123, v123\n"
"s_nop 0\n"
"v_cvt_pk_bf16_f32 v121, v122, v123\n"
"v_exp_f32 v124, v124\n"
"v_exp_f32 v125, v125\n"
"s_nop 0\n"
"v_cvt_pk_bf16_f32 v122, v124, v125\n"
"v_exp_f32 v126, v126\n"
"v_exp_f32 v127, v127\n"
"s_nop 0\n"
"v_cvt_pk_bf16_f32 v123, v126, v127\n"
"s_nop 1\n"
"v_mfma_f32_16x16x32_bf16 v[172:175], v[60:63], v[120:123], v[172:175]\n"
"ds_write_b128 v22, a[8:11] offset:2048\n"
"s_waitcnt lgkmcnt(11)\n"
"v_mfma_f32_16x16x32_bf16 a[32:35], a[192:195], v[120:123], a[32:35]\n"
"s_add_u32 s81, s79, s5\n"
"v_exp_f32 v132, v132\n"
"s_waitcnt lgkmcnt(10)\n"
"v_mfma_f32_16x16x32_bf16 a[36:39], a[196:199], v[120:123], a[36:39]\n"
"s_mov_b64 s[24:25], s[20:21]\n"
"v_exp_f32 v133, v133\n"
"s_waitcnt lgkmcnt(9)\n"
"v_mfma_f32_16x16x32_bf16 a[40:43], a[200:203], v[120:123], a[40:43]\n"
"s_waitcnt lgkmcnt(0)\n"
"s_barrier\n"
"v_exp_f32 v134, v134\n"
"v_mfma_f32_16x16x32_bf16 a[44:47], a[204:207], v[120:123], a[44:47]\n"
"ds_write_b128 v22, a[12:15] offset:3072\n"
"v_mfma_f32_16x16x32_bf16 a[48:51], a[208:211], v[120:123], a[48:51]\n"
"ds_write_b128 v22, a[16:19] offset:4096\n"
"v_mfma_f32_16x16x32_bf16 a[52:55], a[212:215], v[120:123], a[52:55]\n"
"ds_write_b128 v22, a[20:23] offset:5120\n"
"v_mfma_f32_16x16x32_bf16 a[56:59], a[216:219], v[120:123], a[56:59]\n"
"ds_write_b128 v22, a[24:27] offset:6144\n"
"v_mfma_f32_16x16x32_bf16 a[60:63], a[220:223], v[120:123], a[60:63]\n"
"ds_write_b128 v22, a[28:31] offset:7168\n"
"v_cvt_pk_bf16_f32 v130, v132, v133\n"
"v_exp_f32 v135, v135\n"
"s_nop 0\n"
"v_cvt_pk_bf16_f32 v131, v134, v135\n"
"s_nop 1\n"
"v_mfma_f32_16x16x32_bf16 v[160:163], v[60:63], v[128:131], v[160:163]\n"
"ds_write_b128 v23, a[32:35] offset:0\n"
"v_mfma_f32_16x16x32_bf16 v[224:227], v[64:67], v[136:139], v[224:227]\n"
"s_mov_b32 s26, s22\n"
"ds_read_b128 a[224:227], v8 offset:0\n"
"ds_read_b128 a[228:231], v8 offset:1024\n"
"v_mfma_f32_16x16x32_bf16 v[228:231], v[68:71], v[136:139], v[228:231]\n"
"s_xor_b32 s77, s77, 0x8000\n"
"ds_read_b128 a[232:235], v8 offset:2048\n"
"ds_read_b128 a[236:239], v8 offset:3072\n"
"v_mfma_f32_16x16x32_bf16 v[232:235], v[72:75], v[136:139], v[232:235]\n"
"ds_write_b128 v23, a[36:39] offset:1024\n"
"v_mfma_f32_16x16x32_bf16 v[236:239], v[76:79], v[136:139], v[236:239]\n"
"s_add_u32 s78, s78, s92\n"
"ds_read_b128 a[240:243], v8 offset:4096\n"
"ds_read_b128 a[244:247], v8 offset:5120\n"
"v_mfma_f32_16x16x32_bf16 v[240:243], v[80:83], v[136:139], v[240:243]\n"
"s_add_u32 s79, s79, s92\n"
"ds_read_b128 a[248:251], v8 offset:6144\n"
"ds_read_b128 a[252:255], v8 offset:7168\n"
"v_mfma_f32_16x16x32_bf16 v[244:247], v[84:87], v[136:139], v[244:247]\n"
"ds_write_b128 v23, a[40:43] offset:2048\n"
"v_mfma_f32_16x16x32_bf16 v[248:251], v[88:91], v[136:139], v[248:251]\n"
"s_xor_b32 s76, s76, 0x8000\n"
"v_cndmask_b32 v4, v160, v164, s[56:57]\n"
"v_cndmask_b32 v5, v168, v172, s[56:57]\n"
"v_mfma_f32_16x16x32_bf16 v[252:255], v[92:95], v[136:139], v[252:255]\n"
"s_sub_u32 s74, s74, 1\n"
"v_xor_b32 v38, 0x2000, v23\n"
"v_cndmask_b32 v4, v4, v5, s[100:101]\n"
"v_mfma_f32_16x16x32_bf16 v[192:195], v[64:67], v[128:131], v[192:195]\n"
"s_cselect_b64 s[78:79], s[72:73], s[78:79]\n"
"ds_write_b32 v19, v4\n"
"v_xor_b32 v9, 0x8000, v9\n"
"v_mfma_f32_16x16x32_bf16 v[196:199], v[68:71], v[128:131], v[196:199]\n"
"ds_write_b128 v23, a[44:47] offset:3072\n"
"v_mfma_f32_16x16x32_bf16 v[200:203], v[72:75], v[128:131], v[200:203]\n"
"ds_write_b128 v23, a[48:51] offset:4096\n"
"v_mfma_f32_16x16x32_bf16 v[204:207], v[76:79], v[128:131], v[204:207]\n"
"ds_write_b128 v23, a[52:55] offset:5120\n"
"v_mfma_f32_16x16x32_bf16 v[208:211], v[80:83], v[128:131], v[208:211]\n"
"ds_write_b128 v23, a[56:59] offset:6144\n"
"v_mfma_f32_16x16x32_bf16 v[212:215], v[84:87], v[128:131], v[212:215]\n"
"s_cselect_b64 s[72:73], s[16:17], s[72:73]\n"
"s_cselect_b32 s74, s75, s74\n"
"s_cselect_b32 s75, 64, s75\n"
"v_mfma_f32_16x16x32_bf16 v[216:219], v[88:91], v[128:131], v[216:219]\n"
"ds_write_b128 v23, a[60:63] offset:7168\n"
"v_mfma_f32_16x16x32_bf16 v[220:223], v[92:95], v[128:131], v[220:223]\n"
"s_cmp_gt_u32 s74, 32\n"
"s_cselect_b64 s[20:21], s[32:33], s[20:21]\n"
"s_cselect_b32 s22, s34, s22\n"
"s_cselect_b32 s92, 262144, s92\n"
"s_cselect_b32 s5, 524288, s5\n"
"s_waitcnt lgkmcnt(0)\n"
"s_barrier\n"
"s_branch EPI_%=\n"
"STUB0_%=:\n"
"s_cbranch_execz PADRET_%=\n"
"RA_22_%=a:\n"
"v_cmp_ne_u64 vcc, s[64:65], v[20:21]\n"
"s_nop 1\n"
"s_cbranch_vccz FOK_20_%=\n"
"FSP_21_%=:\n"
"s_cmp_eq_u32 s69, 0\n"
"s_cbranch_scc1 FOK_20_%=\n"
"s_sub_u32 s69, s69, 1\n"
"s_sleep 2\n"
"s_mov_b64 exec, vcc\n"
"buffer_load_dwordx2 v[20:21], v18, s[44:47], 0 offen sc1\n"
"s_mov_b64 exec, -1\n"
"s_waitcnt vmcnt(0)\n"
"v_cmp_ne_u64 vcc, s[64:65], v[20:21]\n"
"s_nop 1\n"
"s_cbranch_vccnz FSP_21_%=\n"
"FOK_20_%=:\n"
"s_branch RA_22_%=\n"
"N1_18_%=:\n"
"s_cmp_lg_u32 s67, 0\n"
"s_cbranch_scc1 N1B_19_%=\n"
"v_mfma_f32_16x16x32_bf16 v[144:147], a[224:227], a[96:99], v[184:187]\n"
"ds_read_b128 a[192:195], v8 offset:16384\n"
"ds_read_b128 a[196:199], v8 offset:17408\n"
"v_mfma_f32_16x16x32_bf16 v[144:147], a[228:231], a[100:103], v[144:147]\n"
"ds_read_b128 a[200:203], v8 offset:18432\n"
"ds_read_b128 a[204:207], v8 offset:19456\n"
"v_mfma_f32_16x16x32_bf16 v[144:147], a[232:235], a[104:107], v[144:147]\n"
"ds_read_b128 a[208:211], v8 offset:20480\n"
"v_mfma_f32_16x16x32_bf16 v[144:147], a[236:239], a[108:111], v[144:147]\n"
"ds_read_b128 a[212:215], v8 offset:21504\n"
"ds_read_b128 a[216:219], v8 offset:22528\n"
"v_mfma_f32_16x16x32_bf16 v[148:151], a[252:255], a[108:111], v[184:187]\n"
"ds_read_b128 a[220:223], v8 offset:23552\n"
"v_xor_b32 v8, 0x8000, v8\n"
"v_mfma_f32_16x16x32_bf16 v[148:151], a[248:251], a[104:107], v[148:151]\n"
"v_mfma_f32_16x16x32_bf16 v[148:151], a[244:247], a[100:103], v[148:151]\n"
"v_mfma_f32_16x16x32_bf16 v[148:151], a[240:243], a[96:99], v[148:151]\n"
"v_exp_f32 v144, v144\n"
"v_mfma_f32_16x16x32_bf16 v[156:159], a[240:243], a[112:115], v[188:191]\n"
"s_waitcnt lgkmcnt(0)\n"
"s_barrier\n"
"v_exp_f32 v145, v145\n"
"v_mfma_f32_16x16x32_bf16 v[156:159], a[244:247], a[116:119], v[156:159]\n"
"v_exp_f32 v146, v146\n"
"v_mfma_f32_16x16x32_bf16 v[156:159], a[248:251], a[120:123], v[156:159]\n"
"v_exp_f32 v147, v147\n"
"v_mfma_f32_16x16x32_bf16 v[156:159], a[252:255], a[124:127], v[156:159]\n"
"v_exp_f32 v148, v148\n"
"v_mfma_f32_16x16x32_bf16 v[152:155], a[236:239], a[124:127], v[188:191]\n"
"v_exp_f32 v149, v149\n"
"v_mfma_f32_16x16x32_bf16 v[152:155], a[232:235], a[120:123], v[152:155]\n"
"v_exp_f32 v150, v150\n"
"v_mfma_f32_16x16x32_bf16 v[152:155], a[228:231], a[116:119], v[152:155]\n"
"v_exp_f32 v151, v151\n"
"v_mfma_f32_16x16x32_bf16 v[152:155], a[224:227], a[112:115], v[152:155]\n"
"v_exp_f32 v156, v156\n"
"v_mfma_f32_16x16x32_bf16 v[136:139], a[224:227], a[80:83], v[180:183]\n"
"v_exp_f32 v157, v157\n"
"v_mfma_f32_16x16x32_bf16 v[136:139], a[228:231], a[84:87], v[136:139]\n"
"v_exp_f32 v158, v158\n"
"v_mfma_f32_16x16x32_bf16 v[136:139], a[232:235], a[88:91], v[136:139]\n"
"v_exp_f32 v159, v159\n"
"v_mfma_f32_16x16x32_bf16 v[136:139], a[236:239], a[92:95], v[136:139]\n"
"v_exp_f32 v152, v152\n"
"v_mfma_f32_16x16x32_bf16 v[140:143], a[252:255], a[92:95], v[180:183]\n"
"v_exp_f32 v153, v153\n"
"v_mfma_f32_16x16x32_bf16 v[140:143], a[248:251], a[88:91], v[140:143]\n"
"v_exp_f32 v154, v154\n"
"v_mfma_f32_16x16x32_bf16 v[140:143], a[244:247], a[84:87], v[140:143]\n"
"v_exp_f32 v155, v155\n"
"v_mfma_f32_16x16x32_bf16 v[140:143], a[240:243], a[80:83], v[140:143]\n"
"v_exp_f32 v136, v136\n"
"v_mfma_f32_16x16x32_bf16 v[132:135], a[240:243], a[64:67], v[48:51]\n"
"v_exp_f32 v137, v137\n"
"v_mfma_f32_16x16x32_bf16 v[132:135], a[244:247], a[68:71], v[132:135]\n"
"v_exp_f32 v138, v138\n"
"v_mfma_f32_16x16x32_bf16 v[132:135], a[248:251], a[72:75], v[132:135]\n"
"v_exp_f32 v139, v139\n"
"v_mfma_f32_16x16x32_bf16 v[132:135], a[252:255], a[76:79], v[132:135]\n"
"v_exp_f32 v140, v140\n"
"v_mfma_f32_16x16x32_bf16 v[128:131], a[236:239], a[76:79], v[44:47]\n"
"v_exp_f32 v141, v141\n"
"v_mfma_f32_16x16x32_bf16 v[128:131], a[232:235], a[72:75], v[128:131]\n"
"v_exp_f32 v142, v142\n"
"v_mfma_f32_16x16x32_bf16 v[128:131], a[228:231], a[68:71], v[128:131]\n"
"v_exp_f32 v143, v143\n"
"v_mfma_f32_16x16x32_bf16 v[128:131], a[224:227], a[64:67], v[128:131]\n"
"v_cvt_pk_bf16_f32 v144, v144, v145\n"
"v_cvt_pk_bf16_f32 v145, v146, v147\n"
"v_cvt_pk_bf16_f32 v146, v148, v149\n"
"v_cvt_pk_bf16_f32 v147, v150, v151\n"
"v_cvt_pk_bf16_f32 v152, v152, v153\n"
"v_cvt_pk_bf16_f32 v153, v154, v155\n"
"v_cvt_pk_bf16_f32 v154, v156, v157\n"
"v_cvt_pk_bf16_f32 v155, v158, v159\n"
"v_cvt_pk_bf16_f32 v136, v136, v137\n"
"v_cvt_pk_bf16_f32 v137, v138, v139\n"
"v_cvt_pk_bf16_f32 v138, v140, v141\n"
"v_cvt_pk_bf16_f32 v139, v142, v143\n"
"v_exp_f32 v128, v128\n"
"v_exp_f32 v129, v129\n"
"s_nop 0\n"
"v_cvt_pk_bf16_f32 v128, v128, v129\n"
"v_exp_f32 v130, v130\n"
"v_exp_f32 v131, v131\n"
"s_nop 0\n"
"v_cvt_pk_bf16_f32 v129, v130, v131\n"
"s_waitcnt vmcnt(5) lgkmcnt(0)\n"
"s_barrier\n"
"s_add_u32 m0, s77, 0\n"
"s_nop 0\n"
"buffer_load_dwordx4 v24, s[24:27], s80 offen offset:0 sc1 lds\n"
"buffer_load_dwordx4 v25, s[24:27], s80 offen offset:1024 sc1 lds\n"
"buffer_load_dwordx4 v26, s[24:27], s80 offen offset:2048 sc1 lds\n"
"buffer_load_dwordx4 v27, s[24:27], s80 offen offset:3072 sc1 lds\n"
"buffer_store_dwordx4 v[64:67], v24, s[44:47], s84 offen offset:0 sc1\n"
"buffer_store_dwordx4 v[68:71], v25, s[44:47], s84 offen offset:1024 sc1\n"
"buffer_store_dwordx4 v[72:75], v26, s[44:47], s84 offen offset:2048 sc1\n"
"buffer_store_dwordx4 v[76:79], v27, s[44:47], s84 offen offset:3072 sc1\n"
"buffer_store_dwordx4 v[80:83], v24, s[44:47], s86 offen offset:0 sc1\n"
"buffer_store_dwordx4 v[84:87], v25, s[44:47], s86 offen offset:1024 sc1\n"
"buffer_store_dwordx4 v[88:91], v26, s[44:47], s86 offen offset:2048 sc1\n"
"buffer_store_dwordx4 v[92:95], v27, s[44:47], s86 offen offset:3072 sc1\n"
"v_mfma_f32_16x16x32_bf16 v[120:123], a[192:195], a[112:115], v[52:55]\n"
"s_lshl_b32 s82, s61, 13\n"
"s_and_b32 s83, s77, 0x18000\n"
"s_xor_b32 s83, s83, 0x8000\n"
"v_mfma_f32_16x16x32_bf16 v[120:123], a[196:199], a[116:119], v[120:123]\n"
"s_add_u32 s82, s82, s83\n"
"ds_read_b128 v[64:67], v9 offset:0\n"
"v_add_u32 v23, s82, v1\n"
"v_mfma_f32_16x16x32_bf16 v[120:123], a[200:203], a[120:123], v[120:123]\n"
"s_lshl_b32 s82, s70, 3\n"
"s_lshl_b32 s83, s67, 2\n"
"s_add_u32 s82, s82, s83\n"
"v_mfma_f32_16x16x32_bf16 v[120:123], a[204:207], a[124:127], v[120:123]\n"
"s_add_u32 s82, s82, s66\n"
"s_lshl_b32 s82, s82, 17\n"
"s_lshl_b32 s83, s62, 12\n"
"v_mfma_f32_16x16x32_bf16 v[124:127], a[220:223], a[124:127], v[56:59]\n"
"s_add_u32 s82, s82, s83\n"
"s_add_u32 s82, s82, 37748736\n"
"s_add_u32 s83, s82, 262144\n"
"v_mfma_f32_16x16x32_bf16 v[124:127], a[216:219], a[120:123], v[124:127]\n"
"s_add_u32 m0, s77, 16384\n"
"ds_read_b128 v[68:71], v9 offset:1024\n"
"ds_read_b128 v[72:75], v9 offset:2048\n"
"v_mfma_f32_16x16x32_bf16 v[124:127], a[212:215], a[116:119], v[124:127]\n"
"ds_read_b128 v[76:79], v9 offset:3072\n"
"ds_read_b128 v[80:83], v9 offset:4096\n"
"v_mfma_f32_16x16x32_bf16 v[124:127], a[208:211], a[112:115], v[124:127]\n"
"buffer_load_dwordx4 v24, s[24:27], s81 offen offset:0 sc1 lds\n"
"v_mfma_f32_16x16x32_bf16 v[168:171], v[60:63], v[144:147], 0\n"
"buffer_load_dwordx4 v25, s[24:27], s81 offen offset:1024 sc1 lds\n"
"v_mfma_f32_16x16x32_bf16 v[172:175], v[60:63], v[152:155], 0\n"
"s_waitcnt lgkmcnt(4)\n"
"ds_read_b128 v[84:87], v9 offset:5120\n"
"ds_read_b128 v[88:91], v9 offset:6144\n"
"v_mfma_f32_16x16x32_bf16 a[0:3], v[64:67], v[144:147], 0\n"
"buffer_load_dwordx4 v26, s[24:27], s81 offen offset:2048 sc1 lds\n"
"s_waitcnt lgkmcnt(5)\n"
"v_mfma_f32_16x16x32_bf16 a[4:7], v[68:71], v[144:147], 0\n"
"s_waitcnt lgkmcnt(4)\n"
"ds_read_b128 v[92:95], v9 offset:7168\n"
"ds_read_b128 a[192:195], v9 offset:16384\n"
"v_mfma_f32_16x16x32_bf16 a[8:11], v[72:75], v[144:147], 0\n"
"buffer_load_dwordx4 v27, s[24:27], s81 offen offset:3072 sc1 lds\n"
"s_waitcnt lgkmcnt(5)\n"
"v_mfma_f32_16x16x32_bf16 a[12:15], v[76:79], v[144:147], 0\n"
"s_add_u32 m0, s76, 16384\n"
"v_min_u32 v34, 53, v0\n"
"v_lshlrev_b32 v34, 7, v34\n"
"s_waitcnt lgkmcnt(4)\n"
"v_mfma_f32_16x16x32_bf16 a[16:19], v[80:83], v[144:147], 0\n"
"buffer_load_dwordx4 v24, s[20:23], s79 offen offset:0 sc1 lds\n"
"s_waitcnt lgkmcnt(3)\n"
"v_mfma_f32_16x16x32_bf16 a[20:23], v[84:87], v[144:147], 0\n"
"buffer_load_dwordx4 v25, s[20:23], s79 offen offset:1024 sc1 lds\n"
"s_waitcnt lgkmcnt(2)\n"
"v_mfma_f32_16x16x32_bf16 a[24:27], v[88:91], v[144:147], 0\n"
"buffer_load_dwordx4 v26, s[20:23], s79 offen offset:2048 sc1 lds\n"
"s_waitcnt lgkmcnt(1)\n"
"v_mfma_f32_16x16x32_bf16 a[28:31], v[92:95], v[144:147], 0\n"
"buffer_load_dwordx4 v27, s[20:23], s79 offen offset:3072 sc1 lds\n"
"v_mfma_f32_16x16x32_bf16 a[32:35], v[64:67], v[152:155], 0\n"
"s_add_u32 s90, s89, 0\n"
"ds_read_b128 a[196:199], v9 offset:17408\n"
"ds_read_b128 a[200:203], v9 offset:18432\n"
"v_mfma_f32_16x16x32_bf16 a[36:39], v[68:71], v[152:155], 0\n"
"buffer_load_dword v42, v34, s[28:31], s90 offen\n"
"v_mfma_f32_16x16x32_bf16 a[40:43], v[72:75], v[152:155], 0\n"
"s_add_u32 s90, s89, 6912\n"
"ds_read_b128 a[204:207], v9 offset:19456\n"
"ds_read_b128 a[208:211], v9 offset:20480\n"
"v_mfma_f32_16x16x32_bf16 a[44:47], v[76:79], v[152:155], 0\n"
"buffer_load_dword v42, v34, s[28:31], s90 offen\n"
"v_mfma_f32_16x16x32_bf16 a[48:51], v[80:83], v[152:155], 0\n"
"s_add_u32 s90, s89, 13824\n"
"ds_read_b128 a[212:215], v9 offset:21504\n"
"ds_read_b128 a[216:219], v9 offset:22528\n"
"v_mfma_f32_16x16x32_bf16 a[52:55], v[84:87], v[152:155], 0\n"
"buffer_load_dword v42, v34, s[28:31], s90 offen\n"
"v_mfma_f32_16x16x32_bf16 a[56:59], v[88:91], v[152:155], 0\n"
"s_add_u32 s80, s78, s5\n"
"ds_read_b128 a[220:223], v9 offset:23552\n"
"ds_read_b128 v[96:99], v22 offset:8192\n"
"v_mfma_f32_16x16x32_bf16 a[60:63], v[92:95], v[152:155], 0\n"
"ds_write_b128 v22, a[0:3] offset:0\n"
"v_mfma_f32_16x16x32_bf16 v[164:167], v[60:63], v[136:139], 0\n"
"ds_write_b128 v22, a[4:7] offset:1024\n"
"v_exp_f32 v120, v120\n"
"v_exp_f32 v121, v121\n"
"s_nop 0\n"
"v_cvt_pk_bf16_f32 v120, v120, v121\n"
"v_exp_f32 v122, v122\n"
"v_exp_f32 v123, v123\n"
"s_nop 0\n"
"v_cvt_pk_bf16_f32 v121, v122, v123\n"
"v_exp_f32 v124, v124\n"
"v_exp_f32 v125, v125\n"
"s_nop 0\n"
"v_cvt_pk_bf16_f32 v122, v124, v125\n"
"v_exp_f32 v126, v126\n"
"v_exp_f32 v127, v127\n"
"s_nop 0\n"
"v_cvt_pk_bf16_f32 v123, v126, v127\n"
"s_nop 1\n"
"v_mfma_f32_16x16x32_bf16 v[172:175], v[60:63], v[120:123], v[172:175]\n"
"ds_write_b128 v22, a[8:11] offset:2048\n"
"s_waitcnt lgkmcnt(11)\n"
"v_mfma_f32_16x16x32_bf16 a[32:35], a[192:195], v[120:123], a[32:35]\n"
"s_add_u32 s81, s79, s5\n"
"v_exp_f32 v132, v132\n"
"s_waitcnt lgkmcnt(10)\n"
"v_mfma_f32_16x16x32_bf16 a[36:39], a[196:199], v[120:123], a[36:39]\n"
"s_mov_b64 s[24:25], s[20:21]\n"
"v_exp_f32 v133, v133\n"
"s_waitcnt lgkmcnt(9)\n"
"v_mfma_f32_16x16x32_bf16 a[40:43], a[200:203], v[120:123], a[40:43]\n"
"s_waitcnt lgkmcnt(0)\n"
"s_barrier\n"
"v_exp_f32 v134, v134\n"
"v_mfma_f32_16x16x32_bf16 a[44:47], a[204:207], v[120:123], a[44:47]\n"
"ds_write_b128 v22, a[12:15] offset:3072\n"
"v_mfma_f32_16x16x32_bf16 a[48:51], a[208:211], v[120:123], a[48:51]\n"
"ds_write_b128 v22, a[16:19] offset:4096\n"
"v_mfma_f32_16x16x32_bf16 a[52:55], a[212:215], v[120:123], a[52:55]\n"
"ds_write_b128 v22, a[20:23] offset:5120\n"
"v_mfma_f32_16x16x32_bf16 a[56:59], a[216:219], v[120:123], a[56:59]\n"
"ds_write_b128 v22, a[24:27] offset:6144\n"
"v_mfma_f32_16x16x32_bf16 a[60:63], a[220:223], v[120:123], a[60:63]\n"
"ds_write_b128 v22, a[28:31] offset:7168\n"
"v_cvt_pk_bf16_f32 v130, v132, v133\n"
"v_exp_f32 v135, v135\n"
"s_nop 0\n"
"v_cvt_pk_bf16_f32 v131, v134, v135\n"
"s_nop 1\n"
"v_mfma_f32_16x16x32_bf16 v[160:163], v[60:63], v[128:131], 0\n"
"ds_write_b128 v23, a[32:35] offset:0\n"
"v_mfma_f32_16x16x32_bf16 v[224:227], v[64:67], v[136:139], 0\n"
"s_mov_b32 s26, s22\n"
"ds_read_b128 a[224:227], v8 offset:0\n"
"ds_read_b128 a[228:231], v8 offset:1024\n"
"v_mfma_f32_16x16x32_bf16 v[228:231], v[68:71], v[136:139], 0\n"
"s_xor_b32 s77, s77, 0x8000\n"
"ds_read_b128 a[232:235], v8 offset:2048\n"
"ds_read_b128 a[236:239], v8 offset:3072\n"
"v_mfma_f32_16x16x32_bf16 v[232:235], v[72:75], v[136:139], 0\n"
"ds_write_b128 v23, a[36:39] offset:1024\n"
"v_mfma_f32_16x16x32_bf16 v[236:239], v[76:79], v[136:139], 0\n"
"s_add_u32 s78, s78, s92\n"
"ds_read_b128 a[240:243], v8 offset:4096\n"
"ds_read_b128 a[244:247], v8 offset:5120\n"
"v_mfma_f32_16x16x32_bf16 v[240:243], v[80:83], v[136:139], 0\n"
"s_add_u32 s79, s79, s92\n"
"ds_read_b128 a[248:251], v8 offset:6144\n"
"ds_read_b128 a[252:255], v8 offset:7168\n"
"v_mfma_f32_16x16x32_bf16 v[244:247], v[84:87], v[136:139], 0\n"
"ds_write_b128 v23, a[40:43] offset:2048\n"
"v_mfma_f32_16x16x32_bf16 v[248:251], v[88:91], v[136:139], 0\n"
"s_xor_b32 s76, s76, 0x8000\n"
"v_cndmask_b32 v4, v160, v164, s[56:57]\n"
"v_cndmask_b32 v5, v168, v172, s[56:57]\n"
"v_mfma_f32_16x16x32_bf16 v[252:255], v[92:95], v[136:139], 0\n"
"s_sub_u32 s74, s74, 1\n"
"v_xor_b32 v38, 0x2000, v23\n"
"v_cndmask_b32 v4, v4, v5, s[100:101]\n"
"v_mfma_f32_16x16x32_bf16 v[192:195], v[64:67], v[128:131], 0\n"
"s_cselect_b64 s[78:79], s[72:73], s[78:79]\n"
"ds_write_b32 v19, v4\n"
"v_xor_b32 v9, 0x8000, v9\n"
"v_mfma_f32_16x16x32_bf16 v[196:199], v[68:71], v[128:131], 0\n"
"ds_write_b128 v23, a[44:47] offset:3072\n"
"v_mfma_f32_16x16x32_bf16 v[200:203], v[72:75], v[128:131], 0\n"
"ds_write_b128 v23, a[48:51] offset:4096\n"
"v_mfma_f32_16x16x32_bf16 v[204:207], v[76:79], v[128:131], 0\n"
"ds_write_b128 v23, a[52:55] offset:5120\n"
"v_mfma_f32_16x16x32_bf16 v[208:211], v[80:83], v[128:131], 0\n"
"ds_write_b128 v23, a[56:59] offset:6144\n"
"v_mfma_f32_16x16x32_bf16 v[212:215], v[84:87], v[128:131], 0\n"
"s_cselect_b64 s[72:73], s[16:17], s[72:73]\n"
"s_cselect_b32 s74, s75, s74\n"
"s_cselect_b32 s75, 64, s75\n"
"v_mfma_f32_16x16x32_bf16 v[216:219], v[88:91], v[128:131], 0\n"
"ds_write_b128 v23, a[60:63] offset:7168\n"
"v_mfma_f32_16x16x32_bf16 v[220:223], v[92:95], v[128:131], 0\n"
"s_cmp_gt_u32 s74, 32\n"
"s_cselect_b64 s[20:21], s[32:33], s[20:21]\n"
"s_cselect_b32 s22, s34, s22\n"
"s_cselect_b32 s92, 262144, s92\n"
"s_cselect_b32 s5, 524288, s5\n"
"s_waitcnt lgkmcnt(0)\n"
"s_barrier\n"
"s_branch EPI_%=\n"
"N1B_19_%=:\n"
"v_mfma_f32_16x16x32_bf16 v[152:155], a[224:227], a[112:115], v[44:47]\n"
"ds_read_b128 a[192:195], v8 offset:16384\n"
"ds_read_b128 a[196:199], v8 offset:17408\n"
"v_mfma_f32_16x16x32_bf16 v[152:155], a[228:231], a[116:119], v[152:155]\n"
"ds_read_b128 a[200:203], v8 offset:18432\n"
"ds_read_b128 a[204:207], v8 offset:19456\n"
"v_mfma_f32_16x16x32_bf16 v[152:155], a[232:235], a[120:123], v[152:155]\n"
"ds_read_b128 a[208:211], v8 offset:20480\n"
"v_mov_b32 v168, 0\n"
"v_mfma_f32_16x16x32_bf16 v[152:155], a[236:239], a[124:127], v[152:155]\n"
"ds_read_b128 a[212:215], v8 offset:21504\n"
"ds_read_b128 a[216:219], v8 offset:22528\n"
"v_mfma_f32_16x16x32_bf16 v[156:159], a[252:255], a[124:127], v[48:51]\n"
"ds_read_b128 a[220:223], v8 offset:23552\n"
"v_mov_b32 v169, 0\n"
"v_mfma_f32_16x16x32_bf16 v[156:159], a[248:251], a[120:123], v[156:159]\n"
"v_mov_b32 v170, 0\n"
"v_mov_b32 v171, 0\n"
"v_mfma_f32_16x16x32_bf16 v[156:159], a[244:247], a[116:119], v[156:159]\n"
"v_xor_b32 v8, 0x8000, v8\n"
"v_mfma_f32_16x16x32_bf16 v[156:159], a[240:243], a[112:115], v[156:159]\n"
"v_exp_f32 v152, v152\n"
"v_mfma_f32_16x16x32_bf16 v[132:135], a[240:243], a[64:67], v[176:179]\n"
"s_waitcnt lgkmcnt(0)\n"
"s_barrier\n"
"v_exp_f32 v153, v153\n"
"v_mfma_f32_16x16x32_bf16 v[132:135], a[244:247], a[68:71], v[132:135]\n"
"ds_write_b128 v22, v[168:171] offset:0\n"
"v_mfma_f32_16x16x32_bf16 v[132:135], a[248:251], a[72:75], v[132:135]\n"
"v_exp_f32 v154, v154\n"
"v_mfma_f32_16x16x32_bf16 v[132:135], a[252:255], a[76:79], v[132:135]\n"
"ds_write_b128 v22, v[168:171] offset:1024\n"
"v_mfma_f32_16x16x32_bf16 v[128:131], a[236:239], a[76:79], v[176:179]\n"
"v_exp_f32 v155, v155\n"
"v_mfma_f32_16x16x32_bf16 v[128:131], a[232:235], a[72:75], v[128:131]\n"
"ds_write_b128 v22, v[168:171] offset:2048\n"
"v_mfma_f32_16x16x32_bf16 v[128:131], a[228:231], a[68:71], v[128:131]\n"
"v_exp_f32 v156, v156\n"
"v_mfma_f32_16x16x32_bf16 v[128:131], a[224:227], a[64:67], v[128:131]\n"
"ds_write_b128 v22, v[168:171] offset:3072\n"
"v_mfma_f32_16x16x32_bf16 v[136:139], a[224:227], a[80:83], v[180:183]\n"
"v_exp_f32 v157, v157\n"
"v_mfma_f32_16x16x32_bf16 v[136:139], a[228:231], a[84:87], v[136:139]\n"
"ds_write_b128 v22, v[168:171] offset:4096\n"
"v_mfma_f32_16x16x32_bf16 v[136:139], a[232:235], a[88:91], v[136:139]\n"
"v_exp_f32 v158, v158\n"
"v_mfma_f32_16x16x32_bf16 v[136:139], a[236:239], a[92:95], v[136:139]\n"
"ds_write_b128 v22, v[168:171] offset:5120\n"
"v_mfma_f32_16x16x32_bf16 v[140:143], a[252:255], a[92:95], v[180:183]\n"
"v_exp_f32 v159, v159\n"
"v_mfma_f32_16x16x32_bf16 v[140:143], a[248:251], a[88:91], v[140:143]\n"
"ds_write_b128 v22, v[168:171] offset:6144\n"
"v_mfma_f32_16x16x32_bf16 v[140:143], a[244:247], a[84:87], v[140:143]\n"
"v_exp_f32 v128, v128\n"
"v_mfma_f32_16x16x32_bf16 v[140:143], a[240:243], a[80:83], v[140:143]\n"
"ds_write_b128 v22, v[168:171] offset:7168\n"
"v_cvt_pk_bf16_f32 v152, v152, v153\n"
"v_cvt_pk_bf16_f32 v153, v154, v155\n"
"v_cvt_pk_bf16_f32 v154, v156, v157\n"
"v_cvt_pk_bf16_f32 v155, v158, v159\n"
"v_exp_f32 v129, v129\n"
"s_nop 0\n"
"v_cvt_pk_bf16_f32 v128, v128, v129\n"
"v_exp_f32 v130, v130\n"
"v_exp_f32 v131, v131\n"
"s_nop 0\n"
"v_cvt_pk_bf16_f32 v129, v130, v131\n"
"v_exp_f32 v132, v132\n"
"v_exp_f32 v133, v133\n"
"s_nop 0\n"
"v_cvt_pk_bf16_f32 v130, v132, v133\n"
"v_exp_f32 v134, v134\n"
"v_exp_f32 v135, v135\n"
"s_nop 0\n"
"v_cvt_pk_bf16_f32 v131, v134, v135\n"
"v_exp_f32 v136, v136\n"
"v_exp_f32 v137, v137\n"
"s_nop 0\n"
"v_cvt_pk_bf16_f32 v136, v136, v137\n"
"s_waitcnt vmcnt(5) lgkmcnt(0)\n"
"s_barrier\n"
"PG9_%=:\n"
"s_cbranch_execz PADRET_%=\n"
"s_add_u32 m0, s77, 0\n"
"s_nop 0\n"
"buffer_load_dwordx4 v24, s[24:27], s80 offen offset:0 sc1 lds\n"
"buffer_load_dwordx4 v25, s[24:27], s80 offen offset:1024 sc1 lds\n"
"buffer_load_dwordx4 v26, s[24:27], s80 offen offset:2048 sc1 lds\n"
"buffer_load_dwordx4 v27, s[24:27], s80 offen offset:3072 sc1 lds\n"
"buffer_store_dwordx4 v[64:67], v24, s[44:47], s84 offen offset:0 sc1\n"
"buffer_store_dwordx4 v[68:71], v25, s[44:47], s84 offen offset:1024 sc1\n"
"buffer_store_dwordx4 v[72:75], v26, s[44:47], s84 offen offset:2048 sc1\n"
"buffer_store_dwordx4 v[76:79], v27, s[44:47], s84 offen offset:3072 sc1\n"
"buffer_store_dwordx4 v[80:83], v24, s[44:47], s86 offen offset:0 sc1\n"
"buffer_store_dwordx4 v[84:87], v25, s[44:47], s86 offen offset:1024 sc1\n"
"buffer_store_dwordx4 v[88:91], v26, s[44:47], s86 offen offset:2048 sc1\n"
"buffer_store_dwordx4 v[92:95], v27, s[44:47], s86 offen offset:3072 sc1\n"
"v_mfma_f32_16x16x32_bf16 v[96:99], a[192:195], a[64:67], v[52:55]\n"
"s_lshl_b32 s82, s61, 13\n"
"s_and_b32 s83, s77, 0x18000\n"
"s_xor_b32 s83, s83, 0x8000\n"
"v_mfma_f32_16x16x32_bf16 v[96:99], a[196:199], a[68:71], v[96:99]\n"
"s_add_u32 s82, s82, s83\n"
"ds_read_b128 v[64:67], v9 offset:0\n"
"v_add_u32 v23, s82, v1\n"
"v_mfma_f32_16x16x32_bf16 v[96:99], a[200:203], a[72:75], v[96:99]\n"
"s_lshl_b32 s82, s70, 3\n"
"s_lshl_b32 s83, s67, 2\n"
"s_add_u32 s82, s82, s83\n"
"v_mfma_f32_16x16x32_bf16 v[96:99], a[204:207], a[76:79], v[96:99]\n"
"s_add_u32 s82, s82, s66\n"
"s_lshl_b32 s82, s82, 17\n"
"s_lshl_b32 s83, s62, 12\n"
"v_mfma_f32_16x16x32_bf16 v[100:103], a[220:223], a[76:79], v[56:59]\n"
"s_add_u32 s82, s82, s83\n"
"s_add_u32 s82, s82, 37748736\n"
"s_add_u32 s83, s82, 262144\n"
"v_mfma_f32_16x16x32_bf16 v[100:103], a[216:219], a[72:75], v[100:103]\n"
"s_add_u32 m0, s77, 16384\n"
"ds_read_b128 v[68:71], v9 offset:1024\n"
"ds_read_b128 v[72:75], v9 offset:2048\n"
"v_mfma_f32_16x16x32_bf16 v[100:103], a[212:215], a[68:71], v[100:103]\n"
"ds_read_b128 v[76:79], v9 offset:3072\n"
"ds_read_b128 v[80:83], v9 offset:4096\n"
"v_mfma_f32_16x16x32_bf16 v[100:103], a[208:211], a[64:67], v[100:103]\n"
"buffer_load_dwordx4 v24, s[24:27], s81 offen offset:0 sc1 lds\n"
"v_mfma_f32_16x16x32_bf16 v[108:111], a[208:211], a[80:83], v[180:183]\n"
"buffer_load_dwordx4 v25, s[24:27], s81 offen offset:1024 sc1 lds\n"
"v_mfma_f32_16x16x32_bf16 v[108:111], a[212:215], a[84:87], v[108:111]\n"
"ds_read_b128 v[84:87], v9 offset:5120\n"
"ds_read_b128 v[88:91], v9 offset:6144\n"
"v_mfma_f32_16x16x32_bf16 v[108:111], a[216:219], a[88:91], v[108:111]\n"
"buffer_load_dwordx4 v26, s[24:27], s81 offen offset:2048 sc1 lds\n"
"v_mfma_f32_16x16x32_bf16 v[108:111], a[220:223], a[92:95], v[108:111]\n"
"ds_read_b128 v[92:95], v9 offset:7168\n"
"ds_read_b128 a[224:227], v8 offset:0\n"
"v_mfma_f32_16x16x32_bf16 v[104:107], a[204:207], a[92:95], v[180:183]\n"
"buffer_load_dwordx4 v27, s[24:27], s81 offen offset:3072 sc1 lds\n"
"v_mfma_f32_16x16x32_bf16 v[104:107], a[200:203], a[88:91], v[104:107]\n"
"s_add_u32 m0, s76, 16384\n"
"ds_read_b128 a[228:231], v8 offset:1024\n"
"ds_read_b128 a[232:235], v8 offset:2048\n"
"v_mfma_f32_16x16x32_bf16 v[104:107], a[196:199], a[84:87], v[104:107]\n"
"buffer_load_dwordx4 v24, s[20:23], s79 offen offset:0 sc1 lds\n"
"v_mfma_f32_16x16x32_bf16 v[104:107], a[192:195], a[80:83], v[104:107]\n"
"s_add_u32 s90, s89, 0\n"
"ds_read_b128 a[236:239], v8 offset:3072\n"
"ds_read_b128 a[240:243], v8 offset:4096\n"
"s_waitcnt lgkmcnt(12)\n"
"v_mfma_f32_16x16x32_bf16 a[32:35], v[64:67], v[152:155], 0\n"
"buffer_load_dwordx4 v25, s[20:23], s79 offen offset:1024 sc1 lds\n"
"s_waitcnt lgkmcnt(11)\n"
"v_mfma_f32_16x16x32_bf16 a[36:39], v[68:71], v[152:155], 0\n"
"buffer_load_dwordx4 v26, s[20:23], s79 offen offset:2048 sc1 lds\n"
"s_waitcnt lgkmcnt(10)\n"
"v_mfma_f32_16x16x32_bf16 a[40:43], v[72:75], v[152:155], 0\n"
"s_mov_b64 s[24:25], s[20:21]\n"
"ds_read_b128 a[244:247], v8 offset:5120\n"
"ds_read_b128 a[248:251], v8 offset:6144\n"
"s_waitcnt lgkmcnt(11)\n"
"v_mfma_f32_16x16x32_bf16 a[44:47], v[76:79], v[152:155], 0\n"
"buffer_load_dwordx4 v27, s[20:23], s79 offen offset:3072 sc1 lds\n"
"s_waitcnt lgkmcnt(10)\n"
"v_mfma_f32_16x16x32_bf16 a[48:51], v[80:83], v[152:155], 0\n"
"s_mov_b32 s26, s22\n"
"v_min_u32 v34, 53, v0\n"
"v_lshlrev_b32 v34, 7, v34\n"
"s_waitcnt lgkmcnt(9)\n"
"v_mfma_f32_16x16x32_bf16 a[52:55], v[84:87], v[152:155], 0\n"
"s_waitcnt lgkmcnt(8)\n"
"ds_read_b128 a[252:255], v8 offset:7168\n"
"ds_read_b128 a[192:195], v9 offset:16384\n"
"v_mfma_f32_16x16x32_bf16 a[56:59], v[88:91], v[152:155], 0\n"
"buffer_load_dword v42, v34, s[28:31], s90 offen\n"
"s_waitcnt lgkmcnt(9)\n"
"v_mfma_f32_16x16x32_bf16 a[60:63], v[92:95], v[152:155], 0\n"
"s_add_u32 s90, s89, 6912\n"
"ds_read_b128 a[196:199], v9 offset:17408\n"
"ds_read_b128 a[200:203], v9 offset:18432\n"
"v_mfma_f32_16x16x32_bf16 v[172:175], v[60:63], v[152:155], 0\n"
"buffer_load_dword v42, v34, s[28:31], s90 offen\n"
"v_mfma_f32_16x16x32_bf16 v[160:163], v[60:63], v[128:131], 0\n"
"s_add_u32 s90, s89, 13824\n"
"ds_read_b128 a[204:207], v9 offset:19456\n"
"ds_read_b128 a[208:211], v9 offset:20480\n"
"v_mfma_f32_16x16x32_bf16 v[192:195], v[64:67], v[128:131], 0\n"
"buffer_load_dword v42, v34, s[28:31], s90 offen\n"
"v_mfma_f32_16x16x32_bf16 v[196:199], v[68:71], v[128:131], 0\n"
"s_add_u32 s80, s78, s5\n"
"ds_read_b128 a[212:215], v9 offset:21504\n"
"ds_read_b128 a[216:219], v9 offset:22528\n"
"v_mfma_f32_16x16x32_bf16 v[200:203], v[72:75], v[128:131], 0\n"
"s_add_u32 s81, s79, s5\n"
"ds_read_b128 a[220:223], v9 offset:23552\n"
"v_cndmask_b32 v5, v168, v172, s[56:57]\n"
"v_mfma_f32_16x16x32_bf16 v[204:207], v[76:79], v[128:131], 0\n"
"s_xor_b32 s77, s77, 0x8000\n"
"v_exp_f32 v138, v138\n"
"v_mfma_f32_16x16x32_bf16 v[208:211], v[80:83], v[128:131], 0\n"
"s_add_u32 s78, s78, s92\n"
"v_exp_f32 v139, v139\n"
"v_mfma_f32_16x16x32_bf16 v[212:215], v[84:87], v[128:131], 0\n"
"s_waitcnt lgkmcnt(0)\n"
"s_barrier\n"
"v_exp_f32 v140, v140\n"
"v_mfma_f32_16x16x32_bf16 v[216:219], v[88:91], v[128:131], 0\n"
"ds_write_b128 v23, a[32:35] offset:0\n"
"v_mfma_f32_16x16x32_bf16 v[220:223], v[92:95], v[128:131], 0\n"
"s_add_u32 s79, s79, s92\n"
"v_exp_f32 v141, v141\n"
"v_cvt_pk_bf16_f32 v137, v138, v139\n"
"v_cvt_pk_bf16_f32 v138, v140, v141\n"
"v_exp_f32 v142, v142\n"
"v_exp_f32 v143, v143\n"
"s_nop 0\n"
"v_cvt_pk_bf16_f32 v139, v142, v143\n"
"s_nop 1\n"
"v_mfma_f32_16x16x32_bf16 v[164:167], v[60:63], v[136:139], 0\n"
"ds_write_b128 v23, a[36:39] offset:1024\n"
"v_mfma_f32_16x16x32_bf16 v[224:227], v[64:67], v[136:139], 0\n"
"ds_write_b128 v23, a[40:43] offset:2048\n"
"v_mfma_f32_16x16x32_bf16 v[228:231], v[68:71], v[136:139], 0\n"
"s_xor_b32 s76, s76, 0x8000\n"
"v_exp_f32 v96, v96\n"
"v_mfma_f32_16x16x32_bf16 v[232:235], v[72:75], v[136:139], 0\n"
"ds_write_b128 v23, a[44:47] offset:3072\n"
"v_mfma_f32_16x16x32_bf16 v[236:239], v[76:79], v[136:139], 0\n"
"s_sub_u32 s74, s74, 1\n"
"v_exp_f32 v97, v97\n"
"v_mfma_f32_16x16x32_bf16 v[240:243], v[80:83], v[136:139], 0\n"
"s_cselect_b64 s[78:79], s[72:73], s[78:79]\n"
"v_exp_f32 v98, v98\n"
"v_mfma_f32_16x16x32_bf16 v[244:247], v[84:87], v[136:139], 0\n"
"s_cselect_b64 s[72:73], s[16:17], s[72:73]\n"
"v_exp_f32 v99, v99\n"
"v_mfma_f32_16x16x32_bf16 v[248:251], v[88:91], v[136:139], 0\n"
"s_cselect_b32 s74, s75, s74\n"
"v_exp_f32 v100, v100\n"
"v_mfma_f32_16x16x32_bf16 v[252:255], v[92:95], v[136:139], 0\n"
"s_cselect_b32 s75, 64, s75\n"
"v_exp_f32 v101, v101\n"
"v_cvt_pk_bf16_f32 v96, v96, v97\n"
"v_cvt_pk_bf16_f32 v97, v98, v99\n"
"v_cvt_pk_bf16_f32 v98, v100, v101\n"
"v_exp_f32 v102, v102\n"
"v_exp_f32 v103, v103\n"
"s_nop 0\n"
"v_cvt_pk_bf16_f32 v99, v102, v103\n"
"s_nop 1\n"
"v_mfma_f32_16x16x32_bf16 v[160:163], v[60:63], v[96:99], v[160:163]\n"
"ds_write_b128 v23, a[48:51] offset:4096\n"
"v_mfma_f32_16x16x32_bf16 v[192:195], a[192:195], v[96:99], v[192:195]\n"
"ds_write_b128 v23, a[52:55] offset:5120\n"
"v_mfma_f32_16x16x32_bf16 v[196:199], a[196:199], v[96:99], v[196:199]\n"
"s_cmp_gt_u32 s74, 32\n"
"v_exp_f32 v104, v104\n"
"v_mfma_f32_16x16x32_bf16 v[200:203], a[200:203], v[96:99], v[200:203]\n"
"s_cselect_b64 s[20:21], s[32:33], s[20:21]\n"
"v_exp_f32 v105, v105\n"
"v_mfma_f32_16x16x32_bf16 v[204:207], a[204:207], v[96:99], v[204:207]\n"
"s_cselect_b32 s22, s34, s22\n"
"v_exp_f32 v106, v106\n"
"v_cvt_pk_bf16_f32 v104, v104, v105\n"
"v_exp_f32 v107, v107\n"
"s_nop 0\n"
"v_cvt_pk_bf16_f32 v105, v106, v107\n"
"v_exp_f32 v108, v108\n"
"v_exp_f32 v109, v109\n"
"s_nop 0\n"
"v_cvt_pk_bf16_f32 v106, v108, v109\n"
"v_exp_f32 v110, v110\n"
"v_exp_f32 v111, v111\n"
"s_nop 0\n"
"v_cvt_pk_bf16_f32 v107, v110, v111\n"
"s_nop 1\n"
"v_mfma_f32_16x16x32_bf16 v[164:167], v[60:63], v[104:107], v[164:167]\n"
"ds_write_b128 v23, a[56:59] offset:6144\n"
"v_mfma_f32_16x16x32_bf16 v[208:211], a[208:211], v[96:99], v[208:211]\n"
"ds_write_b128 v23, a[60:63] offset:7168\n"
"v_mfma_f32_16x16x32_bf16 v[212:215], a[212:215], v[96:99], v[212:215]\n"
"s_cselect_b32 s92, 262144, s92\n"
"v_xor_b32 v38, 0x2000, v23\n"
"v_xor_b32 v9, 0x8000, v9\n"
"v_mfma_f32_16x16x32_bf16 v[216:219], a[216:219], v[96:99], v[216:219]\n"
"s_cselect_b32 s5, 524288, s5\n"
"v_mfma_f32_16x16x32_bf16 v[220:223], a[220:223], v[96:99], v[220:223]\n"
"v_cndmask_b32 v4, v160, v164, s[56:57]\n"
"v_cndmask_b32 v4, v4, v5, s[100:101]\n"
"v_mfma_f32_16x16x32_bf16 v[224:227], a[192:195], v[104:107], v[224:227]\n"
"ds_write_b32 v19, v4\n"
"v_mfma_f32_16x16x32_bf16 v[228:231], a[196:199], v[104:107], v[228:231]\n"
"v_mfma_f32_16x16x32_bf16 v[232:235], a[200:203], v[104:107], v[232:235]\n"
"v_mfma_f32_16x16x32_bf16 v[236:239], a[204:207], v[104:107], v[236:239]\n"
"v_mfma_f32_16x16x32_bf16 v[240:243], a[208:211], v[104:107], v[240:243]\n"
"v_mfma_f32_16x16x32_bf16 v[244:247], a[212:215], v[104:107], v[244:247]\n"
"v_mfma_f32_16x16x32_bf16 v[248:251], a[216:219], v[104:107], v[248:251]\n"
"v_mfma_f32_16x16x32_bf16 v[252:255], a[220:223], v[104:107], v[252:255]\n"
"s_waitcnt lgkmcnt(0)\n"
"s_barrier\n"
"s_branch EPI_%=\n"
"B1_15_%=:\n"
"ODD0_%=:\n"
"s_cbranch_execz PADRET_%=\n"
"v_mfma_f32_16x16x32_bf16 v[152:155], a[192:195], a[112:115], v[44:47]\n"
"s_add_u32 m0, s77, 0\n"
"ds_read_b128 a[224:227], v9 offset:16384\n"
"v_mfma_f32_16x16x32_bf16 v[152:155], a[196:199], a[116:119], v[152:155]\n"
"ds_read_b128 a[228:231], v9 offset:17408\n"
"v_mfma_f32_16x16x32_bf16 v[152:155], a[200:203], a[120:123], v[152:155]\n"
"buffer_load_dwordx4 v24, s[24:27], s80 offen offset:0 sc1 lds\n"
"v_mfma_f32_16x16x32_bf16 v[152:155], a[204:207], a[124:127], v[152:155]\n"
"ds_read_b128 a[232:235], v9 offset:18432\n"
"v_mfma_f32_16x16x32_bf16 v[156:159], a[220:223], a[124:127], v[48:51]\n"
"v_exp_f32 v118, v118\n"
"v_mfma_f32_16x16x32_bf16 v[156:159], a[216:219], a[120:123], v[156:159]\n"
"ds_read_b128 a[236:239], v9 offset:19456\n"
"v_mfma_f32_16x16x32_bf16 v[156:159], a[212:215], a[116:119], v[156:159]\n"
"buffer_load_dwordx4 v25, s[24:27], s80 offen offset:1024 sc1 lds\n"
"v_mfma_f32_16x16x32_bf16 v[156:159], a[208:211], a[112:115], v[156:159]\n"
"ds_read_b128 a[240:243], v9 offset:20480\n"
"v_mfma_f32_16x16x32_bf16 v[132:135], a[208:211], a[64:67], v[176:179]\n"
"v_exp_f32 v119, v119\n"
"v_mfma_f32_16x16x32_bf16 v[132:135], a[212:215], a[68:71], v[132:135]\n"
"ds_read_b128 a[244:247], v9 offset:21504\n"
"v_cvt_pk_bf16_f32 v115, v118, v119\n"
"v_mfma_f32_16x16x32_bf16 v[132:135], a[216:219], a[72:75], v[132:135]\n"
"buffer_load_dwordx4 v26, s[24:27], s80 offen offset:2048 sc1 lds\n"
"v_mfma_f32_16x16x32_bf16 v[132:135], a[220:223], a[76:79], v[132:135]\n"
"ds_read_b128 a[248:251], v9 offset:22528\n"
"v_mfma_f32_16x16x32_bf16 v[128:131], a[204:207], a[76:79], v[176:179]\n"
"v_exp_f32 v120, v120\n"
"v_mfma_f32_16x16x32_bf16 v[128:131], a[200:203], a[72:75], v[128:131]\n"
"ds_read_b128 a[252:255], v9 offset:23552\n"
"v_xor_b32 v9, 0x8000, v9\n"
"v_mfma_f32_16x16x32_bf16 v[128:131], a[196:199], a[68:71], v[128:131]\n"
"buffer_load_dwordx4 v27, s[24:27], s80 offen offset:3072 sc1 lds\n"
"v_mfma_f32_16x16x32_bf16 v[128:131], a[192:195], a[64:67], v[128:131]\n"
"s_add_u32 m0, s76, 0\n"
"v_exp_f32 v121, v121\n"
"v_mfma_f32_16x16x32_bf16 v[136:139], a[192:195], a[80:83], v[180:183]\n"
"v_exp_f32 v122, v122\n"
"v_mfma_f32_16x16x32_bf16 v[136:139], a[196:199], a[84:87], v[136:139]\n"
"buffer_load_dwordx4 v24, s[20:23], s78 offen offset:0 sc1 lds\n"
"v_mfma_f32_16x16x32_bf16 v[136:139], a[200:203], a[88:91], v[136:139]\n"
"v_exp_f32 v123, v123\n"
"v_mfma_f32_16x16x32_bf16 v[136:139], a[204:207], a[92:95], v[136:139]\n"
"v_exp_f32 v124, v124\n"
"v_mfma_f32_16x16x32_bf16 v[140:143], a[220:223], a[92:95], v[180:183]\n"
"v_exp_f32 v125, v125\n"
"v_mfma_f32_16x16x32_bf16 v[140:143], a[216:219], a[88:91], v[140:143]\n"
"buffer_load_dwordx4 v25, s[20:23], s78 offen offset:1024 sc1 lds\n"
"v_mfma_f32_16x16x32_bf16 v[140:143], a[212:215], a[84:87], v[140:143]\n"
"v_exp_f32 v126, v126\n"
"v_mfma_f32_16x16x32_bf16 v[140:143], a[208:211], a[80:83], v[140:143]\n"
"s_waitcnt lgkmcnt(7)\n"
"v_exp_f32 v127, v127\n"
"v_mfma_f32_16x16x32_bf16 a[0:3], a[224:227], v[112:115], a[0:3]\n"
"s_waitcnt lgkmcnt(6)\n"
"ds_read_b128 a[192:195], v8 offset:16384\n"
"v_cvt_pk_bf16_f32 v120, v120, v121\n"
"v_mfma_f32_16x16x32_bf16 a[4:7], a[228:231], v[112:115], a[4:7]\n"
"s_waitcnt lgkmcnt(6)\n"
"ds_read_b128 a[196:199], v8 offset:17408\n"
"v_cvt_pk_bf16_f32 v121, v122, v123\n"
"v_mfma_f32_16x16x32_bf16 a[8:11], a[232:235], v[112:115], a[8:11]\n"
"buffer_load_dwordx4 v26, s[20:23], s78 offen offset:2048 sc1 lds\n"
"s_waitcnt lgkmcnt(6)\n"
"v_mfma_f32_16x16x32_bf16 a[12:15], a[236:239], v[112:115], a[12:15]\n"
"s_waitcnt lgkmcnt(5)\n"
"ds_read_b128 a[200:203], v8 offset:18432\n"
"ds_read_b128 a[204:207], v8 offset:19456\n"
"v_mfma_f32_16x16x32_bf16 a[16:19], a[240:243], v[112:115], a[16:19]\n"
"s_waitcnt lgkmcnt(6)\n"
"ds_read_b128 a[208:211], v8 offset:20480\n"
"v_cvt_pk_bf16_f32 v122, v124, v125\n"
"v_mfma_f32_16x16x32_bf16 a[20:23], a[244:247], v[112:115], a[20:23]\n"
"s_waitcnt lgkmcnt(6)\n"
"ds_read_b128 a[212:215], v8 offset:21504\n"
"v_cvt_pk_bf16_f32 v123, v126, v127\n"
"v_mfma_f32_16x16x32_bf16 a[24:27], a[248:251], v[112:115], a[24:27]\n"
"buffer_load_dwordx4 v27, s[20:23], s78 offen offset:3072 sc1 lds\n"
"s_waitcnt lgkmcnt(6)\n"
"v_mfma_f32_16x16x32_bf16 a[28:31], a[252:255], v[112:115], a[28:31]\n"
"ds_read_b128 a[216:219], v8 offset:22528\n"
"ds_read_b128 a[220:223], v8 offset:23552\n"
"v_mfma_f32_16x16x32_bf16 v[168:171], v[60:63], v[112:115], v[168:171]\n"
"v_exp_f32 v152, v152\n"
"v_mfma_f32_16x16x32_bf16 v[192:195], a[224:227], v[96:99], v[192:195]\n"
"v_exp_f32 v153, v153\n"
"v_mfma_f32_16x16x32_bf16 v[196:199], a[228:231], v[96:99], v[196:199]\n"
"ds_write_b128 v22, a[0:3] offset:0\n"
"v_mfma_f32_16x16x32_bf16 v[200:203], a[232:235], v[96:99], v[200:203]\n"
"v_exp_f32 v154, v154\n"
"v_mfma_f32_16x16x32_bf16 v[204:207], a[236:239], v[96:99], v[204:207]\n"
"v_exp_f32 v155, v155\n"
"v_mfma_f32_16x16x32_bf16 v[208:211], a[240:243], v[96:99], v[208:211]\n"
"ds_write_b128 v22, a[4:7] offset:1024\n"
"v_mfma_f32_16x16x32_bf16 v[212:215], a[244:247], v[96:99], v[212:215]\n"
"v_exp_f32 v156, v156\n"
"v_mfma_f32_16x16x32_bf16 v[216:219], a[248:251], v[96:99], v[216:219]\n"
"v_exp_f32 v157, v157\n"
"v_mfma_f32_16x16x32_bf16 v[220:223], a[252:255], v[96:99], v[220:223]\n"
"ds_write_b128 v22, a[8:11] offset:2048\n"
"v_mfma_f32_16x16x32_bf16 v[160:163], v[60:63], v[96:99], v[160:163]\n"
"v_exp_f32 v158, v158\n"
"v_mfma_f32_16x16x32_bf16 v[224:227], a[224:227], v[104:107], v[224:227]\n"
"v_exp_f32 v159, v159\n"
"v_mfma_f32_16x16x32_bf16 v[228:231], a[228:231], v[104:107], v[228:231]\n"
"ds_write_b128 v22, a[12:15] offset:3072\n"
"v_mfma_f32_16x16x32_bf16 v[232:235], a[232:235], v[104:107], v[232:235]\n"
"v_exp_f32 v128, v128\n"
"v_mfma_f32_16x16x32_bf16 v[236:239], a[236:239], v[104:107], v[236:239]\n"
"v_exp_f32 v129, v129\n"
"v_mfma_f32_16x16x32_bf16 v[240:243], a[240:243], v[104:107], v[240:243]\n"
"ds_write_b128 v22, a[16:19] offset:4096\n"
"v_mfma_f32_16x16x32_bf16 v[244:247], a[244:247], v[104:107], v[244:247]\n"
"v_exp_f32 v130, v130\n"
"v_mfma_f32_16x16x32_bf16 v[248:251], a[248:251], v[104:107], v[248:251]\n"
"v_exp_f32 v131, v131\n"
"v_mfma_f32_16x16x32_bf16 v[252:255], a[252:255], v[104:107], v[252:255]\n"
"ds_write_b128 v22, a[20:23] offset:5120\n"
"v_mfma_f32_16x16x32_bf16 v[164:167], v[60:63], v[104:107], v[164:167]\n"
"v_exp_f32 v132, v132\n"
"v_mfma_f32_16x16x32_bf16 a[32:35], a[224:227], v[120:123], a[32:35]\n"
"v_exp_f32 v133, v133\n"
"v_mfma_f32_16x16x32_bf16 a[36:39], a[228:231], v[120:123], a[36:39]\n"
"ds_write_b128 v22, a[24:27] offset:6144\n"
"v_mfma_f32_16x16x32_bf16 a[40:43], a[232:235], v[120:123], a[40:43]\n"
"v_exp_f32 v134, v134\n"
"v_mfma_f32_16x16x32_bf16 a[44:47], a[236:239], v[120:123], a[44:47]\n"
"v_exp_f32 v135, v135\n"
"v_mfma_f32_16x16x32_bf16 a[48:51], a[240:243], v[120:123], a[48:51]\n"
"ds_write_b128 v22, a[28:31] offset:7168\n"
"v_mfma_f32_16x16x32_bf16 a[52:55], a[244:247], v[120:123], a[52:55]\n"
"v_exp_f32 v136, v136\n"
"v_mfma_f32_16x16x32_bf16 a[56:59], a[248:251], v[120:123], a[56:59]\n"
"v_exp_f32 v137, v137\n"
"v_mfma_f32_16x16x32_bf16 a[60:63], a[252:255], v[120:123], a[60:63]\n"
"s_waitcnt vmcnt(8)\n"
"v_xor_b32 v8, 0x8000, v8\n"
"v_cvt_pk_bf16_f32 v152, v152, v153\n"
"v_mfma_f32_16x16x32_bf16 v[172:175], v[60:63], v[120:123], v[172:175]\n"
"s_waitcnt lgkmcnt(0)\n"
"s_barrier\n"
"v_cvt_pk_bf16_f32 v153, v154, v155\n"
"v_cvt_pk_bf16_f32 v154, v156, v157\n"
"v_cvt_pk_bf16_f32 v155, v158, v159\n"
"v_cvt_pk_bf16_f32 v128, v128, v129\n"
"v_cvt_pk_bf16_f32 v129, v130, v131\n"
"v_cvt_pk_bf16_f32 v130, v132, v133\n"
"v_cvt_pk_bf16_f32 v131, v134, v135\n"
"v_cvt_pk_bf16_f32 v136, v136, v137\n"
"s_mov_b32 exec_lo, s58\n"
"s_mov_b32 exec_hi, 0\n"
"buffer_store_dwordx2 v[10:11], off, s[44:47], s85 sc1\n"
"s_mov_b64 exec, -1\n"
"s_mov_b32 s58, 0\n"
"F1_17_%=:\n"
"v_mfma_f32_16x16x32_bf16 v[96:99], a[192:195], a[64:67], v[52:55]\n"
"s_lshl_b32 s82, s61, 13\n"
"s_and_b32 s83, s77, 0x18000\n"
"s_xor_b32 s83, s83, 0x8000\n"
"v_mfma_f32_16x16x32_bf16 v[96:99], a[196:199], a[68:71], v[96:99]\n"
"s_add_u32 s82, s82, s83\n"
"ds_read_b128 v[64:67], v9 offset:0\n"
"v_add_u32 v23, s82, v1\n"
"v_mfma_f32_16x16x32_bf16 v[96:99], a[200:203], a[72:75], v[96:99]\n"
"s_lshl_b32 s82, s70, 3\n"
"s_lshl_b32 s83, s67, 2\n"
"s_add_u32 s82, s82, s83\n"
"v_mfma_f32_16x16x32_bf16 v[96:99], a[204:207], a[76:79], v[96:99]\n"
"s_add_u32 s82, s82, s66\n"
"s_lshl_b32 s82, s82, 17\n"
"s_lshl_b32 s83, s62, 12\n"
"v_mfma_f32_16x16x32_bf16 v[100:103], a[220:223], a[76:79], v[56:59]\n"
"s_add_u32 s82, s82, s83\n"
"s_add_u32 s82, s82, 37748736\n"
"s_add_u32 s83, s82, 262144\n"
"v_mfma_f32_16x16x32_bf16 v[100:103], a[216:219], a[72:75], v[100:103]\n"
"s_add_u32 m0, s77, 16384\n"
"ds_read_b128 v[68:71], v9 offset:1024\n"
"ds_read_b128 v[72:75], v9 offset:2048\n"
"v_mfma_f32_16x16x32_bf16 v[100:103], a[212:215], a[68:71], v[100:103]\n"
"ds_read_b128 v[76:79], v9 offset:3072\n"
"ds_read_b128 v[80:83], v9 offset:4096\n"
"v_mfma_f32_16x16x32_bf16 v[100:103], a[208:211], a[64:67], v[100:103]\n"
"buffer_load_dwordx4 v24, s[24:27], s81 offen offset:0 sc1 lds\n"
"v_mfma_f32_16x16x32_bf16 v[108:111], a[208:211], a[80:83], v[180:183]\n"
"buffer_load_dwordx4 v25, s[24:27], s81 offen offset:1024 sc1 lds\n"
"v_mfma_f32_16x16x32_bf16 v[108:111], a[212:215], a[84:87], v[108:111]\n"
"ds_read_b128 v[84:87], v9 offset:5120\n"
"ds_read_b128 v[88:91], v9 offset:6144\n"
"v_mfma_f32_16x16x32_bf16 v[108:111], a[216:219], a[88:91], v[108:111]\n"
"buffer_load_dwordx4 v26, s[24:27], s81 offen offset:2048 sc1 lds\n"
"v_mfma_f32_16x16x32_bf16 v[108:111], a[220:223], a[92:95], v[108:111]\n"
"ds_read_b128 v[92:95], v9 offset:7168\n"
"ds_read_b128 a[224:227], v8 offset:0\n"
"v_mfma_f32_16x16x32_bf16 v[104:107], a[204:207], a[92:95], v[180:183]\n"
"buffer_load_dwordx4 v27, s[24:27], s81 offen offset:3072 sc1 lds\n"
"v_mfma_f32_16x16x32_bf16 v[104:107], a[200:203], a[88:91], v[104:107]\n"
"s_add_u32 m0, s76, 16384\n"
"ds_read_b128 a[228:231], v8 offset:1024\n"
"ds_read_b128 a[232:235], v8 offset:2048\n"
"v_mfma_f32_16x16x32_bf16 v[104:107], a[196:199], a[84:87], v[104:107]\n"
"buffer_load_dwordx4 v24, s[20:23], s79 offen offset:0 sc1 lds\n"
"v_mfma_f32_16x16x32_bf16 v[104:107], a[192:195], a[80:83], v[104:107]\n"
"s_add_u32 s90, s89, 0\n"
"ds_read_b128 a[236:239], v8 offset:3072\n"
"ds_read_b128 a[240:243], v8 offset:4096\n"
"s_waitcnt lgkmcnt(12)\n"
"v_mfma_f32_16x16x32_bf16 a[32:35], v[64:67], v[152:155], a[32:35]\n"
"buffer_load_dwordx4 v25, s[20:23], s79 offen offset:1024 sc1 lds\n"
"s_waitcnt lgkmcnt(11)\n"
"v_mfma_f32_16x16x32_bf16 a[36:39], v[68:71], v[152:155], a[36:39]\n"
"buffer_load_dwordx4 v26, s[20:23], s79 offen offset:2048 sc1 lds\n"
"s_waitcnt lgkmcnt(10)\n"
"v_mfma_f32_16x16x32_bf16 a[40:43], v[72:75], v[152:155], a[40:43]\n"
"s_mov_b64 s[24:25], s[20:21]\n"
"ds_read_b128 a[244:247], v8 offset:5120\n"
"ds_read_b128 a[248:251], v8 offset:6144\n"
"s_waitcnt lgkmcnt(11)\n"
"v_mfma_f32_16x16x32_bf16 a[44:47], v[76:79], v[152:155], a[44:47]\n"
"buffer_load_dwordx4 v27, s[20:23], s79 offen offset:3072 sc1 lds\n"
"s_waitcnt lgkmcnt(10)\n"
"v_mfma_f32_16x16x32_bf16 a[48:51], v[80:83], v[152:155], a[48:51]\n"
"s_mov_b32 s26, s22\n"
"v_min_u32 v34, 53, v0\n"
"v_lshlrev_b32 v34, 7, v34\n"
"s_waitcnt lgkmcnt(9)\n"
"v_mfma_f32_16x16x32_bf16 a[52:55], v[84:87], v[152:155], a[52:55]\n"
"s_waitcnt lgkmcnt(8)\n"
"ds_read_b128 a[252:255], v8 offset:7168\n"
"ds_read_b128 a[192:195], v9 offset:16384\n"
"v_mfma_f32_16x16x32_bf16 a[56:59], v[88:91], v[152:155], a[56:59]\n"
"buffer_load_dword v42, v34, s[28:31], s90 offen\n"
"s_waitcnt lgkmcnt(9)\n"
"v_mfma_f32_16x16x32_bf16 a[60:63], v[92:95], v[152:155], a[60:63]\n"
"s_add_u32 s90, s89, 6912\n"
"ds_read_b128 a[196:199], v9 offset:17408\n"
"ds_read_b128 a[200:203], v9 offset:18432\n"
"v_mfma_f32_16x16x32_bf16 v[172:175], v[60:63], v[152:155], v[172:175]\n"
"buffer_load_dword v42, v34, s[28:31], s90 offen\n"
"v_mfma_f32_16x16x32_bf16 v[160:163], v[60:63], v[128:131], v[160:163]\n"
"s_add_u32 s90, s89, 13824\n"
"ds_read_b128 a[204:207], v9 offset:19456\n"
"ds_read_b128 a[208:211], v9 offset:20480\n"
"v_mfma_f32_16x16x32_bf16 v[192:195], v[64:67], v[128:131], v[192:195]\n"
"buffer_load_dword v42, v34, s[28:31], s90 offen\n"
"v_mfma_f32_16x16x32_bf16 v[196:199], v[68:71], v[128:131], v[196:199]\n"
"s_add_u32 s80, s78, s5\n"
"ds_read_b128 a[212:215], v9 offset:21504\n"
"ds_read_b128 a[216:219], v9 offset:22528\n"
"v_mfma_f32_16x16x32_bf16 v[200:203], v[72:75], v[128:131], v[200:203]\n"
"s_add_u32 s81, s79, s5\n"
"ds_read_b128 a[220:223], v9 offset:23552\n"
"v_cndmask_b32 v5, v168, v172, s[56:57]\n"
"v_mfma_f32_16x16x32_bf16 v[204:207], v[76:79], v[128:131], v[204:207]\n"
"s_xor_b32 s77, s77, 0x8000\n"
"v_exp_f32 v138, v138\n"
"v_mfma_f32_16x16x32_bf16 v[208:211], v[80:83], v[128:131], v[208:211]\n"
"s_add_u32 s78, s78, s92\n"
"v_exp_f32 v139, v139\n"
"v_mfma_f32_16x16x32_bf16 v[212:215], v[84:87], v[128:131], v[212:215]\n"
"s_waitcnt lgkmcnt(0)\n"
"s_barrier\n"
"v_exp_f32 v140, v140\n"
"v_mfma_f32_16x16x32_bf16 v[216:219], v[88:91], v[128:131], v[216:219]\n"
"ds_write_b128 v23, a[32:35] offset:0\n"
"v_mfma_f32_16x16x32_bf16 v[220:223], v[92:95], v[128:131], v[220:223]\n"
"s_add_u32 s79, s79, s92\n"
"v_exp_f32 v141, v141\n"
"v_cvt_pk_bf16_f32 v137, v138, v139\n"
"v_cvt_pk_bf16_f32 v138, v140, v141\n"
"v_exp_f32 v142, v142\n"
"v_exp_f32 v143, v143\n"
"s_nop 0\n"
"v_cvt_pk_bf16_f32 v139, v142, v143\n"
"s_nop 1\n"
"v_mfma_f32_16x16x32_bf16 v[164:167], v[60:63], v[136:139], v[164:167]\n"
"ds_write_b128 v23, a[36:39] offset:1024\n"
"v_mfma_f32_16x16x32_bf16 v[224:227], v[64:67], v[136:139], v[224:227]\n"
"ds_write_b128 v23, a[40:43] offset:2048\n"
"v_mfma_f32_16x16x32_bf16 v[228:231], v[68:71], v[136:139], v[228:231]\n"
"s_xor_b32 s76, s76, 0x8000\n"
"v_exp_f32 v96, v96\n"
"v_mfma_f32_16x16x32_bf16 v[232:235], v[72:75], v[136:139], v[232:235]\n"
"ds_write_b128 v23, a[44:47] offset:3072\n"
"v_mfma_f32_16x16x32_bf16 v[236:239], v[76:79], v[136:139], v[236:239]\n"
"s_sub_u32 s74, s74, 1\n"
"v_exp_f32 v97, v97\n"
"v_mfma_f32_16x16x32_bf16 v[240:243], v[80:83], v[136:139], v[240:243]\n"
"s_cselect_b64 s[78:79], s[72:73], s[78:79]\n"
"v_exp_f32 v98, v98\n"
"v_mfma_f32_16x16x32_bf16 v[244:247], v[84:87], v[136:139], v[244:247]\n"
"s_cselect_b64 s[72:73], s[16:17], s[72:73]\n"
"v_exp_f32 v99, v99\n"
"v_mfma_f32_16x16x32_bf16 v[248:251], v[88:91], v[136:139], v[248:251]\n"
"s_cselect_b32 s74, s75, s74\n"
"v_exp_f32 v100, v100\n"
"v_mfma_f32_16x16x32_bf16 v[252:255], v[92:95], v[136:139], v[252:255]\n"
"s_cselect_b32 s75, 64, s75\n"
"v_exp_f32 v101, v101\n"
"v_cvt_pk_bf16_f32 v96, v96, v97\n"
"v_cvt_pk_bf16_f32 v97, v98, v99\n"
"v_cvt_pk_bf16_f32 v98, v100, v101\n"
"v_exp_f32 v102, v102\n"
"v_exp_f32 v103, v103\n"
"s_nop 0\n"
"v_cvt_pk_bf16_f32 v99, v102, v103\n"
"s_nop 1\n"
"v_mfma_f32_16x16x32_bf16 v[160:163], v[60:63], v[96:99], v[160:163]\n"
"ds_write_b128 v23, a[48:51] offset:4096\n"
"v_mfma_f32_16x16x32_bf16 v[192:195], a[192:195], v[96:99], v[192:195]\n"
"ds_write_b128 v23, a[52:55] offset:5120\n"
"v_mfma_f32_16x16x32_bf16 v[196:199], a[196:199], v[96:99], v[196:199]\n"
"s_cmp_gt_u32 s74, 32\n"
"v_exp_f32 v104, v104\n"
"v_mfma_f32_16x16x32_bf16 v[200:203], a[200:203], v[96:99], v[200:203]\n"
"s_cselect_b64 s[20:21], s[32:33], s[20:21]\n"
"v_exp_f32 v105, v105\n"
"v_mfma_f32_16x16x32_bf16 v[204:207], a[204:207], v[96:99], v[204:207]\n"
"s_cselect_b32 s22, s34, s22\n"
"v_exp_f32 v106, v106\n"
"v_cvt_pk_bf16_f32 v104, v104, v105\n"
"v_exp_f32 v107, v107\n"
"s_nop 0\n"
"v_cvt_pk_bf16_f32 v105, v106, v107\n"
"v_exp_f32 v108, v108\n"
"v_exp_f32 v109, v109\n"
"s_nop 0\n"
"v_cvt_pk_bf16_f32 v106, v108, v109\n"
"v_exp_f32 v110, v110\n"
"v_exp_f32 v111, v111\n"
"s_nop 0\n"
"v_cvt_pk_bf16_f32 v107, v110, v111\n"
"s_nop 1\n"
"v_mfma_f32_16x16x32_bf16 v[164:167], v[60:63], v[104:107], v[164:167]\n"
"ds_write_b128 v23, a[56:59] offset:6144\n"
"v_mfma_f32_16x16x32_bf16 v[208:211], a[208:211], v[96:99], v[208:211]\n"
"ds_write_b128 v23, a[60:63] offset:7168\n"
"v_mfma_f32_16x16x32_bf16 v[212:215], a[212:215], v[96:99], v[212:215]\n"
"s_cselect_b32 s92, 262144, s92\n"
"v_xor_b32 v38, 0x2000, v23\n"
"v_xor_b32 v9, 0x8000, v9\n"
"v_mfma_f32_16x16x32_bf16 v[216:219], a[216:219], v[96:99], v[216:219]\n"
"s_cselect_b32 s5, 524288, s5\n"
"v_mfma_f32_16x16x32_bf16 v[220:223], a[220:223], v[96:99], v[220:223]\n"
"v_cndmask_b32 v4, v160, v164, s[56:57]\n"
"v_cndmask_b32 v4, v4, v5, s[100:101]\n"
"v_mfma_f32_16x16x32_bf16 v[224:227], a[192:195], v[104:107], v[224:227]\n"
"ds_write_b32 v19, v4\n"
"v_mfma_f32_16x16x32_bf16 v[228:231], a[196:199], v[104:107], v[228:231]\n"
"v_mfma_f32_16x16x32_bf16 v[232:235], a[200:203], v[104:107], v[232:235]\n"
"v_mfma_f32_16x16x32_bf16 v[236:239], a[204:207], v[104:107], v[236:239]\n"
"v_mfma_f32_16x16x32_bf16 v[240:243], a[208:211], v[104:107], v[240:243]\n"
"v_mfma_f32_16x16x32_bf16 v[244:247], a[212:215], v[104:107], v[244:247]\n"
"v_mfma_f32_16x16x32_bf16 v[248:251], a[216:219], v[104:107], v[248:251]\n"
"v_mfma_f32_16x16x32_bf16 v[252:255], a[220:223], v[104:107], v[252:255]\n"
"s_waitcnt lgkmcnt(0)\n"
"s_barrier\n"
"EPI_%=:\n"
"s_cbranch_execz PADRET_%=\n"
"s_cmp_lg_u32 s67, 0\n"
"s_cselect_b64 exec, -1, 0\n"
"ds_read_b128 v[96:99], v37\n"
"s_mov_b64 exec, -1\n"
"ds_read_b128 v[128:131], v39\n"
"ds_read_b128 v[132:135], v40\n"
"ds_read_b128 v[136:139], v41\n"
"ds_read_b128 v[100:103], v37 offset:1024\n"
"ds_read_b128 v[104:107], v37 offset:2048\n"
"ds_read_b128 v[108:111], v37 offset:3072\n"
"ds_read_b128 v[112:115], v37 offset:4096\n"
"ds_read_b128 v[116:119], v37 offset:5120\n"
"ds_read_b128 v[120:123], v37 offset:6144\n"
"ds_read_b128 v[124:127], v37 offset:7168\n"
"ds_read_b128 v[64:67], v38 offset:0\n"
"ds_read_b128 v[68:71], v38 offset:1024\n"
"ds_read_b128 v[72:75], v38 offset:2048\n"
"ds_read_b128 v[76:79], v38 offset:3072\n"
"ds_read_b128 v[80:83], v38 offset:4096\n"
"ds_read_b128 v[84:87], v38 offset:5120\n"
"ds_read_b128 v[88:91], v38 offset:6144\n"
"ds_read_b128 v[92:95], v38 offset:7168\n"
"s_waitcnt lgkmcnt(15)\n"
"v_add_f32 v4, v160, v130\n"
"v_add_f32 v5, v164, v131\n"
"v_add_f32 v6, v168, v128\n"
"v_add_f32 v7, v172, v129\n"
"v_add_f32 v132, v132, v138\n"
"v_add_f32 v133, v133, v139\n"
"v_add_f32 v134, v134, v136\n"
"v_add_f32 v135, v135, v137\n"
"v_min3_f32 v32, v4, v5, v6\n"
"v_min3_f32 v33, v7, v132, v133\n"
"v_min3_f32 v32, v32, v134, v135\n"
"v_min_f32 v32, v32, v33\n"
"v_pk_add_f32 v[6:7], v[6:7], v[132:133]\n"
"v_pk_add_f32 v[134:135], v[134:135], v[4:5]\n"
"PG10_%=:\n"
"s_cbranch_execz PADRET_%=\n"
"v_cmp_nle_f32 vcc, 0x21800000, v32\n"
"v_pk_add_f32 v[6:7], v[6:7], v[134:135]\n"
"s_mov_b64 s[96:97], vcc\n"
"v_add_f32 v33, v6, v7\n"
"v_rcp_f32 v6, v5\n"
"v_rcp_f32 v4, v4\n"
"v_cmp_nge_f32 vcc, 0x71800000, v33\n"
"s_nop 0\n"
"s_or_b64 vcc, vcc, s[96:97]\n"
"s_cmp_lg_u32 s93, 0\n"
"s_cselect_b64 vcc, 0, vcc\n"
"s_cbranch_vccnz SAFE0_%=\n"
"s_waitcnt lgkmcnt(14)\n"
"v_pk_add_f32 v[192:193], v[192:193], v[96:97]\n"
"v_pk_add_f32 v[194:195], v[194:195], v[98:99]\n"
"v_pk_add_f32 v[196:197], v[196:197], v[100:101]\n"
"v_pk_add_f32 v[198:199], v[198:199], v[102:103]\n"
"v_pk_mul_f32 v[192:193], v[192:193], v[4:5] op_sel_hi:[1,0]\n"
"v_pk_mul_f32 v[194:195], v[194:195], v[4:5] op_sel_hi:[1,0]\n"
"v_pk_mul_f32 v[196:197], v[196:197], v[4:5] op_sel_hi:[1,0]\n"
"v_pk_mul_f32 v[198:199], v[198:199], v[4:5] op_sel_hi:[1,0]\n"
"v_cvt_pk_bf16_f32 v192, v192, v193\n"
"v_cvt_pk_bf16_f32 v193, v194, v195\n"
"v_cvt_pk_bf16_f32 v194, v196, v197\n"
"v_cvt_pk_bf16_f32 v195, v198, v199\n"
"s_waitcnt lgkmcnt(12)\n"
"v_pk_add_f32 v[200:201], v[200:201], v[104:105]\n"
"v_pk_add_f32 v[202:203], v[202:203], v[106:107]\n"
"v_pk_add_f32 v[204:205], v[204:205], v[108:109]\n"
"v_pk_add_f32 v[206:207], v[206:207], v[110:111]\n"
"v_pk_mul_f32 v[200:201], v[200:201], v[4:5] op_sel_hi:[1,0]\n"
"v_pk_mul_f32 v[202:203], v[202:203], v[4:5] op_sel_hi:[1,0]\n"
"v_pk_mul_f32 v[204:205], v[204:205], v[4:5] op_sel_hi:[1,0]\n"
"v_pk_mul_f32 v[206:207], v[206:207], v[4:5] op_sel_hi:[1,0]\n"
"v_cvt_pk_bf16_f32 v200, v200, v201\n"
"v_cvt_pk_bf16_f32 v201, v202, v203\n"
"v_cvt_pk_bf16_f32 v202, v204, v205\n"
"v_cvt_pk_bf16_f32 v203, v206, v207\n"
"s_waitcnt lgkmcnt(10)\n"
"v_pk_add_f32 v[208:209], v[208:209], v[112:113]\n"
"v_pk_add_f32 v[210:211], v[210:211], v[114:115]\n"
"v_pk_add_f32 v[212:213], v[212:213], v[116:117]\n"
"v_pk_add_f32 v[214:215], v[214:215], v[118:119]\n"
"v_pk_mul_f32 v[208:209], v[208:209], v[4:5] op_sel_hi:[1,0]\n"
"v_pk_mul_f32 v[210:211], v[210:211], v[4:5] op_sel_hi:[1,0]\n"
"v_pk_mul_f32 v[212:213], v[212:213], v[4:5] op_sel_hi:[1,0]\n"
"v_pk_mul_f32 v[214:215], v[214:215], v[4:5] op_sel_hi:[1,0]\n"
"v_cvt_pk_bf16_f32 v208, v208, v209\n"
"v_cvt_pk_bf16_f32 v209, v210, v211\n"
"v_cvt_pk_bf16_f32 v210, v212, v213\n"
"v_cvt_pk_bf16_f32 v211, v214, v215\n"
"s_waitcnt lgkmcnt(8)\n"
"v_pk_add_f32 v[216:217], v[216:217], v[120:121]\n"
"v_pk_add_f32 v[218:219], v[218:219], v[122:123]\n"
"v_pk_add_f32 v[220:221], v[220:221], v[124:125]\n"
"v_pk_add_f32 v[222:223], v[222:223], v[126:127]\n"
"v_pk_mul_f32 v[216:217], v[216:217], v[4:5] op_sel_hi:[1,0]\n"
"v_pk_mul_f32 v[218:219], v[218:219], v[4:5] op_sel_hi:[1,0]\n"
"v_pk_mul_f32 v[220:221], v[220:221], v[4:5] op_sel_hi:[1,0]\n"
"v_pk_mul_f32 v[222:223], v[222:223], v[4:5] op_sel_hi:[1,0]\n"
"v_cvt_pk_bf16_f32 v216, v216, v217\n"
"v_cvt_pk_bf16_f32 v217, v218, v219\n"
"v_cvt_pk_bf16_f32 v218, v220, v221\n"
"v_cvt_pk_bf16_f32 v219, v222, v223\n"
"s_waitcnt lgkmcnt(6)\n"
"v_pk_add_f32 v[224:225], v[224:225], v[64:65]\n"
"v_pk_add_f32 v[226:227], v[226:227], v[66:67]\n"
"v_pk_add_f32 v[228:229], v[228:229], v[68:69]\n"
"v_pk_add_f32 v[230:231], v[230:231], v[70:71]\n"
"v_pk_mul_f32 v[224:225], v[224:225], v[6:7] op_sel_hi:[1,0]\n"
"v_pk_mul_f32 v[226:227], v[226:227], v[6:7] op_sel_hi:[1,0]\n"
"v_pk_mul_f32 v[228:229], v[228:229], v[6:7] op_sel_hi:[1,0]\n"
"v_pk_mul_f32 v[230:231], v[230:231], v[6:7] op_sel_hi:[1,0]\n"
"v_cvt_pk_bf16_f32 v224, v224, v225\n"
"v_cvt_pk_bf16_f32 v225, v226, v227\n"
"v_cvt_pk_bf16_f32 v226, v228, v229\n"
"v_cvt_pk_bf16_f32 v227, v230, v231\n"
"s_waitcnt lgkmcnt(4)\n"
"v_pk_add_f32 v[232:233], v[232:233], v[72:73]\n"
"v_pk_add_f32 v[234:235], v[234:235], v[74:75]\n"
"v_pk_add_f32 v[236:237], v[236:237], v[76:77]\n"
"v_pk_add_f32 v[238:239], v[238:239], v[78:79]\n"
"v_pk_mul_f32 v[232:233], v[232:233], v[6:7] op_sel_hi:[1,0]\n"
"v_pk_mul_f32 v[234:235], v[234:235], v[6:7] op_sel_hi:[1,0]\n"
"v_pk_mul_f32 v[236:237], v[236:237], v[6:7] op_sel_hi:[1,0]\n"
"v_pk_mul_f32 v[238:239], v[238:239], v[6:7] op_sel_hi:[1,0]\n"
"v_cvt_pk_bf16_f32 v232, v232, v233\n"
"v_cvt_pk_bf16_f32 v233, v234, v235\n"
"v_cvt_pk_bf16_f32 v234, v236, v237\n"
"v_cvt_pk_bf16_f32 v235, v238, v239\n"
"s_waitcnt lgkmcnt(2)\n"
"v_pk_add_f32 v[240:241], v[240:241], v[80:81]\n"
"v_pk_add_f32 v[242:243], v[242:243], v[82:83]\n"
"v_pk_add_f32 v[244:245], v[244:245], v[84:85]\n"
"v_pk_add_f32 v[246:247], v[246:247], v[86:87]\n"
"v_pk_mul_f32 v[240:241], v[240:241], v[6:7] op_sel_hi:[1,0]\n"
"v_pk_mul_f32 v[242:243], v[242:243], v[6:7] op_sel_hi:[1,0]\n"
"v_pk_mul_f32 v[244:245], v[244:245], v[6:7] op_sel_hi:[1,0]\n"
"v_pk_mul_f32 v[246:247], v[246:247], v[6:7] op_sel_hi:[1,0]\n"
"v_cvt_pk_bf16_f32 v240, v240, v241\n"
"v_cvt_pk_bf16_f32 v241, v242, v243\n"
"v_cvt_pk_bf16_f32 v242, v244, v245\n"
"v_cvt_pk_bf16_f32 v243, v246, v247\n"
"s_waitcnt lgkmcnt(0)\n"
"v_pk_add_f32 v[248:249], v[248:249], v[88:89]\n"
"v_pk_add_f32 v[250:251], v[250:251], v[90:91]\n"
"v_pk_add_f32 v[252:253], v[252:253], v[92:93]\n"
"v_pk_add_f32 v[254:255], v[254:255], v[94:95]\n"
"v_pk_mul_f32 v[248:249], v[248:249], v[6:7] op_sel_hi:[1,0]\n"
"v_pk_mul_f32 v[250:251], v[250:251], v[6:7] op_sel_hi:[1,0]\n"
"v_pk_mul_f32 v[252:253], v[252:253], v[6:7] op_sel_hi:[1,0]\n"
"v_pk_mul_f32 v[254:255], v[254:255], v[6:7] op_sel_hi:[1,0]\n"
"v_cvt_pk_bf16_f32 v248, v248, v249\n"
"v_cvt_pk_bf16_f32 v249, v250, v251\n"
"v_cvt_pk_bf16_f32 v250, v252, v253\n"
"v_cvt_pk_bf16_f32 v251, v254, v255\n"
"s_getpc_b64 s[8:9]\n"
"PC_23_%=:\n"
"s_add_u32 s8, s8, TAIL0_%=-PC_23_%=\n"
"s_addc_u32 s9, s9, 0\n"
"s_mov_b32 s10, KEND_%=-TAIL0_%=\n"
"s_mov_b32 s11, 0x20000\n"
"s_lshr_b32 s98, s60, 3\n"
"s_lshl_b32 s98, s98, 2\n"
"s_add_u32 s98, s98, s61\n"
"s_lshl_b32 s98, s98, 7\n"
"v_and_b32 v35, 7, v0\n"
"v_lshlrev_b32 v35, 14, v35\n"
"buffer_load_dword v42, v35, s[8:11], s98 offen\n"
"s_cmp_lg_u32 s93, 0\n"
"s_cbranch_scc1 US_12_%=\n"
"US_12_%=r:\n"
"BLKB_%=:\n"
"s_sub_u32 s91, s71, 1\n"
"s_cbranch_scc1 N1_29_%=\n"
"v_mfma_f32_16x16x32_bf16 v[128:131], a[224:227], a[128:131], v[176:179]\n"
"s_add_u32 m0, s76, 0\n"
"ds_read_b128 a[192:195], v8 offset:16384\n"
"v_mfma_f32_16x16x32_bf16 v[128:131], a[228:231], a[132:135], v[128:131]\n"
"ds_read_b128 a[196:199], v8 offset:17408\n"
"v_mfma_f32_16x16x32_bf16 v[128:131], a[232:235], a[136:139], v[128:131]\n"
"ds_read_b128 a[200:203], v8 offset:18432\n"
"v_mfma_f32_16x16x32_bf16 v[128:131], a[236:239], a[140:143], v[128:131]\n"
"ds_read_b128 a[204:207], v8 offset:19456\n"
"v_mfma_f32_16x16x32_bf16 v[132:135], a[252:255], a[140:143], v[176:179]\n"
"ds_read_b128 a[208:211], v8 offset:20480\n"
"v_mfma_f32_16x16x32_bf16 v[132:135], a[248:251], a[136:139], v[132:135]\n"
"ds_read_b128 a[212:215], v8 offset:21504\n"
"v_mfma_f32_16x16x32_bf16 v[132:135], a[244:247], a[132:135], v[132:135]\n"
"ds_read_b128 a[216:219], v8 offset:22528\n"
"v_mfma_f32_16x16x32_bf16 v[132:135], a[240:243], a[128:131], v[132:135]\n"
"ds_read_b128 a[220:223], v8 offset:23552\n"
"v_xor_b32 v8, 0x8000, v8\n"
"v_mfma_f32_16x16x32_bf16 v[140:143], a[240:243], a[144:147], v[180:183]\n"
"v_exp_f32 v128, v128\n"
"v_mfma_f32_16x16x32_bf16 v[140:143], a[244:247], a[148:151], v[140:143]\n"
"v_exp_f32 v129, v129\n"
"v_mfma_f32_16x16x32_bf16 v[140:143], a[248:251], a[152:155], v[140:143]\n"
"v_exp_f32 v130, v130\n"
"v_mfma_f32_16x16x32_bf16 v[140:143], a[252:255], a[156:159], v[140:143]\n"
"v_exp_f32 v131, v131\n"
"v_mfma_f32_16x16x32_bf16 v[136:139], a[236:239], a[156:159], v[180:183]\n"
"v_exp_f32 v132, v132\n"
"v_mfma_f32_16x16x32_bf16 v[136:139], a[232:235], a[152:155], v[136:139]\n"
"v_exp_f32 v133, v133\n"
"v_mfma_f32_16x16x32_bf16 v[136:139], a[228:231], a[148:151], v[136:139]\n"
"buffer_store_dwordx4 v[192:195], v1, s[44:47], s82 offen offset:0 sc1\n"
"v_mfma_f32_16x16x32_bf16 v[136:139], a[224:227], a[144:147], v[136:139]\n"
"buffer_store_dwordx4 v[200:203], v1, s[44:47], s82 offen offset:1024 sc1\n"
"v_mfma_f32_16x16x32_bf16 v[144:147], a[224:227], a[160:163], v[184:187]\n"
"buffer_store_dwordx4 v[208:211], v1, s[44:47], s82 offen offset:2048 sc1\n"
"v_mfma_f32_16x16x32_bf16 v[144:147], a[228:231], a[164:167], v[144:147]\n"
"v_exp_f32 v134, v134\n"
"v_mfma_f32_16x16x32_bf16 v[144:147], a[232:235], a[168:171], v[144:147]\n"
"buffer_store_dwordx4 v[216:219], v1, s[44:47], s82 offen offset:3072 sc1\n"
"v_mfma_f32_16x16x32_bf16 v[144:147], a[236:239], a[172:175], v[144:147]\n"
"buffer_store_dwordx4 v[224:227], v1, s[44:47], s83 offen offset:0 sc1\n"
"v_mfma_f32_16x16x32_bf16 v[148:151], a[252:255], a[172:175], v[184:187]\n"
"v_exp_f32 v135, v135\n"
"v_mfma_f32_16x16x32_bf16 v[148:151], a[248:251], a[168:171], v[148:151]\n"
"buffer_store_dwordx4 v[232:235], v1, s[44:47], s83 offen offset:1024 sc1\n"
"v_mfma_f32_16x16x32_bf16 v[148:151], a[244:247], a[164:167], v[148:151]\n"
"buffer_store_dwordx4 v[240:243], v1, s[44:47], s83 offen offset:2048 sc1\n"
"v_mfma_f32_16x16x32_bf16 v[148:151], a[240:243], a[160:163], v[148:151]\n"
"buffer_store_dwordx4 v[248:251], v1, s[44:47], s83 offen offset:3072 sc1\n"
"v_mfma_f32_16x16x32_bf16 v[156:159], a[240:243], a[176:179], v[188:191]\n"
"v_exp_f32 v136, v136\n"
"v_mfma_f32_16x16x32_bf16 v[156:159], a[244:247], a[180:183], v[156:159]\n"
"buffer_load_dwordx4 v24, s[20:23], s78 offen offset:0 sc1 lds\n"
"v_mfma_f32_16x16x32_bf16 v[156:159], a[248:251], a[184:187], v[156:159]\n"
"buffer_load_dwordx4 v25, s[20:23], s78 offen offset:1024 sc1 lds\n"
"v_mfma_f32_16x16x32_bf16 v[156:159], a[252:255], a[188:191], v[156:159]\n"
"v_exp_f32 v137, v137\n"
"v_mfma_f32_16x16x32_bf16 v[152:155], a[236:239], a[188:191], v[188:191]\n"
"buffer_load_dwordx4 v26, s[20:23], s78 offen offset:2048 sc1 lds\n"
"v_mfma_f32_16x16x32_bf16 v[152:155], a[232:235], a[184:187], v[152:155]\n"
"buffer_load_dwordx4 v27, s[20:23], s78 offen offset:3072 sc1 lds\n"
"v_mfma_f32_16x16x32_bf16 v[152:155], a[228:231], a[180:183], v[152:155]\n"
"s_waitcnt vmcnt(24)\n"
"v_exp_f32 v138, v138\n"
"v_mfma_f32_16x16x32_bf16 v[152:155], a[224:227], a[176:179], v[152:155]\n"
"s_waitcnt lgkmcnt(0)\n"
"s_barrier\n"
"v_exp_f32 v139, v139\n"
"v_cvt_pk_bf16_f32 v128, v128, v129\n"
"v_cvt_pk_bf16_f32 v129, v130, v131\n"
"v_cvt_pk_bf16_f32 v130, v132, v133\n"
"v_cvt_pk_bf16_f32 v131, v134, v135\n"
"v_cvt_pk_bf16_f32 v136, v136, v137\n"
"v_cvt_pk_bf16_f32 v137, v138, v139\n"
"v_exp_f32 v140, v140\n"
"v_exp_f32 v141, v141\n"
"s_nop 0\n"
"v_cvt_pk_bf16_f32 v138, v140, v141\n"
"v_mfma_f32_16x16x32_bf16 v[96:99], a[192:195], a[128:131], v[176:179]\n"
"s_add_u32 m0, s77, 0\n"
"ds_read_b128 a[224:227], v9 offset:0\n"
"v_mfma_f32_16x16x32_bf16 v[96:99], a[196:199], a[132:135], v[96:99]\n"
"v_exp_f32 v142, v142\n"
"v_mfma_f32_16x16x32_bf16 v[96:99], a[200:203], a[136:139], v[96:99]\n"
"ds_read_b128 a[228:231], v9 offset:1024\n"
"v_mfma_f32_16x16x32_bf16 v[96:99], a[204:207], a[140:143], v[96:99]\n"
"v_exp_f32 v143, v143\n"
"v_mfma_f32_16x16x32_bf16 v[100:103], a[220:223], a[140:143], v[176:179]\n"
"ds_read_b128 a[232:235], v9 offset:2048\n"
"v_cvt_pk_bf16_f32 v139, v142, v143\n"
"v_mfma_f32_16x16x32_bf16 v[100:103], a[216:219], a[136:139], v[100:103]\n"
"v_exp_f32 v144, v144\n"
"v_mfma_f32_16x16x32_bf16 v[100:103], a[212:215], a[132:135], v[100:103]\n"
"v_exp_f32 v145, v145\n"
"v_mfma_f32_16x16x32_bf16 v[100:103], a[208:211], a[128:131], v[100:103]\n"
"ds_read_b128 a[236:239], v9 offset:3072\n"
"v_cvt_pk_bf16_f32 v144, v144, v145\n"
"v_mfma_f32_16x16x32_bf16 v[108:111], a[208:211], a[144:147], v[180:183]\n"
"v_exp_f32 v146, v146\n"
"v_mfma_f32_16x16x32_bf16 v[108:111], a[212:215], a[148:151], v[108:111]\n"
"ds_read_b128 a[240:243], v9 offset:4096\n"
"v_mfma_f32_16x16x32_bf16 v[108:111], a[216:219], a[152:155], v[108:111]\n"
"v_exp_f32 v147, v147\n"
"v_mfma_f32_16x16x32_bf16 v[108:111], a[220:223], a[156:159], v[108:111]\n"
"ds_read_b128 a[244:247], v9 offset:5120\n"
"v_cvt_pk_bf16_f32 v145, v146, v147\n"
"v_mfma_f32_16x16x32_bf16 v[104:107], a[204:207], a[156:159], v[180:183]\n"
"v_exp_f32 v148, v148\n"
"v_mfma_f32_16x16x32_bf16 v[104:107], a[200:203], a[152:155], v[104:107]\n"
"v_exp_f32 v149, v149\n"
"v_mfma_f32_16x16x32_bf16 v[104:107], a[196:199], a[148:151], v[104:107]\n"
"ds_read_b128 a[248:251], v9 offset:6144\n"
"v_cvt_pk_bf16_f32 v146, v148, v149\n"
"v_mfma_f32_16x16x32_bf16 v[104:107], a[192:195], a[144:147], v[104:107]\n"
"v_exp_f32 v150, v150\n"
"v_mfma_f32_16x16x32_bf16 v[112:115], a[192:195], a[160:163], v[184:187]\n"
"ds_read_b128 a[252:255], v9 offset:7168\n"
"v_mfma_f32_16x16x32_bf16 v[112:115], a[196:199], a[164:167], v[112:115]\n"
"v_exp_f32 v151, v151\n"
"v_mfma_f32_16x16x32_bf16 v[112:115], a[200:203], a[168:171], v[112:115]\n"
"v_exp_f32 v152, v152\n"
"v_mfma_f32_16x16x32_bf16 v[112:115], a[204:207], a[172:175], v[112:115]\n"
"v_exp_f32 v153, v153\n"
"v_mfma_f32_16x16x32_bf16 v[116:119], a[220:223], a[172:175], v[184:187]\n"
"v_cvt_pk_bf16_f32 v147, v150, v151\n"
"v_cvt_pk_bf16_f32 v152, v152, v153\n"
"v_mfma_f32_16x16x32_bf16 v[116:119], a[216:219], a[168:171], v[116:119]\n"
"v_exp_f32 v154, v154\n"
"v_mfma_f32_16x16x32_bf16 v[116:119], a[212:215], a[164:167], v[116:119]\n"
"v_exp_f32 v155, v155\n"
"v_mfma_f32_16x16x32_bf16 v[116:119], a[208:211], a[160:163], v[116:119]\n"
"v_exp_f32 v156, v156\n"
"v_mfma_f32_16x16x32_bf16 v[124:127], a[208:211], a[176:179], v[188:191]\n"
"v_exp_f32 v157, v157\n"
"v_mfma_f32_16x16x32_bf16 v[124:127], a[212:215], a[180:183], v[124:127]\n"
"v_exp_f32 v158, v158\n"
"v_mfma_f32_16x16x32_bf16 v[124:127], a[216:219], a[184:187], v[124:127]\n"
"v_exp_f32 v159, v159\n"
"v_mfma_f32_16x16x32_bf16 v[124:127], a[220:223], a[188:191], v[124:127]\n"
"v_cvt_pk_bf16_f32 v153, v154, v155\n"
"v_cvt_pk_bf16_f32 v154, v156, v157\n"
"v_mfma_f32_16x16x32_bf16 v[120:123], a[204:207], a[188:191], v[188:191]\n"
"v_exp_f32 v96, v96\n"
"v_mfma_f32_16x16x32_bf16 v[120:123], a[200:203], a[184:187], v[120:123]\n"
"v_exp_f32 v97, v97\n"
"v_mfma_f32_16x16x32_bf16 v[120:123], a[196:199], a[180:183], v[120:123]\n"
"buffer_load_dwordx4 v24, s[24:27], s80 offen offset:0 sc1 lds\n"
"v_mfma_f32_16x16x32_bf16 v[120:123], a[192:195], a[176:179], v[120:123]\n"
"buffer_load_dwordx4 v25, s[24:27], s80 offen offset:1024 sc1 lds\n"
"s_waitcnt lgkmcnt(7)\n"
"v_mfma_f32_16x16x32_bf16 v[192:195], a[224:227], v[128:131], 0\n"
"s_waitcnt lgkmcnt(6)\n"
"ds_read_b128 a[192:195], v8 offset:0\n"
"v_cvt_pk_bf16_f32 v155, v158, v159\n"
"v_mfma_f32_16x16x32_bf16 v[196:199], a[228:231], v[128:131], 0\n"
"buffer_load_dwordx4 v26, s[24:27], s80 offen offset:2048 sc1 lds\n"
"s_waitcnt lgkmcnt(6)\n"
"v_mfma_f32_16x16x32_bf16 v[200:203], a[232:235], v[128:131], 0\n"
"s_waitcnt lgkmcnt(5)\n"
"ds_read_b128 a[196:199], v8 offset:1024\n"
"v_cvt_pk_bf16_f32 v96, v96, v97\n"
"v_mfma_f32_16x16x32_bf16 v[204:207], a[236:239], v[128:131], 0\n"
"buffer_load_dwordx4 v27, s[24:27], s80 offen offset:3072 sc1 lds\n"
"v_mfma_f32_16x16x32_bf16 v[160:163], v[60:63], v[128:131], 0\n"
"s_add_u32 m0, s77, 16384\n"
"s_add_u32 s80, s78, s5\n"
"s_waitcnt lgkmcnt(5)\n"
"v_mfma_f32_16x16x32_bf16 v[208:211], a[240:243], v[128:131], 0\n"
"s_waitcnt lgkmcnt(4)\n"
"ds_read_b128 a[200:203], v8 offset:2048\n"
"v_mfma_f32_16x16x32_bf16 v[212:215], a[244:247], v[128:131], 0\n"
"buffer_load_dwordx4 v24, s[24:27], s81 offen offset:0 sc1 lds\n"
"s_waitcnt lgkmcnt(4)\n"
"v_mfma_f32_16x16x32_bf16 v[216:219], a[248:251], v[128:131], 0\n"
"buffer_load_dwordx4 v25, s[24:27], s81 offen offset:1024 sc1 lds\n"
"s_waitcnt lgkmcnt(3)\n"
"v_mfma_f32_16x16x32_bf16 v[220:223], a[252:255], v[128:131], 0\n"
"ds_read_b128 a[204:207], v8 offset:3072\n"
"v_mfma_f32_16x16x32_bf16 v[252:255], a[252:255], v[136:139], 0\n"
"buffer_load_dwordx4 v26, s[24:27], s81 offen offset:2048 sc1 lds\n"
"v_mfma_f32_16x16x32_bf16 v[248:251], a[248:251], v[136:139], 0\n"
"ds_read_b128 a[208:211], v8 offset:4096\n"
"v_mfma_f32_16x16x32_bf16 v[244:247], a[244:247], v[136:139], 0\n"
"buffer_load_dwordx4 v27, s[24:27], s81 offen offset:3072 sc1 lds\n"
"v_mfma_f32_16x16x32_bf16 v[240:243], a[240:243], v[136:139], 0\n"
"s_add_u32 s81, s79, s5\n"
"s_xor_b32 s77, s77, 0x8000\n"
"s_add_u32 m0, s76, 16384\n"
"v_mfma_f32_16x16x32_bf16 v[164:167], v[60:63], v[136:139], 0\n"
"buffer_load_dwordx4 v24, s[20:23], s79 offen offset:0 sc1 lds\n"
"v_mfma_f32_16x16x32_bf16 v[236:239], a[236:239], v[136:139], 0\n"
"s_mov_b64 s[24:25], s[20:21]\n"
"s_mov_b32 s26, s22\n"
"ds_read_b128 a[212:215], v8 offset:5120\n"
"v_mfma_f32_16x16x32_bf16 v[232:235], a[232:235], v[136:139], 0\n"
"buffer_load_dwordx4 v25, s[20:23], s79 offen offset:1024 sc1 lds\n"
"v_mfma_f32_16x16x32_bf16 v[228:231], a[228:231], v[136:139], 0\n"
"s_add_u32 s78, s78, s92\n"
"ds_read_b128 a[216:219], v8 offset:6144\n"
"v_mfma_f32_16x16x32_bf16 v[224:227], a[224:227], v[136:139], 0\n"
"buffer_load_dwordx4 v26, s[20:23], s79 offen offset:2048 sc1 lds\n"
"v_mfma_f32_16x16x32_bf16 a[0:3], a[224:227], v[144:147], 0\n"
"buffer_load_dwordx4 v27, s[20:23], s79 offen offset:3072 sc1 lds\n"
"v_mfma_f32_16x16x32_bf16 a[4:7], a[228:231], v[144:147], 0\n"
"s_add_u32 s79, s79, s92\n"
"s_xor_b32 s76, s76, 0x8000\n"
"ds_read_b128 a[220:223], v8 offset:7168\n"
"v_mfma_f32_16x16x32_bf16 a[8:11], a[232:235], v[144:147], 0\n"
"s_sub_u32 s74, s74, 1\n"
"v_exp_f32 v98, v98\n"
"v_mfma_f32_16x16x32_bf16 a[12:15], a[236:239], v[144:147], 0\n"
"s_cselect_b64 s[78:79], s[72:73], s[78:79]\n"
"v_exp_f32 v99, v99\n"
"v_mfma_f32_16x16x32_bf16 v[168:171], v[60:63], v[144:147], 0\n"
"s_cselect_b64 s[72:73], s[16:17], s[72:73]\n"
"v_exp_f32 v100, v100\n"
"v_mfma_f32_16x16x32_bf16 a[16:19], a[240:243], v[144:147], 0\n"
"s_cselect_b32 s74, s75, s74\n"
"v_exp_f32 v101, v101\n"
"v_mfma_f32_16x16x32_bf16 a[20:23], a[244:247], v[144:147], 0\n"
"s_cselect_b32 s75, 64, s75\n"
"v_exp_f32 v102, v102\n"
"v_mfma_f32_16x16x32_bf16 a[24:27], a[248:251], v[144:147], 0\n"
"s_cmp_gt_u32 s74, 32\n"
"v_exp_f32 v103, v103\n"
"v_mfma_f32_16x16x32_bf16 a[28:31], a[252:255], v[144:147], 0\n"
"s_cselect_b64 s[20:21], s[32:33], s[20:21]\n"
"v_exp_f32 v104, v104\n"
"v_mfma_f32_16x16x32_bf16 a[60:63], a[252:255], v[152:155], 0\n"
"s_cselect_b32 s22, s34, s22\n"
"v_exp_f32 v105, v105\n"
"v_mfma_f32_16x16x32_bf16 a[56:59], a[248:251], v[152:155], 0\n"
"s_cselect_b32 s92, 262144, s92\n"
"v_exp_f32 v106, v106\n"
"v_mfma_f32_16x16x32_bf16 a[52:55], a[244:247], v[152:155], 0\n"
"s_cselect_b32 s5, 524288, s5\n"
"v_exp_f32 v107, v107\n"
"v_mfma_f32_16x16x32_bf16 a[48:51], a[240:243], v[152:155], 0\n"
"v_exp_f32 v108, v108\n"
"v_mfma_f32_16x16x32_bf16 v[172:175], v[60:63], v[152:155], 0\n"
"v_exp_f32 v109, v109\n"
"v_mfma_f32_16x16x32_bf16 a[44:47], a[236:239], v[152:155], 0\n"
"v_exp_f32 v110, v110\n"
"v_mfma_f32_16x16x32_bf16 a[40:43], a[232:235], v[152:155], 0\n"
"v_exp_f32 v111, v111\n"
"v_mfma_f32_16x16x32_bf16 a[36:39], a[228:231], v[152:155], 0\n"
"s_waitcnt vmcnt(16)\n"
"v_exp_f32 v112, v112\n"
"v_mfma_f32_16x16x32_bf16 a[32:35], a[224:227], v[152:155], 0\n"
"s_waitcnt lgkmcnt(0)\n"
"s_barrier\n"
"v_exp_f32 v113, v113\n"
"v_cvt_pk_bf16_f32 v97, v98, v99\n"
"v_cvt_pk_bf16_f32 v98, v100, v101\n"
"v_cvt_pk_bf16_f32 v99, v102, v103\n"
"v_cvt_pk_bf16_f32 v104, v104, v105\n"
"v_cvt_pk_bf16_f32 v105, v106, v107\n"
"v_cvt_pk_bf16_f32 v106, v108, v109\n"
"v_cvt_pk_bf16_f32 v107, v110, v111\n"
"v_cvt_pk_bf16_f32 v112, v112, v113\n"
"v_exp_f32 v114, v114\n"
"v_exp_f32 v115, v115\n"
"s_nop 0\n"
"v_cvt_pk_bf16_f32 v113, v114, v115\n"
"v_exp_f32 v116, v116\n"
"v_exp_f32 v117, v117\n"
"s_nop 0\n"
"v_cvt_pk_bf16_f32 v114, v116, v117\n"
"s_mov_b32 exec_lo, s58\n"
"s_mov_b32 exec_hi, 0\n"
"buffer_store_dwordx2 v[10:11], off, s[44:47], s85 sc1\n"
"s_mov_b64 exec, -1\n"
"s_mov_b32 s58, 0\n"
"s_lshl_b32 s99, s60, 7\n"
"s_add_u32 s99, s99, 65536\n"
"v_mov_b32 v28, s64\n"
"v_mov_b32 v29, s65\n"
"s_cmp_eq_u32 s61, 0\n"
"s_cselect_b64 exec, 1, 0\n"
"buffer_store_dwordx2 v[28:29], off, s[44:47], s99 sc1\n"
"s_mov_b64 exec, -1\n"
"s_sub_u32 s91, s91, 1\n"
"s_cbranch_scc1 DG_25_%=\n"
"s_nop 0\n"
"ATL_24_%=:\n"
"v_mfma_f32_16x16x32_bf16 v[128:131], a[192:195], a[128:131], v[176:179]\n"
"s_add_u32 m0, s77, 0\n"
"ds_read_b128 a[224:227], v9 offset:16384\n"
"v_mfma_f32_16x16x32_bf16 v[128:131], a[196:199], a[132:135], v[128:131]\n"
"v_exp_f32 v118, v118\n"
"v_mfma_f32_16x16x32_bf16 v[128:131], a[200:203], a[136:139], v[128:131]\n"
"ds_read_b128 a[228:231], v9 offset:17408\n"
"v_mfma_f32_16x16x32_bf16 v[128:131], a[204:207], a[140:143], v[128:131]\n"
"buffer_load_dwordx2 v[20:21], v18, s[44:47], 0 offen sc1\n"
"v_mfma_f32_16x16x32_bf16 v[132:135], a[220:223], a[140:143], v[176:179]\n"
"ds_read_b128 a[232:235], v9 offset:18432\n"
"v_mfma_f32_16x16x32_bf16 v[132:135], a[216:219], a[136:139], v[132:135]\n"
"v_exp_f32 v119, v119\n"
"v_mfma_f32_16x16x32_bf16 v[132:135], a[212:215], a[132:135], v[132:135]\n"
"v_exp_f32 v120, v120\n"
"v_mfma_f32_16x16x32_bf16 v[132:135], a[208:211], a[128:131], v[132:135]\n"
"ds_read_b128 a[236:239], v9 offset:19456\n"
"v_cvt_pk_bf16_f32 v115, v118, v119\n"
"v_mfma_f32_16x16x32_bf16 v[140:143], a[208:211], a[144:147], v[180:183]\n"
"buffer_load_dwordx4 v24, s[24:27], s80 offen offset:0 sc1 lds\n"
"v_mfma_f32_16x16x32_bf16 v[140:143], a[212:215], a[148:151], v[140:143]\n"
"ds_read_b128 a[240:243], v9 offset:20480\n"
"v_mfma_f32_16x16x32_bf16 v[140:143], a[216:219], a[152:155], v[140:143]\n"
"v_exp_f32 v121, v121\n"
"v_mfma_f32_16x16x32_bf16 v[140:143], a[220:223], a[156:159], v[140:143]\n"
"ds_read_b128 a[244:247], v9 offset:21504\n"
"v_cvt_pk_bf16_f32 v120, v120, v121\n"
"v_mfma_f32_16x16x32_bf16 v[136:139], a[204:207], a[156:159], v[180:183]\n"
"v_exp_f32 v122, v122\n"
"v_mfma_f32_16x16x32_bf16 v[136:139], a[200:203], a[152:155], v[136:139]\n"
"v_exp_f32 v123, v123\n"
"v_mfma_f32_16x16x32_bf16 v[136:139], a[196:199], a[148:151], v[136:139]\n"
"ds_read_b128 a[248:251], v9 offset:22528\n"
"v_cvt_pk_bf16_f32 v121, v122, v123\n"
"v_mfma_f32_16x16x32_bf16 v[136:139], a[192:195], a[144:147], v[136:139]\n"
"buffer_load_dwordx4 v25, s[24:27], s80 offen offset:1024 sc1 lds\n"
"v_mfma_f32_16x16x32_bf16 v[144:147], a[192:195], a[160:163], v[184:187]\n"
"ds_read_b128 a[252:255], v9 offset:23552\n"
"v_xor_b32 v9, 0x8000, v9\n"
"v_mfma_f32_16x16x32_bf16 v[144:147], a[196:199], a[164:167], v[144:147]\n"
"v_exp_f32 v124, v124\n"
"v_mfma_f32_16x16x32_bf16 v[144:147], a[200:203], a[168:171], v[144:147]\n"
"v_exp_f32 v125, v125\n"
"v_mfma_f32_16x16x32_bf16 v[144:147], a[204:207], a[172:175], v[144:147]\n"
"v_exp_f32 v126, v126\n"
"v_mfma_f32_16x16x32_bf16 v[148:151], a[220:223], a[172:175], v[184:187]\n"
"v_exp_f32 v127, v127\n"
"v_mfma_f32_16x16x32_bf16 v[148:151], a[216:219], a[168:171], v[148:151]\n"
"buffer_load_dwordx4 v26, s[24:27], s80 offen offset:2048 sc1 lds\n"
"v_mfma_f32_16x16x32_bf16 v[148:151], a[212:215], a[164:167], v[148:151]\n"
"v_cvt_pk_bf16_f32 v122, v124, v125\n"
"v_cvt_pk_bf16_f32 v123, v126, v127\n"
"v_mfma_f32_16x16x32_bf16 v[148:151], a[208:211], a[160:163], v[148:151]\n"
"v_exp_f32 v128, v128\n"
"v_mfma_f32_16x16x32_bf16 v[156:159], a[208:211], a[176:179], v[188:191]\n"
"v_exp_f32 v129, v129\n"
"v_mfma_f32_16x16x32_bf16 v[156:159], a[212:215], a[180:183], v[156:159]\n"
"v_exp_f32 v130, v130\n"
"v_mfma_f32_16x16x32_bf16 v[156:159], a[216:219], a[184:187], v[156:159]\n"
"v_exp_f32 v131, v131\n"
"v_mfma_f32_16x16x32_bf16 v[156:159], a[220:223], a[188:191], v[156:159]\n"
"v_exp_f32 v132, v132\n"
"v_mfma_f32_16x16x32_bf16 v[152:155], a[204:207], a[188:191], v[188:191]\n"
"buffer_load_dwordx4 v27, s[24:27], s80 offen offset:3072 sc1 lds\n"
"v_mfma_f32_16x16x32_bf16 v[152:155], a[200:203], a[184:187], v[152:155]\n"
"s_add_u32 m0, s76, 0\n"
"v_exp_f32 v133, v133\n"
"v_mfma_f32_16x16x32_bf16 v[152:155], a[196:199], a[180:183], v[152:155]\n"
"v_exp_f32 v134, v134\n"
"v_mfma_f32_16x16x32_bf16 v[152:155], a[192:195], a[176:179], v[152:155]\n"
"s_waitcnt lgkmcnt(7)\n"
"v_exp_f32 v135, v135\n"
"v_mfma_f32_16x16x32_bf16 v[192:195], a[224:227], v[96:99], v[192:195]\n"
"s_waitcnt lgkmcnt(6)\n"
"ds_read_b128 a[192:195], v8 offset:16384\n"
"v_cvt_pk_bf16_f32 v128, v128, v129\n"
"v_mfma_f32_16x16x32_bf16 v[196:199], a[228:231], v[96:99], v[196:199]\n"
"s_waitcnt lgkmcnt(6)\n"
"v_exp_f32 v136, v136\n"
"v_mfma_f32_16x16x32_bf16 v[200:203], a[232:235], v[96:99], v[200:203]\n"
"s_waitcnt lgkmcnt(5)\n"
"ds_read_b128 a[196:199], v8 offset:17408\n"
"v_cvt_pk_bf16_f32 v129, v130, v131\n"
"v_mfma_f32_16x16x32_bf16 v[204:207], a[236:239], v[96:99], v[204:207]\n"
"buffer_load_dwordx4 v24, s[20:23], s78 offen offset:0 sc1 lds\n"
"v_mfma_f32_16x16x32_bf16 v[160:163], v[60:63], v[96:99], v[160:163]\n"
"s_waitcnt lgkmcnt(5)\n"
"v_exp_f32 v137, v137\n"
"v_mfma_f32_16x16x32_bf16 v[208:211], a[240:243], v[96:99], v[208:211]\n"
"s_waitcnt lgkmcnt(4)\n"
"ds_read_b128 a[200:203], v8 offset:18432\n"
"v_cvt_pk_bf16_f32 v130, v132, v133\n"
"v_mfma_f32_16x16x32_bf16 v[212:215], a[244:247], v[96:99], v[212:215]\n"
"s_waitcnt lgkmcnt(4)\n"
"v_exp_f32 v138, v138\n"
"v_mfma_f32_16x16x32_bf16 v[216:219], a[248:251], v[96:99], v[216:219]\n"
"s_waitcnt lgkmcnt(3)\n"
"ds_read_b128 a[204:207], v8 offset:19456\n"
"v_cvt_pk_bf16_f32 v131, v134, v135\n"
"v_mfma_f32_16x16x32_bf16 v[220:223], a[252:255], v[96:99], v[220:223]\n"
"v_exp_f32 v139, v139\n"
"v_mfma_f32_16x16x32_bf16 v[252:255], a[252:255], v[104:107], v[252:255]\n"
"buffer_load_dwordx4 v25, s[20:23], s78 offen offset:1024 sc1 lds\n"
"v_mfma_f32_16x16x32_bf16 v[248:251], a[248:251], v[104:107], v[248:251]\n"
"ds_read_b128 a[208:211], v8 offset:20480\n"
"v_cvt_pk_bf16_f32 v136, v136, v137\n"
"v_mfma_f32_16x16x32_bf16 v[244:247], a[244:247], v[104:107], v[244:247]\n"
"v_exp_f32 v140, v140\n"
"v_mfma_f32_16x16x32_bf16 v[240:243], a[240:243], v[104:107], v[240:243]\n"
"v_exp_f32 v141, v141\n"
"v_mfma_f32_16x16x32_bf16 v[164:167], v[60:63], v[104:107], v[164:167]\n"
"ds_read_b128 a[212:215], v8 offset:21504\n"
"v_cvt_pk_bf16_f32 v137, v138, v139\n"
"v_mfma_f32_16x16x32_bf16 v[236:239], a[236:239], v[104:107], v[236:239]\n"
"v_exp_f32 v142, v142\n"
"v_mfma_f32_16x16x32_bf16 v[232:235], a[232:235], v[104:107], v[232:235]\n"
"ds_read_b128 a[216:219], v8 offset:22528\n"
"v_cvt_pk_bf16_f32 v138, v140, v141\n"
"v_mfma_f32_16x16x32_bf16 v[228:231], a[228:231], v[104:107], v[228:231]\n"
"buffer_load_dwordx4 v26, s[20:23], s78 offen offset:2048 sc1 lds\n"
"v_mfma_f32_16x16x32_bf16 v[224:227], a[224:227], v[104:107], v[224:227]\n"
"v_exp_f32 v143, v143\n"
"v_mfma_f32_16x16x32_bf16 a[0:3], a[224:227], v[112:115], a[0:3]\n"
"ds_read_b128 a[220:223], v8 offset:23552\n"
"v_xor_b32 v8, 0x8000, v8\n"
"v_mfma_f32_16x16x32_bf16 a[4:7], a[228:231], v[112:115], a[4:7]\n"
"v_exp_f32 v144, v144\n"
"v_mfma_f32_16x16x32_bf16 a[8:11], a[232:235], v[112:115], a[8:11]\n"
"v_exp_f32 v145, v145\n"
"v_mfma_f32_16x16x32_bf16 a[12:15], a[236:239], v[112:115], a[12:15]\n"
"v_exp_f32 v146, v146\n"
"v_mfma_f32_16x16x32_bf16 v[168:171], v[60:63], v[112:115], v[168:171]\n"
"buffer_load_dwordx4 v27, s[20:23], s78 offen offset:3072 sc1 lds\n"
"v_mfma_f32_16x16x32_bf16 a[16:19], a[240:243], v[112:115], a[16:19]\n"
"v_exp_f32 v147, v147\n"
"v_mfma_f32_16x16x32_bf16 a[20:23], a[244:247], v[112:115], a[20:23]\n"
"v_exp_f32 v148, v148\n"
"v_mfma_f32_16x16x32_bf16 a[24:27], a[248:251], v[112:115], a[24:27]\n"
"v_exp_f32 v149, v149\n"
"v_mfma_f32_16x16x32_bf16 a[28:31], a[252:255], v[112:115], a[28:31]\n"
"v_cvt_pk_bf16_f32 v139, v142, v143\n"
"v_cvt_pk_bf16_f32 v144, v144, v145\n"
"v_mfma_f32_16x16x32_bf16 a[60:63], a[252:255], v[120:123], a[60:63]\n"
"v_cvt_pk_bf16_f32 v145, v146, v147\n"
"v_cvt_pk_bf16_f32 v146, v148, v149\n"
"v_mfma_f32_16x16x32_bf16 a[56:59], a[248:251], v[120:123], a[56:59]\n"
"v_mfma_f32_16x16x32_bf16 a[52:55], a[244:247], v[120:123], a[52:55]\n"
"v_mfma_f32_16x16x32_bf16 a[48:51], a[240:243], v[120:123], a[48:51]\n"
"v_mfma_f32_16x16x32_bf16 v[172:175], v[60:63], v[120:123], v[172:175]\n"
"v_mfma_f32_16x16x32_bf16 a[44:47], a[236:239], v[120:123], a[44:47]\n"
"v_mfma_f32_16x16x32_bf16 a[40:43], a[232:235], v[120:123], a[40:43]\n"
"v_mfma_f32_16x16x32_bf16 a[36:39], a[228:231], v[120:123], a[36:39]\n"
"s_waitcnt vmcnt(17) lgkmcnt(0)\n"
"s_barrier\n"
"v_mfma_f32_16x16x32_bf16 a[32:35], a[224:227], v[120:123], a[32:35]\n"
"v_mfma_f32_16x16x32_bf16 v[96:99], a[192:195], a[128:131], v[176:179]\n"
"s_add_u32 m0, s77, 16384\n"
"s_add_u32 s80, s78, s5\n"
"ds_read_b128 a[224:227], v9 offset:0\n"
"v_mfma_f32_16x16x32_bf16 v[96:99], a[196:199], a[132:135], v[96:99]\n"
"v_exp_f32 v150, v150\n"
"v_mfma_f32_16x16x32_bf16 v[96:99], a[200:203], a[136:139], v[96:99]\n"
"ds_read_b128 a[228:231], v9 offset:1024\n"
"v_mfma_f32_16x16x32_bf16 v[96:99], a[204:207], a[140:143], v[96:99]\n"
"buffer_load_dwordx4 v24, s[24:27], s81 offen offset:0 sc1 lds\n"
"v_mfma_f32_16x16x32_bf16 v[100:103], a[220:223], a[140:143], v[176:179]\n"
"ds_read_b128 a[232:235], v9 offset:2048\n"
"v_mfma_f32_16x16x32_bf16 v[100:103], a[216:219], a[136:139], v[100:103]\n"
"v_exp_f32 v151, v151\n"
"v_mfma_f32_16x16x32_bf16 v[100:103], a[212:215], a[132:135], v[100:103]\n"
"v_exp_f32 v152, v152\n"
"v_mfma_f32_16x16x32_bf16 v[100:103], a[208:211], a[128:131], v[100:103]\n"
"ds_read_b128 a[236:239], v9 offset:3072\n"
"v_cvt_pk_bf16_f32 v147, v150, v151\n"
"v_mfma_f32_16x16x32_bf16 v[108:111], a[208:211], a[144:147], v[180:183]\n"
"buffer_load_dwordx4 v25, s[24:27], s81 offen offset:1024 sc1 lds\n"
"v_mfma_f32_16x16x32_bf16 v[108:111], a[212:215], a[148:151], v[108:111]\n"
"ds_read_b128 a[240:243], v9 offset:4096\n"
"v_mfma_f32_16x16x32_bf16 v[108:111], a[216:219], a[152:155], v[108:111]\n"
"v_exp_f32 v153, v153\n"
"v_mfma_f32_16x16x32_bf16 v[108:111], a[220:223], a[156:159], v[108:111]\n"
"ds_read_b128 a[244:247], v9 offset:5120\n"
"v_cvt_pk_bf16_f32 v152, v152, v153\n"
"s_nop 0\n"
"PG11_%=:\n"
"s_cbranch_execz PADRET_%=\n"
"v_mfma_f32_16x16x32_bf16 v[104:107], a[204:207], a[156:159], v[180:183]\n"
"v_exp_f32 v154, v154\n"
"v_mfma_f32_16x16x32_bf16 v[104:107], a[200:203], a[152:155], v[104:107]\n"
"v_exp_f32 v155, v155\n"
"v_mfma_f32_16x16x32_bf16 v[104:107], a[196:199], a[148:151], v[104:107]\n"
"ds_read_b128 a[248:251], v9 offset:6144\n"
"v_cvt_pk_bf16_f32 v153, v154, v155\n"
"v_mfma_f32_16x16x32_bf16 v[104:107], a[192:195], a[144:147], v[104:107]\n"
"buffer_load_dwordx4 v26, s[24:27], s81 offen offset:2048 sc1 lds\n"
"v_mfma_f32_16x16x32_bf16 v[112:115], a[192:195], a[160:163], v[184:187]\n"
"ds_read_b128 a[252:255], v9 offset:7168\n"
"v_mfma_f32_16x16x32_bf16 v[112:115], a[196:199], a[164:167], v[112:115]\n"
"v_exp_f32 v156, v156\n"
"v_mfma_f32_16x16x32_bf16 v[112:115], a[200:203], a[168:171], v[112:115]\n"
"v_exp_f32 v157, v157\n"
"v_mfma_f32_16x16x32_bf16 v[112:115], a[204:207], a[172:175], v[112:115]\n"
"v_exp_f32 v158, v158\n"
"v_mfma_f32_16x16x32_bf16 v[116:119], a[220:223], a[172:175], v[184:187]\n"
"buffer_load_dwordx4 v27, s[24:27], s81 offen offset:3072 sc1 lds\n"
"v_mfma_f32_16x16x32_bf16 v[116:119], a[216:219], a[168:171], v[116:119]\n"
"s_add_u32 s81, s79, s5\n"
"s_xor_b32 s77, s77, 0x8000\n"
"s_add_u32 m0, s76, 16384\n"
"v_mfma_f32_16x16x32_bf16 v[116:119], a[212:215], a[164:167], v[116:119]\n"
"s_mov_b64 s[24:25], s[20:21]\n"
"v_exp_f32 v159, v159\n"
"v_mfma_f32_16x16x32_bf16 v[116:119], a[208:211], a[160:163], v[116:119]\n"
"s_mov_b32 s26, s22\n"
"v_cvt_pk_bf16_f32 v154, v156, v157\n"
"v_cvt_pk_bf16_f32 v155, v158, v159\n"
"v_mfma_f32_16x16x32_bf16 v[124:127], a[208:211], a[176:179], v[188:191]\n"
"s_add_u32 s78, s78, s92\n"
"v_exp_f32 v96, v96\n"
"v_mfma_f32_16x16x32_bf16 v[124:127], a[212:215], a[180:183], v[124:127]\n"
"v_exp_f32 v97, v97\n"
"v_mfma_f32_16x16x32_bf16 v[124:127], a[216:219], a[184:187], v[124:127]\n"
"buffer_load_dwordx4 v24, s[20:23], s79 offen offset:0 sc1 lds\n"
"v_mfma_f32_16x16x32_bf16 v[124:127], a[220:223], a[188:191], v[124:127]\n"
"v_exp_f32 v98, v98\n"
"v_mfma_f32_16x16x32_bf16 v[120:123], a[204:207], a[188:191], v[188:191]\n"
"v_exp_f32 v99, v99\n"
"v_mfma_f32_16x16x32_bf16 v[120:123], a[200:203], a[184:187], v[120:123]\n"
"v_exp_f32 v100, v100\n"
"v_mfma_f32_16x16x32_bf16 v[120:123], a[196:199], a[180:183], v[120:123]\n"
"v_exp_f32 v101, v101\n"
"v_mfma_f32_16x16x32_bf16 v[120:123], a[192:195], a[176:179], v[120:123]\n"
"s_waitcnt lgkmcnt(7)\n"
"v_exp_f32 v102, v102\n"
"v_mfma_f32_16x16x32_bf16 v[192:195], a[224:227], v[128:131], v[192:195]\n"
"s_waitcnt lgkmcnt(6)\n"
"ds_read_b128 a[192:195], v8 offset:0\n"
"v_cvt_pk_bf16_f32 v96, v96, v97\n"
"v_mfma_f32_16x16x32_bf16 v[196:199], a[228:231], v[128:131], v[196:199]\n"
"buffer_load_dwordx4 v25, s[20:23], s79 offen offset:1024 sc1 lds\n"
"s_waitcnt lgkmcnt(6)\n"
"v_mfma_f32_16x16x32_bf16 v[200:203], a[232:235], v[128:131], v[200:203]\n"
"s_waitcnt lgkmcnt(5)\n"
"ds_read_b128 a[196:199], v8 offset:1024\n"
"v_cvt_pk_bf16_f32 v97, v98, v99\n"
"v_mfma_f32_16x16x32_bf16 v[204:207], a[236:239], v[128:131], v[204:207]\n"
"v_exp_f32 v103, v103\n"
"v_mfma_f32_16x16x32_bf16 v[160:163], v[60:63], v[128:131], v[160:163]\n"
"s_waitcnt lgkmcnt(5)\n"
"v_exp_f32 v104, v104\n"
"v_mfma_f32_16x16x32_bf16 v[208:211], a[240:243], v[128:131], v[208:211]\n"
"s_waitcnt lgkmcnt(4)\n"
"ds_read_b128 a[200:203], v8 offset:2048\n"
"v_cvt_pk_bf16_f32 v98, v100, v101\n"
"v_mfma_f32_16x16x32_bf16 v[212:215], a[244:247], v[128:131], v[212:215]\n"
"buffer_load_dwordx4 v26, s[20:23], s79 offen offset:2048 sc1 lds\n"
"s_waitcnt lgkmcnt(4)\n"
"v_mfma_f32_16x16x32_bf16 v[216:219], a[248:251], v[128:131], v[216:219]\n"
"s_waitcnt lgkmcnt(3)\n"
"ds_read_b128 a[204:207], v8 offset:3072\n"
"v_cvt_pk_bf16_f32 v99, v102, v103\n"
"v_mfma_f32_16x16x32_bf16 v[220:223], a[252:255], v[128:131], v[220:223]\n"
"v_exp_f32 v105, v105\n"
"v_mfma_f32_16x16x32_bf16 v[252:255], a[252:255], v[136:139], v[252:255]\n"
"v_exp_f32 v106, v106\n"
"v_mfma_f32_16x16x32_bf16 v[248:251], a[248:251], v[136:139], v[248:251]\n"
"ds_read_b128 a[208:211], v8 offset:4096\n"
"v_cvt_pk_bf16_f32 v104, v104, v105\n"
"v_mfma_f32_16x16x32_bf16 v[244:247], a[244:247], v[136:139], v[244:247]\n"
"v_exp_f32 v107, v107\n"
"v_mfma_f32_16x16x32_bf16 v[240:243], a[240:243], v[136:139], v[240:243]\n"
"buffer_load_dwordx4 v27, s[20:23], s79 offen offset:3072 sc1 lds\n"
"v_mfma_f32_16x16x32_bf16 v[164:167], v[60:63], v[136:139], v[164:167]\n"
"s_add_u32 s79, s79, s92\n"
"ds_read_b128 a[212:215], v8 offset:5120\n"
"v_cvt_pk_bf16_f32 v105, v106, v107\n"
"v_mfma_f32_16x16x32_bf16 v[236:239], a[236:239], v[136:139], v[236:239]\n"
"s_xor_b32 s76, s76, 0x8000\n"
"v_exp_f32 v108, v108\n"
"v_mfma_f32_16x16x32_bf16 v[232:235], a[232:235], v[136:139], v[232:235]\n"
"s_sub_u32 s74, s74, 1\n"
"s_cselect_b64 s[78:79], s[72:73], s[78:79]\n"
"ds_read_b128 a[216:219], v8 offset:6144\n"
"v_mfma_f32_16x16x32_bf16 v[228:231], a[228:231], v[136:139], v[228:231]\n"
"s_cselect_b64 s[72:73], s[16:17], s[72:73]\n"
"v_exp_f32 v109, v109\n"
"v_mfma_f32_16x16x32_bf16 v[224:227], a[224:227], v[136:139], v[224:227]\n"
"s_cselect_b32 s74, s75, s74\n"
"v_exp_f32 v110, v110\n"
"v_mfma_f32_16x16x32_bf16 a[0:3], a[224:227], v[144:147], a[0:3]\n"
"s_cselect_b32 s75, 64, s75\n"
"ds_read_b128 a[220:223], v8 offset:7168\n"
"v_cvt_pk_bf16_f32 v106, v108, v109\n"
"v_mfma_f32_16x16x32_bf16 a[4:7], a[228:231], v[144:147], a[4:7]\n"
"s_cmp_gt_u32 s74, 32\n"
"v_exp_f32 v111, v111\n"
"v_mfma_f32_16x16x32_bf16 a[8:11], a[232:235], v[144:147], a[8:11]\n"
"s_cselect_b64 s[20:21], s[32:33], s[20:21]\n"
"v_exp_f32 v112, v112\n"
"v_mfma_f32_16x16x32_bf16 a[12:15], a[236:239], v[144:147], a[12:15]\n"
"s_cselect_b32 s22, s34, s22\n"
"v_exp_f32 v113, v113\n"
"v_mfma_f32_16x16x32_bf16 v[168:171], v[60:63], v[144:147], v[168:171]\n"
"s_cselect_b32 s92, 262144, s92\n"
"v_exp_f32 v114, v114\n"
"v_mfma_f32_16x16x32_bf16 a[16:19], a[240:243], v[144:147], a[16:19]\n"
"s_cselect_b32 s5, 524288, s5\n"
"v_exp_f32 v115, v115\n"
"v_mfma_f32_16x16x32_bf16 a[20:23], a[244:247], v[144:147], a[20:23]\n"
"s_cmp_eq_u32 s78, s95\n"
"v_exp_f32 v116, v116\n"
"v_mfma_f32_16x16x32_bf16 a[24:27], a[248:251], v[144:147], a[24:27]\n"
"v_exp_f32 v117, v117\n"
"v_mfma_f32_16x16x32_bf16 a[28:31], a[252:255], v[144:147], a[28:31]\n"
"v_cvt_pk_bf16_f32 v107, v110, v111\n"
"v_cvt_pk_bf16_f32 v112, v112, v113\n"
"v_mfma_f32_16x16x32_bf16 a[60:63], a[252:255], v[152:155], a[60:63]\n"
"v_cvt_pk_bf16_f32 v113, v114, v115\n"
"v_cvt_pk_bf16_f32 v114, v116, v117\n"
"v_mfma_f32_16x16x32_bf16 a[56:59], a[248:251], v[152:155], a[56:59]\n"
"v_mfma_f32_16x16x32_bf16 a[52:55], a[244:247], v[152:155], a[52:55]\n"
"s_waitcnt vmcnt(16)\n"
"v_cmp_ne_u64 vcc, s[64:65], v[20:21]\n"
"v_mfma_f32_16x16x32_bf16 a[48:51], a[240:243], v[152:155], a[48:51]\n"
"s_cselect_b64 vcc, vcc, 0\n"
"s_cbranch_vccnz RA_33_%=a\n"
"RA_33_%=:\n"
"s_sub_u32 s91, s91, 1\n"
"v_mfma_f32_16x16x32_bf16 v[172:175], v[60:63], v[152:155], v[172:175]\n"
"v_mfma_f32_16x16x32_bf16 a[44:47], a[236:239], v[152:155], a[44:47]\n"
"v_mfma_f32_16x16x32_bf16 a[40:43], a[232:235], v[152:155], a[40:43]\n"
"s_waitcnt lgkmcnt(0)\n"
"s_barrier\n"
"v_mfma_f32_16x16x32_bf16 a[36:39], a[228:231], v[152:155], a[36:39]\n"
"v_mfma_f32_16x16x32_bf16 a[32:35], a[224:227], v[152:155], a[32:35]\n"
"s_cbranch_scc0 ATL_24_%=\n"
"DG_25_%=:\n"
"s_cmp_lg_u32 s67, 0\n"
"s_cbranch_scc1 B1_26_%=\n"
"v_mfma_f32_16x16x32_bf16 v[144:147], a[192:195], a[160:163], v[184:187]\n"
"s_add_u32 m0, s77, 0\n"
"ds_read_b128 a[224:227], v9 offset:16384\n"
"v_mfma_f32_16x16x32_bf16 v[144:147], a[196:199], a[164:167], v[144:147]\n"
"ds_read_b128 a[228:231], v9 offset:17408\n"
"v_mfma_f32_16x16x32_bf16 v[144:147], a[200:203], a[168:171], v[144:147]\n"
"buffer_load_dwordx4 v24, s[24:27], s80 offen offset:0 sc1 lds\n"
"v_mfma_f32_16x16x32_bf16 v[144:147], a[204:207], a[172:175], v[144:147]\n"
"ds_read_b128 a[232:235], v9 offset:18432\n"
"v_mfma_f32_16x16x32_bf16 v[148:151], a[220:223], a[172:175], v[184:187]\n"
"v_exp_f32 v118, v118\n"
"v_mfma_f32_16x16x32_bf16 v[148:151], a[216:219], a[168:171], v[148:151]\n"
"ds_read_b128 a[236:239], v9 offset:19456\n"
"v_mfma_f32_16x16x32_bf16 v[148:151], a[212:215], a[164:167], v[148:151]\n"
"v_exp_f32 v119, v119\n"
"v_mfma_f32_16x16x32_bf16 v[148:151], a[208:211], a[160:163], v[148:151]\n"
"ds_read_b128 a[240:243], v9 offset:20480\n"
"v_cvt_pk_bf16_f32 v115, v118, v119\n"
"v_mfma_f32_16x16x32_bf16 v[156:159], a[208:211], a[176:179], v[188:191]\n"
"v_exp_f32 v120, v120\n"
"v_mfma_f32_16x16x32_bf16 v[156:159], a[212:215], a[180:183], v[156:159]\n"
"ds_read_b128 a[244:247], v9 offset:21504\n"
"v_mfma_f32_16x16x32_bf16 v[156:159], a[216:219], a[184:187], v[156:159]\n"
"buffer_load_dwordx4 v25, s[24:27], s80 offen offset:1024 sc1 lds\n"
"v_mfma_f32_16x16x32_bf16 v[156:159], a[220:223], a[188:191], v[156:159]\n"
"ds_read_b128 a[248:251], v9 offset:22528\n"
"v_mfma_f32_16x16x32_bf16 v[152:155], a[204:207], a[188:191], v[188:191]\n"
"v_exp_f32 v121, v121\n"
"v_mfma_f32_16x16x32_bf16 v[152:155], a[200:203], a[184:187], v[152:155]\n"
"ds_read_b128 a[252:255], v9 offset:23552\n"
"v_cvt_pk_bf16_f32 v120, v120, v121\n"
"v_mfma_f32_16x16x32_bf16 v[152:155], a[196:199], a[180:183], v[152:155]\n"
"v_exp_f32 v122, v122\n"
"v_mfma_f32_16x16x32_bf16 v[152:155], a[192:195], a[176:179], v[152:155]\n"
"v_exp_f32 v123, v123\n"
"v_mfma_f32_16x16x32_bf16 v[136:139], a[192:195], a[144:147], v[180:183]\n"
"buffer_load_dwordx4 v26, s[24:27], s80 offen offset:2048 sc1 lds\n"
"v_mfma_f32_16x16x32_bf16 v[136:139], a[196:199], a[148:151], v[136:139]\n"
"v_exp_f32 v124, v124\n"
"v_mfma_f32_16x16x32_bf16 v[136:139], a[200:203], a[152:155], v[136:139]\n"
"v_exp_f32 v125, v125\n"
"v_mfma_f32_16x16x32_bf16 v[136:139], a[204:207], a[156:159], v[136:139]\n"
"v_exp_f32 v126, v126\n"
"v_mfma_f32_16x16x32_bf16 v[140:143], a[220:223], a[156:159], v[180:183]\n"
"v_exp_f32 v127, v127\n"
"v_mfma_f32_16x16x32_bf16 v[140:143], a[216:219], a[152:155], v[140:143]\n"
"v_cvt_pk_bf16_f32 v121, v122, v123\n"
"v_cvt_pk_bf16_f32 v122, v124, v125\n"
"v_mfma_f32_16x16x32_bf16 v[140:143], a[212:215], a[148:151], v[140:143]\n"
"v_cvt_pk_bf16_f32 v123, v126, v127\n"
"v_xor_b32 v9, 0x8000, v9\n"
"v_mfma_f32_16x16x32_bf16 v[140:143], a[208:211], a[144:147], v[140:143]\n"
"v_exp_f32 v144, v144\n"
"v_mfma_f32_16x16x32_bf16 v[132:135], a[208:211], a[128:131], v[48:51]\n"
"buffer_load_dwordx4 v27, s[24:27], s80 offen offset:3072 sc1 lds\n"
"v_mfma_f32_16x16x32_bf16 v[132:135], a[212:215], a[132:135], v[132:135]\n"
"s_add_u32 m0, s76, 0\n"
"v_exp_f32 v145, v145\n"
"v_mfma_f32_16x16x32_bf16 v[132:135], a[216:219], a[136:139], v[132:135]\n"
"v_exp_f32 v146, v146\n"
"v_mfma_f32_16x16x32_bf16 v[132:135], a[220:223], a[140:143], v[132:135]\n"
"v_exp_f32 v147, v147\n"
"v_mfma_f32_16x16x32_bf16 v[128:131], a[204:207], a[140:143], v[44:47]\n"
"v_exp_f32 v148, v148\n"
"v_mfma_f32_16x16x32_bf16 v[128:131], a[200:203], a[136:139], v[128:131]\n"
"v_exp_f32 v149, v149\n"
"v_mfma_f32_16x16x32_bf16 v[128:131], a[196:199], a[132:135], v[128:131]\n"
"v_exp_f32 v150, v150\n"
"v_mfma_f32_16x16x32_bf16 v[128:131], a[192:195], a[128:131], v[128:131]\n"
"buffer_load_dwordx4 v24, s[20:23], s78 offen offset:0 sc1 lds\n"
"s_waitcnt lgkmcnt(7)\n"
"v_mfma_f32_16x16x32_bf16 v[192:195], a[224:227], v[96:99], v[192:195]\n"
"s_waitcnt lgkmcnt(6)\n"
"ds_read_b128 a[192:195], v8 offset:16384\n"
"v_cvt_pk_bf16_f32 v144, v144, v145\n"
"v_mfma_f32_16x16x32_bf16 v[196:199], a[228:231], v[96:99], v[196:199]\n"
"s_waitcnt lgkmcnt(6)\n"
"v_exp_f32 v151, v151\n"
"v_mfma_f32_16x16x32_bf16 v[200:203], a[232:235], v[96:99], v[200:203]\n"
"s_waitcnt lgkmcnt(5)\n"
"ds_read_b128 a[196:199], v8 offset:17408\n"
"v_cvt_pk_bf16_f32 v145, v146, v147\n"
"v_mfma_f32_16x16x32_bf16 v[204:207], a[236:239], v[96:99], v[204:207]\n"
"s_waitcnt lgkmcnt(5)\n"
"v_exp_f32 v152, v152\n"
"v_mfma_f32_16x16x32_bf16 v[208:211], a[240:243], v[96:99], v[208:211]\n"
"s_waitcnt lgkmcnt(4)\n"
"v_exp_f32 v153, v153\n"
"v_mfma_f32_16x16x32_bf16 v[212:215], a[244:247], v[96:99], v[212:215]\n"
"s_waitcnt lgkmcnt(3)\n"
"ds_read_b128 a[200:203], v8 offset:18432\n"
"v_cvt_pk_bf16_f32 v146, v148, v149\n"
"v_mfma_f32_16x16x32_bf16 v[216:219], a[248:251], v[96:99], v[216:219]\n"
"buffer_load_dwordx4 v25, s[20:23], s78 offen offset:1024 sc1 lds\n"
"s_waitcnt lgkmcnt(3)\n"
"v_mfma_f32_16x16x32_bf16 v[220:223], a[252:255], v[96:99], v[220:223]\n"
"ds_read_b128 a[204:207], v8 offset:19456\n"
"v_cvt_pk_bf16_f32 v147, v150, v151\n"
"v_mfma_f32_16x16x32_bf16 v[160:163], v[60:63], v[96:99], v[160:163]\n"
"v_exp_f32 v154, v154\n"
"v_mfma_f32_16x16x32_bf16 v[224:227], a[224:227], v[104:107], v[224:227]\n"
"v_exp_f32 v155, v155\n"
"v_mfma_f32_16x16x32_bf16 v[228:231], a[228:231], v[104:107], v[228:231]\n"
"ds_read_b128 a[208:211], v8 offset:20480\n"
"v_cvt_pk_bf16_f32 v152, v152, v153\n"
"v_mfma_f32_16x16x32_bf16 v[232:235], a[232:235], v[104:107], v[232:235]\n"
"v_exp_f32 v156, v156\n"
"v_mfma_f32_16x16x32_bf16 v[236:239], a[236:239], v[104:107], v[236:239]\n"
"v_exp_f32 v157, v157\n"
"v_mfma_f32_16x16x32_bf16 v[240:243], a[240:243], v[104:107], v[240:243]\n"
"ds_read_b128 a[212:215], v8 offset:21504\n"
"v_cvt_pk_bf16_f32 v153, v154, v155\n"
"v_mfma_f32_16x16x32_bf16 v[244:247], a[244:247], v[104:107], v[244:247]\n"
"buffer_load_dwordx4 v26, s[20:23], s78 offen offset:2048 sc1 lds\n"
"v_mfma_f32_16x16x32_bf16 v[248:251], a[248:251], v[104:107], v[248:251]\n"
"ds_read_b128 a[216:219], v8 offset:22528\n"
"v_cvt_pk_bf16_f32 v154, v156, v157\n"
"v_mfma_f32_16x16x32_bf16 v[252:255], a[252:255], v[104:107], v[252:255]\n"
"v_exp_f32 v158, v158\n"
"v_mfma_f32_16x16x32_bf16 v[164:167], v[60:63], v[104:107], v[164:167]\n"
"v_exp_f32 v159, v159\n"
"v_mfma_f32_16x16x32_bf16 a[0:3], a[224:227], v[112:115], a[0:3]\n"
"ds_read_b128 a[220:223], v8 offset:23552\n"
"v_xor_b32 v8, 0x8000, v8\n"
"v_mfma_f32_16x16x32_bf16 a[4:7], a[228:231], v[112:115], a[4:7]\n"
"v_exp_f32 v136, v136\n"
"v_mfma_f32_16x16x32_bf16 a[8:11], a[232:235], v[112:115], a[8:11]\n"
"v_exp_f32 v137, v137\n"
"v_mfma_f32_16x16x32_bf16 a[12:15], a[236:239], v[112:115], a[12:15]\n"
"buffer_load_dwordx4 v27, s[20:23], s78 offen offset:3072 sc1 lds\n"
"v_mfma_f32_16x16x32_bf16 a[16:19], a[240:243], v[112:115], a[16:19]\n"
"v_exp_f32 v138, v138\n"
"v_mfma_f32_16x16x32_bf16 a[20:23], a[244:247], v[112:115], a[20:23]\n"
"v_exp_f32 v139, v139\n"
"v_mfma_f32_16x16x32_bf16 a[24:27], a[248:251], v[112:115], a[24:27]\n"
"v_exp_f32 v140, v140\n"
"v_mfma_f32_16x16x32_bf16 a[28:31], a[252:255], v[112:115], a[28:31]\n"
"v_exp_f32 v141, v141\n"
"v_mfma_f32_16x16x32_bf16 v[168:171], v[60:63], v[112:115], v[168:171]\n"
"v_exp_f32 v142, v142\n"
"v_mfma_f32_16x16x32_bf16 a[32:35], a[224:227], v[120:123], a[32:35]\n"
"v_exp_f32 v143, v143\n"
"v_mfma_f32_16x16x32_bf16 a[36:39], a[228:231], v[120:123], a[36:39]\n"
"v_exp_f32 v128, v128\n"
"v_mfma_f32_16x16x32_bf16 a[40:43], a[232:235], v[120:123], a[40:43]\n"
"v_exp_f32 v129, v129\n"
"v_mfma_f32_16x16x32_bf16 a[44:47], a[236:239], v[120:123], a[44:47]\n"
"v_exp_f32 v130, v130\n"
"v_mfma_f32_16x16x32_bf16 a[48:51], a[240:243], v[120:123], a[48:51]\n"
"v_exp_f32 v131, v131\n"
"v_mfma_f32_16x16x32_bf16 a[52:55], a[244:247], v[120:123], a[52:55]\n"
"v_cvt_pk_bf16_f32 v155, v158, v159\n"
"v_cvt_pk_bf16_f32 v136, v136, v137\n"
"v_mfma_f32_16x16x32_bf16 a[56:59], a[248:251], v[120:123], a[56:59]\n"
"v_cvt_pk_bf16_f32 v137, v138, v139\n"
"v_cvt_pk_bf16_f32 v138, v140, v141\n"
"v_mfma_f32_16x16x32_bf16 a[60:63], a[252:255], v[120:123], a[60:63]\n"
"s_waitcnt vmcnt(8)\n"
"v_cvt_pk_bf16_f32 v139, v142, v143\n"
"v_cvt_pk_bf16_f32 v128, v128, v129\n"
"v_mfma_f32_16x16x32_bf16 v[172:175], v[60:63], v[120:123], v[172:175]\n"
"s_waitcnt lgkmcnt(0)\n"
"s_barrier\n"
"v_cvt_pk_bf16_f32 v129, v130, v131\n"
"F0_27_%=:\n"
"v_mfma_f32_16x16x32_bf16 v[120:123], a[192:195], a[176:179], v[52:55]\n"
"s_add_u32 m0, s76, 16384\n"
"ds_read_b128 v[64:67], v9 offset:0\n"
"ds_read_b128 v[68:71], v9 offset:1024\n"
"v_mfma_f32_16x16x32_bf16 v[120:123], a[196:199], a[180:183], v[120:123]\n"
"s_lshl_b32 s82, s61, 13\n"
"ds_read_b128 v[72:75], v9 offset:2048\n"
"ds_read_b128 v[76:79], v9 offset:3072\n"
"v_mfma_f32_16x16x32_bf16 v[120:123], a[200:203], a[184:187], v[120:123]\n"
"s_and_b32 s83, s77, 0x18000\n"
"s_xor_b32 s83, s83, 0x8000\n"
"ds_read_b128 v[80:83], v9 offset:4096\n"
"v_mfma_f32_16x16x32_bf16 v[120:123], a[204:207], a[188:191], v[120:123]\n"
"s_add_u32 s82, s82, s83\n"
"ds_read_b128 v[84:87], v9 offset:5120\n"
"ds_read_b128 v[88:91], v9 offset:6144\n"
"v_mfma_f32_16x16x32_bf16 v[124:127], a[220:223], a[188:191], v[56:59]\n"
"s_lshl_b32 s83, s67, 2\n"
"ds_read_b128 v[92:95], v9 offset:7168\n"
"v_add_u32 v23, s82, v1\n"
"v_mfma_f32_16x16x32_bf16 v[124:127], a[216:219], a[184:187], v[124:127]\n"
"s_lshl_b32 s82, s71, 3\n"
"v_exp_f32 v132, v132\n"
"v_mfma_f32_16x16x32_bf16 v[124:127], a[212:215], a[180:183], v[124:127]\n"
"s_add_u32 s82, s82, s83\n"
"v_exp_f32 v133, v133\n"
"v_mfma_f32_16x16x32_bf16 v[124:127], a[208:211], a[176:179], v[124:127]\n"
"s_add_u32 s82, s82, s66\n"
"v_exp_f32 v120, v120\n"
"v_mfma_f32_16x16x32_bf16 v[168:171], v[60:63], v[144:147], v[168:171]\n"
"s_lshl_b32 s82, s82, 17\n"
"ds_read_b128 a[192:195], v9 offset:16384\n"
"v_cvt_pk_bf16_f32 v130, v132, v133\n"
"v_mfma_f32_16x16x32_bf16 v[172:175], v[60:63], v[152:155], v[172:175]\n"
"s_lshl_b32 s83, s62, 12\n"
"s_add_u32 s82, s82, s83\n"
"ds_read_b128 a[196:199], v9 offset:17408\n"
"s_waitcnt lgkmcnt(9)\n"
"v_mfma_f32_16x16x32_bf16 a[0:3], v[64:67], v[144:147], a[0:3]\n"
"s_add_u32 s82, s82, 37748736\n"
"s_add_u32 s83, s82, 262144\n"
"ds_read_b128 a[200:203], v9 offset:18432\n"
"s_waitcnt lgkmcnt(9)\n"
"v_mfma_f32_16x16x32_bf16 a[4:7], v[68:71], v[144:147], a[4:7]\n"
"s_waitcnt lgkmcnt(8)\n"
"ds_read_b128 a[204:207], v9 offset:19456\n"
"v_xor_b32 v38, 0x2000, v23\n"
"v_mfma_f32_16x16x32_bf16 a[8:11], v[72:75], v[144:147], a[8:11]\n"
"s_waitcnt lgkmcnt(8)\n"
"ds_read_b128 a[208:211], v9 offset:20480\n"
"v_mfma_f32_16x16x32_bf16 a[12:15], v[76:79], v[144:147], a[12:15]\n"
"s_waitcnt lgkmcnt(8)\n"
"ds_read_b128 a[212:215], v9 offset:21504\n"
"v_mfma_f32_16x16x32_bf16 a[16:19], v[80:83], v[144:147], a[16:19]\n"
"s_waitcnt lgkmcnt(8)\n"
"ds_read_b128 a[216:219], v9 offset:22528\n"
"v_mfma_f32_16x16x32_bf16 a[20:23], v[84:87], v[144:147], a[20:23]\n"
"s_waitcnt lgkmcnt(8)\n"
"ds_read_b128 a[220:223], v9 offset:23552\n"
"ds_read_b128 v[96:99], v22 offset:8192\n"
"v_mfma_f32_16x16x32_bf16 a[24:27], v[88:91], v[144:147], a[24:27]\n"
"s_waitcnt lgkmcnt(9)\n"
"v_exp_f32 v121, v121\n"
"v_mfma_f32_16x16x32_bf16 a[28:31], v[92:95], v[144:147], a[28:31]\n"
"ds_write_b128 v22, a[0:3] offset:0\n"
"v_mfma_f32_16x16x32_bf16 a[32:35], v[64:67], v[152:155], a[32:35]\n"
"v_exp_f32 v122, v122\n"
"v_mfma_f32_16x16x32_bf16 a[36:39], v[68:71], v[152:155], a[36:39]\n"
"ds_write_b128 v22, a[4:7] offset:1024\n"
"v_mfma_f32_16x16x32_bf16 a[40:43], v[72:75], v[152:155], a[40:43]\n"
"v_exp_f32 v123, v123\n"
"v_mfma_f32_16x16x32_bf16 a[44:47], v[76:79], v[152:155], a[44:47]\n"
"ds_write_b128 v22, a[8:11] offset:2048\n"
"v_mfma_f32_16x16x32_bf16 a[48:51], v[80:83], v[152:155], a[48:51]\n"
"v_exp_f32 v124, v124\n"
"v_mfma_f32_16x16x32_bf16 a[52:55], v[84:87], v[152:155], a[52:55]\n"
"v_exp_f32 v125, v125\n"
"v_mfma_f32_16x16x32_bf16 a[56:59], v[88:91], v[152:155], a[56:59]\n"
"v_exp_f32 v126, v126\n"
"v_mfma_f32_16x16x32_bf16 a[60:63], v[92:95], v[152:155], a[60:63]\n"
"s_waitcnt lgkmcnt(0)\n"
"s_barrier\n"
"v_exp_f32 v127, v127\n"
"v_mfma_f32_16x16x32_bf16 v[164:167], v[60:63], v[136:139], v[164:167]\n"
"ds_write_b128 v22, a[12:15] offset:3072\n"
"v_cvt_pk_bf16_f32 v120, v120, v121\n"
"v_cvt_pk_bf16_f32 v121, v122, v123\n"
"v_cvt_pk_bf16_f32 v122, v124, v125\n"
"v_cvt_pk_bf16_f32 v123, v126, v127\n"
"s_nop 1\n"
"v_mfma_f32_16x16x32_bf16 v[172:175], v[60:63], v[120:123], v[172:175]\n"
"ds_write_b128 v22, a[16:19] offset:4096\n"
"v_mfma_f32_16x16x32_bf16 a[32:35], a[192:195], v[120:123], a[32:35]\n"
"buffer_load_dwordx4 v24, s[20:23], s79 offen offset:0 sc1 lds\n"
"v_mfma_f32_16x16x32_bf16 a[36:39], a[196:199], v[120:123], a[36:39]\n"
"ds_write_b128 v22, a[20:23] offset:5120\n"
"v_mfma_f32_16x16x32_bf16 a[40:43], a[200:203], v[120:123], a[40:43]\n"
"buffer_load_dwordx4 v25, s[20:23], s79 offen offset:1024 sc1 lds\n"
"v_mfma_f32_16x16x32_bf16 a[44:47], a[204:207], v[120:123], a[44:47]\n"
"ds_write_b128 v22, a[24:27] offset:6144\n"
"v_mfma_f32_16x16x32_bf16 a[48:51], a[208:211], v[120:123], a[48:51]\n"
"ds_write_b128 v22, a[28:31] offset:7168\n"
"v_mfma_f32_16x16x32_bf16 a[52:55], a[212:215], v[120:123], a[52:55]\n"
"buffer_load_dwordx4 v26, s[20:23], s79 offen offset:2048 sc1 lds\n"
"v_mfma_f32_16x16x32_bf16 a[56:59], a[216:219], v[120:123], a[56:59]\n"
"v_exp_f32 v134, v134\n"
"v_mfma_f32_16x16x32_bf16 a[60:63], a[220:223], v[120:123], a[60:63]\n"
"v_exp_f32 v135, v135\n"
"s_nop 0\n"
"v_cvt_pk_bf16_f32 v131, v134, v135\n"
"s_nop 1\n"
"v_mfma_f32_16x16x32_bf16 v[160:163], v[60:63], v[128:131], v[160:163]\n"
"buffer_load_dwordx4 v27, s[20:23], s79 offen offset:3072 sc1 lds\n"
"v_mfma_f32_16x16x32_bf16 v[224:227], v[64:67], v[136:139], v[224:227]\n"
"ds_write_b128 v23, a[32:35] offset:0\n"
"v_mfma_f32_16x16x32_bf16 v[228:231], v[68:71], v[136:139], v[228:231]\n"
"s_add_u32 m0, s77, 16384\n"
"ds_read_b128 a[224:227], v8 offset:0\n"
"ds_read_b128 a[228:231], v8 offset:1024\n"
"v_mfma_f32_16x16x32_bf16 v[232:235], v[72:75], v[136:139], v[232:235]\n"
"ds_read_b128 a[232:235], v8 offset:2048\n"
"ds_read_b128 a[236:239], v8 offset:3072\n"
"v_mfma_f32_16x16x32_bf16 v[236:239], v[76:79], v[136:139], v[236:239]\n"
"ds_write_b128 v23, a[36:39] offset:1024\n"
"v_mfma_f32_16x16x32_bf16 v[240:243], v[80:83], v[136:139], v[240:243]\n"
"buffer_load_dwordx2 v[20:21], v30, s[44:47], 0 offen sc1\n"
"v_mfma_f32_16x16x32_bf16 v[244:247], v[84:87], v[136:139], v[244:247]\n"
"ds_read_b128 a[240:243], v8 offset:4096\n"
"ds_read_b128 a[244:247], v8 offset:5120\n"
"v_mfma_f32_16x16x32_bf16 v[248:251], v[88:91], v[136:139], v[248:251]\n"
"ds_read_b128 a[248:251], v8 offset:6144\n"
"ds_read_b128 a[252:255], v8 offset:7168\n"
"v_mfma_f32_16x16x32_bf16 v[252:255], v[92:95], v[136:139], v[252:255]\n"
"ds_write_b128 v23, a[40:43] offset:2048\n"
"v_mfma_f32_16x16x32_bf16 v[192:195], v[64:67], v[128:131], v[192:195]\n"
"v_cndmask_b32 v4, v160, v164, s[56:57]\n"
"v_cndmask_b32 v5, v168, v172, s[56:57]\n"
"v_mfma_f32_16x16x32_bf16 v[196:199], v[68:71], v[128:131], v[196:199]\n"
"v_xor_b32 v9, 0x8000, v9\n"
"v_cndmask_b32 v4, v4, v5, s[100:101]\n"
"v_mfma_f32_16x16x32_bf16 v[200:203], v[72:75], v[128:131], v[200:203]\n"
"ds_write_b32 v19, v4\n"
"v_mfma_f32_16x16x32_bf16 v[204:207], v[76:79], v[128:131], v[204:207]\n"
"buffer_load_dwordx4 v24, s[24:27], s81 offen offset:0 sc1 lds\n"
"v_mfma_f32_16x16x32_bf16 v[208:211], v[80:83], v[128:131], v[208:211]\n"
"ds_write_b128 v23, a[44:47] offset:3072\n"
"v_mfma_f32_16x16x32_bf16 v[212:215], v[84:87], v[128:131], v[212:215]\n"
"ds_write_b128 v23, a[48:51] offset:4096\n"
"v_mfma_f32_16x16x32_bf16 v[216:219], v[88:91], v[128:131], v[216:219]\n"
"buffer_load_dwordx4 v25, s[24:27], s81 offen offset:1024 sc1 lds\n"
"v_mfma_f32_16x16x32_bf16 v[220:223], v[92:95], v[128:131], v[220:223]\n"
"ds_write_b128 v23, a[52:55] offset:5120\n"
"ds_write_b128 v23, a[56:59] offset:6144\n"
"ds_write_b128 v23, a[60:63] offset:7168\n"
"buffer_load_dwordx4 v26, s[24:27], s81 offen offset:2048 sc1 lds\n"
"buffer_load_dwordx4 v27, s[24:27], s81 offen offset:3072 sc1 lds\n"
"s_waitcnt lgkmcnt(0)\n"
"s_barrier\n"
"s_branch TAIL0_%=\n"
"STUB1_%=:\n"
"s_cbranch_execz PADRET_%=\n"
"RA_33_%=a:\n"
"v_cmp_ne_u64 vcc, s[64:65], v[20:21]\n"
"s_nop 1\n"
"s_cbranch_vccz FOK_31_%=\n"
"FSP_32_%=:\n"
"s_cmp_eq_u32 s69, 0\n"
"s_cbranch_scc1 FOK_31_%=\n"
"s_sub_u32 s69, s69, 1\n"
"s_sleep 2\n"
"s_mov_b64 exec, vcc\n"
"buffer_load_dwordx2 v[20:21], v18, s[44:47], 0 offen sc1\n"
"s_mov_b64 exec, -1\n"
"s_waitcnt vmcnt(0)\n"
"v_cmp_ne_u64 vcc, s[64:65], v[20:21]\n"
"s_nop 1\n"
"s_cbranch_vccnz FSP_32_%=\n"
"FOK_31_%=:\n"
"s_branch RA_33_%=\n"
"N1_29_%=:\n"
"s_cmp_lg_u32 s67, 0\n"
"s_cbranch_scc1 N1B_30_%=\n"
"buffer_store_dwordx4 v[192:195], v1, s[44:47], s82 offen offset:0 sc1\n"
"buffer_store_dwordx4 v[200:203], v1, s[44:47], s82 offen offset:1024 sc1\n"
"buffer_store_dwordx4 v[208:211], v1, s[44:47], s82 offen offset:2048 sc1\n"
"buffer_store_dwordx4 v[216:219], v1, s[44:47], s82 offen offset:3072 sc1\n"
"buffer_store_dwordx4 v[224:227], v1, s[44:47], s83 offen offset:0 sc1\n"
"buffer_store_dwordx4 v[232:235], v1, s[44:47], s83 offen offset:1024 sc1\n"
"buffer_store_dwordx4 v[240:243], v1, s[44:47], s83 offen offset:2048 sc1\n"
"buffer_store_dwordx4 v[248:251], v1, s[44:47], s83 offen offset:3072 sc1\n"
"s_add_u32 m0, s76, 0\n"
"s_nop 0\n"
"buffer_load_dwordx4 v24, s[20:23], s78 offen offset:0 sc1 lds\n"
"buffer_load_dwordx4 v25, s[20:23], s78 offen offset:1024 sc1 lds\n"
"buffer_load_dwordx4 v26, s[20:23], s78 offen offset:2048 sc1 lds\n"
"buffer_load_dwordx4 v27, s[20:23], s78 offen offset:3072 sc1 lds\n"
"v_mfma_f32_16x16x32_bf16 v[144:147], a[224:227], a[160:163], v[184:187]\n"
"ds_read_b128 a[192:195], v8 offset:16384\n"
"ds_read_b128 a[196:199], v8 offset:17408\n"
"v_mfma_f32_16x16x32_bf16 v[144:147], a[228:231], a[164:167], v[144:147]\n"
"ds_read_b128 a[200:203], v8 offset:18432\n"
"ds_read_b128 a[204:207], v8 offset:19456\n"
"v_mfma_f32_16x16x32_bf16 v[144:147], a[232:235], a[168:171], v[144:147]\n"
"ds_read_b128 a[208:211], v8 offset:20480\n"
"v_mfma_f32_16x16x32_bf16 v[144:147], a[236:239], a[172:175], v[144:147]\n"
"ds_read_b128 a[212:215], v8 offset:21504\n"
"ds_read_b128 a[216:219], v8 offset:22528\n"
"v_mfma_f32_16x16x32_bf16 v[148:151], a[252:255], a[172:175], v[184:187]\n"
"ds_read_b128 a[220:223], v8 offset:23552\n"
"v_xor_b32 v8, 0x8000, v8\n"
"v_mfma_f32_16x16x32_bf16 v[148:151], a[248:251], a[168:171], v[148:151]\n"
"v_mfma_f32_16x16x32_bf16 v[148:151], a[244:247], a[164:167], v[148:151]\n"
"v_mfma_f32_16x16x32_bf16 v[148:151], a[240:243], a[160:163], v[148:151]\n"
"v_exp_f32 v144, v144\n"
"v_mfma_f32_16x16x32_bf16 v[156:159], a[240:243], a[176:179], v[188:191]\n"
"s_waitcnt lgkmcnt(0)\n"
"s_barrier\n"
"v_exp_f32 v145, v145\n"
"v_mfma_f32_16x16x32_bf16 v[156:159], a[244:247], a[180:183], v[156:159]\n"
"v_exp_f32 v146, v146\n"
"v_mfma_f32_16x16x32_bf16 v[156:159], a[248:251], a[184:187], v[156:159]\n"
"v_exp_f32 v147, v147\n"
"v_mfma_f32_16x16x32_bf16 v[156:159], a[252:255], a[188:191], v[156:159]\n"
"v_exp_f32 v148, v148\n"
"v_mfma_f32_16x16x32_bf16 v[152:155], a[236:239], a[188:191], v[188:191]\n"
"v_exp_f32 v149, v149\n"
"v_mfma_f32_16x16x32_bf16 v[152:155], a[232:235], a[184:187], v[152:155]\n"
"v_exp_f32 v150, v150\n"
"v_mfma_f32_16x16x32_bf16 v[152:155], a[228:231], a[180:183], v[152:155]\n"
"v_exp_f32 v151, v151\n"
"v_mfma_f32_16x16x32_bf16 v[152:155], a[224:227], a[176:179], v[152:155]\n"
"v_exp_f32 v156, v156\n"
"v_mfma_f32_16x16x32_bf16 v[136:139], a[224:227], a[144:147], v[180:183]\n"
"v_exp_f32 v157, v157\n"
"v_mfma_f32_16x16x32_bf16 v[136:139], a[228:231], a[148:151], v[136:139]\n"
"v_exp_f32 v158, v158\n"
"v_mfma_f32_16x16x32_bf16 v[136:139], a[232:235], a[152:155], v[136:139]\n"
"v_exp_f32 v159, v159\n"
"v_mfma_f32_16x16x32_bf16 v[136:139], a[236:239], a[156:159], v[136:139]\n"
"v_exp_f32 v152, v152\n"
"v_mfma_f32_16x16x32_bf16 v[140:143], a[252:255], a[156:159], v[180:183]\n"
"v_exp_f32 v153, v153\n"
"v_mfma_f32_16x16x32_bf16 v[140:143], a[248:251], a[152:155], v[140:143]\n"
"v_exp_f32 v154, v154\n"
"v_mfma_f32_16x16x32_bf16 v[140:143], a[244:247], a[148:151], v[140:143]\n"
"v_exp_f32 v155, v155\n"
"v_mfma_f32_16x16x32_bf16 v[140:143], a[240:243], a[144:147], v[140:143]\n"
"v_exp_f32 v136, v136\n"
"v_mfma_f32_16x16x32_bf16 v[132:135], a[240:243], a[128:131], v[48:51]\n"
"v_exp_f32 v137, v137\n"
"v_mfma_f32_16x16x32_bf16 v[132:135], a[244:247], a[132:135], v[132:135]\n"
"v_exp_f32 v138, v138\n"
"v_mfma_f32_16x16x32_bf16 v[132:135], a[248:251], a[136:139], v[132:135]\n"
"v_exp_f32 v139, v139\n"
"v_mfma_f32_16x16x32_bf16 v[132:135], a[252:255], a[140:143], v[132:135]\n"
"v_exp_f32 v140, v140\n"
"v_mfma_f32_16x16x32_bf16 v[128:131], a[236:239], a[140:143], v[44:47]\n"
"v_exp_f32 v141, v141\n"
"v_mfma_f32_16x16x32_bf16 v[128:131], a[232:235], a[136:139], v[128:131]\n"
"v_exp_f32 v142, v142\n"
"v_mfma_f32_16x16x32_bf16 v[128:131], a[228:231], a[132:135], v[128:131]\n"
"v_exp_f32 v143, v143\n"
"v_mfma_f32_16x16x32_bf16 v[128:131], a[224:227], a[128:131], v[128:131]\n"
"v_cvt_pk_bf16_f32 v144, v144, v145\n"
"v_cvt_pk_bf16_f32 v145, v146, v147\n"
"v_cvt_pk_bf16_f32 v146, v148, v149\n"
"v_cvt_pk_bf16_f32 v147, v150, v151\n"
"v_cvt_pk_bf16_f32 v152, v152, v153\n"
"v_cvt_pk_bf16_f32 v153, v154, v155\n"
"v_cvt_pk_bf16_f32 v154, v156, v157\n"
"v_cvt_pk_bf16_f32 v155, v158, v159\n"
"v_cvt_pk_bf16_f32 v136, v136, v137\n"
"v_cvt_pk_bf16_f32 v137, v138, v139\n"
"v_cvt_pk_bf16_f32 v138, v140, v141\n"
"v_cvt_pk_bf16_f32 v139, v142, v143\n"
"v_exp_f32 v128, v128\n"
"v_exp_f32 v129, v129\n"
"s_nop 0\n"
"v_cvt_pk_bf16_f32 v128, v128, v129\n"
"v_exp_f32 v130, v130\n"
"v_exp_f32 v131, v131\n"
"s_nop 0\n"
"v_cvt_pk_bf16_f32 v129, v130, v131\n"
"s_waitcnt vmcnt(4) lgkmcnt(0)\n"
"s_barrier\n"
"s_add_u32 m0, s77, 0\n"
"s_nop 0\n"
"buffer_load_dwordx4 v24, s[24:27], s80 offen offset:0 sc1 lds\n"
"buffer_load_dwordx4 v25, s[24:27], s80 offen offset:1024 sc1 lds\n"
"buffer_load_dwordx4 v26, s[24:27], s80 offen offset:2048 sc1 lds\n"
"buffer_load_dwordx4 v27, s[24:27], s80 offen offset:3072 sc1 lds\n"
"s_lshl_b32 s99, s60, 7\n"
"s_add_u32 s99, s99, 65536\n"
"v_mov_b32 v28, s64\n"
"v_mov_b32 v29, s65\n"
"s_cmp_eq_u32 s61, 0\n"
"s_cselect_b64 exec, 1, 0\n"
"buffer_store_dwordx2 v[28:29], off, s[44:47], s99 sc1\n"
"s_mov_b64 exec, -1\n"
"v_mfma_f32_16x16x32_bf16 v[120:123], a[192:195], a[176:179], v[52:55]\n"
"s_add_u32 m0, s76, 16384\n"
"ds_read_b128 v[64:67], v9 offset:0\n"
"ds_read_b128 v[68:71], v9 offset:1024\n"
"v_mfma_f32_16x16x32_bf16 v[120:123], a[196:199], a[180:183], v[120:123]\n"
"s_lshl_b32 s82, s61, 13\n"
"ds_read_b128 v[72:75], v9 offset:2048\n"
"ds_read_b128 v[76:79], v9 offset:3072\n"
"PG12_%=:\n"
"s_cbranch_execz PADRET_%=\n"
"v_mfma_f32_16x16x32_bf16 v[120:123], a[200:203], a[184:187], v[120:123]\n"
"s_and_b32 s83, s77, 0x18000\n"
"s_xor_b32 s83, s83, 0x8000\n"
"ds_read_b128 v[80:83], v9 offset:4096\n"
"v_mfma_f32_16x16x32_bf16 v[120:123], a[204:207], a[188:191], v[120:123]\n"
"s_add_u32 s82, s82, s83\n"
"ds_read_b128 v[84:87], v9 offset:5120\n"
"ds_read_b128 v[88:91], v9 offset:6144\n"
"v_mfma_f32_16x16x32_bf16 v[124:127], a[220:223], a[188:191], v[56:59]\n"
"s_lshl_b32 s83, s67, 2\n"
"ds_read_b128 v[92:95], v9 offset:7168\n"
"v_add_u32 v23, s82, v1\n"
"v_mfma_f32_16x16x32_bf16 v[124:127], a[216:219], a[184:187], v[124:127]\n"
"s_lshl_b32 s82, s71, 3\n"
"v_exp_f32 v132, v132\n"
"v_mfma_f32_16x16x32_bf16 v[124:127], a[212:215], a[180:183], v[124:127]\n"
"s_add_u32 s82, s82, s83\n"
"v_exp_f32 v133, v133\n"
"v_mfma_f32_16x16x32_bf16 v[124:127], a[208:211], a[176:179], v[124:127]\n"
"s_add_u32 s82, s82, s66\n"
"v_exp_f32 v120, v120\n"
"v_mfma_f32_16x16x32_bf16 v[168:171], v[60:63], v[144:147], 0\n"
"s_lshl_b32 s82, s82, 17\n"
"ds_read_b128 a[192:195], v9 offset:16384\n"
"v_cvt_pk_bf16_f32 v130, v132, v133\n"
"v_mfma_f32_16x16x32_bf16 v[172:175], v[60:63], v[152:155], 0\n"
"s_lshl_b32 s83, s62, 12\n"
"s_add_u32 s82, s82, s83\n"
"ds_read_b128 a[196:199], v9 offset:17408\n"
"s_waitcnt lgkmcnt(9)\n"
"v_mfma_f32_16x16x32_bf16 a[0:3], v[64:67], v[144:147], 0\n"
"s_add_u32 s82, s82, 37748736\n"
"s_add_u32 s83, s82, 262144\n"
"ds_read_b128 a[200:203], v9 offset:18432\n"
"s_waitcnt lgkmcnt(9)\n"
"v_mfma_f32_16x16x32_bf16 a[4:7], v[68:71], v[144:147], 0\n"
"s_waitcnt lgkmcnt(8)\n"
"ds_read_b128 a[204:207], v9 offset:19456\n"
"v_xor_b32 v38, 0x2000, v23\n"
"v_mfma_f32_16x16x32_bf16 a[8:11], v[72:75], v[144:147], 0\n"
"s_waitcnt lgkmcnt(8)\n"
"ds_read_b128 a[208:211], v9 offset:20480\n"
"v_mfma_f32_16x16x32_bf16 a[12:15], v[76:79], v[144:147], 0\n"
"s_waitcnt lgkmcnt(8)\n"
"ds_read_b128 a[212:215], v9 offset:21504\n"
"v_mfma_f32_16x16x32_bf16 a[16:19], v[80:83], v[144:147], 0\n"
"s_waitcnt lgkmcnt(8)\n"
"ds_read_b128 a[216:219], v9 offset:22528\n"
"v_mfma_f32_16x16x32_bf16 a[20:23], v[84:87], v[144:147], 0\n"
"s_waitcnt lgkmcnt(8)\n"
"ds_read_b128 a[220:223], v9 offset:23552\n"
"ds_read_b128 v[96:99], v22 offset:8192\n"
"v_mfma_f32_16x16x32_bf16 a[24:27], v[88:91], v[144:147], 0\n"
"s_waitcnt lgkmcnt(9)\n"
"v_exp_f32 v121, v121\n"
"v_mfma_f32_16x16x32_bf16 a[28:31], v[92:95], v[144:147], 0\n"
"ds_write_b128 v22, a[0:3] offset:0\n"
"v_mfma_f32_16x16x32_bf16 a[32:35], v[64:67], v[152:155], 0\n"
"v_exp_f32 v122, v122\n"
"v_mfma_f32_16x16x32_bf16 a[36:39], v[68:71], v[152:155], 0\n"
"ds_write_b128 v22, a[4:7] offset:1024\n"
"v_mfma_f32_16x16x32_bf16 a[40:43], v[72:75], v[152:155], 0\n"
"v_exp_f32 v123, v123\n"
"v_mfma_f32_16x16x32_bf16 a[44:47], v[76:79], v[152:155], 0\n"
"ds_write_b128 v22, a[8:11] offset:2048\n"
"v_mfma_f32_16x16x32_bf16 a[48:51], v[80:83], v[152:155], 0\n"
"v_exp_f32 v124, v124\n"
"v_mfma_f32_16x16x32_bf16 a[52:55], v[84:87], v[152:155], 0\n"
"v_exp_f32 v125, v125\n"
"v_mfma_f32_16x16x32_bf16 a[56:59], v[88:91], v[152:155], 0\n"
"v_exp_f32 v126, v126\n"
"v_mfma_f32_16x16x32_bf16 a[60:63], v[92:95], v[152:155], 0\n"
"s_waitcnt lgkmcnt(0)\n"
"s_barrier\n"
"v_exp_f32 v127, v127\n"
"v_mfma_f32_16x16x32_bf16 v[164:167], v[60:63], v[136:139], 0\n"
"ds_write_b128 v22, a[12:15] offset:3072\n"
"v_cvt_pk_bf16_f32 v120, v120, v121\n"
"v_cvt_pk_bf16_f32 v121, v122, v123\n"
"v_cvt_pk_bf16_f32 v122, v124, v125\n"
"v_cvt_pk_bf16_f32 v123, v126, v127\n"
"s_nop 1\n"
"v_mfma_f32_16x16x32_bf16 v[172:175], v[60:63], v[120:123], v[172:175]\n"
"ds_write_b128 v22, a[16:19] offset:4096\n"
"v_mfma_f32_16x16x32_bf16 a[32:35], a[192:195], v[120:123], a[32:35]\n"
"buffer_load_dwordx4 v24, s[20:23], s79 offen offset:0 sc1 lds\n"
"v_mfma_f32_16x16x32_bf16 a[36:39], a[196:199], v[120:123], a[36:39]\n"
"ds_write_b128 v22, a[20:23] offset:5120\n"
"v_mfma_f32_16x16x32_bf16 a[40:43], a[200:203], v[120:123], a[40:43]\n"
"buffer_load_dwordx4 v25, s[20:23], s79 offen offset:1024 sc1 lds\n"
"v_mfma_f32_16x16x32_bf16 a[44:47], a[204:207], v[120:123], a[44:47]\n"
"ds_write_b128 v22, a[24:27] offset:6144\n"
"v_mfma_f32_16x16x32_bf16 a[48:51], a[208:211], v[120:123], a[48:51]\n"
"ds_write_b128 v22, a[28:31] offset:7168\n"
"v_mfma_f32_16x16x32_bf16 a[52:55], a[212:215], v[120:123], a[52:55]\n"
"buffer_load_dwordx4 v26, s[20:23], s79 offen offset:2048 sc1 lds\n"
"v_mfma_f32_16x16x32_bf16 a[56:59], a[216:219], v[120:123], a[56:59]\n"
"v_exp_f32 v134, v134\n"
"v_mfma_f32_16x16x32_bf16 a[60:63], a[220:223], v[120:123], a[60:63]\n"
"v_exp_f32 v135, v135\n"
"s_nop 0\n"
"v_cvt_pk_bf16_f32 v131, v134, v135\n"
"s_nop 1\n"
"v_mfma_f32_16x16x32_bf16 v[160:163], v[60:63], v[128:131], 0\n"
"buffer_load_dwordx4 v27, s[20:23], s79 offen offset:3072 sc1 lds\n"
"v_mfma_f32_16x16x32_bf16 v[224:227], v[64:67], v[136:139], 0\n"
"ds_write_b128 v23, a[32:35] offset:0\n"
"v_mfma_f32_16x16x32_bf16 v[228:231], v[68:71], v[136:139], 0\n"
"s_add_u32 m0, s77, 16384\n"
"ds_read_b128 a[224:227], v8 offset:0\n"
"ds_read_b128 a[228:231], v8 offset:1024\n"
"v_mfma_f32_16x16x32_bf16 v[232:235], v[72:75], v[136:139], 0\n"
"ds_read_b128 a[232:235], v8 offset:2048\n"
"ds_read_b128 a[236:239], v8 offset:3072\n"
"v_mfma_f32_16x16x32_bf16 v[236:239], v[76:79], v[136:139], 0\n"
"ds_write_b128 v23, a[36:39] offset:1024\n"
"v_mfma_f32_16x16x32_bf16 v[240:243], v[80:83], v[136:139], 0\n"
"buffer_load_dwordx2 v[20:21], v30, s[44:47], 0 offen sc1\n"
"v_mfma_f32_16x16x32_bf16 v[244:247], v[84:87], v[136:139], 0\n"
"ds_read_b128 a[240:243], v8 offset:4096\n"
"ds_read_b128 a[244:247], v8 offset:5120\n"
"v_mfma_f32_16x16x32_bf16 v[248:251], v[88:91], v[136:139], 0\n"
"ds_read_b128 a[248:251], v8 offset:6144\n"
"ds_read_b128 a[252:255], v8 offset:7168\n"
"v_mfma_f32_16x16x32_bf16 v[252:255], v[92:95], v[136:139], 0\n"
"ds_write_b128 v23, a[40:43] offset:2048\n"
"v_mfma_f32_16x16x32_bf16 v[192:195], v[64:67], v[128:131], 0\n"
"v_cndmask_b32 v4, v160, v164, s[56:57]\n"
"v_cndmask_b32 v5, v168, v172, s[56:57]\n"
"v_mfma_f32_16x16x32_bf16 v[196:199], v[68:71], v[128:131], 0\n"
"v_xor_b32 v9, 0x8000, v9\n"
"v_cndmask_b32 v4, v4, v5, s[100:101]\n"
"v_mfma_f32_16x16x32_bf16 v[200:203], v[72:75], v[128:131], 0\n"
"ds_write_b32 v19, v4\n"
"v_mfma_f32_16x16x32_bf16 v[204:207], v[76:79], v[128:131], 0\n"
"buffer_load_dwordx4 v24, s[24:27], s81 offen offset:0 sc1 lds\n"
"v_mfma_f32_16x16x32_bf16 v[208:211], v[80:83], v[128:131], 0\n"
"ds_write_b128 v23, a[44:47] offset:3072\n"
"v_mfma_f32_16x16x32_bf16 v[212:215], v[84:87], v[128:131], 0\n"
"ds_write_b128 v23, a[48:51] offset:4096\n"
"v_mfma_f32_16x16x32_bf16 v[216:219], v[88:91], v[128:131], 0\n"
"buffer_load_dwordx4 v25, s[24:27], s81 offen offset:1024 sc1 lds\n"
"v_mfma_f32_16x16x32_bf16 v[220:223], v[92:95], v[128:131], 0\n"
"ds_write_b128 v23, a[52:55] offset:5120\n"
"ds_write_b128 v23, a[56:59] offset:6144\n"
"ds_write_b128 v23, a[60:63] offset:7168\n"
"buffer_load_dwordx4 v26, s[24:27], s81 offen offset:2048 sc1 lds\n"
"buffer_load_dwordx4 v27, s[24:27], s81 offen offset:3072 sc1 lds\n"
"s_waitcnt lgkmcnt(0)\n"
"s_barrier\n"
"s_branch TAIL0_%=\n"
"N1B_30_%=:\n"
"buffer_store_dwordx4 v[192:195], v1, s[44:47], s82 offen offset:0 sc1\n"
"buffer_store_dwordx4 v[200:203], v1, s[44:47], s82 offen offset:1024 sc1\n"
"buffer_store_dwordx4 v[208:211], v1, s[44:47], s82 offen offset:2048 sc1\n"
"buffer_store_dwordx4 v[216:219], v1, s[44:47], s82 offen offset:3072 sc1\n"
"buffer_store_dwordx4 v[224:227], v1, s[44:47], s83 offen offset:0 sc1\n"
"buffer_store_dwordx4 v[232:235], v1, s[44:47], s83 offen offset:1024 sc1\n"
"buffer_store_dwordx4 v[240:243], v1, s[44:47], s83 offen offset:2048 sc1\n"
"buffer_store_dwordx4 v[248:251], v1, s[44:47], s83 offen offset:3072 sc1\n"
"s_add_u32 m0, s76, 0\n"
"s_nop 0\n"
"buffer_load_dwordx4 v24, s[20:23], s78 offen offset:0 sc1 lds\n"
"buffer_load_dwordx4 v25, s[20:23], s78 offen offset:1024 sc1 lds\n"
"buffer_load_dwordx4 v26, s[20:23], s78 offen offset:2048 sc1 lds\n"
"buffer_load_dwordx4 v27, s[20:23], s78 offen offset:3072 sc1 lds\n"
"v_mfma_f32_16x16x32_bf16 v[152:155], a[224:227], a[176:179], v[44:47]\n"
"ds_read_b128 a[192:195], v8 offset:16384\n"
"ds_read_b128 a[196:199], v8 offset:17408\n"
"v_mfma_f32_16x16x32_bf16 v[152:155], a[228:231], a[180:183], v[152:155]\n"
"ds_read_b128 a[200:203], v8 offset:18432\n"
"ds_read_b128 a[204:207], v8 offset:19456\n"
"v_mfma_f32_16x16x32_bf16 v[152:155], a[232:235], a[184:187], v[152:155]\n"
"ds_read_b128 a[208:211], v8 offset:20480\n"
"v_mov_b32 v168, 0\n"
"v_mfma_f32_16x16x32_bf16 v[152:155], a[236:239], a[188:191], v[152:155]\n"
"ds_read_b128 a[212:215], v8 offset:21504\n"
"ds_read_b128 a[216:219], v8 offset:22528\n"
"v_mfma_f32_16x16x32_bf16 v[156:159], a[252:255], a[188:191], v[48:51]\n"
"ds_read_b128 a[220:223], v8 offset:23552\n"
"v_mov_b32 v169, 0\n"
"v_mfma_f32_16x16x32_bf16 v[156:159], a[248:251], a[184:187], v[156:159]\n"
"v_mov_b32 v170, 0\n"
"v_mov_b32 v171, 0\n"
"v_mfma_f32_16x16x32_bf16 v[156:159], a[244:247], a[180:183], v[156:159]\n"
"v_xor_b32 v8, 0x8000, v8\n"
"v_mfma_f32_16x16x32_bf16 v[156:159], a[240:243], a[176:179], v[156:159]\n"
"v_exp_f32 v152, v152\n"
"v_mfma_f32_16x16x32_bf16 v[132:135], a[240:243], a[128:131], v[176:179]\n"
"s_waitcnt lgkmcnt(0)\n"
"s_barrier\n"
"v_exp_f32 v153, v153\n"
"v_mfma_f32_16x16x32_bf16 v[132:135], a[244:247], a[132:135], v[132:135]\n"
"ds_write_b128 v22, v[168:171] offset:0\n"
"v_mfma_f32_16x16x32_bf16 v[132:135], a[248:251], a[136:139], v[132:135]\n"
"v_exp_f32 v154, v154\n"
"v_mfma_f32_16x16x32_bf16 v[132:135], a[252:255], a[140:143], v[132:135]\n"
"ds_write_b128 v22, v[168:171] offset:1024\n"
"v_mfma_f32_16x16x32_bf16 v[128:131], a[236:239], a[140:143], v[176:179]\n"
"v_exp_f32 v155, v155\n"
"v_mfma_f32_16x16x32_bf16 v[128:131], a[232:235], a[136:139], v[128:131]\n"
"ds_write_b128 v22, v[168:171] offset:2048\n"
"v_mfma_f32_16x16x32_bf16 v[128:131], a[228:231], a[132:135], v[128:131]\n"
"v_exp_f32 v156, v156\n"
"v_mfma_f32_16x16x32_bf16 v[128:131], a[224:227], a[128:131], v[128:131]\n"
"ds_write_b128 v22, v[168:171] offset:3072\n"
"v_mfma_f32_16x16x32_bf16 v[136:139], a[224:227], a[144:147], v[180:183]\n"
"v_exp_f32 v157, v157\n"
"v_mfma_f32_16x16x32_bf16 v[136:139], a[228:231], a[148:151], v[136:139]\n"
"ds_write_b128 v22, v[168:171] offset:4096\n"
"v_mfma_f32_16x16x32_bf16 v[136:139], a[232:235], a[152:155], v[136:139]\n"
"v_exp_f32 v158, v158\n"
"v_mfma_f32_16x16x32_bf16 v[136:139], a[236:239], a[156:159], v[136:139]\n"
"ds_write_b128 v22, v[168:171] offset:5120\n"
"v_mfma_f32_16x16x32_bf16 v[140:143], a[252:255], a[156:159], v[180:183]\n"
"v_exp_f32 v159, v159\n"
"v_mfma_f32_16x16x32_bf16 v[140:143], a[248:251], a[152:155], v[140:143]\n"
"ds_write_b128 v22, v[168:171] offset:6144\n"
"v_mfma_f32_16x16x32_bf16 v[140:143], a[244:247], a[148:151], v[140:143]\n"
"v_exp_f32 v128, v128\n"
"v_mfma_f32_16x16x32_bf16 v[140:143], a[240:243], a[144:147], v[140:143]\n"
"ds_write_b128 v22, v[168:171] offset:7168\n"
"v_cvt_pk_bf16_f32 v152, v152, v153\n"
"v_cvt_pk_bf16_f32 v153, v154, v155\n"
"v_cvt_pk_bf16_f32 v154, v156, v157\n"
"v_cvt_pk_bf16_f32 v155, v158, v159\n"
"v_exp_f32 v129, v129\n"
"s_nop 0\n"
"v_cvt_pk_bf16_f32 v128, v128, v129\n"
"v_exp_f32 v130, v130\n"
"v_exp_f32 v131, v131\n"
"s_nop 0\n"
"v_cvt_pk_bf16_f32 v129, v130, v131\n"
"v_exp_f32 v132, v132\n"
"v_exp_f32 v133, v133\n"
"s_nop 0\n"
"v_cvt_pk_bf16_f32 v130, v132, v133\n"
"v_exp_f32 v134, v134\n"
"v_exp_f32 v135, v135\n"
"s_nop 0\n"
"v_cvt_pk_bf16_f32 v131, v134, v135\n"
"v_exp_f32 v136, v136\n"
"v_exp_f32 v137, v137\n"
"s_nop 0\n"
"v_cvt_pk_bf16_f32 v136, v136, v137\n"
"s_waitcnt vmcnt(4) lgkmcnt(0)\n"
"s_barrier\n"
"s_add_u32 m0, s77, 0\n"
"s_nop 0\n"
"buffer_load_dwordx4 v24, s[24:27], s80 offen offset:0 sc1 lds\n"
"buffer_load_dwordx4 v25, s[24:27], s80 offen offset:1024 sc1 lds\n"
"buffer_load_dwordx4 v26, s[24:27], s80 offen offset:2048 sc1 lds\n"
"buffer_load_dwordx4 v27, s[24:27], s80 offen offset:3072 sc1 lds\n"
"s_lshl_b32 s99, s60, 7\n"
"s_add_u32 s99, s99, 65536\n"
"v_mov_b32 v28, s64\n"
"v_mov_b32 v29, s65\n"
"s_cmp_eq_u32 s61, 0\n"
"s_cselect_b64 exec, 1, 0\n"
"buffer_store_dwordx2 v[28:29], off, s[44:47], s99 sc1\n"
"s_mov_b64 exec, -1\n"
"v_mfma_f32_16x16x32_bf16 v[96:99], a[192:195], a[128:131], v[52:55]\n"
"s_add_u32 m0, s76, 16384\n"
"ds_read_b128 v[64:67], v9 offset:0\n"
"ds_read_b128 v[68:71], v9 offset:1024\n"
"v_mfma_f32_16x16x32_bf16 v[96:99], a[196:199], a[132:135], v[96:99]\n"
"s_lshl_b32 s82, s61, 13\n"
"ds_read_b128 v[72:75], v9 offset:2048\n"
"ds_read_b128 v[76:79], v9 offset:3072\n"
"v_mfma_f32_16x16x32_bf16 v[96:99], a[200:203], a[136:139], v[96:99]\n"
"s_and_b32 s83, s77, 0x18000\n"
"s_xor_b32 s83, s83, 0x8000\n"
"ds_read_b128 v[80:83], v9 offset:4096\n"
"v_mfma_f32_16x16x32_bf16 v[96:99], a[204:207], a[140:143], v[96:99]\n"
"s_add_u32 s82, s82, s83\n"
"ds_read_b128 v[84:87], v9 offset:5120\n"
"ds_read_b128 v[88:91], v9 offset:6144\n"
"v_mfma_f32_16x16x32_bf16 v[100:103], a[220:223], a[140:143], v[56:59]\n"
"s_lshl_b32 s83, s67, 2\n"
"ds_read_b128 v[92:95], v9 offset:7168\n"
"v_add_u32 v23, s82, v1\n"
"v_mfma_f32_16x16x32_bf16 v[100:103], a[216:219], a[136:139], v[100:103]\n"
"s_lshl_b32 s82, s71, 3\n"
"ds_read_b128 a[224:227], v8 offset:0\n"
"ds_read_b128 a[228:231], v8 offset:1024\n"
"v_mfma_f32_16x16x32_bf16 v[100:103], a[212:215], a[132:135], v[100:103]\n"
"s_add_u32 s82, s82, s83\n"
"ds_read_b128 a[232:235], v8 offset:2048\n"
"ds_read_b128 a[236:239], v8 offset:3072\n"
"v_mfma_f32_16x16x32_bf16 v[100:103], a[208:211], a[128:131], v[100:103]\n"
"s_add_u32 s82, s82, s66\n"
"s_lshl_b32 s82, s82, 17\n"
"ds_read_b128 a[240:243], v8 offset:4096\n"
"v_mfma_f32_16x16x32_bf16 v[108:111], a[208:211], a[144:147], v[180:183]\n"
"s_lshl_b32 s83, s62, 12\n"
"ds_read_b128 a[244:247], v8 offset:5120\n"
"ds_read_b128 a[248:251], v8 offset:6144\n"
"v_mfma_f32_16x16x32_bf16 v[108:111], a[212:215], a[148:151], v[108:111]\n"
"s_add_u32 s82, s82, s83\n"
"s_add_u32 s82, s82, 37748736\n"
"ds_read_b128 a[252:255], v8 offset:7168\n"
"v_mfma_f32_16x16x32_bf16 v[108:111], a[216:219], a[152:155], v[108:111]\n"
"s_add_u32 s83, s82, 262144\n"
"v_exp_f32 v138, v138\n"
"v_mfma_f32_16x16x32_bf16 v[108:111], a[220:223], a[156:159], v[108:111]\n"
"v_exp_f32 v139, v139\n"
"v_mfma_f32_16x16x32_bf16 v[104:107], a[204:207], a[156:159], v[180:183]\n"
"v_exp_f32 v140, v140\n"
"v_mfma_f32_16x16x32_bf16 v[104:107], a[200:203], a[152:155], v[104:107]\n"
"v_exp_f32 v141, v141\n"
"v_mfma_f32_16x16x32_bf16 v[104:107], a[196:199], a[148:151], v[104:107]\n"
"v_exp_f32 v142, v142\n"
"v_mfma_f32_16x16x32_bf16 v[104:107], a[192:195], a[144:147], v[104:107]\n"
"s_waitcnt lgkmcnt(15)\n"
"ds_read_b128 a[192:195], v9 offset:16384\n"
"ds_read_b128 a[196:199], v9 offset:17408\n"
"v_mfma_f32_16x16x32_bf16 a[32:35], v[64:67], v[152:155], 0\n"
"s_waitcnt lgkmcnt(15)\n"
"ds_read_b128 a[200:203], v9 offset:18432\n"
"ds_read_b128 a[204:207], v9 offset:19456\n"
"v_mfma_f32_16x16x32_bf16 a[36:39], v[68:71], v[152:155], 0\n"
"s_waitcnt lgkmcnt(15)\n"
"ds_read_b128 a[208:211], v9 offset:20480\n"
"ds_read_b128 a[212:215], v9 offset:21504\n"
"v_mfma_f32_16x16x32_bf16 a[40:43], v[72:75], v[152:155], 0\n"
"s_waitcnt lgkmcnt(15)\n"
"ds_read_b128 a[216:219], v9 offset:22528\n"
"ds_read_b128 a[220:223], v9 offset:23552\n"
"v_mfma_f32_16x16x32_bf16 a[44:47], v[76:79], v[152:155], 0\n"
"s_waitcnt lgkmcnt(15)\n"
"v_exp_f32 v143, v143\n"
"v_mfma_f32_16x16x32_bf16 a[48:51], v[80:83], v[152:155], 0\n"
"s_waitcnt lgkmcnt(15)\n"
"v_cvt_pk_bf16_f32 v137, v138, v139\n"
"v_cvt_pk_bf16_f32 v138, v140, v141\n"
"v_mfma_f32_16x16x32_bf16 a[52:55], v[84:87], v[152:155], 0\n"
"s_waitcnt lgkmcnt(15)\n"
"v_xor_b32 v38, 0x2000, v23\n"
"v_cvt_pk_bf16_f32 v139, v142, v143\n"
"v_mfma_f32_16x16x32_bf16 a[56:59], v[88:91], v[152:155], 0\n"
"s_waitcnt lgkmcnt(15)\n"
"v_exp_f32 v96, v96\n"
"v_mfma_f32_16x16x32_bf16 a[60:63], v[92:95], v[152:155], 0\n"
"v_exp_f32 v97, v97\n"
"v_mfma_f32_16x16x32_bf16 v[172:175], v[60:63], v[152:155], 0\n"
"v_exp_f32 v98, v98\n"
"v_mfma_f32_16x16x32_bf16 v[160:163], v[60:63], v[128:131], 0\n"
"v_exp_f32 v99, v99\n"
"v_mfma_f32_16x16x32_bf16 v[192:195], v[64:67], v[128:131], 0\n"
"s_waitcnt lgkmcnt(0)\n"
"s_barrier\n"
"v_exp_f32 v100, v100\n"
"v_mfma_f32_16x16x32_bf16 v[196:199], v[68:71], v[128:131], 0\n"
"buffer_load_dwordx4 v24, s[20:23], s79 offen offset:0 sc1 lds\n"
"v_mfma_f32_16x16x32_bf16 v[200:203], v[72:75], v[128:131], 0\n"
"ds_write_b128 v23, a[32:35] offset:0\n"
"v_mfma_f32_16x16x32_bf16 v[204:207], v[76:79], v[128:131], 0\n"
"v_exp_f32 v101, v101\n"
"v_mfma_f32_16x16x32_bf16 v[208:211], v[80:83], v[128:131], 0\n"
"buffer_load_dwordx4 v25, s[20:23], s79 offen offset:1024 sc1 lds\n"
"v_mfma_f32_16x16x32_bf16 v[212:215], v[84:87], v[128:131], 0\n"
"ds_write_b128 v23, a[36:39] offset:1024\n"
"v_mfma_f32_16x16x32_bf16 v[216:219], v[88:91], v[128:131], 0\n"
"v_exp_f32 v102, v102\n"
"v_mfma_f32_16x16x32_bf16 v[220:223], v[92:95], v[128:131], 0\n"
"v_exp_f32 v103, v103\n"
"v_mfma_f32_16x16x32_bf16 v[164:167], v[60:63], v[136:139], 0\n"
"ds_write_b128 v23, a[40:43] offset:2048\n"
"v_mfma_f32_16x16x32_bf16 v[224:227], v[64:67], v[136:139], 0\n"
"buffer_load_dwordx4 v26, s[20:23], s79 offen offset:2048 sc1 lds\n"
"v_mfma_f32_16x16x32_bf16 v[228:231], v[68:71], v[136:139], 0\n"
"v_cvt_pk_bf16_f32 v96, v96, v97\n"
"v_cvt_pk_bf16_f32 v97, v98, v99\n"
"v_mfma_f32_16x16x32_bf16 v[232:235], v[72:75], v[136:139], 0\n"
"ds_write_b128 v23, a[44:47] offset:3072\n"
"v_mfma_f32_16x16x32_bf16 v[236:239], v[76:79], v[136:139], 0\n"
"buffer_load_dwordx4 v27, s[20:23], s79 offen offset:3072 sc1 lds\n"
"v_mfma_f32_16x16x32_bf16 v[240:243], v[80:83], v[136:139], 0\n"
"s_add_u32 m0, s77, 16384\n"
"v_cvt_pk_bf16_f32 v98, v100, v101\n"
"v_cvt_pk_bf16_f32 v99, v102, v103\n"
"v_mfma_f32_16x16x32_bf16 v[244:247], v[84:87], v[136:139], 0\n"
"ds_write_b128 v23, a[48:51] offset:4096\n"
"v_mfma_f32_16x16x32_bf16 v[248:251], v[88:91], v[136:139], 0\n"
"buffer_load_dwordx2 v[20:21], v30, s[44:47], 0 offen sc1\n"
"v_mfma_f32_16x16x32_bf16 v[252:255], v[92:95], v[136:139], 0\n"
"v_exp_f32 v104, v104\n"
"v_mfma_f32_16x16x32_bf16 v[160:163], v[60:63], v[96:99], v[160:163]\n"
"ds_write_b128 v23, a[52:55] offset:5120\n"
"v_mfma_f32_16x16x32_bf16 v[192:195], a[192:195], v[96:99], v[192:195]\n"
"buffer_load_dwordx4 v24, s[24:27], s81 offen offset:0 sc1 lds\n"
"v_mfma_f32_16x16x32_bf16 v[196:199], a[196:199], v[96:99], v[196:199]\n"
"v_exp_f32 v105, v105\n"
"v_mfma_f32_16x16x32_bf16 v[200:203], a[200:203], v[96:99], v[200:203]\n"
"v_exp_f32 v106, v106\n"
"v_mfma_f32_16x16x32_bf16 v[204:207], a[204:207], v[96:99], v[204:207]\n"
"v_exp_f32 v107, v107\n"
"v_cvt_pk_bf16_f32 v104, v104, v105\n"
"v_cvt_pk_bf16_f32 v105, v106, v107\n"
"v_exp_f32 v108, v108\n"
"v_exp_f32 v109, v109\n"
"s_nop 0\n"
"v_cvt_pk_bf16_f32 v106, v108, v109\n"
"v_exp_f32 v110, v110\n"
"v_exp_f32 v111, v111\n"
"s_nop 0\n"
"v_cvt_pk_bf16_f32 v107, v110, v111\n"
"s_nop 1\n"
"v_mfma_f32_16x16x32_bf16 v[164:167], v[60:63], v[104:107], v[164:167]\n"
"ds_write_b128 v23, a[56:59] offset:6144\n"
"v_mfma_f32_16x16x32_bf16 v[208:211], a[208:211], v[96:99], v[208:211]\n"
"buffer_load_dwordx4 v25, s[24:27], s81 offen offset:1024 sc1 lds\n"
"v_mfma_f32_16x16x32_bf16 v[212:215], a[212:215], v[96:99], v[212:215]\n"
"ds_write_b128 v23, a[60:63] offset:7168\n"
"v_mfma_f32_16x16x32_bf16 v[216:219], a[216:219], v[96:99], v[216:219]\n"
"buffer_load_dwordx4 v26, s[24:27], s81 offen offset:2048 sc1 lds\n"
"v_mfma_f32_16x16x32_bf16 v[220:223], a[220:223], v[96:99], v[220:223]\n"
"v_cndmask_b32 v4, v160, v164, s[56:57]\n"
"v_cndmask_b32 v5, v168, v172, s[56:57]\n"
"v_mfma_f32_16x16x32_bf16 v[224:227], a[192:195], v[104:107], v[224:227]\n"
"v_xor_b32 v9, 0x8000, v9\n"
"v_cndmask_b32 v4, v4, v5, s[100:101]\n"
"v_mfma_f32_16x16x32_bf16 v[228:231], a[196:199], v[104:107], v[228:231]\n"
"ds_write_b32 v19, v4\n"
"v_mfma_f32_16x16x32_bf16 v[232:235], a[200:203], v[104:107], v[232:235]\n"
"buffer_load_dwordx4 v27, s[24:27], s81 offen offset:3072 sc1 lds\n"
"v_mfma_f32_16x16x32_bf16 v[236:239], a[204:207], v[104:107], v[236:239]\n"
"v_mfma_f32_16x16x32_bf16 v[240:243], a[208:211], v[104:107], v[240:243]\n"
"v_mfma_f32_16x16x32_bf16 v[244:247], a[212:215], v[104:107], v[244:247]\n"
"v_mfma_f32_16x16x32_bf16 v[248:251], a[216:219], v[104:107], v[248:251]\n"
"v_mfma_f32_16x16x32_bf16 v[252:255], a[220:223], v[104:107], v[252:255]\n"
"s_waitcnt lgkmcnt(0)\n"
"s_barrier\n"
"s_branch TAIL0_%=\n"
"B1_26_%=:\n"
"ODD1_%=:\n"
"s_cbranch_execz PADRET_%=\n"
"v_mfma_f32_16x16x32_bf16 v[152:155], a[192:195], a[176:179], v[44:47]\n"
"s_add_u32 m0, s77, 0\n"
"ds_read_b128 a[224:227], v9 offset:16384\n"
"v_mfma_f32_16x16x32_bf16 v[152:155], a[196:199], a[180:183], v[152:155]\n"
"ds_read_b128 a[228:231], v9 offset:17408\n"
"v_mfma_f32_16x16x32_bf16 v[152:155], a[200:203], a[184:187], v[152:155]\n"
"buffer_load_dwordx4 v24, s[24:27], s80 offen offset:0 sc1 lds\n"
"v_mfma_f32_16x16x32_bf16 v[152:155], a[204:207], a[188:191], v[152:155]\n"
"ds_read_b128 a[232:235], v9 offset:18432\n"
"v_mfma_f32_16x16x32_bf16 v[156:159], a[220:223], a[188:191], v[48:51]\n"
"v_exp_f32 v118, v118\n"
"v_mfma_f32_16x16x32_bf16 v[156:159], a[216:219], a[184:187], v[156:159]\n"
"ds_read_b128 a[236:239], v9 offset:19456\n"
"v_mfma_f32_16x16x32_bf16 v[156:159], a[212:215], a[180:183], v[156:159]\n"
"buffer_load_dwordx4 v25, s[24:27], s80 offen offset:1024 sc1 lds\n"
"v_mfma_f32_16x16x32_bf16 v[156:159], a[208:211], a[176:179], v[156:159]\n"
"ds_read_b128 a[240:243], v9 offset:20480\n"
"v_mfma_f32_16x16x32_bf16 v[132:135], a[208:211], a[128:131], v[176:179]\n"
"v_exp_f32 v119, v119\n"
"v_mfma_f32_16x16x32_bf16 v[132:135], a[212:215], a[132:135], v[132:135]\n"
"ds_read_b128 a[244:247], v9 offset:21504\n"
"v_cvt_pk_bf16_f32 v115, v118, v119\n"
"v_mfma_f32_16x16x32_bf16 v[132:135], a[216:219], a[136:139], v[132:135]\n"
"buffer_load_dwordx4 v26, s[24:27], s80 offen offset:2048 sc1 lds\n"
"v_mfma_f32_16x16x32_bf16 v[132:135], a[220:223], a[140:143], v[132:135]\n"
"ds_read_b128 a[248:251], v9 offset:22528\n"
"v_mfma_f32_16x16x32_bf16 v[128:131], a[204:207], a[140:143], v[176:179]\n"
"v_exp_f32 v120, v120\n"
"v_mfma_f32_16x16x32_bf16 v[128:131], a[200:203], a[136:139], v[128:131]\n"
"ds_read_b128 a[252:255], v9 offset:23552\n"
"v_xor_b32 v9, 0x8000, v9\n"
"v_mfma_f32_16x16x32_bf16 v[128:131], a[196:199], a[132:135], v[128:131]\n"
"buffer_load_dwordx4 v27, s[24:27], s80 offen offset:3072 sc1 lds\n"
"v_mfma_f32_16x16x32_bf16 v[128:131], a[192:195], a[128:131], v[128:131]\n"
"s_add_u32 m0, s76, 0\n"
"v_exp_f32 v121, v121\n"
"v_mfma_f32_16x16x32_bf16 v[136:139], a[192:195], a[144:147], v[180:183]\n"
"v_exp_f32 v122, v122\n"
"v_mfma_f32_16x16x32_bf16 v[136:139], a[196:199], a[148:151], v[136:139]\n"
"buffer_load_dwordx4 v24, s[20:23], s78 offen offset:0 sc1 lds\n"
"v_mfma_f32_16x16x32_bf16 v[136:139], a[200:203], a[152:155], v[136:139]\n"
"v_exp_f32 v123, v123\n"
"v_mfma_f32_16x16x32_bf16 v[136:139], a[204:207], a[156:159], v[136:139]\n"
"v_exp_f32 v124, v124\n"
"v_mfma_f32_16x16x32_bf16 v[140:143], a[220:223], a[156:159], v[180:183]\n"
"v_exp_f32 v125, v125\n"
"v_mfma_f32_16x16x32_bf16 v[140:143], a[216:219], a[152:155], v[140:143]\n"
"buffer_load_dwordx4 v25, s[20:23], s78 offen offset:1024 sc1 lds\n"
"v_mfma_f32_16x16x32_bf16 v[140:143], a[212:215], a[148:151], v[140:143]\n"
"v_exp_f32 v126, v126\n"
"v_mfma_f32_16x16x32_bf16 v[140:143], a[208:211], a[144:147], v[140:143]\n"
"s_waitcnt lgkmcnt(7)\n"
"v_exp_f32 v127, v127\n"
"v_mfma_f32_16x16x32_bf16 a[0:3], a[224:227], v[112:115], a[0:3]\n"
"s_waitcnt lgkmcnt(6)\n"
"ds_read_b128 a[192:195], v8 offset:16384\n"
"v_cvt_pk_bf16_f32 v120, v120, v121\n"
"v_mfma_f32_16x16x32_bf16 a[4:7], a[228:231], v[112:115], a[4:7]\n"
"s_waitcnt lgkmcnt(6)\n"
"ds_read_b128 a[196:199], v8 offset:17408\n"
"v_cvt_pk_bf16_f32 v121, v122, v123\n"
"v_mfma_f32_16x16x32_bf16 a[8:11], a[232:235], v[112:115], a[8:11]\n"
"buffer_load_dwordx4 v26, s[20:23], s78 offen offset:2048 sc1 lds\n"
"s_waitcnt lgkmcnt(6)\n"
"v_mfma_f32_16x16x32_bf16 a[12:15], a[236:239], v[112:115], a[12:15]\n"
"s_waitcnt lgkmcnt(5)\n"
"ds_read_b128 a[200:203], v8 offset:18432\n"
"ds_read_b128 a[204:207], v8 offset:19456\n"
"v_mfma_f32_16x16x32_bf16 a[16:19], a[240:243], v[112:115], a[16:19]\n"
"s_waitcnt lgkmcnt(6)\n"
"ds_read_b128 a[208:211], v8 offset:20480\n"
"v_cvt_pk_bf16_f32 v122, v124, v125\n"
"v_mfma_f32_16x16x32_bf16 a[20:23], a[244:247], v[112:115], a[20:23]\n"
"s_waitcnt lgkmcnt(6)\n"
"ds_read_b128 a[212:215], v8 offset:21504\n"
"v_cvt_pk_bf16_f32 v123, v126, v127\n"
"v_mfma_f32_16x16x32_bf16 a[24:27], a[248:251], v[112:115], a[24:27]\n"
"buffer_load_dwordx4 v27, s[20:23], s78 offen offset:3072 sc1 lds\n"
"s_waitcnt lgkmcnt(6)\n"
"v_mfma_f32_16x16x32_bf16 a[28:31], a[252:255], v[112:115], a[28:31]\n"
"ds_read_b128 a[216:219], v8 offset:22528\n"
"ds_read_b128 a[220:223], v8 offset:23552\n"
"v_mfma_f32_16x16x32_bf16 v[168:171], v[60:63], v[112:115], v[168:171]\n"
"v_exp_f32 v152, v152\n"
"v_mfma_f32_16x16x32_bf16 v[192:195], a[224:227], v[96:99], v[192:195]\n"
"v_exp_f32 v153, v153\n"
"v_mfma_f32_16x16x32_bf16 v[196:199], a[228:231], v[96:99], v[196:199]\n"
"ds_write_b128 v22, a[0:3] offset:0\n"
"v_mfma_f32_16x16x32_bf16 v[200:203], a[232:235], v[96:99], v[200:203]\n"
"v_exp_f32 v154, v154\n"
"v_mfma_f32_16x16x32_bf16 v[204:207], a[236:239], v[96:99], v[204:207]\n"
"v_exp_f32 v155, v155\n"
"v_mfma_f32_16x16x32_bf16 v[208:211], a[240:243], v[96:99], v[208:211]\n"
"ds_write_b128 v22, a[4:7] offset:1024\n"
"v_mfma_f32_16x16x32_bf16 v[212:215], a[244:247], v[96:99], v[212:215]\n"
"v_exp_f32 v156, v156\n"
"v_mfma_f32_16x16x32_bf16 v[216:219], a[248:251], v[96:99], v[216:219]\n"
"v_exp_f32 v157, v157\n"
"v_mfma_f32_16x16x32_bf16 v[220:223], a[252:255], v[96:99], v[220:223]\n"
"ds_write_b128 v22, a[8:11] offset:2048\n"
"v_mfma_f32_16x16x32_bf16 v[160:163], v[60:63], v[96:99], v[160:163]\n"
"v_exp_f32 v158, v158\n"
"v_mfma_f32_16x16x32_bf16 v[224:227], a[224:227], v[104:107], v[224:227]\n"
"v_exp_f32 v159, v159\n"
"v_mfma_f32_16x16x32_bf16 v[228:231], a[228:231], v[104:107], v[228:231]\n"
"ds_write_b128 v22, a[12:15] offset:3072\n"
"v_mfma_f32_16x16x32_bf16 v[232:235], a[232:235], v[104:107], v[232:235]\n"
"v_exp_f32 v128, v128\n"
"v_mfma_f32_16x16x32_bf16 v[236:239], a[236:239], v[104:107], v[236:239]\n"
"v_exp_f32 v129, v129\n"
"v_mfma_f32_16x16x32_bf16 v[240:243], a[240:243], v[104:107], v[240:243]\n"
"ds_write_b128 v22, a[16:19] offset:4096\n"
"v_mfma_f32_16x16x32_bf16 v[244:247], a[244:247], v[104:107], v[244:247]\n"
"v_exp_f32 v130, v130\n"
"v_mfma_f32_16x16x32_bf16 v[248:251], a[248:251], v[104:107], v[248:251]\n"
"v_exp_f32 v131, v131\n"
"v_mfma_f32_16x16x32_bf16 v[252:255], a[252:255], v[104:107], v[252:255]\n"
"ds_write_b128 v22, a[20:23] offset:5120\n"
"v_mfma_f32_16x16x32_bf16 v[164:167], v[60:63], v[104:107], v[164:167]\n"
"v_exp_f32 v132, v132\n"
"v_mfma_f32_16x16x32_bf16 a[32:35], a[224:227], v[120:123], a[32:35]\n"
"v_exp_f32 v133, v133\n"
"v_mfma_f32_16x16x32_bf16 a[36:39], a[228:231], v[120:123], a[36:39]\n"
"ds_write_b128 v22, a[24:27] offset:6144\n"
"v_mfma_f32_16x16x32_bf16 a[40:43], a[232:235], v[120:123], a[40:43]\n"
"v_exp_f32 v134, v134\n"
"v_mfma_f32_16x16x32_bf16 a[44:47], a[236:239], v[120:123], a[44:47]\n"
"v_exp_f32 v135, v135\n"
"v_mfma_f32_16x16x32_bf16 a[48:51], a[240:243], v[120:123], a[48:51]\n"
"ds_write_b128 v22, a[28:31] offset:7168\n"
"v_mfma_f32_16x16x32_bf16 a[52:55], a[244:247], v[120:123], a[52:55]\n"
"v_exp_f32 v136, v136\n"
"v_mfma_f32_16x16x32_bf16 a[56:59], a[248:251], v[120:123], a[56:59]\n"
"v_exp_f32 v137, v137\n"
"v_mfma_f32_16x16x32_bf16 a[60:63], a[252:255], v[120:123], a[60:63]\n"
"s_waitcnt vmcnt(8)\n"
"v_xor_b32 v8, 0x8000, v8\n"
"v_cvt_pk_bf16_f32 v152, v152, v153\n"
"v_mfma_f32_16x16x32_bf16 v[172:175], v[60:63], v[120:123], v[172:175]\n"
"s_waitcnt lgkmcnt(0)\n"
"s_barrier\n"
"v_cvt_pk_bf16_f32 v153, v154, v155\n"
"v_cvt_pk_bf16_f32 v154, v156, v157\n"
"v_cvt_pk_bf16_f32 v155, v158, v159\n"
"v_cvt_pk_bf16_f32 v128, v128, v129\n"
"v_cvt_pk_bf16_f32 v129, v130, v131\n"
"v_cvt_pk_bf16_f32 v130, v132, v133\n"
"v_cvt_pk_bf16_f32 v131, v134, v135\n"
"v_cvt_pk_bf16_f32 v136, v136, v137\n"
"F1_28_%=:\n"
"v_mfma_f32_16x16x32_bf16 v[96:99], a[192:195], a[128:131], v[52:55]\n"
"s_add_u32 m0, s76, 16384\n"
"ds_read_b128 v[64:67], v9 offset:0\n"
"ds_read_b128 v[68:71], v9 offset:1024\n"
"v_mfma_f32_16x16x32_bf16 v[96:99], a[196:199], a[132:135], v[96:99]\n"
"s_lshl_b32 s82, s61, 13\n"
"ds_read_b128 v[72:75], v9 offset:2048\n"
"ds_read_b128 v[76:79], v9 offset:3072\n"
"v_mfma_f32_16x16x32_bf16 v[96:99], a[200:203], a[136:139], v[96:99]\n"
"s_and_b32 s83, s77, 0x18000\n"
"s_xor_b32 s83, s83, 0x8000\n"
"ds_read_b128 v[80:83], v9 offset:4096\n"
"v_mfma_f32_16x16x32_bf16 v[96:99], a[204:207], a[140:143], v[96:99]\n"
"PG13_%=:\n"
"s_cbranch_execz PADRET_%=\n"
"s_add_u32 s82, s82, s83\n"
"ds_read_b128 v[84:87], v9 offset:5120\n"
"ds_read_b128 v[88:91], v9 offset:6144\n"
"v_mfma_f32_16x16x32_bf16 v[100:103], a[220:223], a[140:143], v[56:59]\n"
"s_lshl_b32 s83, s67, 2\n"
"ds_read_b128 v[92:95], v9 offset:7168\n"
"v_add_u32 v23, s82, v1\n"
"v_mfma_f32_16x16x32_bf16 v[100:103], a[216:219], a[136:139], v[100:103]\n"
"s_lshl_b32 s82, s71, 3\n"
"ds_read_b128 a[224:227], v8 offset:0\n"
"ds_read_b128 a[228:231], v8 offset:1024\n"
"v_mfma_f32_16x16x32_bf16 v[100:103], a[212:215], a[132:135], v[100:103]\n"
"s_add_u32 s82, s82, s83\n"
"ds_read_b128 a[232:235], v8 offset:2048\n"
"ds_read_b128 a[236:239], v8 offset:3072\n"
"v_mfma_f32_16x16x32_bf16 v[100:103], a[208:211], a[128:131], v[100:103]\n"
"s_add_u32 s82, s82, s66\n"
"s_lshl_b32 s82, s82, 17\n"
"ds_read_b128 a[240:243], v8 offset:4096\n"
"v_mfma_f32_16x16x32_bf16 v[108:111], a[208:211], a[144:147], v[180:183]\n"
"s_lshl_b32 s83, s62, 12\n"
"ds_read_b128 a[244:247], v8 offset:5120\n"
"ds_read_b128 a[248:251], v8 offset:6144\n"
"v_mfma_f32_16x16x32_bf16 v[108:111], a[212:215], a[148:151], v[108:111]\n"
"s_add_u32 s82, s82, s83\n"
"s_add_u32 s82, s82, 37748736\n"
"ds_read_b128 a[252:255], v8 offset:7168\n"
"v_mfma_f32_16x16x32_bf16 v[108:111], a[216:219], a[152:155], v[108:111]\n"
"s_add_u32 s83, s82, 262144\n"
"v_exp_f32 v138, v138\n"
"v_mfma_f32_16x16x32_bf16 v[108:111], a[220:223], a[156:159], v[108:111]\n"
"v_exp_f32 v139, v139\n"
"v_mfma_f32_16x16x32_bf16 v[104:107], a[204:207], a[156:159], v[180:183]\n"
"v_exp_f32 v140, v140\n"
"v_mfma_f32_16x16x32_bf16 v[104:107], a[200:203], a[152:155], v[104:107]\n"
"v_exp_f32 v141, v141\n"
"v_mfma_f32_16x16x32_bf16 v[104:107], a[196:199], a[148:151], v[104:107]\n"
"v_exp_f32 v142, v142\n"
"v_mfma_f32_16x16x32_bf16 v[104:107], a[192:195], a[144:147], v[104:107]\n"
"s_waitcnt lgkmcnt(15)\n"
"ds_read_b128 a[192:195], v9 offset:16384\n"
"ds_read_b128 a[196:199], v9 offset:17408\n"
"v_mfma_f32_16x16x32_bf16 a[32:35], v[64:67], v[152:155], a[32:35]\n"
"s_waitcnt lgkmcnt(15)\n"
"ds_read_b128 a[200:203], v9 offset:18432\n"
"ds_read_b128 a[204:207], v9 offset:19456\n"
"v_mfma_f32_16x16x32_bf16 a[36:39], v[68:71], v[152:155], a[36:39]\n"
"s_waitcnt lgkmcnt(15)\n"
"ds_read_b128 a[208:211], v9 offset:20480\n"
"ds_read_b128 a[212:215], v9 offset:21504\n"
"v_mfma_f32_16x16x32_bf16 a[40:43], v[72:75], v[152:155], a[40:43]\n"
"s_waitcnt lgkmcnt(15)\n"
"ds_read_b128 a[216:219], v9 offset:22528\n"
"ds_read_b128 a[220:223], v9 offset:23552\n"
"v_mfma_f32_16x16x32_bf16 a[44:47], v[76:79], v[152:155], a[44:47]\n"
"s_waitcnt lgkmcnt(15)\n"
"v_exp_f32 v143, v143\n"
"v_mfma_f32_16x16x32_bf16 a[48:51], v[80:83], v[152:155], a[48:51]\n"
"s_waitcnt lgkmcnt(15)\n"
"v_cvt_pk_bf16_f32 v137, v138, v139\n"
"v_cvt_pk_bf16_f32 v138, v140, v141\n"
"v_mfma_f32_16x16x32_bf16 a[52:55], v[84:87], v[152:155], a[52:55]\n"
"s_waitcnt lgkmcnt(15)\n"
"v_xor_b32 v38, 0x2000, v23\n"
"v_cvt_pk_bf16_f32 v139, v142, v143\n"
"v_mfma_f32_16x16x32_bf16 a[56:59], v[88:91], v[152:155], a[56:59]\n"
"s_waitcnt lgkmcnt(15)\n"
"v_exp_f32 v96, v96\n"
"v_mfma_f32_16x16x32_bf16 a[60:63], v[92:95], v[152:155], a[60:63]\n"
"v_exp_f32 v97, v97\n"
"v_mfma_f32_16x16x32_bf16 v[172:175], v[60:63], v[152:155], v[172:175]\n"
"v_exp_f32 v98, v98\n"
"v_mfma_f32_16x16x32_bf16 v[160:163], v[60:63], v[128:131], v[160:163]\n"
"v_exp_f32 v99, v99\n"
"v_mfma_f32_16x16x32_bf16 v[192:195], v[64:67], v[128:131], v[192:195]\n"
"s_waitcnt lgkmcnt(0)\n"
"s_barrier\n"
"v_exp_f32 v100, v100\n"
"v_mfma_f32_16x16x32_bf16 v[196:199], v[68:71], v[128:131], v[196:199]\n"
"buffer_load_dwordx4 v24, s[20:23], s79 offen offset:0 sc1 lds\n"
"v_mfma_f32_16x16x32_bf16 v[200:203], v[72:75], v[128:131], v[200:203]\n"
"ds_write_b128 v23, a[32:35] offset:0\n"
"v_mfma_f32_16x16x32_bf16 v[204:207], v[76:79], v[128:131], v[204:207]\n"
"v_exp_f32 v101, v101\n"
"v_mfma_f32_16x16x32_bf16 v[208:211], v[80:83], v[128:131], v[208:211]\n"
"buffer_load_dwordx4 v25, s[20:23], s79 offen offset:1024 sc1 lds\n"
"v_mfma_f32_16x16x32_bf16 v[212:215], v[84:87], v[128:131], v[212:215]\n"
"ds_write_b128 v23, a[36:39] offset:1024\n"
"v_mfma_f32_16x16x32_bf16 v[216:219], v[88:91], v[128:131], v[216:219]\n"
"v_exp_f32 v102, v102\n"
"v_mfma_f32_16x16x32_bf16 v[220:223], v[92:95], v[128:131], v[220:223]\n"
"v_exp_f32 v103, v103\n"
"v_mfma_f32_16x16x32_bf16 v[164:167], v[60:63], v[136:139], v[164:167]\n"
"ds_write_b128 v23, a[40:43] offset:2048\n"
"v_mfma_f32_16x16x32_bf16 v[224:227], v[64:67], v[136:139], v[224:227]\n"
"buffer_load_dwordx4 v26, s[20:23], s79 offen offset:2048 sc1 lds\n"
"v_mfma_f32_16x16x32_bf16 v[228:231], v[68:71], v[136:139], v[228:231]\n"
"v_cvt_pk_bf16_f32 v96, v96, v97\n"
"v_cvt_pk_bf16_f32 v97, v98, v99\n"
"v_mfma_f32_16x16x32_bf16 v[232:235], v[72:75], v[136:139], v[232:235]\n"
"ds_write_b128 v23, a[44:47] offset:3072\n"
"v_mfma_f32_16x16x32_bf16 v[236:239], v[76:79], v[136:139], v[236:239]\n"
"buffer_load_dwordx4 v27, s[20:23], s79 offen offset:3072 sc1 lds\n"
"v_mfma_f32_16x16x32_bf16 v[240:243], v[80:83], v[136:139], v[240:243]\n"
"s_add_u32 m0, s77, 16384\n"
"v_cvt_pk_bf16_f32 v98, v100, v101\n"
"v_cvt_pk_bf16_f32 v99, v102, v103\n"
"v_mfma_f32_16x16x32_bf16 v[244:247], v[84:87], v[136:139], v[244:247]\n"
"ds_write_b128 v23, a[48:51] offset:4096\n"
"v_mfma_f32_16x16x32_bf16 v[248:251], v[88:91], v[136:139], v[248:251]\n"
"buffer_load_dwordx2 v[20:21], v30, s[44:47], 0 offen sc1\n"
"v_mfma_f32_16x16x32_bf16 v[252:255], v[92:95], v[136:139], v[252:255]\n"
"v_exp_f32 v104, v104\n"
"v_mfma_f32_16x16x32_bf16 v[160:163], v[60:63], v[96:99], v[160:163]\n"
"ds_write_b128 v23, a[52:55] offset:5120\n"
"v_mfma_f32_16x16x32_bf16 v[192:195], a[192:195], v[96:99], v[192:195]\n"
"buffer_load_dwordx4 v24, s[24:27], s81 offen offset:0 sc1 lds\n"
"v_mfma_f32_16x16x32_bf16 v[196:199], a[196:199], v[96:99], v[196:199]\n"
"v_exp_f32 v105, v105\n"
"v_mfma_f32_16x16x32_bf16 v[200:203], a[200:203], v[96:99], v[200:203]\n"
"v_exp_f32 v106, v106\n"
"v_mfma_f32_16x16x32_bf16 v[204:207], a[204:207], v[96:99], v[204:207]\n"
"v_exp_f32 v107, v107\n"
"v_cvt_pk_bf16_f32 v104, v104, v105\n"
"v_cvt_pk_bf16_f32 v105, v106, v107\n"
"v_exp_f32 v108, v108\n"
"v_exp_f32 v109, v109\n"
"s_nop 0\n"
"v_cvt_pk_bf16_f32 v106, v108, v109\n"
"v_exp_f32 v110, v110\n"
"v_exp_f32 v111, v111\n"
"s_nop 0\n"
"v_cvt_pk_bf16_f32 v107, v110, v111\n"
"s_nop 1\n"
"v_mfma_f32_16x16x32_bf16 v[164:167], v[60:63], v[104:107], v[164:167]\n"
"ds_write_b128 v23, a[56:59] offset:6144\n"
"v_mfma_f32_16x16x32_bf16 v[208:211], a[208:211], v[96:99], v[208:211]\n"
"buffer_load_dwordx4 v25, s[24:27], s81 offen offset:1024 sc1 lds\n"
"v_mfma_f32_16x16x32_bf16 v[212:215], a[212:215], v[96:99], v[212:215]\n"
"ds_write_b128 v23, a[60:63] offset:7168\n"
"v_mfma_f32_16x16x32_bf16 v[216:219], a[216:219], v[96:99], v[216:219]\n"
"buffer_load_dwordx4 v26, s[24:27], s81 offen offset:2048 sc1 lds\n"
"v_mfma_f32_16x16x32_bf16 v[220:223], a[220:223], v[96:99], v[220:223]\n"
"v_cndmask_b32 v4, v160, v164, s[56:57]\n"
"v_cndmask_b32 v5, v168, v172, s[56:57]\n"
"v_mfma_f32_16x16x32_bf16 v[224:227], a[192:195], v[104:107], v[224:227]\n"
"v_xor_b32 v9, 0x8000, v9\n"
"v_cndmask_b32 v4, v4, v5, s[100:101]\n"
"v_mfma_f32_16x16x32_bf16 v[228:231], a[196:199], v[104:107], v[228:231]\n"
"ds_write_b32 v19, v4\n"
"v_mfma_f32_16x16x32_bf16 v[232:235], a[200:203], v[104:107], v[232:235]\n"
"buffer_load_dwordx4 v27, s[24:27], s81 offen offset:3072 sc1 lds\n"
"v_mfma_f32_16x16x32_bf16 v[236:239], a[204:207], v[104:107], v[236:239]\n"
"v_mfma_f32_16x16x32_bf16 v[240:243], a[208:211], v[104:107], v[240:243]\n"
"v_mfma_f32_16x16x32_bf16 v[244:247], a[212:215], v[104:107], v[244:247]\n"
"v_mfma_f32_16x16x32_bf16 v[248:251], a[216:219], v[104:107], v[248:251]\n"
"v_mfma_f32_16x16x32_bf16 v[252:255], a[220:223], v[104:107], v[252:255]\n"
"s_waitcnt lgkmcnt(0)\n"
"s_barrier\n"
"TAIL0_%=:\n"
"s_cbranch_execz PADRET_%=\n"
"s_cmp_lg_u32 s67, 0\n"
"s_cselect_b64 exec, -1, 0\n"
"ds_read_b128 v[96:99], v37\n"
"s_mov_b64 exec, -1\n"
"ds_read_b128 v[128:131], v39\n"
"ds_read_b128 v[132:135], v40\n"
"ds_read_b128 v[136:139], v41\n"
"ds_read_b128 v[100:103], v37 offset:1024\n"
"ds_read_b128 v[104:107], v37 offset:2048\n"
"ds_read_b128 v[108:111], v37 offset:3072\n"
"ds_read_b128 v[112:115], v37 offset:4096\n"
"ds_read_b128 v[116:119], v37 offset:5120\n"
"ds_read_b128 v[120:123], v37 offset:6144\n"
"ds_read_b128 v[124:127], v37 offset:7168\n"
"ds_read_b128 v[64:67], v38 offset:0\n"
"ds_read_b128 v[68:71], v38 offset:1024\n"
"ds_read_b128 v[72:75], v38 offset:2048\n"
"ds_read_b128 v[76:79], v38 offset:3072\n"
"ds_read_b128 v[80:83], v38 offset:4096\n"
"ds_read_b128 v[84:87], v38 offset:5120\n"
"ds_read_b128 v[88:91], v38 offset:6144\n"
"ds_read_b128 v[92:95], v38 offset:7168\n"
"s_waitcnt lgkmcnt(15)\n"
"v_add_f32 v4, v160, v130\n"
"v_add_f32 v5, v164, v131\n"
"v_add_f32 v6, v168, v128\n"
"v_add_f32 v7, v172, v129\n"
"v_add_f32 v132, v132, v138\n"
"v_add_f32 v133, v133, v139\n"
"v_add_f32 v134, v134, v136\n"
"v_add_f32 v135, v135, v137\n"
"v_min3_f32 v32, v4, v5, v6\n"
"v_min3_f32 v33, v7, v132, v133\n"
"v_min3_f32 v32, v32, v134, v135\n"
"v_min_f32 v32, v32, v33\n"
"v_pk_add_f32 v[6:7], v[6:7], v[132:133]\n"
"v_pk_add_f32 v[134:135], v[134:135], v[4:5]\n"
"v_cmp_nle_f32 vcc, 0x21800000, v32\n"
"v_pk_add_f32 v[6:7], v[6:7], v[134:135]\n"
"s_mov_b64 s[96:97], vcc\n"
"v_add_f32 v33, v6, v7\n"
"v_rcp_f32 v6, v5\n"
"v_rcp_f32 v4, v4\n"
"v_cmp_nge_f32 vcc, 0x71800000, v33\n"
"s_nop 0\n"
"s_or_b64 vcc, vcc, s[96:97]\n"
"s_cmp_lg_u32 s93, 0\n"
"s_cselect_b64 vcc, 0, vcc\n"
"s_cbranch_vccnz SAFE_%=\n"
"s_xor_b32 s72, s88, 0x1000\n"
"s_xor_b32 s73, s16, 0x20000\n"
"s_lshl_b32 s74, s61, 12\n"
"v_xor_b32 v16, 0x2000, v30\n"
"v_add_u32 v16, 32768, v16\n"
"v_lshrrev_b32 v34, 2, v0\n"
"v_and_b32 v35, 7, v34\n"
"v_lshlrev_b32 v34, 8, v34\n"
"v_lshl_add_u32 v34, v35, 5, v34\n"
"v_and_b32 v35, 3, v0\n"
"v_lshl_add_u32 v34, v35, 3, v34\n"
"v_xor_b32 v8, 0, v34\n"
"v_xor_b32 v9, 32, v34\n"
"v_xor_b32 v10, 64, v34\n"
"v_xor_b32 v11, 96, v34\n"
"v_xor_b32 v12, 128, v34\n"
"v_xor_b32 v13, 160, v34\n"
"v_xor_b32 v14, 192, v34\n"
"v_xor_b32 v15, 224, v34\n"
"s_waitcnt vmcnt(4)\n"
"v_cmp_ne_u64 vcc, s[64:65], v[20:21]\n"
"s_nop 1\n"
"s_cbranch_vccz FOK_40_%=\n"
"FSP_41_%=:\n"
"s_cmp_eq_u32 s69, 0\n"
"s_cbranch_scc1 FOK_40_%=\n"
"s_sub_u32 s69, s69, 1\n"
"s_sleep 2\n"
"s_mov_b64 exec, vcc\n"
"buffer_load_dwordx2 v[20:21], v30, s[44:47], 0 offen sc1\n"
"s_mov_b64 exec, -1\n"
"s_waitcnt vmcnt(0)\n"
"v_cmp_ne_u64 vcc, s[64:65], v[20:21]\n"
"s_nop 1\n"
"s_cbranch_vccnz FSP_41_%=\n"
"FOK_40_%=:\n"
"s_add_u32 s84, s88, 0\n"
"buffer_load_dwordx4 a[64:67], v1, s[44:47], s84 offen offset:0 sc1\n"
"buffer_load_dwordx4 a[68:71], v31, s[44:47], s84 offen offset:0 sc1\n"
"buffer_load_dwordx4 a[72:75], v1, s[44:47], s84 offen offset:1024 sc1\n"
"buffer_load_dwordx4 a[76:79], v31, s[44:47], s84 offen offset:1024 sc1\n"
"buffer_load_dwordx4 a[80:83], v1, s[44:47], s84 offen offset:2048 sc1\n"
"buffer_load_dwordx4 a[84:87], v31, s[44:47], s84 offen offset:2048 sc1\n"
"buffer_load_dwordx4 a[88:91], v1, s[44:47], s84 offen offset:3072 sc1\n"
"buffer_load_dwordx4 a[92:95], v31, s[44:47], s84 offen offset:3072 sc1\n"
"s_waitcnt lgkmcnt(14)\n"
"v_pk_add_f32 v[192:193], v[192:193], v[96:97]\n"
"v_pk_add_f32 v[194:195], v[194:195], v[98:99]\n"
"v_pk_add_f32 v[196:197], v[196:197], v[100:101]\n"
"v_pk_add_f32 v[198:199], v[198:199], v[102:103]\n"
"v_pk_mul_f32 v[192:193], v[192:193], v[4:5] op_sel_hi:[1,0]\n"
"v_pk_mul_f32 v[194:195], v[194:195], v[4:5] op_sel_hi:[1,0]\n"
"v_pk_mul_f32 v[196:197], v[196:197], v[4:5] op_sel_hi:[1,0]\n"
"v_pk_mul_f32 v[198:199], v[198:199], v[4:5] op_sel_hi:[1,0]\n"
"v_cvt_pk_bf16_f32 v192, v192, v193\n"
"v_cvt_pk_bf16_f32 v193, v194, v195\n"
"v_cvt_pk_bf16_f32 v194, v196, v197\n"
"v_cvt_pk_bf16_f32 v195, v198, v199\n"
"buffer_store_dwordx4 v[192:195], v1, s[44:47], s82 offen offset:0 sc1\n"
"s_waitcnt lgkmcnt(12)\n"
"v_pk_add_f32 v[200:201], v[200:201], v[104:105]\n"
"v_pk_add_f32 v[202:203], v[202:203], v[106:107]\n"
"v_pk_add_f32 v[204:205], v[204:205], v[108:109]\n"
"v_pk_add_f32 v[206:207], v[206:207], v[110:111]\n"
"v_pk_mul_f32 v[200:201], v[200:201], v[4:5] op_sel_hi:[1,0]\n"
"v_pk_mul_f32 v[202:203], v[202:203], v[4:5] op_sel_hi:[1,0]\n"
"v_pk_mul_f32 v[204:205], v[204:205], v[4:5] op_sel_hi:[1,0]\n"
"v_pk_mul_f32 v[206:207], v[206:207], v[4:5] op_sel_hi:[1,0]\n"
"v_cvt_pk_bf16_f32 v200, v200, v201\n"
"v_cvt_pk_bf16_f32 v201, v202, v203\n"
"v_cvt_pk_bf16_f32 v202, v204, v205\n"
"v_cvt_pk_bf16_f32 v203, v206, v207\n"
"buffer_store_dwordx4 v[200:203], v1, s[44:47], s82 offen offset:1024 sc1\n"
"s_waitcnt lgkmcnt(10)\n"
"v_pk_add_f32 v[208:209], v[208:209], v[112:113]\n"
"v_pk_add_f32 v[210:211], v[210:211], v[114:115]\n"
"v_pk_add_f32 v[212:213], v[212:213], v[116:117]\n"
"v_pk_add_f32 v[214:215], v[214:215], v[118:119]\n"
"v_pk_mul_f32 v[208:209], v[208:209], v[4:5] op_sel_hi:[1,0]\n"
"v_pk_mul_f32 v[210:211], v[210:211], v[4:5] op_sel_hi:[1,0]\n"
"v_pk_mul_f32 v[212:213], v[212:213], v[4:5] op_sel_hi:[1,0]\n"
"v_pk_mul_f32 v[214:215], v[214:215], v[4:5] op_sel_hi:[1,0]\n"
"v_cvt_pk_bf16_f32 v208, v208, v209\n"
"v_cvt_pk_bf16_f32 v209, v210, v211\n"
"v_cvt_pk_bf16_f32 v210, v212, v213\n"
"v_cvt_pk_bf16_f32 v211, v214, v215\n"
"buffer_store_dwordx4 v[208:211], v1, s[44:47], s82 offen offset:2048 sc1\n"
"s_waitcnt lgkmcnt(8)\n"
"v_pk_add_f32 v[216:217], v[216:217], v[120:121]\n"
"v_pk_add_f32 v[218:219], v[218:219], v[122:123]\n"
"v_pk_add_f32 v[220:221], v[220:221], v[124:125]\n"
"v_pk_add_f32 v[222:223], v[222:223], v[126:127]\n"
"v_pk_mul_f32 v[216:217], v[216:217], v[4:5] op_sel_hi:[1,0]\n"
"v_pk_mul_f32 v[218:219], v[218:219], v[4:5] op_sel_hi:[1,0]\n"
"v_pk_mul_f32 v[220:221], v[220:221], v[4:5] op_sel_hi:[1,0]\n"
"v_pk_mul_f32 v[222:223], v[222:223], v[4:5] op_sel_hi:[1,0]\n"
"v_cvt_pk_bf16_f32 v216, v216, v217\n"
"v_cvt_pk_bf16_f32 v217, v218, v219\n"
"v_cvt_pk_bf16_f32 v218, v220, v221\n"
"v_cvt_pk_bf16_f32 v219, v222, v223\n"
"buffer_store_dwordx4 v[216:219], v1, s[44:47], s82 offen offset:3072 sc1\n"
"s_waitcnt lgkmcnt(6)\n"
"v_pk_add_f32 v[224:225], v[224:225], v[64:65]\n"
"v_pk_add_f32 v[226:227], v[226:227], v[66:67]\n"
"v_pk_add_f32 v[228:229], v[228:229], v[68:69]\n"
"v_pk_add_f32 v[230:231], v[230:231], v[70:71]\n"
"v_pk_mul_f32 v[224:225], v[224:225], v[6:7] op_sel_hi:[1,0]\n"
"v_pk_mul_f32 v[226:227], v[226:227], v[6:7] op_sel_hi:[1,0]\n"
"v_pk_mul_f32 v[228:229], v[228:229], v[6:7] op_sel_hi:[1,0]\n"
"v_pk_mul_f32 v[230:231], v[230:231], v[6:7] op_sel_hi:[1,0]\n"
"v_cvt_pk_bf16_f32 v224, v224, v225\n"
"v_cvt_pk_bf16_f32 v225, v226, v227\n"
"v_cvt_pk_bf16_f32 v226, v228, v229\n"
"v_cvt_pk_bf16_f32 v227, v230, v231\n"
"buffer_store_dwordx4 v[224:227], v1, s[44:47], s83 offen offset:0 sc1\n"
"s_waitcnt lgkmcnt(4)\n"
"v_pk_add_f32 v[232:233], v[232:233], v[72:73]\n"
"v_pk_add_f32 v[234:235], v[234:235], v[74:75]\n"
"v_pk_add_f32 v[236:237], v[236:237], v[76:77]\n"
"v_pk_add_f32 v[238:239], v[238:239], v[78:79]\n"
"v_pk_mul_f32 v[232:233], v[232:233], v[6:7] op_sel_hi:[1,0]\n"
"v_pk_mul_f32 v[234:235], v[234:235], v[6:7] op_sel_hi:[1,0]\n"
"v_pk_mul_f32 v[236:237], v[236:237], v[6:7] op_sel_hi:[1,0]\n"
"v_pk_mul_f32 v[238:239], v[238:239], v[6:7] op_sel_hi:[1,0]\n"
"v_cvt_pk_bf16_f32 v232, v232, v233\n"
"v_cvt_pk_bf16_f32 v233, v234, v235\n"
"v_cvt_pk_bf16_f32 v234, v236, v237\n"
"v_cvt_pk_bf16_f32 v235, v238, v239\n"
"buffer_store_dwordx4 v[232:235], v1, s[44:47], s83 offen offset:1024 sc1\n"
"s_waitcnt lgkmcnt(2)\n"
"v_pk_add_f32 v[240:241], v[240:241], v[80:81]\n"
"v_pk_add_f32 v[242:243], v[242:243], v[82:83]\n"
"v_pk_add_f32 v[244:245], v[244:245], v[84:85]\n"
"v_pk_add_f32 v[246:247], v[246:247], v[86:87]\n"
"v_pk_mul_f32 v[240:241], v[240:241], v[6:7] op_sel_hi:[1,0]\n"
"v_pk_mul_f32 v[242:243], v[242:243], v[6:7] op_sel_hi:[1,0]\n"
"v_pk_mul_f32 v[244:245], v[244:245], v[6:7] op_sel_hi:[1,0]\n"
"v_pk_mul_f32 v[246:247], v[246:247], v[6:7] op_sel_hi:[1,0]\n"
"v_cvt_pk_bf16_f32 v240, v240, v241\n"
"v_cvt_pk_bf16_f32 v241, v242, v243\n"
"v_cvt_pk_bf16_f32 v242, v244, v245\n"
"v_cvt_pk_bf16_f32 v243, v246, v247\n"
"buffer_store_dwordx4 v[240:243], v1, s[44:47], s83 offen offset:2048 sc1\n"
"s_waitcnt lgkmcnt(0)\n"
"v_pk_add_f32 v[248:249], v[248:249], v[88:89]\n"
"v_pk_add_f32 v[250:251], v[250:251], v[90:91]\n"
"v_pk_add_f32 v[252:253], v[252:253], v[92:93]\n"
"v_pk_add_f32 v[254:255], v[254:255], v[94:95]\n"
"v_pk_mul_f32 v[248:249], v[248:249], v[6:7] op_sel_hi:[1,0]\n"
"v_pk_mul_f32 v[250:251], v[250:251], v[6:7] op_sel_hi:[1,0]\n"
"v_pk_mul_f32 v[252:253], v[252:253], v[6:7] op_sel_hi:[1,0]\n"
"v_pk_mul_f32 v[254:255], v[254:255], v[6:7] op_sel_hi:[1,0]\n"
"v_cvt_pk_bf16_f32 v248, v248, v249\n"
"v_cvt_pk_bf16_f32 v249, v250, v251\n"
"v_cvt_pk_bf16_f32 v250, v252, v253\n"
"v_cvt_pk_bf16_f32 v251, v254, v255\n"
"buffer_store_dwordx4 v[248:251], v1, s[44:47], s83 offen offset:3072 sc1\n"
"s_barrier\n"
"ds_read_b64_tr_b16 v[64:65], v8 offset:32768\n"
"ds_read_b64_tr_b16 v[66:67], v8 offset:36864\n"
"ds_read_b64_tr_b16 v[68:69], v8 offset:40960\n"
"ds_read_b64_tr_b16 v[70:71], v8 offset:45056\n"
"ds_read_b64_tr_b16 v[72:73], v8 offset:49152\n"
"ds_read_b64_tr_b16 v[74:75], v8 offset:53248\n"
"ds_read_b64_tr_b16 v[76:77], v8 offset:57344\n"
"ds_read_b64_tr_b16 v[78:79], v8 offset:61440\n"
"ds_read_b64_tr_b16 v[80:81], v9 offset:32768\n"
"ds_read_b64_tr_b16 v[82:83], v9 offset:36864\n"
"ds_read_b64_tr_b16 v[84:85], v9 offset:40960\n"
"ds_read_b64_tr_b16 v[86:87], v9 offset:45056\n"
"ds_read_b64_tr_b16 v[88:89], v9 offset:49152\n"
"ds_read_b64_tr_b16 v[90:91], v9 offset:53248\n"
"ds_read_b64_tr_b16 v[92:93], v9 offset:57344\n"
"ds_read_b64_tr_b16 v[94:95], v9 offset:61440\n"
"s_waitcnt vmcnt(8)\n"
"OP0_%=:\n"
"s_waitcnt lgkmcnt(14)\n"
"v_mfma_f32_16x16x32_bf16 v[128:131], a[64:67], v[64:67], 0\n"
"s_add_u32 s84, s88, 8192\n"
"ds_read_b64_tr_b16 v[96:97], v10 offset:32768\n"
"ds_read_b64_tr_b16 v[98:99], v10 offset:36864\n"
"s_waitcnt lgkmcnt(14)\n"
"v_mfma_f32_16x16x32_bf16 v[128:131], a[72:75], v[68:71], v[128:131]\n"
"buffer_load_dwordx4 a[96:99], v1, s[44:47], s84 offen offset:0 sc1\n"
"s_waitcnt lgkmcnt(12)\n"
"v_mfma_f32_16x16x32_bf16 v[128:131], a[80:83], v[72:75], v[128:131]\n"
"s_waitcnt lgkmcnt(10)\n"
"ds_read_b64_tr_b16 v[100:101], v10 offset:40960\n"
"ds_read_b64_tr_b16 v[102:103], v10 offset:45056\n"
"v_mfma_f32_16x16x32_bf16 v[128:131], a[88:91], v[76:79], v[128:131]\n"
"ds_read_b64_tr_b16 v[104:105], v10 offset:49152\n"
"ds_read_b64_tr_b16 v[106:107], v10 offset:53248\n"
"v_mfma_f32_16x16x32_bf16 v[132:135], a[92:95], v[76:79], 0\n"
"buffer_load_dwordx4 a[100:103], v31, s[44:47], s84 offen offset:0 sc1\n"
"v_mfma_f32_16x16x32_bf16 v[132:135], a[84:87], v[72:75], v[132:135]\n"
"ds_read_b64_tr_b16 v[108:109], v10 offset:57344\n"
"ds_read_b64_tr_b16 v[110:111], v10 offset:61440\n"
"v_mfma_f32_16x16x32_bf16 v[132:135], a[76:79], v[68:71], v[132:135]\n"
"ds_read_b64_tr_b16 v[112:113], v11 offset:32768\n"
"ds_read_b64_tr_b16 v[114:115], v11 offset:36864\n"
"v_mfma_f32_16x16x32_bf16 v[132:135], a[68:71], v[64:67], v[132:135]\n"
"s_waitcnt lgkmcnt(15)\n"
"ds_read_b64_tr_b16 v[116:117], v11 offset:40960\n"
"ds_read_b64_tr_b16 v[118:119], v11 offset:45056\n"
"v_mfma_f32_16x16x32_bf16 v[140:143], a[68:71], v[80:83], 0\n"
"buffer_load_dwordx4 a[104:107], v1, s[44:47], s84 offen offset:1024 sc1\n"
"s_waitcnt lgkmcnt(15)\n"
"v_mfma_f32_16x16x32_bf16 v[140:143], a[76:79], v[84:87], v[140:143]\n"
"s_waitcnt lgkmcnt(14)\n"
"ds_read_b64_tr_b16 v[120:121], v11 offset:49152\n"
"ds_read_b64_tr_b16 v[122:123], v11 offset:53248\n"
"v_mfma_f32_16x16x32_bf16 v[140:143], a[84:87], v[88:91], v[140:143]\n"
"s_waitcnt lgkmcnt(14)\n"
"ds_read_b64_tr_b16 v[124:125], v11 offset:57344\n"
"ds_read_b64_tr_b16 v[126:127], v11 offset:61440\n"
"v_mfma_f32_16x16x32_bf16 v[140:143], a[92:95], v[92:95], v[140:143]\n"
"buffer_load_dwordx4 a[108:111], v31, s[44:47], s84 offen offset:1024 sc1\n"
"v_mfma_f32_16x16x32_bf16 v[136:139], a[88:91], v[92:95], 0\n"
"ds_read_b64_tr_b16 v[64:65], v12 offset:32768\n"
"ds_read_b64_tr_b16 v[66:67], v12 offset:36864\n"
"v_mfma_f32_16x16x32_bf16 v[136:139], a[80:83], v[88:91], v[136:139]\n"
"ds_read_b64_tr_b16 v[68:69], v12 offset:40960\n"
"ds_read_b64_tr_b16 v[70:71], v12 offset:45056\n"
"v_mfma_f32_16x16x32_bf16 v[136:139], a[72:75], v[84:87], v[136:139]\n"
"ds_read_b64_tr_b16 v[72:73], v12 offset:49152\n"
"ds_read_b64_tr_b16 v[74:75], v12 offset:53248\n"
"v_mfma_f32_16x16x32_bf16 v[136:139], a[64:67], v[80:83], v[136:139]\n"
"buffer_load_dwordx4 a[112:115], v1, s[44:47], s84 offen offset:2048 sc1\n"
"s_waitcnt lgkmcnt(15)\n"
"v_mfma_f32_16x16x32_bf16 v[144:147], a[64:67], v[96:99], 0\n"
"s_waitcnt lgkmcnt(15)\n"
"ds_read_b64_tr_b16 v[76:77], v12 offset:57344\n"
"ds_read_b64_tr_b16 v[78:79], v12 offset:61440\n"
"v_mfma_f32_16x16x32_bf16 v[144:147], a[72:75], v[100:103], v[144:147]\n"
"s_waitcnt lgkmcnt(15)\n"
"ds_read_b64_tr_b16 v[80:81], v13 offset:32768\n"
"ds_read_b64_tr_b16 v[82:83], v13 offset:36864\n"
"v_mfma_f32_16x16x32_bf16 v[144:147], a[80:83], v[104:107], v[144:147]\n"
"buffer_load_dwordx4 a[116:119], v31, s[44:47], s84 offen offset:2048 sc1\n"
"s_waitcnt lgkmcnt(15)\n"
"v_mfma_f32_16x16x32_bf16 v[144:147], a[88:91], v[108:111], v[144:147]\n"
"ds_read_b64_tr_b16 v[84:85], v13 offset:40960\n"
"ds_read_b64_tr_b16 v[86:87], v13 offset:45056\n"
"v_mfma_f32_16x16x32_bf16 v[148:151], a[92:95], v[108:111], 0\n"
"ds_read_b64_tr_b16 v[88:89], v13 offset:49152\n"
"ds_read_b64_tr_b16 v[90:91], v13 offset:53248\n"
"v_mfma_f32_16x16x32_bf16 v[148:151], a[84:87], v[104:107], v[148:151]\n"
"ds_read_b64_tr_b16 v[92:93], v13 offset:57344\n"
"ds_read_b64_tr_b16 v[94:95], v13 offset:61440\n"
"v_mfma_f32_16x16x32_bf16 v[148:151], a[76:79], v[100:103], v[148:151]\n"
"buffer_load_dwordx4 a[120:123], v1, s[44:47], s84 offen offset:3072 sc1\n"
"v_mfma_f32_16x16x32_bf16 v[148:151], a[68:71], v[96:99], v[148:151]\n"
"s_waitcnt lgkmcnt(15)\n"
"ds_read_b64_tr_b16 v[96:97], v14 offset:32768\n"
"ds_read_b64_tr_b16 v[98:99], v14 offset:36864\n"
"v_mfma_f32_16x16x32_bf16 v[156:159], a[68:71], v[112:115], 0\n"
"s_waitcnt lgkmcnt(15)\n"
"ds_read_b64_tr_b16 v[100:101], v14 offset:40960\n"
"ds_read_b64_tr_b16 v[102:103], v14 offset:45056\n"
"v_mfma_f32_16x16x32_bf16 v[156:159], a[76:79], v[116:119], v[156:159]\n"
"buffer_load_dwordx4 a[124:127], v31, s[44:47], s84 offen offset:3072 sc1\n"
"s_waitcnt lgkmcnt(15)\n"
"v_mfma_f32_16x16x32_bf16 v[156:159], a[84:87], v[120:123], v[156:159]\n"
"s_waitcnt lgkmcnt(15)\n"
"ds_read_b64_tr_b16 v[104:105], v14 offset:49152\n"
"ds_read_b64_tr_b16 v[106:107], v14 offset:53248\n"
"v_mfma_f32_16x16x32_bf16 v[156:159], a[92:95], v[124:127], v[156:159]\n"
"ds_read_b64_tr_b16 v[108:109], v14 offset:57344\n"
"ds_read_b64_tr_b16 v[110:111], v14 offset:61440\n"
"v_mfma_f32_16x16x32_bf16 v[152:155], a[88:91], v[124:127], 0\n"
"v_mfma_f32_16x16x32_bf16 v[152:155], a[80:83], v[120:123], v[152:155]\n"
"v_mfma_f32_16x16x32_bf16 v[152:155], a[72:75], v[116:119], v[152:155]\n"
"v_mfma_f32_16x16x32_bf16 v[152:155], a[64:67], v[112:115], v[152:155]\n"
"s_waitcnt lgkmcnt(15)\n"
"ds_read_b64_tr_b16 v[112:113], v15 offset:32768\n"
"ds_read_b64_tr_b16 v[114:115], v15 offset:36864\n"
"v_mfma_f32_16x16x32_bf16 v[160:163], a[64:67], v[64:67], 0\n"
"s_waitcnt lgkmcnt(15)\n"
"ds_read_b64_tr_b16 v[116:117], v15 offset:40960\n"
"ds_read_b64_tr_b16 v[118:119], v15 offset:45056\n"
"v_mfma_f32_16x16x32_bf16 v[160:163], a[72:75], v[68:71], v[160:163]\n"
"s_waitcnt lgkmcnt(15)\n"
"ds_read_b64_tr_b16 v[120:121], v15 offset:49152\n"
"ds_read_b64_tr_b16 v[122:123], v15 offset:53248\n"
"v_mfma_f32_16x16x32_bf16 v[160:163], a[80:83], v[72:75], v[160:163]\n"
"s_waitcnt lgkmcnt(15)\n"
"ds_read_b64_tr_b16 v[124:125], v15 offset:57344\n"
"ds_read_b64_tr_b16 v[126:127], v15 offset:61440\n"
"v_mfma_f32_16x16x32_bf16 v[160:163], a[88:91], v[76:79], v[160:163]\n"
"v_mfma_f32_16x16x32_bf16 v[164:167], a[92:95], v[76:79], 0\n"
"v_mfma_f32_16x16x32_bf16 v[164:167], a[84:87], v[72:75], v[164:167]\n"
"v_mfma_f32_16x16x32_bf16 v[164:167], a[76:79], v[68:71], v[164:167]\n"
"v_mfma_f32_16x16x32_bf16 v[164:167], a[68:71], v[64:67], v[164:167]\n"
"s_waitcnt vmcnt(8) lgkmcnt(0)\n"
"s_barrier\n"
"s_lshl_b32 s99, s60, 7\n"
"s_add_u32 s99, s99, 98304\n"
"v_mov_b32 v28, s64\n"
"v_mov_b32 v29, s65\n"
"s_cmp_eq_u32 s61, 0\n"
"s_cselect_b64 exec, 1, 0\n"
"buffer_store_dwordx2 v[28:29], off, s[44:47], s99 sc1\n"
"s_mov_b64 exec, -1\n"
"v_mfma_f32_16x16x32_bf16 v[172:175], a[68:71], v[80:83], 0\n"
"s_add_u32 s84, s88, 16384\n"
"ds_read_b64_tr_b16 v[64:65], v8 offset:0\n"
"ds_read_b64_tr_b16 v[66:67], v8 offset:4096\n"
"v_mfma_f32_16x16x32_bf16 v[172:175], a[76:79], v[84:87], v[172:175]\n"
"ds_read_b64_tr_b16 v[68:69], v8 offset:8192\n"
"ds_read_b64_tr_b16 v[70:71], v8 offset:12288\n"
"v_mfma_f32_16x16x32_bf16 v[172:175], a[84:87], v[88:91], v[172:175]\n"
"ds_read_b64_tr_b16 v[72:73], v8 offset:16384\n"
"ds_read_b64_tr_b16 v[74:75], v8 offset:20480\n"
"v_mfma_f32_16x16x32_bf16 v[172:175], a[92:95], v[92:95], v[172:175]\n"
"ds_read_b64_tr_b16 v[76:77], v8 offset:24576\n"
"ds_read_b64_tr_b16 v[78:79], v8 offset:28672\n"
"v_mfma_f32_16x16x32_bf16 v[168:171], a[88:91], v[92:95], 0\n"
"buffer_load_dwordx4 a[128:131], v1, s[44:47], s84 offen offset:0 sc1\n"
"v_mfma_f32_16x16x32_bf16 v[168:171], a[80:83], v[88:91], v[168:171]\n"
"buffer_load_dwordx4 a[132:135], v31, s[44:47], s84 offen offset:0 sc1\n"
"v_mfma_f32_16x16x32_bf16 v[168:171], a[72:75], v[84:87], v[168:171]\n"
"v_mov_b32 v208, 0\n"
"v_mov_b32 v209, 0\n"
"v_mfma_f32_16x16x32_bf16 v[168:171], a[64:67], v[80:83], v[168:171]\n"
"v_mov_b32 v210, 0\n"
"v_mov_b32 v211, 0\n"
"v_mfma_f32_16x16x32_bf16 v[176:179], a[64:67], v[96:99], 0\n"
"buffer_load_dwordx4 a[136:139], v1, s[44:47], s84 offen offset:1024 sc1\n"
"v_mfma_f32_16x16x32_bf16 v[176:179], a[72:75], v[100:103], v[176:179]\n"
"v_mov_b32 v212, 0\n"
"v_mov_b32 v213, 0\n"
"v_mfma_f32_16x16x32_bf16 v[176:179], a[80:83], v[104:107], v[176:179]\n"
"v_mov_b32 v214, 0\n"
"v_mov_b32 v215, 0\n"
"v_mfma_f32_16x16x32_bf16 v[176:179], a[88:91], v[108:111], v[176:179]\n"
"ds_read_b64_tr_b16 v[80:81], v9 offset:0\n"
"ds_read_b64_tr_b16 v[82:83], v9 offset:4096\n"
"v_mfma_f32_16x16x32_bf16 v[180:183], a[92:95], v[108:111], 0\n"
"buffer_load_dwordx4 a[140:143], v31, s[44:47], s84 offen offset:1024 sc1\n"
"v_mfma_f32_16x16x32_bf16 v[180:183], a[84:87], v[104:107], v[180:183]\n"
"ds_read_b64_tr_b16 v[84:85], v9 offset:8192\n"
"ds_read_b64_tr_b16 v[86:87], v9 offset:12288\n"
"v_mfma_f32_16x16x32_bf16 v[180:183], a[76:79], v[100:103], v[180:183]\n"
"ds_read_b64_tr_b16 v[88:89], v9 offset:16384\n"
"ds_read_b64_tr_b16 v[90:91], v9 offset:20480\n"
"v_mfma_f32_16x16x32_bf16 v[180:183], a[68:71], v[96:99], v[180:183]\n"
"buffer_load_dwordx4 a[144:147], v1, s[44:47], s84 offen offset:2048 sc1\n"
"v_mfma_f32_16x16x32_bf16 v[188:191], a[68:71], v[112:115], 0\n"
"ds_read_b64_tr_b16 v[92:93], v9 offset:24576\n"
"ds_read_b64_tr_b16 v[94:95], v9 offset:28672\n"
"v_mfma_f32_16x16x32_bf16 v[188:191], a[76:79], v[116:119], v[188:191]\n"
"ds_read_b64_tr_b16 v[96:97], v10 offset:0\n"
"ds_read_b64_tr_b16 v[98:99], v10 offset:4096\n"
"v_mfma_f32_16x16x32_bf16 v[188:191], a[84:87], v[120:123], v[188:191]\n"
"ds_read_b64_tr_b16 v[100:101], v10 offset:8192\n"
"ds_read_b64_tr_b16 v[102:103], v10 offset:12288\n"
"v_mfma_f32_16x16x32_bf16 v[188:191], a[92:95], v[124:127], v[188:191]\n"
"buffer_load_dwordx4 a[148:151], v31, s[44:47], s84 offen offset:2048 sc1\n"
"v_mfma_f32_16x16x32_bf16 v[184:187], a[88:91], v[124:127], 0\n"
"ds_read_b64_tr_b16 v[104:105], v10 offset:16384\n"
"ds_read_b64_tr_b16 v[106:107], v10 offset:20480\n"
"v_mfma_f32_16x16x32_bf16 v[184:187], a[80:83], v[120:123], v[184:187]\n"
"ds_read_b64_tr_b16 v[108:109], v10 offset:24576\n"
"ds_read_b64_tr_b16 v[110:111], v10 offset:28672\n"
"v_mfma_f32_16x16x32_bf16 v[184:187], a[72:75], v[116:119], v[184:187]\n"
"v_mfma_f32_16x16x32_bf16 v[184:187], a[64:67], v[112:115], v[184:187]\n"
"s_waitcnt vmcnt(7)\n"
"s_waitcnt lgkmcnt(15)\n"
"v_mfma_f32_16x16x32_bf16 v[128:131], a[96:99], v[64:67], v[128:131]\n"
"s_add_u32 m0, s74, 65536\n"
"s_add_u32 s85, s16, 786432\n"
"ds_read_b64_tr_b16 v[112:113], v11 offset:0\n"
"s_waitcnt lgkmcnt(15)\n"
"v_mfma_f32_16x16x32_bf16 v[128:131], a[104:107], v[68:71], v[128:131]\n"
"buffer_load_dwordx4 a[152:155], v1, s[44:47], s84 offen offset:3072 sc1\n"
"s_waitcnt lgkmcnt(15)\n"
"s_nop 0\n"
"PG14_%=:\n"
"s_cbranch_execz PADRET_%=\n"
"v_mfma_f32_16x16x32_bf16 v[128:131], a[112:115], v[72:75], v[128:131]\n"
"s_mov_b64 s[56:57], s[6:7]\n"
"ds_read_b64_tr_b16 v[114:115], v11 offset:4096\n"
"ds_read_b64_tr_b16 v[116:117], v11 offset:8192\n"
"s_waitcnt lgkmcnt(15)\n"
"v_mfma_f32_16x16x32_bf16 v[128:131], a[120:123], v[76:79], v[128:131]\n"
"s_mov_b32 s58, 4096\n"
"ds_read_b64_tr_b16 v[118:119], v11 offset:12288\n"
"ds_read_b64_tr_b16 v[120:121], v11 offset:16384\n"
"v_mfma_f32_16x16x32_bf16 v[132:135], a[124:127], v[76:79], v[132:135]\n"
"buffer_load_dwordx4 a[156:159], v31, s[44:47], s84 offen offset:3072 sc1\n"
"v_mfma_f32_16x16x32_bf16 v[132:135], a[116:119], v[72:75], v[132:135]\n"
"s_lshl_b32 s91, s61, 13\n"
"ds_read_b64_tr_b16 v[122:123], v11 offset:20480\n"
"ds_read_b64_tr_b16 v[124:125], v11 offset:24576\n"
"v_mfma_f32_16x16x32_bf16 v[132:135], a[108:111], v[68:71], v[132:135]\n"
"s_lshr_b32 s86, s60, 3\n"
"s_min_u32 s86, s86, 27\n"
"ds_read_b64_tr_b16 v[126:127], v11 offset:28672\n"
"v_mfma_f32_16x16x32_bf16 v[132:135], a[100:103], v[64:67], v[132:135]\n"
"s_and_b32 s77, s86, 3\n"
"ds_read_b64_tr_b16 v[64:65], v12 offset:0\n"
"ds_read_b64_tr_b16 v[66:67], v12 offset:4096\n"
"s_waitcnt lgkmcnt(15)\n"
"v_mfma_f32_16x16x32_bf16 v[140:143], a[100:103], v[80:83], v[140:143]\n"
"buffer_load_dwordx4 v24, s[32:35], s85 offen offset:0 lds\n"
"s_waitcnt lgkmcnt(15)\n"
"v_mfma_f32_16x16x32_bf16 v[140:143], a[108:111], v[84:87], v[140:143]\n"
"s_lshr_b32 s86, s86, 2\n"
"ds_read_b64_tr_b16 v[68:69], v12 offset:8192\n"
"ds_read_b64_tr_b16 v[70:71], v12 offset:12288\n"
"s_waitcnt lgkmcnt(15)\n"
"v_mfma_f32_16x16x32_bf16 v[140:143], a[116:119], v[88:91], v[140:143]\n"
"s_mul_i32 s78, s19, 7\n"
"ds_read_b64_tr_b16 v[72:73], v12 offset:16384\n"
"ds_read_b64_tr_b16 v[74:75], v12 offset:20480\n"
"s_waitcnt lgkmcnt(15)\n"
"v_mfma_f32_16x16x32_bf16 v[140:143], a[124:127], v[92:95], v[140:143]\n"
"buffer_load_dwordx4 v25, s[32:35], s85 offen offset:1024 lds\n"
"v_mfma_f32_16x16x32_bf16 v[136:139], a[120:123], v[92:95], v[136:139]\n"
"s_add_u32 s78, s78, s86\n"
"ds_read_b64_tr_b16 v[76:77], v12 offset:24576\n"
"ds_read_b64_tr_b16 v[78:79], v12 offset:28672\n"
"v_mfma_f32_16x16x32_bf16 v[136:139], a[112:115], v[88:91], v[136:139]\n"
"s_and_b32 s86, s60, 1\n"
"s_lshl_b32 s86, s86, 2\n"
"s_add_u32 s77, s77, s86\n"
"v_mfma_f32_16x16x32_bf16 v[136:139], a[104:107], v[84:87], v[136:139]\n"
"s_lshl_b32 s76, s78, 3\n"
"s_add_u32 s76, s76, s77\n"
"s_mul_i32 s75, s61, 1835008\n"
"v_mfma_f32_16x16x32_bf16 v[136:139], a[96:99], v[80:83], v[136:139]\n"
"buffer_load_dwordx4 v26, s[32:35], s85 offen offset:2048 lds\n"
"s_waitcnt lgkmcnt(15)\n"
"v_mfma_f32_16x16x32_bf16 v[144:147], a[96:99], v[96:99], v[144:147]\n"
"s_lshl_b32 s86, s78, 7\n"
"s_add_u32 s75, s75, s86\n"
"s_and_b32 s86, s78, 0\n"
"s_waitcnt lgkmcnt(15)\n"
"v_mfma_f32_16x16x32_bf16 v[144:147], a[104:107], v[100:103], v[144:147]\n"
"s_mul_i32 s86, s86, 917504\n"
"s_sub_u32 s92, s75, s86\n"
"s_add_u32 s75, s75, s86\n"
"s_waitcnt lgkmcnt(15)\n"
"v_mfma_f32_16x16x32_bf16 v[144:147], a[112:115], v[104:107], v[144:147]\n"
"s_lshr_b32 s79, s60, 2\n"
"s_and_b32 s80, s60, 3\n"
"s_lshl_b32 s81, s60, 2\n"
"s_waitcnt lgkmcnt(15)\n"
"v_mfma_f32_16x16x32_bf16 v[144:147], a[120:123], v[108:111], v[144:147]\n"
"buffer_load_dwordx4 v27, s[32:35], s85 offen offset:3072 lds\n"
"v_mfma_f32_16x16x32_bf16 v[148:151], a[124:127], v[108:111], v[148:151]\n"
"s_add_u32 s81, s81, s61\n"
"s_add_u32 m0, s74, 81920\n"
"s_add_u32 s85, s16, 851968\n"
"v_mfma_f32_16x16x32_bf16 v[148:151], a[116:119], v[104:107], v[148:151]\n"
"v_lshrrev_b32 v34, 3, v0\n"
"v_lshlrev_b32 v35, 1, v3\n"
"v_mfma_f32_16x16x32_bf16 v[148:151], a[108:111], v[100:103], v[148:151]\n"
"buffer_load_dwordx4 v24, s[32:35], s85 offen offset:0 lds\n"
"v_mfma_f32_16x16x32_bf16 v[148:151], a[100:103], v[96:99], v[148:151]\n"
"s_waitcnt lgkmcnt(14)\n"
"v_xor_b32 v35, v35, v0\n"
"v_and_b32 v35, 7, v35\n"
"v_mfma_f32_16x16x32_bf16 v[156:159], a[100:103], v[112:115], v[156:159]\n"
"s_waitcnt lgkmcnt(12)\n"
"v_lshlrev_b32 v35, 4, v35\n"
"v_mul_u32_u24 v34, 3584, v34\n"
"v_mfma_f32_16x16x32_bf16 v[156:159], a[108:111], v[116:119], v[156:159]\n"
"s_waitcnt lgkmcnt(10)\n"
"v_add_u32 v34, v34, v35\n"
"v_add_u32 v17, 0, v34\n"
"v_mfma_f32_16x16x32_bf16 v[156:159], a[116:119], v[120:123], v[156:159]\n"
"buffer_load_dwordx4 v25, s[32:35], s85 offen offset:1024 lds\n"
"s_waitcnt lgkmcnt(8)\n"
"v_mfma_f32_16x16x32_bf16 v[156:159], a[124:127], v[124:127], v[156:159]\n"
"v_add_u32 v18, 27648, v34\n"
"v_add_u32 v19, 55296, v34\n"
"v_mfma_f32_16x16x32_bf16 v[152:155], a[120:123], v[124:127], v[152:155]\n"
"ds_read_b64_tr_b16 v[80:81], v13 offset:0\n"
"v_add_u32 v22, 82944, v34\n"
"v_mfma_f32_16x16x32_bf16 v[152:155], a[112:115], v[120:123], v[152:155]\n"
"buffer_load_dwordx4 v26, s[32:35], s85 offen offset:2048 lds\n"
"v_mfma_f32_16x16x32_bf16 v[152:155], a[104:107], v[116:119], v[152:155]\n"
"ds_read_b64_tr_b16 v[82:83], v13 offset:4096\n"
"ds_read_b64_tr_b16 v[84:85], v13 offset:8192\n"
"v_mfma_f32_16x16x32_bf16 v[152:155], a[96:99], v[112:115], v[152:155]\n"
"s_waitcnt lgkmcnt(9)\n"
"ds_read_b64_tr_b16 v[86:87], v13 offset:12288\n"
"ds_read_b64_tr_b16 v[88:89], v13 offset:16384\n"
"v_mfma_f32_16x16x32_bf16 v[160:163], a[96:99], v[64:67], v[160:163]\n"
"s_waitcnt lgkmcnt(9)\n"
"ds_read_b64_tr_b16 v[90:91], v13 offset:20480\n"
"ds_read_b64_tr_b16 v[92:93], v13 offset:24576\n"
"v_mfma_f32_16x16x32_bf16 v[160:163], a[104:107], v[68:71], v[160:163]\n"
"buffer_load_dwordx4 v27, s[32:35], s85 offen offset:3072 lds\n"
"s_waitcnt lgkmcnt(9)\n"
"v_mfma_f32_16x16x32_bf16 v[160:163], a[112:115], v[72:75], v[160:163]\n"
"s_waitcnt lgkmcnt(7)\n"
"ds_read_b64_tr_b16 v[94:95], v13 offset:28672\n"
"ds_read_b64_tr_b16 v[96:97], v14 offset:0\n"
"v_mfma_f32_16x16x32_bf16 v[160:163], a[120:123], v[76:79], v[160:163]\n"
"ds_read_b64_tr_b16 v[98:99], v14 offset:4096\n"
"ds_read_b64_tr_b16 v[100:101], v14 offset:8192\n"
"v_mfma_f32_16x16x32_bf16 v[164:167], a[124:127], v[76:79], v[164:167]\n"
"ds_read_b64_tr_b16 v[102:103], v14 offset:12288\n"
"ds_read_b64_tr_b16 v[104:105], v14 offset:16384\n"
"v_mfma_f32_16x16x32_bf16 v[164:167], a[116:119], v[72:75], v[164:167]\n"
"ds_read_b64_tr_b16 v[106:107], v14 offset:20480\n"
"ds_read_b64_tr_b16 v[108:109], v14 offset:24576\n"
"v_mfma_f32_16x16x32_bf16 v[164:167], a[108:111], v[68:71], v[164:167]\n"
"ds_read_b64_tr_b16 v[110:111], v14 offset:28672\n"
"ds_read_b64_tr_b16 v[112:113], v15 offset:0\n"
"v_mfma_f32_16x16x32_bf16 v[164:167], a[100:103], v[64:67], v[164:167]\n"
"ds_read_b64_tr_b16 v[114:115], v15 offset:4096\n"
"ds_read_b64_tr_b16 v[116:117], v15 offset:8192\n"
"ds_read_b64_tr_b16 v[118:119], v15 offset:12288\n"
"ds_read_b64_tr_b16 v[120:121], v15 offset:16384\n"
"ds_read_b64_tr_b16 v[122:123], v15 offset:20480\n"
"ds_read_b64_tr_b16 v[124:125], v15 offset:24576\n"
"ds_read_b64_tr_b16 v[126:127], v15 offset:28672\n"
"s_waitcnt lgkmcnt(0)\n"
"s_barrier\n"
"v_mfma_f32_16x16x32_bf16 v[172:175], a[100:103], v[80:83], v[172:175]\n"
"s_add_u32 s84, s88, 24576\n"
"v_add_u32 v8, 65536, v8\n"
"v_add_u32 v9, 65536, v9\n"
"v_mfma_f32_16x16x32_bf16 v[172:175], a[108:111], v[84:87], v[172:175]\n"
"ds_read_b64_tr_b16 v[64:65], v8 offset:32768\n"
"ds_read_b64_tr_b16 v[66:67], v8 offset:36864\n"
"v_mfma_f32_16x16x32_bf16 v[172:175], a[116:119], v[88:91], v[172:175]\n"
"ds_read_b64_tr_b16 v[68:69], v8 offset:40960\n"
"ds_read_b64_tr_b16 v[70:71], v8 offset:45056\n"
"v_mfma_f32_16x16x32_bf16 v[172:175], a[124:127], v[92:95], v[172:175]\n"
"ds_read_b64_tr_b16 v[72:73], v8 offset:49152\n"
"ds_read_b64_tr_b16 v[74:75], v8 offset:53248\n"
"v_mfma_f32_16x16x32_bf16 v[168:171], a[120:123], v[92:95], v[168:171]\n"
"ds_read_b64_tr_b16 v[76:77], v8 offset:57344\n"
"ds_read_b64_tr_b16 v[78:79], v8 offset:61440\n"
"v_mfma_f32_16x16x32_bf16 v[168:171], a[112:115], v[88:91], v[168:171]\n"
"buffer_load_dwordx4 a[160:163], v1, s[44:47], s84 offen offset:0 sc1\n"
"v_mfma_f32_16x16x32_bf16 v[168:171], a[104:107], v[84:87], v[168:171]\n"
"buffer_load_dwordx4 a[164:167], v31, s[44:47], s84 offen offset:0 sc1\n"
"v_mfma_f32_16x16x32_bf16 v[168:171], a[96:99], v[80:83], v[168:171]\n"
"ds_read_b64_tr_b16 v[80:81], v9 offset:32768\n"
"v_add_u32 v10, 65536, v10\n"
"v_mfma_f32_16x16x32_bf16 v[176:179], a[96:99], v[96:99], v[176:179]\n"
"buffer_load_dwordx4 a[168:171], v1, s[44:47], s84 offen offset:1024 sc1\n"
"v_mfma_f32_16x16x32_bf16 v[176:179], a[104:107], v[100:103], v[176:179]\n"
"ds_read_b64_tr_b16 v[82:83], v9 offset:36864\n"
"ds_read_b64_tr_b16 v[84:85], v9 offset:40960\n"
"v_mfma_f32_16x16x32_bf16 v[176:179], a[112:115], v[104:107], v[176:179]\n"
"ds_read_b64_tr_b16 v[86:87], v9 offset:45056\n"
"ds_read_b64_tr_b16 v[88:89], v9 offset:49152\n"
"v_mfma_f32_16x16x32_bf16 v[176:179], a[120:123], v[108:111], v[176:179]\n"
"ds_read_b64_tr_b16 v[90:91], v9 offset:53248\n"
"ds_read_b64_tr_b16 v[92:93], v9 offset:57344\n"
"v_mfma_f32_16x16x32_bf16 v[180:183], a[124:127], v[108:111], v[180:183]\n"
"buffer_load_dwordx4 a[172:175], v31, s[44:47], s84 offen offset:1024 sc1\n"
"v_mfma_f32_16x16x32_bf16 v[180:183], a[116:119], v[104:107], v[180:183]\n"
"ds_read_b64_tr_b16 v[94:95], v9 offset:61440\n"
"v_mfma_f32_16x16x32_bf16 v[180:183], a[108:111], v[100:103], v[180:183]\n"
"v_mfma_f32_16x16x32_bf16 v[180:183], a[100:103], v[96:99], v[180:183]\n"
"buffer_load_dwordx4 a[176:179], v1, s[44:47], s84 offen offset:2048 sc1\n"
"v_mfma_f32_16x16x32_bf16 v[188:191], a[100:103], v[112:115], v[188:191]\n"
"ds_read_b64_tr_b16 v[96:97], v10 offset:32768\n"
"ds_read_b64_tr_b16 v[98:99], v10 offset:36864\n"
"v_mfma_f32_16x16x32_bf16 v[188:191], a[108:111], v[116:119], v[188:191]\n"
"ds_read_b64_tr_b16 v[100:101], v10 offset:40960\n"
"ds_read_b64_tr_b16 v[102:103], v10 offset:45056\n"
"v_mfma_f32_16x16x32_bf16 v[188:191], a[116:119], v[120:123], v[188:191]\n"
"ds_read_b64_tr_b16 v[104:105], v10 offset:49152\n"
"ds_read_b64_tr_b16 v[106:107], v10 offset:53248\n"
"v_mfma_f32_16x16x32_bf16 v[188:191], a[124:127], v[124:127], v[188:191]\n"
"buffer_load_dwordx4 a[180:183], v31, s[44:47], s84 offen offset:2048 sc1\n"
"v_mfma_f32_16x16x32_bf16 v[184:187], a[120:123], v[124:127], v[184:187]\n"
"ds_read_b64_tr_b16 v[108:109], v10 offset:57344\n"
"ds_read_b64_tr_b16 v[110:111], v10 offset:61440\n"
"v_mfma_f32_16x16x32_bf16 v[184:187], a[112:115], v[120:123], v[184:187]\n"
"v_mfma_f32_16x16x32_bf16 v[184:187], a[104:107], v[116:119], v[184:187]\n"
"v_mfma_f32_16x16x32_bf16 v[184:187], a[96:99], v[112:115], v[184:187]\n"
"s_waitcnt vmcnt(14)\n"
"s_waitcnt lgkmcnt(15)\n"
"v_mfma_f32_16x16x32_bf16 v[128:131], a[128:131], v[64:67], v[128:131]\n"
"s_add_u32 m0, s74, 131072\n"
"s_add_u32 s85, s73, 0\n"
"v_add_u32 v11, 65536, v11\n"
"s_waitcnt lgkmcnt(15)\n"
"v_mfma_f32_16x16x32_bf16 v[128:131], a[136:139], v[68:71], v[128:131]\n"
"buffer_load_dwordx4 a[184:187], v1, s[44:47], s84 offen offset:3072 sc1\n"
"s_waitcnt lgkmcnt(15)\n"
"v_mfma_f32_16x16x32_bf16 v[128:131], a[144:147], v[72:75], v[128:131]\n"
"s_waitcnt lgkmcnt(15)\n"
"ds_read_b64_tr_b16 v[112:113], v11 offset:32768\n"
"ds_read_b64_tr_b16 v[114:115], v11 offset:36864\n"
"v_mfma_f32_16x16x32_bf16 v[128:131], a[152:155], v[76:79], v[128:131]\n"
"ds_read_b64_tr_b16 v[116:117], v11 offset:40960\n"
"ds_read_b64_tr_b16 v[118:119], v11 offset:45056\n"
"v_mfma_f32_16x16x32_bf16 v[132:135], a[156:159], v[76:79], v[132:135]\n"
"buffer_load_dwordx4 a[188:191], v31, s[44:47], s84 offen offset:3072 sc1\n"
"v_mfma_f32_16x16x32_bf16 v[132:135], a[148:151], v[72:75], v[132:135]\n"
"ds_read_b64_tr_b16 v[120:121], v11 offset:49152\n"
"ds_read_b64_tr_b16 v[122:123], v11 offset:53248\n"
"v_mfma_f32_16x16x32_bf16 v[132:135], a[140:143], v[68:71], v[132:135]\n"
"ds_read_b64_tr_b16 v[124:125], v11 offset:57344\n"
"ds_read_b64_tr_b16 v[126:127], v11 offset:61440\n"
"v_mfma_f32_16x16x32_bf16 v[132:135], a[132:135], v[64:67], v[132:135]\n"
"buffer_load_dwordx4 v24, s[32:35], s85 offen offset:0 lds\n"
"s_waitcnt lgkmcnt(15)\n"
"v_mfma_f32_16x16x32_bf16 v[140:143], a[132:135], v[80:83], v[140:143]\n"
"s_waitcnt lgkmcnt(15)\n"
"v_add_u32 v12, 65536, v12\n"
"v_add_u32 v13, 65536, v13\n"
"v_mfma_f32_16x16x32_bf16 v[140:143], a[140:143], v[84:87], v[140:143]\n"
"s_waitcnt lgkmcnt(15)\n"
"ds_read_b64_tr_b16 v[64:65], v12 offset:32768\n"
"ds_read_b64_tr_b16 v[66:67], v12 offset:36864\n"
"v_mfma_f32_16x16x32_bf16 v[140:143], a[148:151], v[88:91], v[140:143]\n"
"buffer_load_dwordx4 v25, s[32:35], s85 offen offset:1024 lds\n"
"s_waitcnt lgkmcnt(15)\n"
"v_mfma_f32_16x16x32_bf16 v[140:143], a[156:159], v[92:95], v[140:143]\n"
"ds_read_b64_tr_b16 v[68:69], v12 offset:40960\n"
"ds_read_b64_tr_b16 v[70:71], v12 offset:45056\n"
"v_mfma_f32_16x16x32_bf16 v[136:139], a[152:155], v[92:95], v[136:139]\n"
"ds_read_b64_tr_b16 v[72:73], v12 offset:49152\n"
"ds_read_b64_tr_b16 v[74:75], v12 offset:53248\n"
"v_mfma_f32_16x16x32_bf16 v[136:139], a[144:147], v[88:91], v[136:139]\n"
"ds_read_b64_tr_b16 v[76:77], v12 offset:57344\n"
"ds_read_b64_tr_b16 v[78:79], v12 offset:61440\n"
"v_mfma_f32_16x16x32_bf16 v[136:139], a[136:139], v[84:87], v[136:139]\n"
"buffer_load_dwordx4 v26, s[32:35], s85 offen offset:2048 lds\n"
"v_mfma_f32_16x16x32_bf16 v[136:139], a[128:131], v[80:83], v[136:139]\n"
"s_waitcnt lgkmcnt(15)\n"
"v_add_u32 v14, 65536, v14\n"
"v_add_u32 v15, 65536, v15\n"
"v_mfma_f32_16x16x32_bf16 v[144:147], a[128:131], v[96:99], v[144:147]\n"
"s_waitcnt lgkmcnt(15)\n"
"ds_read_b64_tr_b16 v[80:81], v13 offset:32768\n"
"ds_read_b64_tr_b16 v[82:83], v13 offset:36864\n"
"v_mfma_f32_16x16x32_bf16 v[144:147], a[136:139], v[100:103], v[144:147]\n"
"buffer_load_dwordx4 v27, s[32:35], s85 offen offset:3072 lds\n"
"s_waitcnt lgkmcnt(15)\n"
"v_mfma_f32_16x16x32_bf16 v[144:147], a[144:147], v[104:107], v[144:147]\n"
"s_add_u32 m0, s74, 147456\n"
"s_add_u32 s85, s73, 65536\n"
"ds_read_b64_tr_b16 v[84:85], v13 offset:40960\n"
"s_waitcnt lgkmcnt(15)\n"
"v_mfma_f32_16x16x32_bf16 v[144:147], a[152:155], v[108:111], v[144:147]\n"
"ds_read_b64_tr_b16 v[86:87], v13 offset:45056\n"
"ds_read_b64_tr_b16 v[88:89], v13 offset:49152\n"
"v_mfma_f32_16x16x32_bf16 v[148:151], a[156:159], v[108:111], v[148:151]\n"
"buffer_load_dwordx2 v[20:21], v16, s[44:47], 0 offen sc1\n"
"v_mfma_f32_16x16x32_bf16 v[148:151], a[148:151], v[104:107], v[148:151]\n"
"ds_read_b64_tr_b16 v[90:91], v13 offset:53248\n"
"ds_read_b64_tr_b16 v[92:93], v13 offset:57344\n"
"v_mfma_f32_16x16x32_bf16 v[148:151], a[140:143], v[100:103], v[148:151]\n"
"ds_read_b64_tr_b16 v[94:95], v13 offset:61440\n"
"v_mfma_f32_16x16x32_bf16 v[148:151], a[132:135], v[96:99], v[148:151]\n"
"buffer_load_dwordx4 v24, s[32:35], s85 offen offset:0 lds\n"
"s_waitcnt lgkmcnt(15)\n"
"v_mfma_f32_16x16x32_bf16 v[156:159], a[132:135], v[112:115], v[156:159]\n"
"s_waitcnt lgkmcnt(15)\n"
"ds_read_b64_tr_b16 v[96:97], v14 offset:32768\n"
"ds_read_b64_tr_b16 v[98:99], v14 offset:36864\n"
"v_mfma_f32_16x16x32_bf16 v[156:159], a[140:143], v[116:119], v[156:159]\n"
"s_waitcnt lgkmcnt(15)\n"
"ds_read_b64_tr_b16 v[100:101], v14 offset:40960\n"
"ds_read_b64_tr_b16 v[102:103], v14 offset:45056\n"
"v_mfma_f32_16x16x32_bf16 v[156:159], a[148:151], v[120:123], v[156:159]\n"
"s_waitcnt lgkmcnt(15)\n"
"ds_read_b64_tr_b16 v[104:105], v14 offset:49152\n"
"ds_read_b64_tr_b16 v[106:107], v14 offset:53248\n"
"v_mfma_f32_16x16x32_bf16 v[156:159], a[156:159], v[124:127], v[156:159]\n"
"buffer_load_dwordx4 v25, s[32:35], s85 offen offset:1024 lds\n"
"v_mfma_f32_16x16x32_bf16 v[152:155], a[152:155], v[124:127], v[152:155]\n"
"ds_read_b64_tr_b16 v[108:109], v14 offset:57344\n"
"ds_read_b64_tr_b16 v[110:111], v14 offset:61440\n"
"v_mfma_f32_16x16x32_bf16 v[152:155], a[144:147], v[120:123], v[152:155]\n"
"v_mfma_f32_16x16x32_bf16 v[152:155], a[136:139], v[116:119], v[152:155]\n"
"buffer_load_dwordx4 v26, s[32:35], s85 offen offset:2048 lds\n"
"v_mfma_f32_16x16x32_bf16 v[152:155], a[128:131], v[112:115], v[152:155]\n"
"s_waitcnt lgkmcnt(15)\n"
"ds_read_b64_tr_b16 v[112:113], v15 offset:32768\n"
"ds_read_b64_tr_b16 v[114:115], v15 offset:36864\n"
"v_mfma_f32_16x16x32_bf16 v[160:163], a[128:131], v[64:67], v[160:163]\n"
"s_waitcnt lgkmcnt(15)\n"
"ds_read_b64_tr_b16 v[116:117], v15 offset:40960\n"
"ds_read_b64_tr_b16 v[118:119], v15 offset:45056\n"
"v_mfma_f32_16x16x32_bf16 v[160:163], a[136:139], v[68:71], v[160:163]\n"
"buffer_load_dwordx4 v27, s[32:35], s85 offen offset:3072 lds\n"
"s_waitcnt lgkmcnt(15)\n"
"v_mfma_f32_16x16x32_bf16 v[160:163], a[144:147], v[72:75], v[160:163]\n"
"s_waitcnt lgkmcnt(15)\n"
"ds_read_b64_tr_b16 v[120:121], v15 offset:49152\n"
"ds_read_b64_tr_b16 v[122:123], v15 offset:53248\n"
"v_mfma_f32_16x16x32_bf16 v[160:163], a[152:155], v[76:79], v[160:163]\n"
"ds_read_b64_tr_b16 v[124:125], v15 offset:57344\n"
"ds_read_b64_tr_b16 v[126:127], v15 offset:61440\n"
"v_mfma_f32_16x16x32_bf16 v[164:167], a[156:159], v[76:79], v[164:167]\n"
"v_mfma_f32_16x16x32_bf16 v[164:167], a[148:151], v[72:75], v[164:167]\n"
"v_mfma_f32_16x16x32_bf16 v[164:167], a[140:143], v[68:71], v[164:167]\n"
"v_mfma_f32_16x16x32_bf16 v[164:167], a[132:135], v[64:67], v[164:167]\n"
"s_waitcnt vmcnt(4) lgkmcnt(0)\n"
"s_barrier\n"
"v_cmp_ne_u64 vcc, s[64:65], v[20:21]\n"
"s_nop 1\n"
"s_cbranch_vccz FOK_42_%=\n"
"FSP_43_%=:\n"
"s_cmp_eq_u32 s69, 0\n"
"s_cbranch_scc1 FOK_42_%=\n"
"s_sub_u32 s69, s69, 1\n"
"s_sleep 2\n"
"s_mov_b64 exec, vcc\n"
"buffer_load_dwordx2 v[20:21], v16, s[44:47], 0 offen sc1\n"
"s_mov_b64 exec, -1\n"
"s_waitcnt vmcnt(0)\n"
"v_cmp_ne_u64 vcc, s[64:65], v[20:21]\n"
"s_nop 1\n"
"s_cbranch_vccnz FSP_43_%=\n"
"FOK_42_%=:\n"
"v_mfma_f32_16x16x32_bf16 v[172:175], a[132:135], v[80:83], v[172:175]\n"
"s_add_u32 s84, s72, 0\n"
"ds_read_b64_tr_b16 v[64:65], v8 offset:0\n"
"ds_read_b64_tr_b16 v[66:67], v8 offset:4096\n"
"v_mfma_f32_16x16x32_bf16 v[172:175], a[140:143], v[84:87], v[172:175]\n"
"ds_read_b64_tr_b16 v[68:69], v8 offset:8192\n"
"ds_read_b64_tr_b16 v[70:71], v8 offset:12288\n"
"v_mfma_f32_16x16x32_bf16 v[172:175], a[148:151], v[88:91], v[172:175]\n"
"ds_read_b64_tr_b16 v[72:73], v8 offset:16384\n"
"ds_read_b64_tr_b16 v[74:75], v8 offset:20480\n"
"v_mfma_f32_16x16x32_bf16 v[172:175], a[156:159], v[92:95], v[172:175]\n"
"ds_read_b64_tr_b16 v[76:77], v8 offset:24576\n"
"ds_read_b64_tr_b16 v[78:79], v8 offset:28672\n"
"v_mfma_f32_16x16x32_bf16 v[168:171], a[152:155], v[92:95], v[168:171]\n"
"buffer_load_dwordx4 a[0:3], v1, s[44:47], s84 offen offset:0 sc1\n"
"v_mfma_f32_16x16x32_bf16 v[168:171], a[144:147], v[88:91], v[168:171]\n"
"buffer_load_dwordx4 a[4:7], v31, s[44:47], s84 offen offset:0 sc1\n"
"v_mfma_f32_16x16x32_bf16 v[168:171], a[136:139], v[84:87], v[168:171]\n"
"v_mov_b32 v216, 0\n"
"v_mov_b32 v217, 0\n"
"v_mfma_f32_16x16x32_bf16 v[168:171], a[128:131], v[80:83], v[168:171]\n"
"v_mov_b32 v218, 0\n"
"v_mov_b32 v219, 0\n"
"v_mfma_f32_16x16x32_bf16 v[176:179], a[128:131], v[96:99], v[176:179]\n"
"buffer_load_dwordx4 a[8:11], v1, s[44:47], s84 offen offset:1024 sc1\n"
"v_mfma_f32_16x16x32_bf16 v[176:179], a[136:139], v[100:103], v[176:179]\n"
"v_mov_b32 v220, 0\n"
"v_mov_b32 v221, 0\n"
"v_mfma_f32_16x16x32_bf16 v[176:179], a[144:147], v[104:107], v[176:179]\n"
"v_mov_b32 v222, 0\n"
"v_mov_b32 v223, 0\n"
"v_mfma_f32_16x16x32_bf16 v[176:179], a[152:155], v[108:111], v[176:179]\n"
"ds_read_b64_tr_b16 v[80:81], v9 offset:0\n"
"ds_read_b64_tr_b16 v[82:83], v9 offset:4096\n"
"v_mfma_f32_16x16x32_bf16 v[180:183], a[156:159], v[108:111], v[180:183]\n"
"buffer_load_dwordx4 a[12:15], v31, s[44:47], s84 offen offset:1024 sc1\n"
"v_mfma_f32_16x16x32_bf16 v[180:183], a[148:151], v[104:107], v[180:183]\n"
"ds_read_b64_tr_b16 v[84:85], v9 offset:8192\n"
"ds_read_b64_tr_b16 v[86:87], v9 offset:12288\n"
"v_mfma_f32_16x16x32_bf16 v[180:183], a[140:143], v[100:103], v[180:183]\n"
"ds_read_b64_tr_b16 v[88:89], v9 offset:16384\n"
"ds_read_b64_tr_b16 v[90:91], v9 offset:20480\n"
"v_mfma_f32_16x16x32_bf16 v[180:183], a[132:135], v[96:99], v[180:183]\n"
"buffer_load_dwordx4 a[16:19], v1, s[44:47], s84 offen offset:2048 sc1\n"
"v_mfma_f32_16x16x32_bf16 v[188:191], a[132:135], v[112:115], v[188:191]\n"
"ds_read_b64_tr_b16 v[92:93], v9 offset:24576\n"
"ds_read_b64_tr_b16 v[94:95], v9 offset:28672\n"
"v_mfma_f32_16x16x32_bf16 v[188:191], a[140:143], v[116:119], v[188:191]\n"
"ds_read_b64_tr_b16 v[96:97], v10 offset:0\n"
"ds_read_b64_tr_b16 v[98:99], v10 offset:4096\n"
"v_mfma_f32_16x16x32_bf16 v[188:191], a[148:151], v[120:123], v[188:191]\n"
"ds_read_b64_tr_b16 v[100:101], v10 offset:8192\n"
"ds_read_b64_tr_b16 v[102:103], v10 offset:12288\n"
"v_mfma_f32_16x16x32_bf16 v[188:191], a[156:159], v[124:127], v[188:191]\n"
"buffer_load_dwordx4 a[20:23], v31, s[44:47], s84 offen offset:2048 sc1\n"
"v_mfma_f32_16x16x32_bf16 v[184:187], a[152:155], v[124:127], v[184:187]\n"
"ds_read_b64_tr_b16 v[104:105], v10 offset:16384\n"
"ds_read_b64_tr_b16 v[106:107], v10 offset:20480\n"
"v_mfma_f32_16x16x32_bf16 v[184:187], a[144:147], v[120:123], v[184:187]\n"
"ds_read_b64_tr_b16 v[108:109], v10 offset:24576\n"
"ds_read_b64_tr_b16 v[110:111], v10 offset:28672\n"
"v_mfma_f32_16x16x32_bf16 v[184:187], a[136:139], v[116:119], v[184:187]\n"
"v_mfma_f32_16x16x32_bf16 v[184:187], a[128:131], v[112:115], v[184:187]\n"
"s_waitcnt lgkmcnt(15)\n"
"v_mfma_f32_16x16x32_bf16 v[128:131], a[160:163], v[64:67], v[128:131]\n"
"s_add_u32 m0, s74, 32768\n"
"s_add_u32 s85, s73, 262144\n"
"ds_read_b64_tr_b16 v[112:113], v11 offset:0\n"
"s_waitcnt lgkmcnt(15)\n"
"v_mfma_f32_16x16x32_bf16 v[128:131], a[168:171], v[68:71], v[128:131]\n"
"buffer_load_dwordx4 a[24:27], v1, s[44:47], s84 offen offset:3072 sc1\n"
"s_waitcnt lgkmcnt(15)\n"
"v_mfma_f32_16x16x32_bf16 v[128:131], a[176:179], v[72:75], v[128:131]\n"
"s_lshl_b32 s86, s61, 6\n"
"ds_read_b64_tr_b16 v[114:115], v11 offset:4096\n"
"ds_read_b64_tr_b16 v[116:117], v11 offset:8192\n"
"s_waitcnt lgkmcnt(15)\n"
"v_mfma_f32_16x16x32_bf16 v[128:131], a[184:187], v[76:79], v[128:131]\n"
"s_lshl_b32 s87, s80, 12\n"
"ds_read_b64_tr_b16 v[118:119], v11 offset:12288\n"
"ds_read_b64_tr_b16 v[120:121], v11 offset:16384\n"
"v_mfma_f32_16x16x32_bf16 v[132:135], a[188:191], v[76:79], v[132:135]\n"
"buffer_load_dwordx4 a[28:31], v31, s[44:47], s84 offen offset:3072 sc1\n"
"v_mfma_f32_16x16x32_bf16 v[132:135], a[180:183], v[72:75], v[132:135]\n"
"ds_read_b64_tr_b16 v[122:123], v11 offset:20480\n"
"ds_read_b64_tr_b16 v[124:125], v11 offset:24576\n"
"v_mfma_f32_16x16x32_bf16 v[132:135], a[172:175], v[68:71], v[132:135]\n"
"ds_read_b64_tr_b16 v[126:127], v11 offset:28672\n"
"v_and_b32 v23, 0x23, v0\n"
"v_mfma_f32_16x16x32_bf16 v[132:135], a[164:167], v[64:67], v[132:135]\n"
"s_waitcnt lgkmcnt(15)\n"
"ds_read_b64_tr_b16 v[64:65], v12 offset:0\n"
"ds_read_b64_tr_b16 v[66:67], v12 offset:4096\n"
"v_mfma_f32_16x16x32_bf16 v[140:143], a[164:167], v[80:83], v[140:143]\n"
"buffer_load_dwordx4 v24, s[32:35], s85 offen offset:0 lds\n"
"s_waitcnt lgkmcnt(15)\n"
"v_mfma_f32_16x16x32_bf16 v[140:143], a[172:175], v[84:87], v[140:143]\n"
"s_waitcnt lgkmcnt(15)\n"
"ds_read_b64_tr_b16 v[68:69], v12 offset:8192\n"
"ds_read_b64_tr_b16 v[70:71], v12 offset:12288\n"
"v_mfma_f32_16x16x32_bf16 v[140:143], a[180:183], v[88:91], v[140:143]\n"
"s_waitcnt lgkmcnt(15)\n"
"ds_read_b64_tr_b16 v[72:73], v12 offset:16384\n"
"ds_read_b64_tr_b16 v[74:75], v12 offset:20480\n"
"v_mfma_f32_16x16x32_bf16 v[140:143], a[188:191], v[92:95], v[140:143]\n"
"buffer_load_dwordx4 v25, s[32:35], s85 offen offset:1024 lds\n"
"v_mfma_f32_16x16x32_bf16 v[136:139], a[184:187], v[92:95], v[136:139]\n"
"ds_read_b64_tr_b16 v[76:77], v12 offset:24576\n"
"ds_read_b64_tr_b16 v[78:79], v12 offset:28672\n"
"v_mfma_f32_16x16x32_bf16 v[136:139], a[176:179], v[88:91], v[136:139]\n"
"v_bfe_u32 v34, v0, 2, 2\n"
"v_lshl_add_u32 v23, v34, 3, v23\n"
"v_mfma_f32_16x16x32_bf16 v[136:139], a[168:171], v[84:87], v[136:139]\n"
"v_bfe_u32 v34, v0, 4, 1\n"
"v_lshl_add_u32 v23, v34, 2, v23\n"
"v_mfma_f32_16x16x32_bf16 v[136:139], a[160:163], v[80:83], v[136:139]\n"
"buffer_load_dwordx4 v26, s[32:35], s85 offen offset:2048 lds\n"
"s_waitcnt lgkmcnt(15)\n"
"v_mfma_f32_16x16x32_bf16 v[144:147], a[160:163], v[96:99], v[144:147]\n"
"s_waitcnt lgkmcnt(15)\n"
"v_add_u32 v23, s86, v23\n"
"v_lshlrev_b32 v34, 1, v23\n"
"v_mfma_f32_16x16x32_bf16 v[144:147], a[168:171], v[100:103], v[144:147]\n"
"s_lshl_b32 s86, s79, 15\n"
"s_add_u32 s86, s86, s87\n"
"v_lshlrev_b32 v23, 4, v23\n"
"s_waitcnt lgkmcnt(15)\n"
"v_mfma_f32_16x16x32_bf16 v[144:147], a[176:179], v[104:107], v[144:147]\n"
"s_add_u32 s87, s86, 16384\n"
"ds_read_b64_tr_b16 v[80:81], v13 offset:0\n"
"ds_read_b64_tr_b16 v[82:83], v13 offset:4096\n"
"s_waitcnt lgkmcnt(15)\n"
"v_mfma_f32_16x16x32_bf16 v[144:147], a[184:187], v[108:111], v[144:147]\n"
"buffer_load_dwordx4 v27, s[32:35], s85 offen offset:3072 lds\n"
"v_mfma_f32_16x16x32_bf16 v[148:151], a[188:191], v[108:111], v[148:151]\n"
"s_add_u32 m0, s74, 49152\n"
"s_add_u32 s85, s73, 327680\n"
"ds_read_b64_tr_b16 v[84:85], v13 offset:8192\n"
"v_mfma_f32_16x16x32_bf16 v[148:151], a[180:183], v[104:107], v[148:151]\n"
"ds_read_b64_tr_b16 v[86:87], v13 offset:12288\n"
"ds_read_b64_tr_b16 v[88:89], v13 offset:16384\n"
"v_mfma_f32_16x16x32_bf16 v[148:151], a[172:175], v[100:103], v[148:151]\n"
"buffer_load_dwordx4 v24, s[32:35], s85 offen offset:0 lds\n"
"v_mfma_f32_16x16x32_bf16 v[148:151], a[164:167], v[96:99], v[148:151]\n"
"s_waitcnt lgkmcnt(15)\n"
"ds_read_b64_tr_b16 v[90:91], v13 offset:20480\n"
"ds_read_b64_tr_b16 v[92:93], v13 offset:24576\n"
"v_mfma_f32_16x16x32_bf16 v[156:159], a[164:167], v[112:115], v[156:159]\n"
"s_waitcnt lgkmcnt(15)\n"
"ds_read_b64_tr_b16 v[94:95], v13 offset:28672\n"
"ds_read_b64_tr_b16 v[96:97], v14 offset:0\n"
"v_mfma_f32_16x16x32_bf16 v[156:159], a[172:175], v[116:119], v[156:159]\n"
"s_waitcnt lgkmcnt(15)\n"
"ds_read_b64_tr_b16 v[98:99], v14 offset:4096\n"
"ds_read_b64_tr_b16 v[100:101], v14 offset:8192\n"
"v_mfma_f32_16x16x32_bf16 v[156:159], a[180:183], v[120:123], v[156:159]\n"
"buffer_load_dwordx4 v25, s[32:35], s85 offen offset:1024 lds\n"
"s_waitcnt lgkmcnt(15)\n"
"v_mfma_f32_16x16x32_bf16 v[156:159], a[188:191], v[124:127], v[156:159]\n"
"ds_read_b64_tr_b16 v[102:103], v14 offset:12288\n"
"ds_read_b64_tr_b16 v[104:105], v14 offset:16384\n"
"v_mfma_f32_16x16x32_bf16 v[152:155], a[184:187], v[124:127], v[152:155]\n"
"ds_read_b64_tr_b16 v[106:107], v14 offset:20480\n"
"ds_read_b64_tr_b16 v[108:109], v14 offset:24576\n"
"v_mfma_f32_16x16x32_bf16 v[152:155], a[176:179], v[120:123], v[152:155]\n"
"buffer_load_dwordx4 v26, s[32:35], s85 offen offset:2048 lds\n"
"v_mfma_f32_16x16x32_bf16 v[152:155], a[168:171], v[116:119], v[152:155]\n"
"ds_read_b64_tr_b16 v[110:111], v14 offset:28672\n"
"v_mfma_f32_16x16x32_bf16 v[152:155], a[160:163], v[112:115], v[152:155]\n"
"s_waitcnt lgkmcnt(15)\n"
"ds_read_b64_tr_b16 v[112:113], v15 offset:0\n"
"ds_read_b64_tr_b16 v[114:115], v15 offset:4096\n"
"v_mfma_f32_16x16x32_bf16 v[160:163], a[160:163], v[64:67], v[160:163]\n"
"s_waitcnt lgkmcnt(15)\n"
"ds_read_b64_tr_b16 v[116:117], v15 offset:8192\n"
"ds_read_b64_tr_b16 v[118:119], v15 offset:12288\n"
"v_mfma_f32_16x16x32_bf16 v[160:163], a[168:171], v[68:71], v[160:163]\n"
"buffer_load_dwordx4 v27, s[32:35], s85 offen offset:3072 lds\n"
"s_waitcnt lgkmcnt(15)\n"
"v_mfma_f32_16x16x32_bf16 v[160:163], a[176:179], v[72:75], v[160:163]\n"
"s_waitcnt lgkmcnt(15)\n"
"ds_read_b64_tr_b16 v[120:121], v15 offset:16384\n"
"ds_read_b64_tr_b16 v[122:123], v15 offset:20480\n"
"v_mfma_f32_16x16x32_bf16 v[160:163], a[184:187], v[76:79], v[160:163]\n"
"ds_read_b64_tr_b16 v[124:125], v15 offset:24576\n"
"ds_read_b64_tr_b16 v[126:127], v15 offset:28672\n"
"v_mfma_f32_16x16x32_bf16 v[164:167], a[188:191], v[76:79], v[164:167]\n"
"v_mfma_f32_16x16x32_bf16 v[164:167], a[180:183], v[72:75], v[164:167]\n"
"v_mfma_f32_16x16x32_bf16 v[164:167], a[172:175], v[68:71], v[164:167]\n"
"v_mfma_f32_16x16x32_bf16 v[164:167], a[164:167], v[64:67], v[164:167]\n"
"s_waitcnt vmcnt(16) lgkmcnt(0)\n"
"s_barrier\n"
"v_mfma_f32_16x16x32_bf16 v[172:175], a[164:167], v[80:83], v[172:175]\n"
"s_add_u32 s84, s72, 8192\n"
"v_add_u32 v8, 65536, v8\n"
"v_add_u32 v9, 65536, v9\n"
"v_mfma_f32_16x16x32_bf16 v[172:175], a[172:175], v[84:87], v[172:175]\n"
"ds_read_b64_tr_b16 v[64:65], v8 offset:0\n"
"ds_read_b64_tr_b16 v[66:67], v8 offset:4096\n"
"v_mfma_f32_16x16x32_bf16 v[172:175], a[180:183], v[88:91], v[172:175]\n"
"ds_read_b64_tr_b16 v[68:69], v8 offset:8192\n"
"ds_read_b64_tr_b16 v[70:71], v8 offset:12288\n"
"v_mfma_f32_16x16x32_bf16 v[172:175], a[188:191], v[92:95], v[172:175]\n"
"ds_read_b64_tr_b16 v[72:73], v8 offset:16384\n"
"ds_read_b64_tr_b16 v[74:75], v8 offset:20480\n"
"v_mfma_f32_16x16x32_bf16 v[168:171], a[184:187], v[92:95], v[168:171]\n"
"ds_read_b64_tr_b16 v[76:77], v8 offset:24576\n"
"ds_read_b64_tr_b16 v[78:79], v8 offset:28672\n"
"v_mfma_f32_16x16x32_bf16 v[168:171], a[176:179], v[88:91], v[168:171]\n"
"buffer_load_dwordx4 a[32:35], v1, s[44:47], s84 offen offset:0 sc1\n"
"v_mfma_f32_16x16x32_bf16 v[168:171], a[168:171], v[84:87], v[168:171]\n"
"buffer_load_dwordx4 a[36:39], v31, s[44:47], s84 offen offset:0 sc1\n"
"v_mfma_f32_16x16x32_bf16 v[168:171], a[160:163], v[80:83], v[168:171]\n"
"ds_read_b64_tr_b16 v[80:81], v9 offset:0\n"
"v_add_u32 v10, 65536, v10\n"
"v_mfma_f32_16x16x32_bf16 v[176:179], a[160:163], v[96:99], v[176:179]\n"
"buffer_load_dwordx4 a[40:43], v1, s[44:47], s84 offen offset:1024 sc1\n"
"v_mfma_f32_16x16x32_bf16 v[176:179], a[168:171], v[100:103], v[176:179]\n"
"ds_read_b64_tr_b16 v[82:83], v9 offset:4096\n"
"ds_read_b64_tr_b16 v[84:85], v9 offset:8192\n"
"v_mfma_f32_16x16x32_bf16 v[176:179], a[176:179], v[104:107], v[176:179]\n"
"ds_read_b64_tr_b16 v[86:87], v9 offset:12288\n"
"ds_read_b64_tr_b16 v[88:89], v9 offset:16384\n"
"v_mfma_f32_16x16x32_bf16 v[176:179], a[184:187], v[108:111], v[176:179]\n"
"ds_read_b64_tr_b16 v[90:91], v9 offset:20480\n"
"ds_read_b64_tr_b16 v[92:93], v9 offset:24576\n"
"v_mfma_f32_16x16x32_bf16 v[180:183], a[188:191], v[108:111], v[180:183]\n"
"buffer_load_dwordx4 a[44:47], v31, s[44:47], s84 offen offset:1024 sc1\n"
"v_mfma_f32_16x16x32_bf16 v[180:183], a[180:183], v[104:107], v[180:183]\n"
"ds_read_b64_tr_b16 v[94:95], v9 offset:28672\n"
"v_mfma_f32_16x16x32_bf16 v[180:183], a[172:175], v[100:103], v[180:183]\n"
"v_mfma_f32_16x16x32_bf16 v[180:183], a[164:167], v[96:99], v[180:183]\n"
"buffer_load_dwordx4 a[48:51], v1, s[44:47], s84 offen offset:2048 sc1\n"
"v_mfma_f32_16x16x32_bf16 v[188:191], a[164:167], v[112:115], v[188:191]\n"
"ds_read_b64_tr_b16 v[96:97], v10 offset:0\n"
"s_nop 0\n"
"PG15_%=:\n"
"s_cbranch_execz PADRET_%=\n"
"ds_read_b64_tr_b16 v[98:99], v10 offset:4096\n"
"v_mfma_f32_16x16x32_bf16 v[188:191], a[172:175], v[116:119], v[188:191]\n"
"ds_read_b64_tr_b16 v[100:101], v10 offset:8192\n"
"ds_read_b64_tr_b16 v[102:103], v10 offset:12288\n"
"v_mfma_f32_16x16x32_bf16 v[188:191], a[180:183], v[120:123], v[188:191]\n"
"ds_read_b64_tr_b16 v[104:105], v10 offset:16384\n"
"ds_read_b64_tr_b16 v[106:107], v10 offset:20480\n"
"v_mfma_f32_16x16x32_bf16 v[188:191], a[188:191], v[124:127], v[188:191]\n"
"buffer_load_dwordx4 a[52:55], v31, s[44:47], s84 offen offset:2048 sc1\n"
"v_mfma_f32_16x16x32_bf16 v[184:187], a[184:187], v[124:127], v[184:187]\n"
"ds_read_b64_tr_b16 v[108:109], v10 offset:24576\n"
"ds_read_b64_tr_b16 v[110:111], v10 offset:28672\n"
"v_mfma_f32_16x16x32_bf16 v[184:187], a[176:179], v[120:123], v[184:187]\n"
"v_mfma_f32_16x16x32_bf16 v[184:187], a[168:171], v[116:119], v[184:187]\n"
"v_mfma_f32_16x16x32_bf16 v[184:187], a[160:163], v[112:115], v[184:187]\n"
"s_waitcnt vmcnt(14)\n"
"s_waitcnt lgkmcnt(15)\n"
"v_mfma_f32_16x16x32_bf16 v[128:131], a[0:3], v[64:67], v[128:131]\n"
"s_add_u32 m0, s74, 0\n"
"s_add_u32 s85, s73, 524288\n"
"v_add_u32 v11, 65536, v11\n"
"s_waitcnt lgkmcnt(15)\n"
"v_mfma_f32_16x16x32_bf16 v[128:131], a[8:11], v[68:71], v[128:131]\n"
"buffer_load_dwordx4 a[56:59], v1, s[44:47], s84 offen offset:3072 sc1\n"
"s_waitcnt lgkmcnt(15)\n"
"v_mfma_f32_16x16x32_bf16 v[128:131], a[16:19], v[72:75], v[128:131]\n"
"s_waitcnt lgkmcnt(15)\n"
"ds_read_b64_tr_b16 v[112:113], v11 offset:0\n"
"ds_read_b64_tr_b16 v[114:115], v11 offset:4096\n"
"v_mfma_f32_16x16x32_bf16 v[128:131], a[24:27], v[76:79], v[128:131]\n"
"ds_read_b64_tr_b16 v[116:117], v11 offset:8192\n"
"ds_read_b64_tr_b16 v[118:119], v11 offset:12288\n"
"v_mfma_f32_16x16x32_bf16 v[132:135], a[28:31], v[76:79], v[132:135]\n"
"buffer_load_dwordx4 a[60:63], v31, s[44:47], s84 offen offset:3072 sc1\n"
"v_mfma_f32_16x16x32_bf16 v[132:135], a[20:23], v[72:75], v[132:135]\n"
"ds_read_b64_tr_b16 v[120:121], v11 offset:16384\n"
"ds_read_b64_tr_b16 v[122:123], v11 offset:20480\n"
"v_mfma_f32_16x16x32_bf16 v[132:135], a[12:15], v[68:71], v[132:135]\n"
"ds_read_b64_tr_b16 v[124:125], v11 offset:24576\n"
"ds_read_b64_tr_b16 v[126:127], v11 offset:28672\n"
"v_mfma_f32_16x16x32_bf16 v[132:135], a[4:7], v[64:67], v[132:135]\n"
"s_waitcnt lgkmcnt(15)\n"
"v_add_u32 v12, 65536, v12\n"
"v_add_u32 v13, 65536, v13\n"
"v_mfma_f32_16x16x32_bf16 v[140:143], a[4:7], v[80:83], v[140:143]\n"
"buffer_load_dwordx4 v24, s[32:35], s85 offen offset:0 lds\n"
"s_waitcnt lgkmcnt(15)\n"
"v_mfma_f32_16x16x32_bf16 v[140:143], a[12:15], v[84:87], v[140:143]\n"
"s_waitcnt lgkmcnt(15)\n"
"ds_read_b64_tr_b16 v[64:65], v12 offset:0\n"
"ds_read_b64_tr_b16 v[66:67], v12 offset:4096\n"
"v_mfma_f32_16x16x32_bf16 v[140:143], a[20:23], v[88:91], v[140:143]\n"
"s_waitcnt lgkmcnt(15)\n"
"ds_read_b64_tr_b16 v[68:69], v12 offset:8192\n"
"ds_read_b64_tr_b16 v[70:71], v12 offset:12288\n"
"v_mfma_f32_16x16x32_bf16 v[140:143], a[28:31], v[92:95], v[140:143]\n"
"buffer_load_dwordx4 v25, s[32:35], s85 offen offset:1024 lds\n"
"v_mfma_f32_16x16x32_bf16 v[136:139], a[24:27], v[92:95], v[136:139]\n"
"ds_read_b64_tr_b16 v[72:73], v12 offset:16384\n"
"ds_read_b64_tr_b16 v[74:75], v12 offset:20480\n"
"v_mfma_f32_16x16x32_bf16 v[136:139], a[16:19], v[88:91], v[136:139]\n"
"ds_read_b64_tr_b16 v[76:77], v12 offset:24576\n"
"ds_read_b64_tr_b16 v[78:79], v12 offset:28672\n"
"v_mfma_f32_16x16x32_bf16 v[136:139], a[8:11], v[84:87], v[136:139]\n"
"v_add_u32 v14, 65536, v14\n"
"v_add_u32 v15, 65536, v15\n"
"v_mfma_f32_16x16x32_bf16 v[136:139], a[0:3], v[80:83], v[136:139]\n"
"buffer_load_dwordx4 v26, s[32:35], s85 offen offset:2048 lds\n"
"s_waitcnt lgkmcnt(15)\n"
"v_mfma_f32_16x16x32_bf16 v[144:147], a[0:3], v[96:99], v[144:147]\n"
"s_waitcnt lgkmcnt(15)\n"
"ds_read_b64_tr_b16 v[80:81], v13 offset:0\n"
"ds_read_b64_tr_b16 v[82:83], v13 offset:4096\n"
"v_mfma_f32_16x16x32_bf16 v[144:147], a[8:11], v[100:103], v[144:147]\n"
"s_waitcnt lgkmcnt(15)\n"
"ds_read_b64_tr_b16 v[84:85], v13 offset:8192\n"
"ds_read_b64_tr_b16 v[86:87], v13 offset:12288\n"
"v_mfma_f32_16x16x32_bf16 v[144:147], a[16:19], v[104:107], v[144:147]\n"
"s_waitcnt lgkmcnt(15)\n"
"ds_read_b64_tr_b16 v[88:89], v13 offset:16384\n"
"ds_read_b64_tr_b16 v[90:91], v13 offset:20480\n"
"v_mfma_f32_16x16x32_bf16 v[144:147], a[24:27], v[108:111], v[144:147]\n"
"buffer_load_dwordx4 v27, s[32:35], s85 offen offset:3072 lds\n"
"v_mfma_f32_16x16x32_bf16 v[148:151], a[28:31], v[108:111], v[148:151]\n"
"s_add_u32 m0, s74, 16384\n"
"s_add_u32 s85, s73, 589824\n"
"ds_read_b64_tr_b16 v[92:93], v13 offset:24576\n"
"v_mfma_f32_16x16x32_bf16 v[148:151], a[20:23], v[104:107], v[148:151]\n"
"ds_read_b64_tr_b16 v[94:95], v13 offset:28672\n"
"v_mfma_f32_16x16x32_bf16 v[148:151], a[12:15], v[100:103], v[148:151]\n"
"buffer_load_dwordx4 v24, s[32:35], s85 offen offset:0 lds\n"
"v_mfma_f32_16x16x32_bf16 v[148:151], a[4:7], v[96:99], v[148:151]\n"
"s_waitcnt lgkmcnt(15)\n"
"ds_read_b64_tr_b16 v[96:97], v14 offset:0\n"
"ds_read_b64_tr_b16 v[98:99], v14 offset:4096\n"
"v_mfma_f32_16x16x32_bf16 v[156:159], a[4:7], v[112:115], v[156:159]\n"
"s_waitcnt lgkmcnt(15)\n"
"ds_read_b64_tr_b16 v[100:101], v14 offset:8192\n"
"ds_read_b64_tr_b16 v[102:103], v14 offset:12288\n"
"v_mfma_f32_16x16x32_bf16 v[156:159], a[12:15], v[116:119], v[156:159]\n"
"s_waitcnt lgkmcnt(15)\n"
"ds_read_b64_tr_b16 v[104:105], v14 offset:16384\n"
"ds_read_b64_tr_b16 v[106:107], v14 offset:20480\n"
"v_mfma_f32_16x16x32_bf16 v[156:159], a[20:23], v[120:123], v[156:159]\n"
"buffer_load_dwordx4 v25, s[32:35], s85 offen offset:1024 lds\n"
"s_waitcnt lgkmcnt(15)\n"
"v_mfma_f32_16x16x32_bf16 v[156:159], a[28:31], v[124:127], v[156:159]\n"
"ds_read_b64_tr_b16 v[108:109], v14 offset:24576\n"
"ds_read_b64_tr_b16 v[110:111], v14 offset:28672\n"
"v_mfma_f32_16x16x32_bf16 v[152:155], a[24:27], v[124:127], v[152:155]\n"
"v_mfma_f32_16x16x32_bf16 v[152:155], a[16:19], v[120:123], v[152:155]\n"
"buffer_load_dwordx4 v26, s[32:35], s85 offen offset:2048 lds\n"
"v_mfma_f32_16x16x32_bf16 v[152:155], a[8:11], v[116:119], v[152:155]\n"
"v_mfma_f32_16x16x32_bf16 v[152:155], a[0:3], v[112:115], v[152:155]\n"
"s_waitcnt lgkmcnt(15)\n"
"ds_read_b64_tr_b16 v[112:113], v15 offset:0\n"
"ds_read_b64_tr_b16 v[114:115], v15 offset:4096\n"
"v_mfma_f32_16x16x32_bf16 v[160:163], a[0:3], v[64:67], v[160:163]\n"
"s_waitcnt lgkmcnt(15)\n"
"ds_read_b64_tr_b16 v[116:117], v15 offset:8192\n"
"ds_read_b64_tr_b16 v[118:119], v15 offset:12288\n"
"v_mfma_f32_16x16x32_bf16 v[160:163], a[8:11], v[68:71], v[160:163]\n"
"buffer_load_dwordx4 v27, s[32:35], s85 offen offset:3072 lds\n"
"s_waitcnt lgkmcnt(15)\n"
"v_mfma_f32_16x16x32_bf16 v[160:163], a[16:19], v[72:75], v[160:163]\n"
"s_waitcnt lgkmcnt(15)\n"
"ds_read_b64_tr_b16 v[120:121], v15 offset:16384\n"
"ds_read_b64_tr_b16 v[122:123], v15 offset:20480\n"
"v_mfma_f32_16x16x32_bf16 v[160:163], a[24:27], v[76:79], v[160:163]\n"
"ds_read_b64_tr_b16 v[124:125], v15 offset:24576\n"
"ds_read_b64_tr_b16 v[126:127], v15 offset:28672\n"
"v_mfma_f32_16x16x32_bf16 v[164:167], a[28:31], v[76:79], v[164:167]\n"
"v_mfma_f32_16x16x32_bf16 v[164:167], a[20:23], v[72:75], v[164:167]\n"
"v_mfma_f32_16x16x32_bf16 v[164:167], a[12:15], v[68:71], v[164:167]\n"
"v_mfma_f32_16x16x32_bf16 v[164:167], a[4:7], v[64:67], v[164:167]\n"
"s_waitcnt vmcnt(16) lgkmcnt(0)\n"
"s_barrier\n"
"v_mfma_f32_16x16x32_bf16 v[172:175], a[4:7], v[80:83], v[172:175]\n"
"s_add_u32 s84, s72, 16384\n"
"v_add_u32 v8, 4294836224, v8\n"
"v_add_u32 v9, 4294836224, v9\n"
"v_mfma_f32_16x16x32_bf16 v[172:175], a[12:15], v[84:87], v[172:175]\n"
"ds_read_b64_tr_b16 v[64:65], v8 offset:32768\n"
"ds_read_b64_tr_b16 v[66:67], v8 offset:36864\n"
"v_mfma_f32_16x16x32_bf16 v[172:175], a[20:23], v[88:91], v[172:175]\n"
"ds_read_b64_tr_b16 v[68:69], v8 offset:40960\n"
"ds_read_b64_tr_b16 v[70:71], v8 offset:45056\n"
"v_mfma_f32_16x16x32_bf16 v[172:175], a[28:31], v[92:95], v[172:175]\n"
"ds_read_b64_tr_b16 v[72:73], v8 offset:49152\n"
"ds_read_b64_tr_b16 v[74:75], v8 offset:53248\n"
"v_mfma_f32_16x16x32_bf16 v[168:171], a[24:27], v[92:95], v[168:171]\n"
"ds_read_b64_tr_b16 v[76:77], v8 offset:57344\n"
"ds_read_b64_tr_b16 v[78:79], v8 offset:61440\n"
"v_mfma_f32_16x16x32_bf16 v[168:171], a[16:19], v[88:91], v[168:171]\n"
"buffer_load_dwordx4 a[192:195], v1, s[44:47], s84 offen offset:0 sc1\n"
"v_mfma_f32_16x16x32_bf16 v[168:171], a[8:11], v[84:87], v[168:171]\n"
"buffer_load_dwordx4 a[196:199], v31, s[44:47], s84 offen offset:0 sc1\n"
"v_mfma_f32_16x16x32_bf16 v[168:171], a[0:3], v[80:83], v[168:171]\n"
"ds_read_b64_tr_b16 v[80:81], v9 offset:32768\n"
"v_add_u32 v10, 4294836224, v10\n"
"v_mfma_f32_16x16x32_bf16 v[176:179], a[0:3], v[96:99], v[176:179]\n"
"buffer_load_dwordx4 a[200:203], v1, s[44:47], s84 offen offset:1024 sc1\n"
"v_mfma_f32_16x16x32_bf16 v[176:179], a[8:11], v[100:103], v[176:179]\n"
"ds_read_b64_tr_b16 v[82:83], v9 offset:36864\n"
"ds_read_b64_tr_b16 v[84:85], v9 offset:40960\n"
"v_mfma_f32_16x16x32_bf16 v[176:179], a[16:19], v[104:107], v[176:179]\n"
"ds_read_b64_tr_b16 v[86:87], v9 offset:45056\n"
"ds_read_b64_tr_b16 v[88:89], v9 offset:49152\n"
"v_mfma_f32_16x16x32_bf16 v[176:179], a[24:27], v[108:111], v[176:179]\n"
"ds_read_b64_tr_b16 v[90:91], v9 offset:53248\n"
"ds_read_b64_tr_b16 v[92:93], v9 offset:57344\n"
"v_mfma_f32_16x16x32_bf16 v[180:183], a[28:31], v[108:111], v[180:183]\n"
"buffer_load_dwordx4 a[204:207], v31, s[44:47], s84 offen offset:1024 sc1\n"
"v_mfma_f32_16x16x32_bf16 v[180:183], a[20:23], v[104:107], v[180:183]\n"
"ds_read_b64_tr_b16 v[94:95], v9 offset:61440\n"
"v_mfma_f32_16x16x32_bf16 v[180:183], a[12:15], v[100:103], v[180:183]\n"
"v_mfma_f32_16x16x32_bf16 v[180:183], a[4:7], v[96:99], v[180:183]\n"
"buffer_load_dwordx4 a[208:211], v1, s[44:47], s84 offen offset:2048 sc1\n"
"v_mfma_f32_16x16x32_bf16 v[188:191], a[4:7], v[112:115], v[188:191]\n"
"ds_read_b64_tr_b16 v[96:97], v10 offset:32768\n"
"ds_read_b64_tr_b16 v[98:99], v10 offset:36864\n"
"v_mfma_f32_16x16x32_bf16 v[188:191], a[12:15], v[116:119], v[188:191]\n"
"ds_read_b64_tr_b16 v[100:101], v10 offset:40960\n"
"ds_read_b64_tr_b16 v[102:103], v10 offset:45056\n"
"v_mfma_f32_16x16x32_bf16 v[188:191], a[20:23], v[120:123], v[188:191]\n"
"ds_read_b64_tr_b16 v[104:105], v10 offset:49152\n"
"ds_read_b64_tr_b16 v[106:107], v10 offset:53248\n"
"v_mfma_f32_16x16x32_bf16 v[188:191], a[28:31], v[124:127], v[188:191]\n"
"buffer_load_dwordx4 a[212:215], v31, s[44:47], s84 offen offset:2048 sc1\n"
"v_mfma_f32_16x16x32_bf16 v[184:187], a[24:27], v[124:127], v[184:187]\n"
"ds_read_b64_tr_b16 v[108:109], v10 offset:57344\n"
"ds_read_b64_tr_b16 v[110:111], v10 offset:61440\n"
"v_mfma_f32_16x16x32_bf16 v[184:187], a[16:19], v[120:123], v[184:187]\n"
"v_mfma_f32_16x16x32_bf16 v[184:187], a[8:11], v[116:119], v[184:187]\n"
"v_mfma_f32_16x16x32_bf16 v[184:187], a[0:3], v[112:115], v[184:187]\n"
"s_waitcnt vmcnt(14)\n"
"s_waitcnt lgkmcnt(15)\n"
"v_mfma_f32_16x16x32_bf16 v[128:131], a[32:35], v[64:67], v[128:131]\n"
"s_add_u32 m0, s74, 98304\n"
"s_add_u32 s85, s73, 786432\n"
"v_add_u32 v11, 4294836224, v11\n"
"s_waitcnt lgkmcnt(15)\n"
"v_mfma_f32_16x16x32_bf16 v[128:131], a[40:43], v[68:71], v[128:131]\n"
"buffer_load_dwordx4 a[216:219], v1, s[44:47], s84 offen offset:3072 sc1\n"
"s_waitcnt lgkmcnt(15)\n"
"v_mfma_f32_16x16x32_bf16 v[128:131], a[48:51], v[72:75], v[128:131]\n"
"s_waitcnt lgkmcnt(15)\n"
"ds_read_b64_tr_b16 v[112:113], v11 offset:32768\n"
"ds_read_b64_tr_b16 v[114:115], v11 offset:36864\n"
"v_mfma_f32_16x16x32_bf16 v[128:131], a[56:59], v[76:79], v[128:131]\n"
"ds_read_b64_tr_b16 v[116:117], v11 offset:40960\n"
"ds_read_b64_tr_b16 v[118:119], v11 offset:45056\n"
"v_mfma_f32_16x16x32_bf16 v[132:135], a[60:63], v[76:79], v[132:135]\n"
"buffer_load_dwordx4 a[220:223], v31, s[44:47], s84 offen offset:3072 sc1\n"
"v_mfma_f32_16x16x32_bf16 v[132:135], a[52:55], v[72:75], v[132:135]\n"
"ds_read_b64_tr_b16 v[120:121], v11 offset:49152\n"
"ds_read_b64_tr_b16 v[122:123], v11 offset:53248\n"
"v_mfma_f32_16x16x32_bf16 v[132:135], a[44:47], v[68:71], v[132:135]\n"
"ds_read_b64_tr_b16 v[124:125], v11 offset:57344\n"
"ds_read_b64_tr_b16 v[126:127], v11 offset:61440\n"
"v_mfma_f32_16x16x32_bf16 v[132:135], a[36:39], v[64:67], v[132:135]\n"
"s_waitcnt lgkmcnt(15)\n"
"v_add_u32 v12, 4294836224, v12\n"
"v_add_u32 v13, 4294836224, v13\n"
"v_mfma_f32_16x16x32_bf16 v[140:143], a[36:39], v[80:83], v[140:143]\n"
"buffer_load_dwordx4 v24, s[32:35], s85 offen offset:0 lds\n"
"s_waitcnt lgkmcnt(15)\n"
"v_mfma_f32_16x16x32_bf16 v[140:143], a[44:47], v[84:87], v[140:143]\n"
"s_waitcnt lgkmcnt(15)\n"
"ds_read_b64_tr_b16 v[64:65], v12 offset:32768\n"
"ds_read_b64_tr_b16 v[66:67], v12 offset:36864\n"
"v_mfma_f32_16x16x32_bf16 v[140:143], a[52:55], v[88:91], v[140:143]\n"
"s_waitcnt lgkmcnt(15)\n"
"ds_read_b64_tr_b16 v[68:69], v12 offset:40960\n"
"ds_read_b64_tr_b16 v[70:71], v12 offset:45056\n"
"v_mfma_f32_16x16x32_bf16 v[140:143], a[60:63], v[92:95], v[140:143]\n"
"buffer_load_dwordx4 v25, s[32:35], s85 offen offset:1024 lds\n"
"v_mfma_f32_16x16x32_bf16 v[136:139], a[56:59], v[92:95], v[136:139]\n"
"ds_read_b64_tr_b16 v[72:73], v12 offset:49152\n"
"ds_read_b64_tr_b16 v[74:75], v12 offset:53248\n"
"v_mfma_f32_16x16x32_bf16 v[136:139], a[48:51], v[88:91], v[136:139]\n"
"ds_read_b64_tr_b16 v[76:77], v12 offset:57344\n"
"ds_read_b64_tr_b16 v[78:79], v12 offset:61440\n"
"v_mfma_f32_16x16x32_bf16 v[136:139], a[40:43], v[84:87], v[136:139]\n"
"v_add_u32 v14, 4294836224, v14\n"
"v_add_u32 v15, 4294836224, v15\n"
"v_mfma_f32_16x16x32_bf16 v[136:139], a[32:35], v[80:83], v[136:139]\n"
"buffer_load_dwordx4 v26, s[32:35], s85 offen offset:2048 lds\n"
"s_waitcnt lgkmcnt(15)\n"
"v_mfma_f32_16x16x32_bf16 v[144:147], a[32:35], v[96:99], v[144:147]\n"
"s_waitcnt lgkmcnt(15)\n"
"ds_read_b64_tr_b16 v[80:81], v13 offset:32768\n"
"ds_read_b64_tr_b16 v[82:83], v13 offset:36864\n"
"v_mfma_f32_16x16x32_bf16 v[144:147], a[40:43], v[100:103], v[144:147]\n"
"s_waitcnt lgkmcnt(15)\n"
"ds_read_b64_tr_b16 v[84:85], v13 offset:40960\n"
"ds_read_b64_tr_b16 v[86:87], v13 offset:45056\n"
"v_mfma_f32_16x16x32_bf16 v[144:147], a[48:51], v[104:107], v[144:147]\n"
"s_waitcnt lgkmcnt(15)\n"
"ds_read_b64_tr_b16 v[88:89], v13 offset:49152\n"
"ds_read_b64_tr_b16 v[90:91], v13 offset:53248\n"
"v_mfma_f32_16x16x32_bf16 v[144:147], a[56:59], v[108:111], v[144:147]\n"
"buffer_load_dwordx4 v27, s[32:35], s85 offen offset:3072 lds\n"
"v_mfma_f32_16x16x32_bf16 v[148:151], a[60:63], v[108:111], v[148:151]\n"
"s_add_u32 m0, s74, 114688\n"
"s_add_u32 s85, s73, 851968\n"
"ds_read_b64_tr_b16 v[92:93], v13 offset:57344\n"
"v_mfma_f32_16x16x32_bf16 v[148:151], a[52:55], v[104:107], v[148:151]\n"
"ds_read_b64_tr_b16 v[94:95], v13 offset:61440\n"
"v_mfma_f32_16x16x32_bf16 v[148:151], a[44:47], v[100:103], v[148:151]\n"
"buffer_load_dwordx4 v24, s[32:35], s85 offen offset:0 lds\n"
"v_mfma_f32_16x16x32_bf16 v[148:151], a[36:39], v[96:99], v[148:151]\n"
"s_waitcnt lgkmcnt(15)\n"
"ds_read_b64_tr_b16 v[96:97], v14 offset:32768\n"
"ds_read_b64_tr_b16 v[98:99], v14 offset:36864\n"
"v_mfma_f32_16x16x32_bf16 v[156:159], a[36:39], v[112:115], v[156:159]\n"
"s_waitcnt lgkmcnt(15)\n"
"ds_read_b64_tr_b16 v[100:101], v14 offset:40960\n"
"ds_read_b64_tr_b16 v[102:103], v14 offset:45056\n"
"v_mfma_f32_16x16x32_bf16 v[156:159], a[44:47], v[116:119], v[156:159]\n"
"s_waitcnt lgkmcnt(15)\n"
"ds_read_b64_tr_b16 v[104:105], v14 offset:49152\n"
"ds_read_b64_tr_b16 v[106:107], v14 offset:53248\n"
"v_mfma_f32_16x16x32_bf16 v[156:159], a[52:55], v[120:123], v[156:159]\n"
"buffer_load_dwordx4 v25, s[32:35], s85 offen offset:1024 lds\n"
"s_waitcnt lgkmcnt(15)\n"
"v_mfma_f32_16x16x32_bf16 v[156:159], a[60:63], v[124:127], v[156:159]\n"
"ds_read_b64_tr_b16 v[108:109], v14 offset:57344\n"
"ds_read_b64_tr_b16 v[110:111], v14 offset:61440\n"
"v_mfma_f32_16x16x32_bf16 v[152:155], a[56:59], v[124:127], v[152:155]\n"
"v_mfma_f32_16x16x32_bf16 v[152:155], a[48:51], v[120:123], v[152:155]\n"
"buffer_load_dwordx4 v26, s[32:35], s85 offen offset:2048 lds\n"
"v_mfma_f32_16x16x32_bf16 v[152:155], a[40:43], v[116:119], v[152:155]\n"
"v_mfma_f32_16x16x32_bf16 v[152:155], a[32:35], v[112:115], v[152:155]\n"
"s_waitcnt lgkmcnt(15)\n"
"ds_read_b64_tr_b16 v[112:113], v15 offset:32768\n"
"ds_read_b64_tr_b16 v[114:115], v15 offset:36864\n"
"v_mfma_f32_16x16x32_bf16 v[160:163], a[32:35], v[64:67], v[160:163]\n"
"s_waitcnt lgkmcnt(15)\n"
"ds_read_b64_tr_b16 v[116:117], v15 offset:40960\n"
"ds_read_b64_tr_b16 v[118:119], v15 offset:45056\n"
"v_mfma_f32_16x16x32_bf16 v[160:163], a[40:43], v[68:71], v[160:163]\n"
"buffer_load_dwordx4 v27, s[32:35], s85 offen offset:3072 lds\n"
"s_waitcnt lgkmcnt(15)\n"
"v_mfma_f32_16x16x32_bf16 v[160:163], a[48:51], v[72:75], v[160:163]\n"
"s_waitcnt lgkmcnt(15)\n"
"ds_read_b64_tr_b16 v[120:121], v15 offset:49152\n"
"ds_read_b64_tr_b16 v[122:123], v15 offset:53248\n"
"v_mfma_f32_16x16x32_bf16 v[160:163], a[56:59], v[76:79], v[160:163]\n"
"ds_read_b64_tr_b16 v[124:125], v15 offset:57344\n"
"ds_read_b64_tr_b16 v[126:127], v15 offset:61440\n"
"v_mfma_f32_16x16x32_bf16 v[164:167], a[60:63], v[76:79], v[164:167]\n"
"v_mfma_f32_16x16x32_bf16 v[164:167], a[52:55], v[72:75], v[164:167]\n"
"v_mfma_f32_16x16x32_bf16 v[164:167], a[44:47], v[68:71], v[164:167]\n"
"v_mfma_f32_16x16x32_bf16 v[164:167], a[36:39], v[64:67], v[164:167]\n"
"s_waitcnt vmcnt(16) lgkmcnt(0)\n"
"s_barrier\n"
"v_mfma_f32_16x16x32_bf16 v[172:175], a[36:39], v[80:83], v[172:175]\n"
"s_add_u32 s84, s72, 24576\n"
"ds_read_b64_tr_b16 v[64:65], v8 offset:0\n"
"ds_read_b64_tr_b16 v[66:67], v8 offset:4096\n"
"v_mfma_f32_16x16x32_bf16 v[172:175], a[44:47], v[84:87], v[172:175]\n"
"s_bfe_u32 s90, s79, 0x30002\n"
"ds_read_b64_tr_b16 v[68:69], v8 offset:8192\n"
"ds_read_b64_tr_b16 v[70:71], v8 offset:12288\n"
"v_mfma_f32_16x16x32_bf16 v[172:175], a[52:55], v[88:91], v[172:175]\n"
"s_lshl_b32 s90, s90, 5\n"
"ds_read_b64_tr_b16 v[72:73], v8 offset:16384\n"
"ds_read_b64_tr_b16 v[74:75], v8 offset:20480\n"
"v_mfma_f32_16x16x32_bf16 v[172:175], a[60:63], v[92:95], v[172:175]\n"
"s_lshr_b32 s89, s79, 5\n"
"ds_read_b64_tr_b16 v[76:77], v8 offset:24576\n"
"ds_read_b64_tr_b16 v[78:79], v8 offset:28672\n"
"v_mfma_f32_16x16x32_bf16 v[168:171], a[56:59], v[92:95], v[168:171]\n"
"buffer_load_dwordx4 a[224:227], v1, s[44:47], s84 offen offset:0 sc1\n"
"v_mfma_f32_16x16x32_bf16 v[168:171], a[48:51], v[88:91], v[168:171]\n"
"buffer_load_dwordx4 a[228:231], v31, s[44:47], s84 offen offset:0 sc1\n"
"v_mfma_f32_16x16x32_bf16 v[168:171], a[40:43], v[84:87], v[168:171]\n"
"s_add_u32 s90, s90, s89\n"
"s_lshl_b32 s90, s90, 2\n"
"s_and_b32 s89, s79, 3\n"
"v_mfma_f32_16x16x32_bf16 v[168:171], a[32:35], v[80:83], v[168:171]\n"
"s_add_u32 s90, s90, s89\n"
"s_lshl_b32 s90, s90, 7\n"
"s_add_u32 s90, s90, 262144\n"
"v_mfma_f32_16x16x32_bf16 v[176:179], a[32:35], v[96:99], v[176:179]\n"
"buffer_load_dwordx4 a[232:235], v1, s[44:47], s84 offen offset:1024 sc1\n"
"v_mfma_f32_16x16x32_bf16 v[176:179], a[40:43], v[100:103], v[176:179]\n"
"s_lshl_b32 s89, s19, 13\n"
"v_lshl_add_u32 v16, v2, 10, s90\n"
"v_lshlrev_b32 v36, 15, v3\n"
"v_mfma_f32_16x16x32_bf16 v[176:179], a[48:51], v[104:107], v[176:179]\n"
"s_lshl_b32 s90, s18, 2\n"
"s_add_u32 s90, s90, s61\n"
"s_lshl_b32 s90, s90, 17\n"
"v_mfma_f32_16x16x32_bf16 v[176:179], a[56:59], v[108:111], v[176:179]\n"
"s_add_u32 s90, s90, s89\n"
"s_bfe_u32 s89, s60, 0x20003\n"
"s_lshl_b32 s89, s89, 11\n"
"v_mfma_f32_16x16x32_bf16 v[180:183], a[60:63], v[108:111], v[180:183]\n"
"buffer_load_dwordx4 a[236:239], v31, s[44:47], s84 offen offset:1024 sc1\n"
"v_mfma_f32_16x16x32_bf16 v[180:183], a[52:55], v[104:107], v[180:183]\n"
"s_add_u32 s90, s90, s89\n"
"s_add_u32 s90, s90, 54525952\n"
"v_lshl_add_u32 v36, v2, 4, v36\n"
"v_mfma_f32_16x16x32_bf16 v[180:183], a[44:47], v[100:103], v[180:183]\n"
"ds_read_b64_tr_b16 v[80:81], v9 offset:0\n"
"ds_read_b64_tr_b16 v[82:83], v9 offset:4096\n"
"v_mfma_f32_16x16x32_bf16 v[180:183], a[36:39], v[96:99], v[180:183]\n"
"buffer_load_dwordx4 a[240:243], v1, s[44:47], s84 offen offset:2048 sc1\n"
"v_mfma_f32_16x16x32_bf16 v[188:191], a[36:39], v[112:115], v[188:191]\n"
"ds_read_b64_tr_b16 v[84:85], v9 offset:8192\n"
"ds_read_b64_tr_b16 v[86:87], v9 offset:12288\n"
"v_mfma_f32_16x16x32_bf16 v[188:191], a[44:47], v[116:119], v[188:191]\n"
"ds_read_b64_tr_b16 v[88:89], v9 offset:16384\n"
"ds_read_b64_tr_b16 v[90:91], v9 offset:20480\n"
"v_mfma_f32_16x16x32_bf16 v[188:191], a[52:55], v[120:123], v[188:191]\n"
"ds_read_b64_tr_b16 v[92:93], v9 offset:24576\n"
"ds_read_b64_tr_b16 v[94:95], v9 offset:28672\n"
"v_mfma_f32_16x16x32_bf16 v[188:191], a[60:63], v[124:127], v[188:191]\n"
"buffer_load_dwordx4 a[244:247], v31, s[44:47], s84 offen offset:2048 sc1\n"
"v_mfma_f32_16x16x32_bf16 v[184:187], a[56:59], v[124:127], v[184:187]\n"
"ds_read_b64_tr_b16 v[96:97], v10 offset:0\n"
"ds_read_b64_tr_b16 v[98:99], v10 offset:4096\n"
"v_mfma_f32_16x16x32_bf16 v[184:187], a[48:51], v[120:123], v[184:187]\n"
"ds_read_b64_tr_b16 v[100:101], v10 offset:8192\n"
"ds_read_b64_tr_b16 v[102:103], v10 offset:12288\n"
"v_mfma_f32_16x16x32_bf16 v[184:187], a[40:43], v[116:119], v[184:187]\n"
"ds_read_b64_tr_b16 v[104:105], v10 offset:16384\n"
"ds_read_b64_tr_b16 v[106:107], v10 offset:20480\n"
"v_mfma_f32_16x16x32_bf16 v[184:187], a[32:35], v[112:115], v[184:187]\n"
"ds_read_b64_tr_b16 v[108:109], v10 offset:24576\n"
"ds_read_b64_tr_b16 v[110:111], v10 offset:28672\n"
"s_waitcnt vmcnt(14)\n"
"s_waitcnt lgkmcnt(15)\n"
"v_mfma_f32_16x16x32_bf16 v[128:131], a[192:195], v[64:67], v[128:131]\n"
"s_add_u32 m0, s91, 65536\n"
"s_add_u32 s85, s75, 0\n"
"ds_read_b64_tr_b16 v[112:113], v11 offset:0\n"
"s_waitcnt lgkmcnt(15)\n"
"v_mfma_f32_16x16x32_bf16 v[128:131], a[200:203], v[68:71], v[128:131]\n"
"buffer_load_dwordx4 a[248:251], v1, s[44:47], s84 offen offset:3072 sc1\n"
"s_waitcnt lgkmcnt(15)\n"
"v_mfma_f32_16x16x32_bf16 v[128:131], a[208:211], v[72:75], v[128:131]\n"
"s_waitcnt lgkmcnt(15)\n"
"ds_read_b64_tr_b16 v[114:115], v11 offset:4096\n"
"ds_read_b64_tr_b16 v[116:117], v11 offset:8192\n"
"v_mfma_f32_16x16x32_bf16 v[128:131], a[216:219], v[76:79], v[128:131]\n"
"buffer_load_dwordx4 a[252:255], v31, s[44:47], s84 offen offset:3072 sc1\n"
"v_mfma_f32_16x16x32_bf16 v[132:135], a[220:223], v[76:79], v[132:135]\n"
"ds_read_b64_tr_b16 v[118:119], v11 offset:12288\n"
"ds_read_b64_tr_b16 v[120:121], v11 offset:16384\n"
"v_mfma_f32_16x16x32_bf16 v[132:135], a[212:215], v[72:75], v[132:135]\n"
"buffer_load_dwordx4 v17, s[36:39], s85 offen offset:0 lds\n"
"v_mfma_f32_16x16x32_bf16 v[132:135], a[204:207], v[68:71], v[132:135]\n"
"ds_read_b64_tr_b16 v[122:123], v11 offset:20480\n"
"ds_read_b64_tr_b16 v[124:125], v11 offset:24576\n"
"v_mfma_f32_16x16x32_bf16 v[132:135], a[196:199], v[64:67], v[132:135]\n"
"buffer_load_dwordx4 v18, s[36:39], s85 offen offset:1024 lds\n"
"s_waitcnt lgkmcnt(15)\n"
"v_mfma_f32_16x16x32_bf16 v[140:143], a[196:199], v[80:83], v[140:143]\n"
"s_waitcnt lgkmcnt(15)\n"
"ds_read_b64_tr_b16 v[126:127], v11 offset:28672\n"
"ds_read_b64_tr_b16 v[64:65], v12 offset:0\n"
"v_mfma_f32_16x16x32_bf16 v[140:143], a[204:207], v[84:87], v[140:143]\n"
"s_waitcnt lgkmcnt(15)\n"
"ds_read_b64_tr_b16 v[66:67], v12 offset:4096\n"
"ds_read_b64_tr_b16 v[68:69], v12 offset:8192\n"
"v_mfma_f32_16x16x32_bf16 v[140:143], a[212:215], v[88:91], v[140:143]\n"
"buffer_load_dwordx4 v19, s[36:39], s85 offen offset:2048 lds\n"
"s_waitcnt lgkmcnt(15)\n"
"v_mfma_f32_16x16x32_bf16 v[140:143], a[220:223], v[92:95], v[140:143]\n"
"ds_read_b64_tr_b16 v[70:71], v12 offset:12288\n"
"ds_read_b64_tr_b16 v[72:73], v12 offset:16384\n"
"v_mfma_f32_16x16x32_bf16 v[136:139], a[216:219], v[92:95], v[136:139]\n"
"buffer_load_dwordx4 v22, s[36:39], s85 offen offset:3072 lds\n"
"v_mfma_f32_16x16x32_bf16 v[136:139], a[208:211], v[88:91], v[136:139]\n"
"ds_read_b64_tr_b16 v[74:75], v12 offset:20480\n"
"ds_read_b64_tr_b16 v[76:77], v12 offset:24576\n"
"v_mfma_f32_16x16x32_bf16 v[136:139], a[200:203], v[84:87], v[136:139]\n"
"buffer_load_short_d16_hi v208, v34, s[12:15], s86 offen offset:0\n"
"v_mfma_f32_16x16x32_bf16 v[136:139], a[192:195], v[80:83], v[136:139]\n"
"s_waitcnt lgkmcnt(15)\n"
"ds_read_b64_tr_b16 v[78:79], v12 offset:28672\n"
"ds_read_b64_tr_b16 v[80:81], v13 offset:0\n"
"v_mfma_f32_16x16x32_bf16 v[144:147], a[192:195], v[96:99], v[144:147]\n"
"buffer_load_short_d16_hi v209, v34, s[12:15], s86 offen offset:1024\n"
"s_waitcnt lgkmcnt(15)\n"
"v_mfma_f32_16x16x32_bf16 v[144:147], a[200:203], v[100:103], v[144:147]\n"
"s_waitcnt lgkmcnt(15)\n"
"ds_read_b64_tr_b16 v[82:83], v13 offset:4096\n"
"ds_read_b64_tr_b16 v[84:85], v13 offset:8192\n"
"v_mfma_f32_16x16x32_bf16 v[144:147], a[208:211], v[104:107], v[144:147]\n"
"s_waitcnt lgkmcnt(15)\n"
"ds_read_b64_tr_b16 v[86:87], v13 offset:12288\n"
"ds_read_b64_tr_b16 v[88:89], v13 offset:16384\n"
"v_mfma_f32_16x16x32_bf16 v[144:147], a[216:219], v[108:111], v[144:147]\n"
"buffer_load_short_d16_hi v210, v34, s[12:15], s86 offen offset:2048\n"
"v_mfma_f32_16x16x32_bf16 v[148:151], a[220:223], v[108:111], v[148:151]\n"
"ds_read_b64_tr_b16 v[90:91], v13 offset:20480\n"
"ds_read_b64_tr_b16 v[92:93], v13 offset:24576\n"
"v_mfma_f32_16x16x32_bf16 v[148:151], a[212:215], v[104:107], v[148:151]\n"
"buffer_load_short_d16_hi v211, v34, s[12:15], s86 offen offset:3072\n"
"v_mfma_f32_16x16x32_bf16 v[148:151], a[204:207], v[100:103], v[148:151]\n"
"ds_read_b64_tr_b16 v[94:95], v13 offset:28672\n"
"v_mfma_f32_16x16x32_bf16 v[148:151], a[196:199], v[96:99], v[148:151]\n"
"buffer_load_short_d16_hi v212, v34, s[12:15], s87 offen offset:0\n"
"s_waitcnt lgkmcnt(15)\n"
"v_mfma_f32_16x16x32_bf16 v[156:159], a[196:199], v[112:115], v[156:159]\n"
"s_waitcnt lgkmcnt(15)\n"
"ds_read_b64_tr_b16 v[96:97], v14 offset:0\n"
"ds_read_b64_tr_b16 v[98:99], v14 offset:4096\n"
"v_mfma_f32_16x16x32_bf16 v[156:159], a[204:207], v[116:119], v[156:159]\n"
"buffer_load_short_d16_hi v213, v34, s[12:15], s87 offen offset:1024\n"
"s_waitcnt lgkmcnt(15)\n"
"v_mfma_f32_16x16x32_bf16 v[156:159], a[212:215], v[120:123], v[156:159]\n"
"s_waitcnt lgkmcnt(15)\n"
"ds_read_b64_tr_b16 v[100:101], v14 offset:8192\n"
"ds_read_b64_tr_b16 v[102:103], v14 offset:12288\n"
"v_mfma_f32_16x16x32_bf16 v[156:159], a[220:223], v[124:127], v[156:159]\n"
"ds_read_b64_tr_b16 v[104:105], v14 offset:16384\n"
"ds_read_b64_tr_b16 v[106:107], v14 offset:20480\n"
"v_mfma_f32_16x16x32_bf16 v[152:155], a[216:219], v[124:127], v[152:155]\n"
"buffer_load_short_d16_hi v214, v34, s[12:15], s87 offen offset:2048\n"
"v_mfma_f32_16x16x32_bf16 v[152:155], a[208:211], v[120:123], v[152:155]\n"
"ds_read_b64_tr_b16 v[108:109], v14 offset:24576\n"
"ds_read_b64_tr_b16 v[110:111], v14 offset:28672\n"
"v_mfma_f32_16x16x32_bf16 v[152:155], a[200:203], v[116:119], v[152:155]\n"
"buffer_load_short_d16_hi v215, v34, s[12:15], s87 offen offset:3072\n"
"v_mfma_f32_16x16x32_bf16 v[152:155], a[192:195], v[112:115], v[152:155]\n"
"s_waitcnt lgkmcnt(15)\n"
"ds_read_b64_tr_b16 v[112:113], v15 offset:0\n"
"ds_read_b64_tr_b16 v[114:115], v15 offset:4096\n"
"v_mfma_f32_16x16x32_bf16 v[160:163], a[192:195], v[64:67], v[160:163]\n"
"buffer_load_short_d16_hi v216, v34, s[12:15], s86 offen offset:512\n"
"s_waitcnt lgkmcnt(15)\n"
"v_mfma_f32_16x16x32_bf16 v[160:163], a[200:203], v[68:71], v[160:163]\n"
"s_waitcnt lgkmcnt(15)\n"
"ds_read_b64_tr_b16 v[116:117], v15 offset:8192\n"
"ds_read_b64_tr_b16 v[118:119], v15 offset:12288\n"
"v_mfma_f32_16x16x32_bf16 v[160:163], a[208:211], v[72:75], v[160:163]\n"
"buffer_load_short_d16_hi v217, v34, s[12:15], s86 offen offset:1536\n"
"s_waitcnt lgkmcnt(15)\n"
"v_mfma_f32_16x16x32_bf16 v[160:163], a[216:219], v[76:79], v[160:163]\n"
"ds_read_b64_tr_b16 v[120:121], v15 offset:16384\n"
"ds_read_b64_tr_b16 v[122:123], v15 offset:20480\n"
"v_mfma_f32_16x16x32_bf16 v[164:167], a[220:223], v[76:79], v[164:167]\n"
"ds_read_b64_tr_b16 v[124:125], v15 offset:24576\n"
"ds_read_b64_tr_b16 v[126:127], v15 offset:28672\n"
"v_mfma_f32_16x16x32_bf16 v[164:167], a[212:215], v[72:75], v[164:167]\n"
"v_mfma_f32_16x16x32_bf16 v[164:167], a[204:207], v[68:71], v[164:167]\n"
"v_mfma_f32_16x16x32_bf16 v[164:167], a[196:199], v[64:67], v[164:167]\n"
"s_waitcnt vmcnt(22) lgkmcnt(0)\n"
"s_barrier\n"
"v_mfma_f32_16x16x32_bf16 v[172:175], a[196:199], v[80:83], v[172:175]\n"
"s_add_u32 m0, s91, 69632\n"
"s_add_u32 s85, s75, 114688\n"
"v_add_u32 v8, 65536, v8\n"
"v_mfma_f32_16x16x32_bf16 v[172:175], a[204:207], v[84:87], v[172:175]\n"
"ds_read_b64_tr_b16 v[64:65], v8 offset:32768\n"
"ds_read_b64_tr_b16 v[66:67], v8 offset:36864\n"
"v_mfma_f32_16x16x32_bf16 v[172:175], a[212:215], v[88:91], v[172:175]\n"
"ds_read_b64_tr_b16 v[68:69], v8 offset:40960\n"
"ds_read_b64_tr_b16 v[70:71], v8 offset:45056\n"
"v_mfma_f32_16x16x32_bf16 v[172:175], a[220:223], v[92:95], v[172:175]\n"
"ds_read_b64_tr_b16 v[72:73], v8 offset:49152\n"
"ds_read_b64_tr_b16 v[74:75], v8 offset:53248\n"
"v_mfma_f32_16x16x32_bf16 v[168:171], a[216:219], v[92:95], v[168:171]\n"
"ds_read_b64_tr_b16 v[76:77], v8 offset:57344\n"
"ds_read_b64_tr_b16 v[78:79], v8 offset:61440\n"
"v_mfma_f32_16x16x32_bf16 v[168:171], a[208:211], v[88:91], v[168:171]\n"
"buffer_load_short_d16_hi v218, v34, s[12:15], s86 offen offset:2560\n"
"v_mfma_f32_16x16x32_bf16 v[168:171], a[200:203], v[84:87], v[168:171]\n"
"buffer_load_short_d16_hi v219, v34, s[12:15], s86 offen offset:3584\n"
"v_mfma_f32_16x16x32_bf16 v[168:171], a[192:195], v[80:83], v[168:171]\n"
"buffer_load_short_d16_hi v220, v34, s[12:15], s87 offen offset:512\n"
"v_mfma_f32_16x16x32_bf16 v[176:179], a[192:195], v[96:99], v[176:179]\n"
"buffer_load_short_d16_hi v221, v34, s[12:15], s87 offen offset:1536\n"
"v_mfma_f32_16x16x32_bf16 v[176:179], a[200:203], v[100:103], v[176:179]\n"
"buffer_load_short_d16_hi v222, v34, s[12:15], s87 offen offset:2560\n"
"v_mfma_f32_16x16x32_bf16 v[176:179], a[208:211], v[104:107], v[176:179]\n"
"v_add_u32 v9, 65536, v9\n"
"v_add_u32 v10, 65536, v10\n"
"v_mfma_f32_16x16x32_bf16 v[176:179], a[216:219], v[108:111], v[176:179]\n"
"ds_read_b64_tr_b16 v[80:81], v9 offset:32768\n"
"ds_read_b64_tr_b16 v[82:83], v9 offset:36864\n"
"v_mfma_f32_16x16x32_bf16 v[180:183], a[220:223], v[108:111], v[180:183]\n"
"buffer_load_short_d16_hi v223, v34, s[12:15], s87 offen offset:3584\n"
"v_mfma_f32_16x16x32_bf16 v[180:183], a[212:215], v[104:107], v[180:183]\n"
"ds_read_b64_tr_b16 v[84:85], v9 offset:40960\n"
"ds_read_b64_tr_b16 v[86:87], v9 offset:45056\n"
"v_mfma_f32_16x16x32_bf16 v[180:183], a[204:207], v[100:103], v[180:183]\n"
"buffer_load_dwordx4 v17, s[36:39], s85 offen offset:0 lds\n"
"v_mfma_f32_16x16x32_bf16 v[180:183], a[196:199], v[96:99], v[180:183]\n"
"s_nop 0\n"
"PG16_%=:\n"
"s_cbranch_execz PADRET_%=\n"
"ds_read_b64_tr_b16 v[88:89], v9 offset:49152\n"
"ds_read_b64_tr_b16 v[90:91], v9 offset:53248\n"
"v_mfma_f32_16x16x32_bf16 v[188:191], a[196:199], v[112:115], v[188:191]\n"
"buffer_load_dwordx4 v18, s[36:39], s85 offen offset:1024 lds\n"
"v_mfma_f32_16x16x32_bf16 v[188:191], a[204:207], v[116:119], v[188:191]\n"
"ds_read_b64_tr_b16 v[92:93], v9 offset:57344\n"
"ds_read_b64_tr_b16 v[94:95], v9 offset:61440\n"
"v_mfma_f32_16x16x32_bf16 v[188:191], a[212:215], v[120:123], v[188:191]\n"
"buffer_load_dwordx4 v19, s[36:39], s85 offen offset:2048 lds\n"
"v_mfma_f32_16x16x32_bf16 v[188:191], a[220:223], v[124:127], v[188:191]\n"
"ds_read_b64_tr_b16 v[96:97], v10 offset:32768\n"
"ds_read_b64_tr_b16 v[98:99], v10 offset:36864\n"
"v_mfma_f32_16x16x32_bf16 v[184:187], a[216:219], v[124:127], v[184:187]\n"
"buffer_load_dwordx4 v22, s[36:39], s85 offen offset:3072 lds\n"
"v_mfma_f32_16x16x32_bf16 v[184:187], a[208:211], v[120:123], v[184:187]\n"
"ds_read_b64_tr_b16 v[100:101], v10 offset:40960\n"
"ds_read_b64_tr_b16 v[102:103], v10 offset:45056\n"
"v_mfma_f32_16x16x32_bf16 v[184:187], a[200:203], v[116:119], v[184:187]\n"
"ds_read_b64_tr_b16 v[104:105], v10 offset:49152\n"
"ds_read_b64_tr_b16 v[106:107], v10 offset:53248\n"
"v_mfma_f32_16x16x32_bf16 v[184:187], a[192:195], v[112:115], v[184:187]\n"
"ds_read_b64_tr_b16 v[108:109], v10 offset:57344\n"
"ds_read_b64_tr_b16 v[110:111], v10 offset:61440\n"
"s_waitcnt vmcnt(24)\n"
"s_waitcnt lgkmcnt(15)\n"
"v_mfma_f32_16x16x32_bf16 v[128:131], a[224:227], v[64:67], v[128:131]\n"
"s_waitcnt lgkmcnt(15)\n"
"v_add_u32 v11, 65536, v11\n"
"v_add_u32 v12, 65536, v12\n"
"v_mfma_f32_16x16x32_bf16 v[128:131], a[232:235], v[68:71], v[128:131]\n"
"s_waitcnt lgkmcnt(15)\n"
"ds_read_b64_tr_b16 v[112:113], v11 offset:32768\n"
"ds_read_b64_tr_b16 v[114:115], v11 offset:36864\n"
"v_mfma_f32_16x16x32_bf16 v[128:131], a[240:243], v[72:75], v[128:131]\n"
"s_waitcnt lgkmcnt(15)\n"
"ds_read_b64_tr_b16 v[116:117], v11 offset:40960\n"
"ds_read_b64_tr_b16 v[118:119], v11 offset:45056\n"
"v_mfma_f32_16x16x32_bf16 v[128:131], a[248:251], v[76:79], v[128:131]\n"
"ds_read_b64_tr_b16 v[120:121], v11 offset:49152\n"
"ds_read_b64_tr_b16 v[122:123], v11 offset:53248\n"
"v_mfma_f32_16x16x32_bf16 v[132:135], a[252:255], v[76:79], v[132:135]\n"
"ds_read_b64_tr_b16 v[124:125], v11 offset:57344\n"
"ds_read_b64_tr_b16 v[126:127], v11 offset:61440\n"
"v_mfma_f32_16x16x32_bf16 v[132:135], a[244:247], v[72:75], v[132:135]\n"
"v_add_u32 v13, 65536, v13\n"
"v_add_u32 v14, 65536, v14\n"
"v_mfma_f32_16x16x32_bf16 v[132:135], a[236:239], v[68:71], v[132:135]\n"
"v_add_u32 v15, 65536, v15\n"
"v_mfma_f32_16x16x32_bf16 v[132:135], a[228:231], v[64:67], v[132:135]\n"
"s_waitcnt lgkmcnt(15)\n"
"ds_read_b64_tr_b16 v[64:65], v12 offset:32768\n"
"ds_read_b64_tr_b16 v[66:67], v12 offset:36864\n"
"v_mfma_f32_16x16x32_bf16 v[140:143], a[228:231], v[80:83], v[140:143]\n"
"s_waitcnt lgkmcnt(15)\n"
"ds_read_b64_tr_b16 v[68:69], v12 offset:40960\n"
"ds_read_b64_tr_b16 v[70:71], v12 offset:45056\n"
"v_mfma_f32_16x16x32_bf16 v[140:143], a[236:239], v[84:87], v[140:143]\n"
"s_waitcnt lgkmcnt(15)\n"
"ds_read_b64_tr_b16 v[72:73], v12 offset:49152\n"
"ds_read_b64_tr_b16 v[74:75], v12 offset:53248\n"
"v_mfma_f32_16x16x32_bf16 v[140:143], a[244:247], v[88:91], v[140:143]\n"
"s_waitcnt lgkmcnt(15)\n"
"ds_read_b64_tr_b16 v[76:77], v12 offset:57344\n"
"ds_read_b64_tr_b16 v[78:79], v12 offset:61440\n"
"v_mfma_f32_16x16x32_bf16 v[140:143], a[252:255], v[92:95], v[140:143]\n"
"v_cvt_pk_bf16_f32 v128, v128, v129\n"
"v_cvt_pk_bf16_f32 v129, v130, v131\n"
"v_mfma_f32_16x16x32_bf16 v[136:139], a[248:251], v[92:95], v[136:139]\n"
"v_cvt_pk_bf16_f32 v130, v132, v133\n"
"v_cvt_pk_bf16_f32 v131, v134, v135\n"
"v_mfma_f32_16x16x32_bf16 v[136:139], a[240:243], v[88:91], v[136:139]\n"
"buffer_store_dwordx4 v[128:131], v36, s[44:47], s90 offen offset:0 sc1\n"
"v_mfma_f32_16x16x32_bf16 v[136:139], a[232:235], v[84:87], v[136:139]\n"
"v_mfma_f32_16x16x32_bf16 v[136:139], a[224:227], v[80:83], v[136:139]\n"
"s_waitcnt lgkmcnt(15)\n"
"ds_read_b64_tr_b16 v[80:81], v13 offset:32768\n"
"ds_read_b64_tr_b16 v[82:83], v13 offset:36864\n"
"v_mfma_f32_16x16x32_bf16 v[144:147], a[224:227], v[96:99], v[144:147]\n"
"s_waitcnt lgkmcnt(15)\n"
"ds_read_b64_tr_b16 v[84:85], v13 offset:40960\n"
"ds_read_b64_tr_b16 v[86:87], v13 offset:45056\n"
"v_mfma_f32_16x16x32_bf16 v[144:147], a[232:235], v[100:103], v[144:147]\n"
"s_waitcnt lgkmcnt(15)\n"
"ds_read_b64_tr_b16 v[88:89], v13 offset:49152\n"
"ds_read_b64_tr_b16 v[90:91], v13 offset:53248\n"
"v_mfma_f32_16x16x32_bf16 v[144:147], a[240:243], v[104:107], v[144:147]\n"
"s_waitcnt lgkmcnt(15)\n"
"ds_read_b64_tr_b16 v[92:93], v13 offset:57344\n"
"ds_read_b64_tr_b16 v[94:95], v13 offset:61440\n"
"v_mfma_f32_16x16x32_bf16 v[144:147], a[248:251], v[108:111], v[144:147]\n"
"v_cvt_pk_bf16_f32 v136, v136, v137\n"
"v_cvt_pk_bf16_f32 v137, v138, v139\n"
"v_mfma_f32_16x16x32_bf16 v[148:151], a[252:255], v[108:111], v[148:151]\n"
"v_cvt_pk_bf16_f32 v138, v140, v141\n"
"v_cvt_pk_bf16_f32 v139, v142, v143\n"
"v_mfma_f32_16x16x32_bf16 v[148:151], a[244:247], v[104:107], v[148:151]\n"
"buffer_store_dwordx4 v[136:139], v36, s[44:47], s90 offen offset:256 sc1\n"
"v_mfma_f32_16x16x32_bf16 v[148:151], a[236:239], v[100:103], v[148:151]\n"
"v_mfma_f32_16x16x32_bf16 v[148:151], a[228:231], v[96:99], v[148:151]\n"
"s_waitcnt lgkmcnt(15)\n"
"v_cvt_pk_bf16_f32 v144, v144, v145\n"
"v_cvt_pk_bf16_f32 v145, v146, v147\n"
"v_mfma_f32_16x16x32_bf16 v[156:159], a[228:231], v[112:115], v[156:159]\n"
"s_waitcnt lgkmcnt(15)\n"
"ds_read_b64_tr_b16 v[96:97], v14 offset:32768\n"
"ds_read_b64_tr_b16 v[98:99], v14 offset:36864\n"
"v_mfma_f32_16x16x32_bf16 v[156:159], a[236:239], v[116:119], v[156:159]\n"
"s_waitcnt lgkmcnt(15)\n"
"ds_read_b64_tr_b16 v[100:101], v14 offset:40960\n"
"ds_read_b64_tr_b16 v[102:103], v14 offset:45056\n"
"v_mfma_f32_16x16x32_bf16 v[156:159], a[244:247], v[120:123], v[156:159]\n"
"s_waitcnt lgkmcnt(15)\n"
"ds_read_b64_tr_b16 v[104:105], v14 offset:49152\n"
"ds_read_b64_tr_b16 v[106:107], v14 offset:53248\n"
"v_mfma_f32_16x16x32_bf16 v[156:159], a[252:255], v[124:127], v[156:159]\n"
"v_cvt_pk_bf16_f32 v146, v148, v149\n"
"v_cvt_pk_bf16_f32 v147, v150, v151\n"
"v_mfma_f32_16x16x32_bf16 v[152:155], a[248:251], v[124:127], v[152:155]\n"
"buffer_store_dwordx4 v[144:147], v36, s[44:47], s90 offen offset:512 sc1\n"
"v_mfma_f32_16x16x32_bf16 v[152:155], a[240:243], v[120:123], v[152:155]\n"
"ds_read_b64_tr_b16 v[108:109], v14 offset:57344\n"
"ds_read_b64_tr_b16 v[110:111], v14 offset:61440\n"
"v_mfma_f32_16x16x32_bf16 v[152:155], a[232:235], v[116:119], v[152:155]\n"
"v_mfma_f32_16x16x32_bf16 v[152:155], a[224:227], v[112:115], v[152:155]\n"
"s_waitcnt lgkmcnt(15)\n"
"ds_read_b64_tr_b16 v[112:113], v15 offset:32768\n"
"ds_read_b64_tr_b16 v[114:115], v15 offset:36864\n"
"v_mfma_f32_16x16x32_bf16 v[160:163], a[224:227], v[64:67], v[160:163]\n"
"s_waitcnt lgkmcnt(15)\n"
"ds_read_b64_tr_b16 v[116:117], v15 offset:40960\n"
"ds_read_b64_tr_b16 v[118:119], v15 offset:45056\n"
"v_mfma_f32_16x16x32_bf16 v[160:163], a[232:235], v[68:71], v[160:163]\n"
"s_waitcnt lgkmcnt(15)\n"
"ds_read_b64_tr_b16 v[120:121], v15 offset:49152\n"
"ds_read_b64_tr_b16 v[122:123], v15 offset:53248\n"
"v_mfma_f32_16x16x32_bf16 v[160:163], a[240:243], v[72:75], v[160:163]\n"
"s_waitcnt lgkmcnt(15)\n"
"ds_read_b64_tr_b16 v[124:125], v15 offset:57344\n"
"ds_read_b64_tr_b16 v[126:127], v15 offset:61440\n"
"v_mfma_f32_16x16x32_bf16 v[160:163], a[248:251], v[76:79], v[160:163]\n"
"v_cvt_pk_bf16_f32 v152, v152, v153\n"
"v_cvt_pk_bf16_f32 v153, v154, v155\n"
"v_mfma_f32_16x16x32_bf16 v[164:167], a[252:255], v[76:79], v[164:167]\n"
"v_cvt_pk_bf16_f32 v154, v156, v157\n"
"v_cvt_pk_bf16_f32 v155, v158, v159\n"
"v_mfma_f32_16x16x32_bf16 v[164:167], a[244:247], v[72:75], v[164:167]\n"
"buffer_store_dwordx4 v[152:155], v36, s[44:47], s90 offen offset:768 sc1\n"
"v_mfma_f32_16x16x32_bf16 v[164:167], a[236:239], v[68:71], v[164:167]\n"
"v_mfma_f32_16x16x32_bf16 v[164:167], a[228:231], v[64:67], v[164:167]\n"
"s_waitcnt lgkmcnt(0)\n"
"s_barrier\n"
"v_mfma_f32_16x16x32_bf16 v[172:175], a[228:231], v[80:83], v[172:175]\n"
"v_cvt_pk_bf16_f32 v160, v160, v161\n"
"v_cvt_pk_bf16_f32 v161, v162, v163\n"
"v_mfma_f32_16x16x32_bf16 v[172:175], a[236:239], v[84:87], v[172:175]\n"
"s_nop 1\n"
"v_cvt_pk_bf16_f32 v162, v164, v165\n"
"v_cvt_pk_bf16_f32 v163, v166, v167\n"
"v_mfma_f32_16x16x32_bf16 v[172:175], a[244:247], v[88:91], v[172:175]\n"
"buffer_store_dwordx4 v[160:163], v36, s[44:47], s90 offen offset:1024 sc1\n"
"v_mfma_f32_16x16x32_bf16 v[172:175], a[252:255], v[92:95], v[172:175]\n"
"v_mfma_f32_16x16x32_bf16 v[168:171], a[248:251], v[92:95], v[168:171]\n"
"v_mfma_f32_16x16x32_bf16 v[168:171], a[240:243], v[88:91], v[168:171]\n"
"v_mfma_f32_16x16x32_bf16 v[168:171], a[232:235], v[84:87], v[168:171]\n"
"v_mfma_f32_16x16x32_bf16 v[168:171], a[224:227], v[80:83], v[168:171]\n"
"v_mfma_f32_16x16x32_bf16 v[176:179], a[224:227], v[96:99], v[176:179]\n"
"v_mfma_f32_16x16x32_bf16 v[176:179], a[232:235], v[100:103], v[176:179]\n"
"v_mfma_f32_16x16x32_bf16 v[176:179], a[240:243], v[104:107], v[176:179]\n"
"v_mfma_f32_16x16x32_bf16 v[176:179], a[248:251], v[108:111], v[176:179]\n"
"s_nop 3\n"
"v_cvt_pk_bf16_f32 v168, v168, v169\n"
"v_cvt_pk_bf16_f32 v169, v170, v171\n"
"v_mfma_f32_16x16x32_bf16 v[180:183], a[252:255], v[108:111], v[180:183]\n"
"v_cvt_pk_bf16_f32 v170, v172, v173\n"
"v_cvt_pk_bf16_f32 v171, v174, v175\n"
"v_mfma_f32_16x16x32_bf16 v[180:183], a[244:247], v[104:107], v[180:183]\n"
"buffer_store_dwordx4 v[168:171], v36, s[44:47], s90 offen offset:1280 sc1\n"
"v_mfma_f32_16x16x32_bf16 v[180:183], a[236:239], v[100:103], v[180:183]\n"
"v_mfma_f32_16x16x32_bf16 v[180:183], a[228:231], v[96:99], v[180:183]\n"
"v_cvt_pk_bf16_f32 v176, v176, v177\n"
"v_cvt_pk_bf16_f32 v177, v178, v179\n"
"v_mfma_f32_16x16x32_bf16 v[188:191], a[228:231], v[112:115], v[188:191]\n"
"v_mfma_f32_16x16x32_bf16 v[188:191], a[236:239], v[116:119], v[188:191]\n"
"v_mfma_f32_16x16x32_bf16 v[188:191], a[244:247], v[120:123], v[188:191]\n"
"v_mfma_f32_16x16x32_bf16 v[188:191], a[252:255], v[124:127], v[188:191]\n"
"s_nop 1\n"
"v_cvt_pk_bf16_f32 v178, v180, v181\n"
"v_cvt_pk_bf16_f32 v179, v182, v183\n"
"v_mfma_f32_16x16x32_bf16 v[184:187], a[248:251], v[124:127], v[184:187]\n"
"buffer_store_dwordx4 v[176:179], v36, s[44:47], s90 offen offset:1536 sc1\n"
"v_mfma_f32_16x16x32_bf16 v[184:187], a[240:243], v[120:123], v[184:187]\n"
"v_mfma_f32_16x16x32_bf16 v[184:187], a[232:235], v[116:119], v[184:187]\n"
"v_mfma_f32_16x16x32_bf16 v[184:187], a[224:227], v[112:115], v[184:187]\n"
"s_nop 7\n"
"v_cvt_pk_bf16_f32 v184, v184, v185\n"
"v_cvt_pk_bf16_f32 v185, v186, v187\n"
"v_cvt_pk_bf16_f32 v186, v188, v189\n"
"v_cvt_pk_bf16_f32 v187, v190, v191\n"
"buffer_store_dwordx4 v[184:187], v36, s[44:47], s90 offen offset:1792 sc1\n"
"v_lshrrev_b32 v34, 2, v0\n"
"v_bfe_u32 v35, v0, 3, 2\n"
"v_lshlrev_b32 v34, 7, v34\n"
"v_lshl_add_u32 v34, v35, 5, v34\n"
"v_and_b32 v35, 3, v0\n"
"v_lshl_add_u32 v34, v35, 3, v34\n"
"v_add_u32 v30, s91, v34\n"
"v_xor_b32 v4, 0, v30\n"
"v_add_u32 v8, 65536, v4\n"
"v_add_u32 v12, 131072, v4\n"
"v_xor_b32 v5, 32, v30\n"
"v_add_u32 v9, 65536, v5\n"
"v_add_u32 v13, 131072, v5\n"
"v_xor_b32 v6, 64, v30\n"
"v_add_u32 v10, 65536, v6\n"
"v_add_u32 v14, 131072, v6\n"
"v_xor_b32 v7, 96, v30\n"
"v_add_u32 v11, 65536, v7\n"
"v_add_u32 v15, 131072, v7\n"
"s_add_u32 m0, s91, 131072\n"
"s_add_u32 s85, s75, 229376\n"
"buffer_load_dwordx4 v17, s[36:39], s85 offen offset:0 lds\n"
"buffer_load_dwordx4 v18, s[36:39], s85 offen offset:1024 lds\n"
"buffer_load_dwordx4 v19, s[36:39], s85 offen offset:2048 lds\n"
"buffer_load_dwordx4 v22, s[36:39], s85 offen offset:3072 lds\n"
"s_add_u32 m0, s91, 135168\n"
"s_add_u32 s85, s75, 344064\n"
"buffer_load_dwordx4 v17, s[36:39], s85 offen offset:0 lds\n"
"buffer_load_dwordx4 v18, s[36:39], s85 offen offset:1024 lds\n"
"buffer_load_dwordx4 v19, s[36:39], s85 offen offset:2048 lds\n"
"buffer_load_dwordx4 v22, s[36:39], s85 offen offset:3072 lds\n"
"s_waitcnt vmcnt(16)\n"
"ds_read_b64_tr_b16 v[48:49], v8 offset:0\n"
"ds_read_b64_tr_b16 v[50:51], v8 offset:2048\n"
"ds_read_b64_tr_b16 v[52:53], v9 offset:0\n"
"ds_read_b64_tr_b16 v[54:55], v9 offset:2048\n"
"ds_read_b64_tr_b16 v[56:57], v10 offset:0\n"
"ds_read_b64_tr_b16 v[58:59], v10 offset:2048\n"
"ds_read_b64_tr_b16 v[60:61], v11 offset:0\n"
"ds_read_b64_tr_b16 v[62:63], v11 offset:2048\n"
"ds_read_b64_tr_b16 v[64:65], v8 offset:4096\n"
"ds_read_b64_tr_b16 v[66:67], v8 offset:6144\n"
"ds_read_b64_tr_b16 v[68:69], v9 offset:4096\n"
"ds_read_b64_tr_b16 v[70:71], v9 offset:6144\n"
"ds_read_b64_tr_b16 v[72:73], v10 offset:4096\n"
"ds_read_b64_tr_b16 v[74:75], v10 offset:6144\n"
"ds_read_b64_tr_b16 v[76:77], v11 offset:4096\n"
"ds_read_b64_tr_b16 v[78:79], v11 offset:6144\n"
"s_and_b32 s73, s60, 7\n"
"s_lshl_b32 s73, s73, 2\n"
"s_lshr_b32 s86, s60, 6\n"
"s_add_u32 s73, s73, s86\n"
"s_bfe_u32 s74, s60, 0x30003\n"
"s_mul_i32 s72, s61, 1835008\n"
"s_lshl_b32 s86, s73, 7\n"
"s_add_u32 s72, s72, s86\n"
"s_and_b32 s86, s73, 0\n"
"s_mul_i32 s86, s86, 917504\n"
"s_sub_u32 s93, s72, s86\n"
"s_add_u32 s72, s72, s86\n"
"s_lshl_b32 s89, s77, 18\n"
"s_lshl_b32 s86, s61, 14\n"
"s_add_u32 s89, s89, s86\n"
"s_add_u32 s89, s89, 62914560\n"
"s_and_b32 s86, s78, 0\n"
"s_mul_i32 s86, s86, 8192\n"
"s_sub_u32 s94, s89, s86\n"
"s_add_u32 s89, s89, s86\n"
"v_lshrrev_b32 v34, 3, v0\n"
"v_lshlrev_b32 v35, 1, v3\n"
"v_xor_b32 v35, v35, v0\n"
"v_and_b32 v35, 7, v35\n"
"v_lshlrev_b32 v35, 4, v35\n"
"v_mul_u32_u24 v34, 4096, v34\n"
"v_add_u32 v34, v34, v35\n"
"v_add_u32 v44, 0, v34\n"
"v_add_u32 v45, 31744, v34\n"
"v_add_u32 v46, 63488, v34\n"
"v_add_u32 v47, 95232, v34\n"
"s_waitcnt vmcnt(8) lgkmcnt(0)\n"
"s_lshl_b32 s99, s81, 7\n"
"s_add_u32 s99, s99, 262144\n"
"v_mov_b32 v28, s64\n"
"v_mov_b32 v29, s65\n"
"s_mov_b64 exec, 1\n"
"buffer_store_dwordx2 v[28:29], off, s[44:47], s99 sc1\n"
"s_mov_b64 exec, -1\n"
"s_add_u32 m0, s91, 32768\n"
"s_add_u32 s85, s75, 458752\n"
"buffer_load_dwordx4 v17, s[36:39], s85 offen offset:0 lds\n"
"buffer_load_dwordx4 v18, s[36:39], s85 offen offset:1024 lds\n"
"buffer_load_dwordx4 v19, s[36:39], s85 offen offset:2048 lds\n"
"buffer_load_dwordx4 v22, s[36:39], s85 offen offset:3072 lds\n"
"s_add_u32 m0, s91, 36864\n"
"s_add_u32 s85, s75, 573440\n"
"buffer_load_dwordx4 v17, s[36:39], s85 offen offset:0 lds\n"
"buffer_load_dwordx4 v18, s[36:39], s85 offen offset:1024 lds\n"
"buffer_load_dwordx4 v19, s[36:39], s85 offen offset:2048 lds\n"
"buffer_load_dwordx4 v22, s[36:39], s85 offen offset:3072 lds\n"
"s_add_u32 m0, s91, 0\n"
"s_add_u32 s85, s75, 688128\n"
"buffer_load_dwordx4 v17, s[36:39], s85 offen offset:0 lds\n"
"buffer_load_dwordx4 v18, s[36:39], s85 offen offset:1024 lds\n"
"buffer_load_dwordx4 v19, s[36:39], s85 offen offset:2048 lds\n"
"buffer_load_dwordx4 v22, s[36:39], s85 offen offset:3072 lds\n"
"s_add_u32 m0, s91, 4096\n"
"s_add_u32 s85, s75, 802816\n"
"buffer_load_dwordx4 v17, s[36:39], s85 offen offset:0 lds\n"
"buffer_load_dwordx4 v18, s[36:39], s85 offen offset:1024 lds\n"
"buffer_load_dwordx4 v19, s[36:39], s85 offen offset:2048 lds\n"
"buffer_load_dwordx4 v22, s[36:39], s85 offen offset:3072 lds\n"
"s_lshl_b32 s86, s78, 7\n"
"v_lshlrev_b32 v40, 3, v3\n"
"buffer_load_dwordx2 v[32:33], v40, s[52:55], s86 offen offset:0\n"
"buffer_load_dwordx2 v[34:35], v40, s[52:55], s86 offen offset:32\n"
"buffer_load_dwordx2 v[36:37], v40, s[52:55], s86 offen offset:64\n"
"buffer_load_dwordx2 v[38:39], v40, s[52:55], s86 offen offset:96\n"
"s_lshl_b32 s86, s61, 8\n"
"s_and_b32 s87, s77, 3\n"
"s_add_u32 s86, s86, s87\n"
"s_lshl_b32 s86, s86, 7\n"
"s_add_u32 s86, s86, 393216\n"
"v_lshl_add_u32 v31, v0, 9, s86\n"
"s_lshl_b32 s86, s61, 6\n"
"s_add_u32 s86, s86, 102400\n"
"v_lshl_add_u32 v41, v3, 4, s86\n"
"v_lshlrev_b32 v42, 2, v0\n"
"v_add_u32 v42, 102400, v42\n"
"s_lshl_b32 s86, s61, 18\n"
"s_lshl_b32 s87, s60, 8\n"
"s_add_u32 s86, s86, s87\n"
"v_lshlrev_b32 v43, 16, v3\n"
"v_lshl_add_u32 v43, v2, 4, v43\n"
"v_add_u32 v43, s86, v43\n"
"s_xor_b32 s86, s61, 0\n"
"s_lshl_b32 s86, s86, 16\n"
"v_add_u32 v24, s86, v1\n"
"s_xor_b32 s86, s61, 1\n"
"s_lshl_b32 s86, s86, 16\n"
"v_add_u32 v25, s86, v1\n"
"s_xor_b32 s86, s61, 2\n"
"s_lshl_b32 s86, s86, 16\n"
"v_add_u32 v26, s86, v1\n"
"s_xor_b32 s86, s61, 3\n"
"s_lshl_b32 s86, s86, 16\n"
"v_add_u32 v27, s86, v1\n"
"s_lshl_b32 s86, s60, 15\n"
"s_add_u32 s86, s86, 54525952\n"
"s_getpc_b64 s[8:9]\n"
"WM_44_%=:\n"
"s_add_u32 s10, s8, WM_44_%=r-WM_44_%=\n"
"s_addc_u32 s11, s9, 0\n"
"s_mov_b32 s98, PG17_%=-WM_44_%=\n"
"s_mov_b32 s99, PG19_%=-WM_44_%=\n"
"s_cmp_eq_u32 s67, 0\n"
"s_cselect_b32 s98, s98, PG18_%=-WM_44_%=\n"
"s_cselect_b32 s99, s99, IDLE_%=-WM_44_%=\n"
"s_cmp_eq_u32 s66, 0\n"
"s_cselect_b32 s98, s98, s99\n"
"s_ashr_i32 s99, s98, 31\n"
"s_add_u32 s8, s8, s98\n"
"s_addc_u32 s9, s9, s99\n"
"s_mov_b64 exec, 0\n"
"s_setpc_b64 s[8:9]\n"
"WM_44_%=r:\n"
"s_mov_b64 exec, -1\n"
"buffer_load_dwordx2 v[20:21], v16, s[44:47], 0 offen sc1\n"
"s_waitcnt vmcnt(0)\n"
"v_cmp_ne_u64 vcc, s[64:65], v[20:21]\n"
"s_nop 1\n"
"s_cbranch_vccz FOK_45_%=\n"
"FSP_46_%=:\n"
"s_cmp_eq_u32 s69, 0\n"
"s_cbranch_scc1 FOK_45_%=\n"
"s_sub_u32 s69, s69, 1\n"
"s_sleep 2\n"
"s_mov_b64 exec, vcc\n"
"buffer_load_dwordx2 v[20:21], v16, s[44:47], 0 offen sc1\n"
"s_mov_b64 exec, -1\n"
"s_waitcnt vmcnt(0)\n"
"v_cmp_ne_u64 vcc, s[64:65], v[20:21]\n"
"s_nop 1\n"
"s_cbranch_vccnz FSP_46_%=\n"
"FOK_45_%=:\n"
"P5_%=:\n"
"buffer_load_dwordx4 v[160:163], v23, s[44:47], s86 offen sc1\n"
"s_add_u32 s86, s86, 4096\n"
"buffer_load_dwordx4 v[164:167], v23, s[44:47], s86 offen sc1\n"
"s_add_u32 s86, s86, 4096\n"
"buffer_load_dwordx4 v[168:171], v23, s[44:47], s86 offen sc1\n"
"s_add_u32 s86, s86, 4096\n"
"buffer_load_dwordx4 v[172:175], v23, s[44:47], s86 offen sc1\n"
"s_add_u32 s86, s86, 4096\n"
"buffer_load_dwordx4 v[176:179], v23, s[44:47], s86 offen sc1\n"
"s_add_u32 s86, s86, 4096\n"
"buffer_load_dwordx4 v[180:183], v23, s[44:47], s86 offen sc1\n"
"s_add_u32 s86, s86, 4096\n"
"buffer_load_dwordx4 v[184:187], v23, s[44:47], s86 offen sc1\n"
"s_add_u32 s86, s86, 4096\n"
"buffer_load_dwordx4 v[188:191], v23, s[44:47], s86 offen sc1\n"
"s_add_u32 s86, s86, 4096\n"
"s_add_u32 m0, s91, 98304\n"
"s_add_u32 s85, s92, 917504\n"
"buffer_load_dwordx4 v17, s[36:39], s85 offen offset:0 lds\n"
"buffer_load_dwordx4 v18, s[36:39], s85 offen offset:1024 lds\n"
"buffer_load_dwordx4 v19, s[36:39], s85 offen offset:2048 lds\n"
"buffer_load_dwordx4 v22, s[36:39], s85 offen offset:3072 lds\n"
"s_add_u32 m0, s91, 65536\n"
"s_add_u32 s85, s92, 1032192\n"
"buffer_load_dwordx4 v17, s[36:39], s85 offen offset:0 lds\n"
"buffer_load_dwordx4 v18, s[36:39], s85 offen offset:1024 lds\n"
"buffer_load_dwordx4 v19, s[36:39], s85 offen offset:2048 lds\n"
"buffer_load_dwordx4 v22, s[36:39], s85 offen offset:3072 lds\n"
"ds_read_b64_tr_b16 v[80:81], v12 offset:0\n"
"ds_read_b64_tr_b16 v[82:83], v12 offset:2048\n"
"ds_read_b64_tr_b16 v[84:85], v13 offset:0\n"
"ds_read_b64_tr_b16 v[86:87], v13 offset:2048\n"
"ds_read_b64_tr_b16 v[88:89], v14 offset:0\n"
"ds_read_b64_tr_b16 v[90:91], v14 offset:2048\n"
"ds_read_b64_tr_b16 v[92:93], v15 offset:0\n"
"ds_read_b64_tr_b16 v[94:95], v15 offset:2048\n"
"ds_read_b64_tr_b16 v[96:97], v12 offset:4096\n"
"ds_read_b64_tr_b16 v[98:99], v12 offset:6144\n"
"ds_read_b64_tr_b16 v[100:101], v13 offset:4096\n"
"ds_read_b64_tr_b16 v[102:103], v13 offset:6144\n"
"ds_read_b64_tr_b16 v[104:105], v14 offset:4096\n"
"ds_read_b64_tr_b16 v[106:107], v14 offset:6144\n"
"ds_read_b64_tr_b16 v[108:109], v15 offset:4096\n"
"ds_read_b64_tr_b16 v[110:111], v15 offset:6144\n"
"ds_read_b64_tr_b16 v[112:113], v4 offset:32768\n"
"ds_read_b64_tr_b16 v[114:115], v4 offset:34816\n"
"ds_read_b64_tr_b16 v[116:117], v5 offset:32768\n"
"ds_read_b64_tr_b16 v[118:119], v5 offset:34816\n"
"ds_read_b64_tr_b16 v[120:121], v6 offset:32768\n"
"ds_read_b64_tr_b16 v[122:123], v6 offset:34816\n"
"ds_read_b64_tr_b16 v[124:125], v7 offset:32768\n"
"ds_read_b64_tr_b16 v[126:127], v7 offset:34816\n"
"ds_read_b64_tr_b16 v[128:129], v4 offset:36864\n"
"ds_read_b64_tr_b16 v[130:131], v4 offset:38912\n"
"ds_read_b64_tr_b16 v[132:133], v5 offset:36864\n"
"ds_read_b64_tr_b16 v[134:135], v5 offset:38912\n"
"ds_read_b64_tr_b16 v[136:137], v6 offset:36864\n"
"ds_read_b64_tr_b16 v[138:139], v6 offset:38912\n"
"ds_read_b64_tr_b16 v[140:141], v7 offset:36864\n"
"ds_read_b64_tr_b16 v[142:143], v7 offset:38912\n"
"ds_read_b64_tr_b16 v[144:145], v4 offset:0\n"
"ds_read_b64_tr_b16 v[146:147], v4 offset:2048\n"
"ds_read_b64_tr_b16 v[148:149], v5 offset:0\n"
"ds_read_b64_tr_b16 v[150:151], v5 offset:2048\n"
"ds_read_b64_tr_b16 v[152:153], v6 offset:0\n"
"ds_read_b64_tr_b16 v[154:155], v6 offset:2048\n"
"ds_read_b64_tr_b16 v[156:157], v7 offset:0\n"
"ds_read_b64_tr_b16 v[158:159], v7 offset:2048\n"
"ds_read_b64_tr_b16 a[112:113], v4 offset:4096\n"
"ds_read_b64_tr_b16 a[114:115], v4 offset:6144\n"
"ds_read_b64_tr_b16 a[116:117], v5 offset:4096\n"
"ds_read_b64_tr_b16 a[118:119], v5 offset:6144\n"
"ds_read_b64_tr_b16 a[120:121], v6 offset:4096\n"
"ds_read_b64_tr_b16 a[122:123], v6 offset:6144\n"
"ds_read_b64_tr_b16 a[124:125], v7 offset:4096\n"
"ds_read_b64_tr_b16 a[126:127], v7 offset:6144\n"
"s_waitcnt vmcnt(14)\n"
"v_lshlrev_b32 v192, 16, v160\n"
"v_and_b32 v193, 0xffff0000, v160\n"
"v_lshlrev_b32 v194, 16, v161\n"
"v_and_b32 v195, 0xffff0000, v161\n"
"v_lshlrev_b32 v196, 16, v162\n"
"v_and_b32 v197, 0xffff0000, v162\n"
"v_lshlrev_b32 v198, 16, v163\n"
"v_and_b32 v199, 0xffff0000, v163\n"
"v_lshlrev_b32 v200, 16, v164\n"
"v_and_b32 v201, 0xffff0000, v164\n"
"v_lshlrev_b32 v202, 16, v165\n"
"v_and_b32 v203, 0xffff0000, v165\n"
"v_lshlrev_b32 v204, 16, v166\n"
"v_and_b32 v205, 0xffff0000, v166\n"
"v_lshlrev_b32 v206, 16, v167\n"
"v_and_b32 v207, 0xffff0000, v167\n"
"v_pk_add_f32 v[208:209], v[208:209], v[192:193]\n"
"v_pk_add_f32 v[210:211], v[210:211], v[194:195]\n"
"v_pk_add_f32 v[212:213], v[212:213], v[196:197]\n"
"v_pk_add_f32 v[214:215], v[214:215], v[198:199]\n"
"v_pk_add_f32 v[216:217], v[216:217], v[200:201]\n"
"v_pk_add_f32 v[218:219], v[218:219], v[202:203]\n"
"v_pk_add_f32 v[220:221], v[220:221], v[204:205]\n"
"v_pk_add_f32 v[222:223], v[222:223], v[206:207]\n"
"s_waitcnt vmcnt(12)\n"
"v_lshlrev_b32 v192, 16, v168\n"
"v_and_b32 v193, 0xffff0000, v168\n"
"v_lshlrev_b32 v194, 16, v169\n"
"v_and_b32 v195, 0xffff0000, v169\n"
"v_lshlrev_b32 v196, 16, v170\n"
"v_and_b32 v197, 0xffff0000, v170\n"
"v_lshlrev_b32 v198, 16, v171\n"
"v_and_b32 v199, 0xffff0000, v171\n"
"v_lshlrev_b32 v200, 16, v172\n"
"v_and_b32 v201, 0xffff0000, v172\n"
"v_lshlrev_b32 v202, 16, v173\n"
"v_and_b32 v203, 0xffff0000, v173\n"
"v_lshlrev_b32 v204, 16, v174\n"
"v_and_b32 v205, 0xffff0000, v174\n"
"v_lshlrev_b32 v206, 16, v175\n"
"v_and_b32 v207, 0xffff0000, v175\n"
"v_pk_add_f32 v[208:209], v[208:209], v[192:193]\n"
"v_pk_add_f32 v[210:211], v[210:211], v[194:195]\n"
"v_pk_add_f32 v[212:213], v[212:213], v[196:197]\n"
"v_pk_add_f32 v[214:215], v[214:215], v[198:199]\n"
"v_pk_add_f32 v[216:217], v[216:217], v[200:201]\n"
"v_pk_add_f32 v[218:219], v[218:219], v[202:203]\n"
"v_pk_add_f32 v[220:221], v[220:221], v[204:205]\n"
"v_pk_add_f32 v[222:223], v[222:223], v[206:207]\n"
"s_waitcnt vmcnt(10)\n"
"v_lshlrev_b32 v192, 16, v176\n"
"v_and_b32 v193, 0xffff0000, v176\n"
"v_lshlrev_b32 v194, 16, v177\n"
"v_and_b32 v195, 0xffff0000, v177\n"
"v_lshlrev_b32 v196, 16, v178\n"
"v_and_b32 v197, 0xffff0000, v178\n"
"v_lshlrev_b32 v198, 16, v179\n"
"v_and_b32 v199, 0xffff0000, v179\n"
"v_lshlrev_b32 v200, 16, v180\n"
"v_and_b32 v201, 0xffff0000, v180\n"
"v_lshlrev_b32 v202, 16, v181\n"
"v_and_b32 v203, 0xffff0000, v181\n"
"v_lshlrev_b32 v204, 16, v182\n"
"v_and_b32 v205, 0xffff0000, v182\n"
"v_lshlrev_b32 v206, 16, v183\n"
"v_and_b32 v207, 0xffff0000, v183\n"
"v_pk_add_f32 v[208:209], v[208:209], v[192:193]\n"
"v_pk_add_f32 v[210:211], v[210:211], v[194:195]\n"
"v_pk_add_f32 v[212:213], v[212:213], v[196:197]\n"
"v_pk_add_f32 v[214:215], v[214:215], v[198:199]\n"
"v_pk_add_f32 v[216:217], v[216:217], v[200:201]\n"
"v_pk_add_f32 v[218:219], v[218:219], v[202:203]\n"
"v_pk_add_f32 v[220:221], v[220:221], v[204:205]\n"
"v_pk_add_f32 v[222:223], v[222:223], v[206:207]\n"
"s_waitcnt vmcnt(8)\n"
"v_lshlrev_b32 v192, 16, v184\n"
"v_and_b32 v193, 0xffff0000, v184\n"
"v_lshlrev_b32 v194, 16, v185\n"
"v_and_b32 v195, 0xffff0000, v185\n"
"v_lshlrev_b32 v196, 16, v186\n"
"v_and_b32 v197, 0xffff0000, v186\n"
"v_lshlrev_b32 v198, 16, v187\n"
"v_and_b32 v199, 0xffff0000, v187\n"
"v_lshlrev_b32 v200, 16, v188\n"
"v_and_b32 v201, 0xffff0000, v188\n"
"v_lshlrev_b32 v202, 16, v189\n"
"v_and_b32 v203, 0xffff0000, v189\n"
"v_lshlrev_b32 v204, 16, v190\n"
"v_and_b32 v205, 0xffff0000, v190\n"
"v_lshlrev_b32 v206, 16, v191\n"
"v_and_b32 v207, 0xffff0000, v191\n"
"v_pk_add_f32 v[208:209], v[208:209], v[192:193]\n"
"v_pk_add_f32 v[210:211], v[210:211], v[194:195]\n"
"v_pk_add_f32 v[212:213], v[212:213], v[196:197]\n"
"v_pk_add_f32 v[214:215], v[214:215], v[198:199]\n"
"v_pk_add_f32 v[216:217], v[216:217], v[200:201]\n"
"v_pk_add_f32 v[218:219], v[218:219], v[202:203]\n"
"v_pk_add_f32 v[220:221], v[220:221], v[204:205]\n"
"v_pk_add_f32 v[222:223], v[222:223], v[206:207]\n"
"s_waitcnt lgkmcnt(0)\n"
"v_pk_add_f32 v[160:161], v[208:209], v[216:217]\n"
"s_add_u32 m0, s91, 69632\n"
"v_pk_add_f32 v[162:163], v[210:211], v[218:219]\n"
"v_pk_add_f32 v[164:165], v[212:213], v[220:221]\n"
"v_pk_add_f32 v[166:167], v[214:215], v[222:223]\n"
"v_pk_mul_f32 v[168:169], v[208:209], v[208:209]\n"
"v_pk_mul_f32 v[170:171], v[210:211], v[210:211]\n"
"v_pk_mul_f32 v[172:173], v[212:213], v[212:213]\n"
"v_pk_mul_f32 v[174:175], v[214:215], v[214:215]\n"
"s_add_u32 s85, s92, 1146880\n"
"v_pk_fma_f32 v[168:169], v[216:217], v[216:217], v[168:169]\n"
"v_pk_fma_f32 v[170:171], v[218:219], v[218:219], v[170:171]\n"
"v_pk_fma_f32 v[172:173], v[220:221], v[220:221], v[172:173]\n"
"v_pk_fma_f32 v[174:175], v[222:223], v[222:223], v[174:175]\n"
"v_permlane32_swap_b32 v160, v168\n"
"v_permlane32_swap_b32 v161, v169\n"
"v_permlane32_swap_b32 v162, v170\n"
"v_permlane32_swap_b32 v163, v171\n"
"buffer_load_dwordx4 v17, s[36:39], s85 offen offset:0 lds\n"
"v_permlane32_swap_b32 v164, v172\n"
"v_permlane32_swap_b32 v165, v173\n"
"v_permlane32_swap_b32 v166, v174\n"
"v_permlane32_swap_b32 v167, v175\n"
"v_pk_add_f32 v[160:161], v[160:161], v[168:169]\n"
"v_pk_add_f32 v[162:163], v[162:163], v[170:171]\n"
"v_pk_add_f32 v[164:165], v[164:165], v[172:173]\n"
"v_pk_add_f32 v[166:167], v[166:167], v[174:175]\n"
"buffer_load_dwordx4 v18, s[36:39], s85 offen offset:1024 lds\n"
"v_permlane16_swap_b32 v160, v164\n"
"v_permlane16_swap_b32 v161, v165\n"
"v_permlane16_swap_b32 v162, v166\n"
"v_permlane16_swap_b32 v163, v167\n"
"v_pk_add_f32 v[160:161], v[160:161], v[164:165]\n"
"v_pk_add_f32 v[162:163], v[162:163], v[166:167]\n"
"s_nop 0\n"
"v_add_f32_dpp v160, v160, v160 row_ror:8 row_mask:0xf bank_mask:0xf\n"
"buffer_load_dwordx4 v19, s[36:39], s85 offen offset:2048 lds\n"
"v_add_f32_dpp v161, v161, v161 row_ror:8 row_mask:0xf bank_mask:0xf\n"
"PG17_%=:\n"
"s_cbranch_execz PADRET_%=\n"
"v_add_f32_dpp v162, v162, v162 row_ror:8 row_mask:0xf bank_mask:0xf\n"
"v_add_f32_dpp v163, v163, v163 row_ror:8 row_mask:0xf bank_mask:0xf\n"
"v_add_f32_dpp v160, v160, v160 row_ror:4 row_mask:0xf bank_mask:0xf\n"
"v_add_f32_dpp v161, v161, v161 row_ror:4 row_mask:0xf bank_mask:0xf\n"
"v_add_f32_dpp v162, v162, v162 row_ror:4 row_mask:0xf bank_mask:0xf\n"
"v_add_f32_dpp v163, v163, v163 row_ror:4 row_mask:0xf bank_mask:0xf\n"
"v_add_f32_dpp v160, v160, v160 row_ror:2 row_mask:0xf bank_mask:0xf\n"
"buffer_load_dwordx4 v22, s[36:39], s85 offen offset:3072 lds\n"
"v_add_f32_dpp v161, v161, v161 row_ror:2 row_mask:0xf bank_mask:0xf\n"
"v_add_f32_dpp v162, v162, v162 row_ror:2 row_mask:0xf bank_mask:0xf\n"
"v_add_f32_dpp v163, v163, v163 row_ror:2 row_mask:0xf bank_mask:0xf\n"
"v_add_f32_dpp v160, v160, v160 row_ror:1 row_mask:0xf bank_mask:0xf\n"
"v_add_f32_dpp v161, v161, v161 row_ror:1 row_mask:0xf bank_mask:0xf\n"
"v_add_f32_dpp v162, v162, v162 row_ror:1 row_mask:0xf bank_mask:0xf\n"
"v_add_f32_dpp v163, v163, v163 row_ror:1 row_mask:0xf bank_mask:0xf\n"
"ds_write_b128 v41, v[160:163]\n"
"v_cvt_pk_bf16_f32 v224, v208, v209\n"
"v_cvt_pk_bf16_f32 v225, v210, v211\n"
"v_cvt_pk_bf16_f32 v226, v212, v213\n"
"v_cvt_pk_bf16_f32 v227, v214, v215\n"
"v_cvt_pk_bf16_f32 v228, v216, v217\n"
"v_cvt_pk_bf16_f32 v229, v218, v219\n"
"v_cvt_pk_bf16_f32 v230, v220, v221\n"
"v_cvt_pk_bf16_f32 v231, v222, v223\n"
"s_mov_b32 s86, 65011712\n"
"buffer_store_dwordx4 v[224:227], v43, s[44:47], s86 offen sc1\n"
"s_mov_b32 s86, 66060288\n"
"buffer_store_dwordx4 v[228:231], v43, s[44:47], s86 offen sc1\n"
"s_waitcnt lgkmcnt(0)\n"
"s_barrier\n"
"ds_read_b32 v160, v42\n"
"s_waitcnt lgkmcnt(0)\n"
"v_mov_b32 v161, v160\n"
"s_nop 1\n"
"v_permlane32_swap_b32 v160, v161\n"
"v_add_f32 v160, v160, v161\n"
"v_mov_b32 v161, v160\n"
"s_nop 1\n"
"v_permlane16_swap_b32 v160, v161\n"
"v_add_f32 v160, v160, v161\n"
"v_mul_f32 v160, 0x3b000000, v160\n"
"s_nop 1\n"
"v_mov_b32_dpp v161, v160 row_ror:8 row_mask:0xf bank_mask:0xf\n"
"v_fma_f32 v161, -v160, v160, v161\n"
"v_mov_b32 v162, 0x2edbe6ff\n"
"v_max_f32 v161, 0, v161\n"
"v_fmac_f32 v162, 0x3f800054, v161\n"
"v_rsq_f32 v162, v162\n"
"v_readlane_b32 s8, v160, 0\n"
"v_readlane_b32 s9, v160, 1\n"
"v_readlane_b32 s10, v160, 2\n"
"v_readlane_b32 s11, v160, 3\n"
"v_readlane_b32 s20, v160, 4\n"
"v_readlane_b32 s21, v160, 5\n"
"v_readlane_b32 s22, v160, 6\n"
"v_readlane_b32 s23, v160, 7\n"
"v_readlane_b32 s24, v162, 0\n"
"v_readlane_b32 s25, v162, 1\n"
"v_readlane_b32 s26, v162, 2\n"
"v_readlane_b32 s27, v162, 3\n"
"v_readlane_b32 s28, v162, 4\n"
"v_readlane_b32 s29, v162, 5\n"
"v_readlane_b32 s30, v162, 6\n"
"v_readlane_b32 s31, v162, 7\n"
"v_pk_add_f32 v[208:209], v[208:209], s[8:9] neg_lo:[0,1] neg_hi:[0,1]\n"
"v_pk_add_f32 v[210:211], v[210:211], s[10:11] neg_lo:[0,1] neg_hi:[0,1]\n"
"v_pk_add_f32 v[212:213], v[212:213], s[20:21] neg_lo:[0,1] neg_hi:[0,1]\n"
"v_pk_add_f32 v[214:215], v[214:215], s[22:23] neg_lo:[0,1] neg_hi:[0,1]\n"
"v_pk_mul_f32 v[208:209], v[208:209], s[24:25]\n"
"v_pk_mul_f32 v[210:211], v[210:211], s[26:27]\n"
"v_pk_mul_f32 v[212:213], v[212:213], s[28:29]\n"
"v_pk_mul_f32 v[214:215], v[214:215], s[30:31]\n"
"v_cvt_pk_bf16_f32 v208, v208, v209\n"
"v_cvt_pk_bf16_f32 v209, v210, v211\n"
"v_cvt_pk_bf16_f32 v210, v212, v213\n"
"v_cvt_pk_bf16_f32 v211, v214, v215\n"
"s_mov_b32 s86, 62914560\n"
"buffer_store_dwordx4 v[208:211], v43, s[44:47], s86 offen sc1\n"
"v_pk_add_f32 v[216:217], v[216:217], s[8:9] neg_lo:[0,1] neg_hi:[0,1]\n"
"v_pk_add_f32 v[218:219], v[218:219], s[10:11] neg_lo:[0,1] neg_hi:[0,1]\n"
"v_pk_add_f32 v[220:221], v[220:221], s[20:21] neg_lo:[0,1] neg_hi:[0,1]\n"
"v_pk_add_f32 v[222:223], v[222:223], s[22:23] neg_lo:[0,1] neg_hi:[0,1]\n"
"v_pk_mul_f32 v[216:217], v[216:217], s[24:25]\n"
"v_pk_mul_f32 v[218:219], v[218:219], s[26:27]\n"
"v_pk_mul_f32 v[220:221], v[220:221], s[28:29]\n"
"v_pk_mul_f32 v[222:223], v[222:223], s[30:31]\n"
"v_cvt_pk_bf16_f32 v216, v216, v217\n"
"v_cvt_pk_bf16_f32 v217, v218, v219\n"
"v_cvt_pk_bf16_f32 v218, v220, v221\n"
"v_cvt_pk_bf16_f32 v219, v222, v223\n"
"s_mov_b32 s86, 63963136\n"
"buffer_store_dwordx4 v[216:219], v43, s[44:47], s86 offen sc1\n"
"s_add_u32 m0, s91, 131072\n"
"s_add_u32 s85, s92, 1261568\n"
"buffer_load_dwordx4 v17, s[36:39], s85 offen offset:0 lds\n"
"buffer_load_dwordx4 v18, s[36:39], s85 offen offset:1024 lds\n"
"buffer_load_dwordx4 v19, s[36:39], s85 offen offset:2048 lds\n"
"buffer_load_dwordx4 v22, s[36:39], s85 offen offset:3072 lds\n"
"s_add_u32 m0, s91, 135168\n"
"s_add_u32 s85, s92, 1376256\n"
"buffer_load_dwordx4 v17, s[36:39], s85 offen offset:0 lds\n"
"buffer_load_dwordx4 v18, s[36:39], s85 offen offset:1024 lds\n"
"buffer_load_dwordx4 v19, s[36:39], s85 offen offset:2048 lds\n"
"buffer_load_dwordx4 v22, s[36:39], s85 offen offset:3072 lds\n"
"s_waitcnt vmcnt(12)\n"
"ds_read_b64_tr_b16 a[128:129], v8 offset:32768\n"
"ds_read_b64_tr_b16 a[130:131], v8 offset:34816\n"
"ds_read_b64_tr_b16 a[132:133], v9 offset:32768\n"
"ds_read_b64_tr_b16 a[134:135], v9 offset:34816\n"
"ds_read_b64_tr_b16 a[136:137], v10 offset:32768\n"
"ds_read_b64_tr_b16 a[138:139], v10 offset:34816\n"
"ds_read_b64_tr_b16 a[140:141], v11 offset:32768\n"
"ds_read_b64_tr_b16 a[142:143], v11 offset:34816\n"
"ds_read_b64_tr_b16 a[144:145], v8 offset:0\n"
"ds_read_b64_tr_b16 a[146:147], v8 offset:2048\n"
"ds_read_b64_tr_b16 a[148:149], v9 offset:0\n"
"ds_read_b64_tr_b16 a[150:151], v9 offset:2048\n"
"ds_read_b64_tr_b16 a[152:153], v10 offset:0\n"
"ds_read_b64_tr_b16 a[154:155], v10 offset:2048\n"
"ds_read_b64_tr_b16 a[156:157], v11 offset:0\n"
"ds_read_b64_tr_b16 a[158:159], v11 offset:2048\n"
"ds_read_b64_tr_b16 a[160:161], v8 offset:4096\n"
"ds_read_b64_tr_b16 a[162:163], v8 offset:6144\n"
"ds_read_b64_tr_b16 a[164:165], v9 offset:4096\n"
"ds_read_b64_tr_b16 a[166:167], v9 offset:6144\n"
"ds_read_b64_tr_b16 a[168:169], v10 offset:4096\n"
"ds_read_b64_tr_b16 a[170:171], v10 offset:6144\n"
"ds_read_b64_tr_b16 a[172:173], v11 offset:4096\n"
"ds_read_b64_tr_b16 a[174:175], v11 offset:6144\n"
"s_waitcnt vmcnt(8)\n"
"s_lshl_b32 s99, s81, 7\n"
"s_add_u32 s99, s99, 393216\n"
"v_mov_b32 v28, s64\n"
"v_mov_b32 v29, s65\n"
"s_mov_b64 exec, 1\n"
"buffer_store_dwordx2 v[28:29], off, s[44:47], s99 sc1\n"
"s_mov_b64 exec, -1\n"
"s_waitcnt lgkmcnt(0)\n"
"s_add_u32 m0, s91, 32768\n"
"s_add_u32 s85, s92, 1490944\n"
"buffer_load_dwordx4 v17, s[36:39], s85 offen offset:0 lds\n"
"buffer_load_dwordx4 v18, s[36:39], s85 offen offset:1024 lds\n"
"buffer_load_dwordx4 v19, s[36:39], s85 offen offset:2048 lds\n"
"buffer_load_dwordx4 v22, s[36:39], s85 offen offset:3072 lds\n"
"s_add_u32 m0, s91, 36864\n"
"s_add_u32 s85, s92, 1605632\n"
"buffer_load_dwordx4 v17, s[36:39], s85 offen offset:0 lds\n"
"buffer_load_dwordx4 v18, s[36:39], s85 offen offset:1024 lds\n"
"buffer_load_dwordx4 v19, s[36:39], s85 offen offset:2048 lds\n"
"buffer_load_dwordx4 v22, s[36:39], s85 offen offset:3072 lds\n"
"s_add_u32 m0, s91, 0\n"
"s_add_u32 s85, s92, 1720320\n"
"buffer_load_dwordx4 v17, s[36:39], s85 offen offset:0 lds\n"
"buffer_load_dwordx4 v18, s[36:39], s85 offen offset:1024 lds\n"
"buffer_load_dwordx4 v19, s[36:39], s85 offen offset:2048 lds\n"
"buffer_load_dwordx4 v22, s[36:39], s85 offen offset:3072 lds\n"
"s_add_u32 m0, s91, 4096\n"
"s_add_u32 s85, s72, 0\n"
"buffer_load_dwordx4 v44, s[40:43], s85 offen offset:0 lds\n"
"buffer_load_dwordx4 v45, s[40:43], s85 offen offset:1024 lds\n"
"buffer_load_dwordx4 v46, s[40:43], s85 offen offset:2048 lds\n"
"buffer_load_dwordx4 v47, s[40:43], s85 offen offset:3072 lds\n"
"s_add_u32 m0, s91, 98304\n"
"s_add_u32 s85, s72, 131072\n"
"buffer_load_dwordx4 v44, s[40:43], s85 offen offset:0 lds\n"
"buffer_load_dwordx4 v45, s[40:43], s85 offen offset:1024 lds\n"
"buffer_load_dwordx4 v46, s[40:43], s85 offen offset:2048 lds\n"
"buffer_load_dwordx4 v47, s[40:43], s85 offen offset:3072 lds\n"
"s_add_u32 m0, s91, 65536\n"
"s_add_u32 s85, s72, 262144\n"
"buffer_load_dwordx4 v44, s[40:43], s85 offen offset:0 lds\n"
"buffer_load_dwordx4 v45, s[40:43], s85 offen offset:1024 lds\n"
"buffer_load_dwordx4 v46, s[40:43], s85 offen offset:2048 lds\n"
"buffer_load_dwordx4 v47, s[40:43], s85 offen offset:3072 lds\n"
"s_add_u32 m0, s91, 69632\n"
"s_add_u32 s85, s72, 393216\n"
"buffer_load_dwordx4 v44, s[40:43], s85 offen offset:0 lds\n"
"buffer_load_dwordx4 v45, s[40:43], s85 offen offset:1024 lds\n"
"buffer_load_dwordx4 v46, s[40:43], s85 offen offset:2048 lds\n"
"buffer_load_dwordx4 v47, s[40:43], s85 offen offset:3072 lds\n"
"s_barrier\n"
"s_cmp_ge_u32 s60, 224\n"
"s_cbranch_scc1 IDLE_%=\n"
"buffer_load_dwordx2 v[20:21], v31, s[44:47], 0 offen sc1\n"
"s_waitcnt vmcnt(0)\n"
"v_cmp_ne_u64 vcc, s[64:65], v[20:21]\n"
"s_nop 1\n"
"s_cbranch_vccz FOK_48_%=\n"
"FSP_49_%=:\n"
"s_cmp_eq_u32 s69, 0\n"
"s_cbranch_scc1 FOK_48_%=\n"
"s_sub_u32 s69, s69, 1\n"
"s_sleep 2\n"
"s_mov_b64 exec, vcc\n"
"buffer_load_dwordx2 v[20:21], v31, s[44:47], 0 offen sc1\n"
"s_mov_b64 exec, -1\n"
"s_waitcnt vmcnt(0)\n"
"v_cmp_ne_u64 vcc, s[64:65], v[20:21]\n"
"s_nop 1\n"
"s_cbranch_vccnz FSP_49_%=\n"
"FOK_48_%=:\n"
"G1_%=:\n"
"s_add_u32 s84, s89, 0\n"
"buffer_load_dwordx4 a[0:3], v24, s[44:47], s84 offen sc1\n"
"s_add_u32 s84, s89, 0\n"
"buffer_load_dwordx4 a[4:7], v25, s[44:47], s84 offen sc1\n"
"s_add_u32 s84, s89, 0\n"
"buffer_load_dwordx4 a[8:11], v26, s[44:47], s84 offen sc1\n"
"s_add_u32 s84, s89, 0\n"
"buffer_load_dwordx4 a[12:15], v27, s[44:47], s84 offen sc1\n"
"s_add_u32 s84, s89, 1024\n"
"buffer_load_dwordx4 a[16:19], v24, s[44:47], s84 offen sc1\n"
"s_add_u32 s84, s89, 1024\n"
"buffer_load_dwordx4 a[20:23], v25, s[44:47], s84 offen sc1\n"
"s_add_u32 s84, s89, 1024\n"
"buffer_load_dwordx4 a[24:27], v26, s[44:47], s84 offen sc1\n"
"s_add_u32 s84, s89, 1024\n"
"buffer_load_dwordx4 a[28:31], v27, s[44:47], s84 offen sc1\n"
"s_add_u32 s84, s89, 2048\n"
"buffer_load_dwordx4 a[32:35], v24, s[44:47], s84 offen sc1\n"
"s_add_u32 s84, s89, 2048\n"
"buffer_load_dwordx4 a[36:39], v25, s[44:47], s84 offen sc1\n"
"s_add_u32 s84, s89, 2048\n"
"buffer_load_dwordx4 a[40:43], v26, s[44:47], s84 offen sc1\n"
"s_add_u32 s84, s89, 2048\n"
"buffer_load_dwordx4 a[44:47], v27, s[44:47], s84 offen sc1\n"
"s_add_u32 s86, s91, 102400\n"
"v_add_u32 v40, s86, v1\n"
"s_add_u32 s86, s91, 4096\n"
"v_add_u32 v41, s86, v1\n"
"s_add_u32 s86, s91, 98304\n"
"v_add_u32 v42, s86, v1\n"
"s_waitcnt vmcnt(8)\n"
"v_mfma_f32_16x16x32_bf16 v[192:195], v[48:51], a[0:3], 0\n"
"s_xor_b32 s86, s61, 0\n"
"ds_read_b64_tr_b16 a[176:177], v12 offset:0\n"
"ds_read_b64_tr_b16 a[178:179], v12 offset:2048\n"
"v_mfma_f32_16x16x32_bf16 v[196:199], v[52:55], a[0:3], 0\n"
"s_lshl_b32 s86, s86, 5\n"
"ds_read_b64_tr_b16 a[180:181], v13 offset:0\n"
"ds_read_b64_tr_b16 a[182:183], v13 offset:2048\n"
"v_mfma_f32_16x16x32_bf16 v[200:203], v[56:59], a[0:3], 0\n"
"s_add_u32 s84, s89, 3072\n"
"ds_read_b64_tr_b16 a[184:185], v14 offset:0\n"
"ds_read_b64_tr_b16 a[186:187], v14 offset:2048\n"
"v_mfma_f32_16x16x32_bf16 v[204:207], v[60:63], a[0:3], 0\n"
"ds_read_b64_tr_b16 a[188:189], v15 offset:0\n"
"ds_read_b64_tr_b16 a[190:191], v15 offset:2048\n"
"v_mfma_f32_16x16x32_bf16 v[220:223], v[60:63], a[4:7], 0\n"
"ds_read_b64_tr_b16 a[192:193], v12 offset:4096\n"
"ds_read_b64_tr_b16 a[194:195], v12 offset:6144\n"
"v_mfma_f32_16x16x32_bf16 v[216:219], v[56:59], a[4:7], 0\n"
"ds_read_b64_tr_b16 a[196:197], v13 offset:4096\n"
"ds_read_b64_tr_b16 a[198:199], v13 offset:6144\n"
"v_mfma_f32_16x16x32_bf16 v[212:215], v[52:55], a[4:7], 0\n"
"ds_read_b64_tr_b16 a[200:201], v14 offset:4096\n"
"ds_read_b64_tr_b16 a[202:203], v14 offset:6144\n"
"v_mfma_f32_16x16x32_bf16 v[208:211], v[48:51], a[4:7], 0\n"
"ds_read_b64_tr_b16 a[204:205], v15 offset:4096\n"
"ds_read_b64_tr_b16 a[206:207], v15 offset:6144\n"
"v_mfma_f32_16x16x32_bf16 v[224:227], v[48:51], a[8:11], 0\n"
"ds_read_b64_tr_b16 a[208:209], v4 offset:32768\n"
"ds_read_b64_tr_b16 a[210:211], v4 offset:34816\n"
"v_mfma_f32_16x16x32_bf16 v[228:231], v[52:55], a[8:11], 0\n"
"ds_read_b64_tr_b16 a[212:213], v5 offset:32768\n"
"ds_read_b64_tr_b16 a[214:215], v5 offset:34816\n"
"v_mfma_f32_16x16x32_bf16 v[232:235], v[56:59], a[8:11], 0\n"
"ds_read_b64_tr_b16 a[216:217], v6 offset:32768\n"
"ds_read_b64_tr_b16 a[218:219], v6 offset:34816\n"
"v_mfma_f32_16x16x32_bf16 v[236:239], v[60:63], a[8:11], 0\n"
"ds_read_b64_tr_b16 a[220:221], v7 offset:32768\n"
"ds_read_b64_tr_b16 a[222:223], v7 offset:34816\n"
"v_mfma_f32_16x16x32_bf16 v[252:255], v[60:63], a[12:15], 0\n"
"ds_read_b64_tr_b16 a[224:225], v4 offset:36864\n"
"ds_read_b64_tr_b16 a[226:227], v4 offset:38912\n"
"v_mfma_f32_16x16x32_bf16 v[248:251], v[56:59], a[12:15], 0\n"
"ds_read_b64_tr_b16 a[228:229], v5 offset:36864\n"
"ds_read_b64_tr_b16 a[230:231], v5 offset:38912\n"
"v_mfma_f32_16x16x32_bf16 v[244:247], v[52:55], a[12:15], 0\n"
"ds_read_b64_tr_b16 a[232:233], v6 offset:36864\n"
"ds_read_b64_tr_b16 a[234:235], v6 offset:38912\n"
"v_mfma_f32_16x16x32_bf16 v[240:243], v[48:51], a[12:15], 0\n"
"ds_read_b64_tr_b16 a[236:237], v7 offset:36864\n"
"ds_read_b64_tr_b16 a[238:239], v7 offset:38912\n"
"ds_read_b64_tr_b16 a[240:241], v4 offset:0\n"
"ds_read_b64_tr_b16 a[242:243], v4 offset:2048\n"
"ds_read_b64_tr_b16 a[244:245], v5 offset:0\n"
"ds_read_b64_tr_b16 a[246:247], v5 offset:2048\n"
"ds_read_b64_tr_b16 a[248:249], v6 offset:0\n"
"ds_read_b64_tr_b16 a[250:251], v6 offset:2048\n"
"ds_read_b64_tr_b16 a[252:253], v7 offset:0\n"
"ds_read_b64_tr_b16 a[254:255], v7 offset:2048\n"
"v_xor_b32 v4, s86, v30\n"
"v_add_u32 v8, 65536, v4\n"
"v_add_u32 v12, 131072, v4\n"
"s_xor_b32 s86, s61, 1\n"
"s_lshl_b32 s86, s86, 5\n"
"v_xor_b32 v5, s86, v30\n"
"v_add_u32 v9, 65536, v5\n"
"v_add_u32 v13, 131072, v5\n"
"s_xor_b32 s86, s61, 2\n"
"s_lshl_b32 s86, s86, 5\n"
"v_xor_b32 v6, s86, v30\n"
"v_add_u32 v10, 65536, v6\n"
"v_add_u32 v14, 131072, v6\n"
"s_xor_b32 s86, s61, 3\n"
"s_lshl_b32 s86, s86, 5\n"
"v_xor_b32 v7, s86, v30\n"
"v_add_u32 v11, 65536, v7\n"
"v_add_u32 v15, 131072, v7\n"
"buffer_load_dwordx4 a[48:51], v24, s[44:47], s84 offen sc1\n"
"s_add_u32 s84, s89, 3072\n"
"buffer_load_dwordx4 a[52:55], v25, s[44:47], s84 offen sc1\n"
"s_add_u32 s84, s89, 3072\n"
"buffer_load_dwordx4 a[56:59], v26, s[44:47], s84 offen sc1\n"
"s_add_u32 s84, s89, 3072\n"
"buffer_load_dwordx4 a[60:63], v27, s[44:47], s84 offen sc1\n"
"s_waitcnt vmcnt(8)\n"
"v_mfma_f32_16x16x32_bf16 v[192:195], v[64:67], a[16:19], v[192:195]\n"
"s_add_u32 s84, s89, 4096\n"
"ds_read_b64_tr_b16 v[48:49], v4 offset:4096\n"
"ds_read_b64_tr_b16 v[50:51], v4 offset:6144\n"
"v_mfma_f32_16x16x32_bf16 v[196:199], v[68:71], a[16:19], v[196:199]\n"
"ds_read_b64_tr_b16 v[52:53], v5 offset:4096\n"
"ds_read_b64_tr_b16 v[54:55], v5 offset:6144\n"
"v_mfma_f32_16x16x32_bf16 v[200:203], v[72:75], a[16:19], v[200:203]\n"
"ds_read_b64_tr_b16 v[56:57], v6 offset:4096\n"
"ds_read_b64_tr_b16 v[58:59], v6 offset:6144\n"
"v_mfma_f32_16x16x32_bf16 v[204:207], v[76:79], a[16:19], v[204:207]\n"
"ds_read_b64_tr_b16 v[60:61], v7 offset:4096\n"
"ds_read_b64_tr_b16 v[62:63], v7 offset:6144\n"
"v_mfma_f32_16x16x32_bf16 v[220:223], v[76:79], a[20:23], v[220:223]\n"
"buffer_load_dwordx4 a[64:67], v24, s[44:47], s84 offen sc1\n"
"v_mfma_f32_16x16x32_bf16 v[216:219], v[72:75], a[20:23], v[216:219]\n"
"s_add_u32 s84, s89, 4096\n"
"v_mfma_f32_16x16x32_bf16 v[212:215], v[68:71], a[20:23], v[212:215]\n"
"buffer_load_dwordx4 a[68:71], v25, s[44:47], s84 offen sc1\n"
"v_mfma_f32_16x16x32_bf16 v[208:211], v[64:67], a[20:23], v[208:211]\n"
"s_add_u32 s84, s89, 4096\n"
"v_mfma_f32_16x16x32_bf16 v[224:227], v[64:67], a[24:27], v[224:227]\n"
"buffer_load_dwordx4 a[72:75], v26, s[44:47], s84 offen sc1\n"
"v_mfma_f32_16x16x32_bf16 v[228:231], v[68:71], a[24:27], v[228:231]\n"
"s_add_u32 s84, s89, 4096\n"
"v_mfma_f32_16x16x32_bf16 v[232:235], v[72:75], a[24:27], v[232:235]\n"
"v_mfma_f32_16x16x32_bf16 v[236:239], v[76:79], a[24:27], v[236:239]\n"
"v_mfma_f32_16x16x32_bf16 v[252:255], v[76:79], a[28:31], v[252:255]\n"
"buffer_load_dwordx4 a[76:79], v27, s[44:47], s84 offen sc1\n"
"v_mfma_f32_16x16x32_bf16 v[248:251], v[72:75], a[28:31], v[248:251]\n"
"v_mfma_f32_16x16x32_bf16 v[244:247], v[68:71], a[28:31], v[244:247]\n"
"v_mfma_f32_16x16x32_bf16 v[240:243], v[64:67], a[28:31], v[240:243]\n"
"s_waitcnt vmcnt(8)\n"
"v_mfma_f32_16x16x32_bf16 v[192:195], v[80:83], a[32:35], v[192:195]\n"
"s_add_u32 s84, s89, 5120\n"
"ds_read_b64_tr_b16 v[64:65], v8 offset:32768\n"
"ds_read_b64_tr_b16 v[66:67], v8 offset:34816\n"
"v_mfma_f32_16x16x32_bf16 v[196:199], v[84:87], a[32:35], v[196:199]\n"
"ds_read_b64_tr_b16 v[68:69], v9 offset:32768\n"
"ds_read_b64_tr_b16 v[70:71], v9 offset:34816\n"
"v_mfma_f32_16x16x32_bf16 v[200:203], v[88:91], a[32:35], v[200:203]\n"
"ds_read_b64_tr_b16 v[72:73], v10 offset:32768\n"
"ds_read_b64_tr_b16 v[74:75], v10 offset:34816\n"
"v_mfma_f32_16x16x32_bf16 v[204:207], v[92:95], a[32:35], v[204:207]\n"
"ds_read_b64_tr_b16 v[76:77], v11 offset:32768\n"
"ds_read_b64_tr_b16 v[78:79], v11 offset:34816\n"
"v_mfma_f32_16x16x32_bf16 v[220:223], v[92:95], a[36:39], v[220:223]\n"
"buffer_load_dwordx4 a[80:83], v24, s[44:47], s84 offen sc1\n"
"v_mfma_f32_16x16x32_bf16 v[216:219], v[88:91], a[36:39], v[216:219]\n"
"s_add_u32 s84, s89, 5120\n"
"v_mfma_f32_16x16x32_bf16 v[212:215], v[84:87], a[36:39], v[212:215]\n"
"buffer_load_dwordx4 a[84:87], v25, s[44:47], s84 offen sc1\n"
"v_mfma_f32_16x16x32_bf16 v[208:211], v[80:83], a[36:39], v[208:211]\n"
"s_add_u32 s84, s89, 5120\n"
"v_mfma_f32_16x16x32_bf16 v[224:227], v[80:83], a[40:43], v[224:227]\n"
"buffer_load_dwordx4 a[88:91], v26, s[44:47], s84 offen sc1\n"
"v_mfma_f32_16x16x32_bf16 v[228:231], v[84:87], a[40:43], v[228:231]\n"
"s_add_u32 s84, s89, 5120\n"
"v_mfma_f32_16x16x32_bf16 v[232:235], v[88:91], a[40:43], v[232:235]\n"
"v_mfma_f32_16x16x32_bf16 v[236:239], v[92:95], a[40:43], v[236:239]\n"
"v_mfma_f32_16x16x32_bf16 v[252:255], v[92:95], a[44:47], v[252:255]\n"
"buffer_load_dwordx4 a[92:95], v27, s[44:47], s84 offen sc1\n"
"v_mfma_f32_16x16x32_bf16 v[248:251], v[88:91], a[44:47], v[248:251]\n"
"v_mfma_f32_16x16x32_bf16 v[244:247], v[84:87], a[44:47], v[244:247]\n"
"v_mfma_f32_16x16x32_bf16 v[240:243], v[80:83], a[44:47], v[240:243]\n"
"s_waitcnt vmcnt(8)\n"
"v_mfma_f32_16x16x32_bf16 v[192:195], v[96:99], a[48:51], v[192:195]\n"
"s_add_u32 s84, s89, 6144\n"
"ds_read_b64_tr_b16 v[80:81], v8 offset:0\n"
"ds_read_b64_tr_b16 v[82:83], v8 offset:2048\n"
"v_mfma_f32_16x16x32_bf16 v[196:199], v[100:103], a[48:51], v[196:199]\n"
"ds_read_b64_tr_b16 v[84:85], v9 offset:0\n"
"ds_read_b64_tr_b16 v[86:87], v9 offset:2048\n"
"v_mfma_f32_16x16x32_bf16 v[200:203], v[104:107], a[48:51], v[200:203]\n"
"ds_read_b64_tr_b16 v[88:89], v10 offset:0\n"
"ds_read_b64_tr_b16 v[90:91], v10 offset:2048\n"
"v_mfma_f32_16x16x32_bf16 v[204:207], v[108:111], a[48:51], v[204:207]\n"
"ds_read_b64_tr_b16 v[92:93], v11 offset:0\n"
"ds_read_b64_tr_b16 v[94:95], v11 offset:2048\n"
"v_mfma_f32_16x16x32_bf16 v[220:223], v[108:111], a[52:55], v[220:223]\n"
"buffer_load_dwordx4 a[96:99], v24, s[44:47], s84 offen sc1\n"
"v_mfma_f32_16x16x32_bf16 v[216:219], v[104:107], a[52:55], v[216:219]\n"
"s_add_u32 s84, s89, 6144\n"
"v_mfma_f32_16x16x32_bf16 v[212:215], v[100:103], a[52:55], v[212:215]\n"
"buffer_load_dwordx4 a[100:103], v25, s[44:47], s84 offen sc1\n"
"v_mfma_f32_16x16x32_bf16 v[208:211], v[96:99], a[52:55], v[208:211]\n"
"s_add_u32 s84, s89, 6144\n"
"v_mfma_f32_16x16x32_bf16 v[224:227], v[96:99], a[56:59], v[224:227]\n"
"buffer_load_dwordx4 a[104:107], v26, s[44:47], s84 offen sc1\n"
"v_mfma_f32_16x16x32_bf16 v[228:231], v[100:103], a[56:59], v[228:231]\n"
"s_add_u32 s84, s89, 6144\n"
"v_mfma_f32_16x16x32_bf16 v[232:235], v[104:107], a[56:59], v[232:235]\n"
"v_mfma_f32_16x16x32_bf16 v[236:239], v[108:111], a[56:59], v[236:239]\n"
"v_mfma_f32_16x16x32_bf16 v[252:255], v[108:111], a[60:63], v[252:255]\n"
"buffer_load_dwordx4 a[108:111], v27, s[44:47], s84 offen sc1\n"
"v_mfma_f32_16x16x32_bf16 v[248:251], v[104:107], a[60:63], v[248:251]\n"
"v_mfma_f32_16x16x32_bf16 v[244:247], v[100:103], a[60:63], v[244:247]\n"
"v_mfma_f32_16x16x32_bf16 v[240:243], v[96:99], a[60:63], v[240:243]\n"
"s_waitcnt vmcnt(8)\n"
"v_mfma_f32_16x16x32_bf16 v[192:195], v[112:115], a[64:67], v[192:195]\n"
"s_add_u32 s84, s89, 7168\n"
"ds_read_b64_tr_b16 v[96:97], v8 offset:4096\n"
"ds_read_b64_tr_b16 v[98:99], v8 offset:6144\n"
"v_mfma_f32_16x16x32_bf16 v[196:199], v[116:119], a[64:67], v[196:199]\n"
"ds_read_b64_tr_b16 v[100:101], v9 offset:4096\n"
"ds_read_b64_tr_b16 v[102:103], v9 offset:6144\n"
"v_mfma_f32_16x16x32_bf16 v[200:203], v[120:123], a[64:67], v[200:203]\n"
"ds_read_b64_tr_b16 v[104:105], v10 offset:4096\n"
"ds_read_b64_tr_b16 v[106:107], v10 offset:6144\n"
"v_mfma_f32_16x16x32_bf16 v[204:207], v[124:127], a[64:67], v[204:207]\n"
"ds_read_b64_tr_b16 v[108:109], v11 offset:4096\n"
"ds_read_b64_tr_b16 v[110:111], v11 offset:6144\n"
"v_mfma_f32_16x16x32_bf16 v[220:223], v[124:127], a[68:71], v[220:223]\n"
"buffer_load_dwordx4 a[0:3], v24, s[44:47], s84 offen sc1\n"
"v_mfma_f32_16x16x32_bf16 v[216:219], v[120:123], a[68:71], v[216:219]\n"
"s_add_u32 s84, s89, 7168\n"
"v_mfma_f32_16x16x32_bf16 v[212:215], v[116:119], a[68:71], v[212:215]\n"
"buffer_load_dwordx4 a[4:7], v25, s[44:47], s84 offen sc1\n"
"v_mfma_f32_16x16x32_bf16 v[208:211], v[112:115], a[68:71], v[208:211]\n"
"s_add_u32 s84, s89, 7168\n"
"v_mfma_f32_16x16x32_bf16 v[224:227], v[112:115], a[72:75], v[224:227]\n"
"buffer_load_dwordx4 a[8:11], v26, s[44:47], s84 offen sc1\n"
"v_mfma_f32_16x16x32_bf16 v[228:231], v[116:119], a[72:75], v[228:231]\n"
"s_add_u32 s84, s89, 7168\n"
"v_mfma_f32_16x16x32_bf16 v[232:235], v[120:123], a[72:75], v[232:235]\n"
"v_mfma_f32_16x16x32_bf16 v[236:239], v[124:127], a[72:75], v[236:239]\n"
"v_mfma_f32_16x16x32_bf16 v[252:255], v[124:127], a[76:79], v[252:255]\n"
"buffer_load_dwordx4 a[12:15], v27, s[44:47], s84 offen sc1\n"
"v_mfma_f32_16x16x32_bf16 v[248:251], v[120:123], a[76:79], v[248:251]\n"
"v_mfma_f32_16x16x32_bf16 v[244:247], v[116:119], a[76:79], v[244:247]\n"
"v_mfma_f32_16x16x32_bf16 v[240:243], v[112:115], a[76:79], v[240:243]\n"
"s_waitcnt vmcnt(8)\n"
"v_mfma_f32_16x16x32_bf16 v[192:195], v[128:131], a[80:83], v[192:195]\n"
"s_add_u32 s84, s94, 8192\n"
"v_mfma_f32_16x16x32_bf16 v[196:199], v[132:135], a[80:83], v[196:199]\n"
"buffer_load_dwordx4 a[16:19], v24, s[44:47], s84 offen sc1\n"
"v_mfma_f32_16x16x32_bf16 v[200:203], v[136:139], a[80:83], v[200:203]\n"
"s_add_u32 s84, s94, 8192\n"
"v_mfma_f32_16x16x32_bf16 v[204:207], v[140:143], a[80:83], v[204:207]\n"
"v_mfma_f32_16x16x32_bf16 v[220:223], v[140:143], a[84:87], v[220:223]\n"
"buffer_load_dwordx4 a[20:23], v25, s[44:47], s84 offen sc1\n"
"v_mfma_f32_16x16x32_bf16 v[216:219], v[136:139], a[84:87], v[216:219]\n"
"s_add_u32 s84, s94, 8192\n"
"v_mfma_f32_16x16x32_bf16 v[212:215], v[132:135], a[84:87], v[212:215]\n"
"v_mfma_f32_16x16x32_bf16 v[208:211], v[128:131], a[84:87], v[208:211]\n"
"v_mfma_f32_16x16x32_bf16 v[224:227], v[128:131], a[88:91], v[224:227]\n"
"buffer_load_dwordx4 a[24:27], v26, s[44:47], s84 offen sc1\n"
"v_mfma_f32_16x16x32_bf16 v[228:231], v[132:135], a[88:91], v[228:231]\n"
"s_add_u32 s84, s94, 8192\n"
"v_mfma_f32_16x16x32_bf16 v[232:235], v[136:139], a[88:91], v[232:235]\n"
"v_mfma_f32_16x16x32_bf16 v[236:239], v[140:143], a[88:91], v[236:239]\n"
"v_mfma_f32_16x16x32_bf16 v[252:255], v[140:143], a[92:95], v[252:255]\n"
"buffer_load_dwordx4 a[28:31], v27, s[44:47], s84 offen sc1\n"
"v_mfma_f32_16x16x32_bf16 v[248:251], v[136:139], a[92:95], v[248:251]\n"
"v_mfma_f32_16x16x32_bf16 v[244:247], v[132:135], a[92:95], v[244:247]\n"
"v_mfma_f32_16x16x32_bf16 v[240:243], v[128:131], a[92:95], v[240:243]\n"
"s_waitcnt vmcnt(8)\n"
"v_mfma_f32_16x16x32_bf16 v[192:195], v[144:147], a[96:99], v[192:195]\n"
"s_add_u32 s84, s94, 9216\n"
"v_mfma_f32_16x16x32_bf16 v[196:199], v[148:151], a[96:99], v[196:199]\n"
"buffer_load_dwordx4 a[32:35], v24, s[44:47], s84 offen sc1\n"
"v_mfma_f32_16x16x32_bf16 v[200:203], v[152:155], a[96:99], v[200:203]\n"
"s_add_u32 s84, s94, 9216\n"
"v_mfma_f32_16x16x32_bf16 v[204:207], v[156:159], a[96:99], v[204:207]\n"
"v_mfma_f32_16x16x32_bf16 v[220:223], v[156:159], a[100:103], v[220:223]\n"
"buffer_load_dwordx4 a[36:39], v25, s[44:47], s84 offen sc1\n"
"v_mfma_f32_16x16x32_bf16 v[216:219], v[152:155], a[100:103], v[216:219]\n"
"s_add_u32 s84, s94, 9216\n"
"v_mfma_f32_16x16x32_bf16 v[212:215], v[148:151], a[100:103], v[212:215]\n"
"v_mfma_f32_16x16x32_bf16 v[208:211], v[144:147], a[100:103], v[208:211]\n"
"v_mfma_f32_16x16x32_bf16 v[224:227], v[144:147], a[104:107], v[224:227]\n"
"buffer_load_dwordx4 a[40:43], v26, s[44:47], s84 offen sc1\n"
"v_mfma_f32_16x16x32_bf16 v[228:231], v[148:151], a[104:107], v[228:231]\n"
"s_add_u32 s84, s94, 9216\n"
"v_mfma_f32_16x16x32_bf16 v[232:235], v[152:155], a[104:107], v[232:235]\n"
"v_mfma_f32_16x16x32_bf16 v[236:239], v[156:159], a[104:107], v[236:239]\n"
"v_mfma_f32_16x16x32_bf16 v[252:255], v[156:159], a[108:111], v[252:255]\n"
"buffer_load_dwordx4 a[44:47], v27, s[44:47], s84 offen sc1\n"
"v_mfma_f32_16x16x32_bf16 v[248:251], v[152:155], a[108:111], v[248:251]\n"
"v_mfma_f32_16x16x32_bf16 v[244:247], v[148:151], a[108:111], v[244:247]\n"
"v_mfma_f32_16x16x32_bf16 v[240:243], v[144:147], a[108:111], v[240:243]\n"
"s_waitcnt vmcnt(8)\n"
"v_mfma_f32_16x16x32_bf16 v[192:195], a[112:115], a[0:3], v[192:195]\n"
"s_add_u32 s84, s94, 10240\n"
"v_mfma_f32_16x16x32_bf16 v[196:199], a[116:119], a[0:3], v[196:199]\n"
"buffer_load_dwordx4 a[48:51], v24, s[44:47], s84 offen sc1\n"
"v_mfma_f32_16x16x32_bf16 v[200:203], a[120:123], a[0:3], v[200:203]\n"
"s_add_u32 s84, s94, 10240\n"
"v_mfma_f32_16x16x32_bf16 v[204:207], a[124:127], a[0:3], v[204:207]\n"
"v_mfma_f32_16x16x32_bf16 v[220:223], a[124:127], a[4:7], v[220:223]\n"
"buffer_load_dwordx4 a[52:55], v25, s[44:47], s84 offen sc1\n"
"v_mfma_f32_16x16x32_bf16 v[216:219], a[120:123], a[4:7], v[216:219]\n"
"s_add_u32 s84, s94, 10240\n"
"v_mfma_f32_16x16x32_bf16 v[212:215], a[116:119], a[4:7], v[212:215]\n"
"v_mfma_f32_16x16x32_bf16 v[208:211], a[112:115], a[4:7], v[208:211]\n"
"v_mfma_f32_16x16x32_bf16 v[224:227], a[112:115], a[8:11], v[224:227]\n"
"buffer_load_dwordx4 a[56:59], v26, s[44:47], s84 offen sc1\n"
"v_mfma_f32_16x16x32_bf16 v[228:231], a[116:119], a[8:11], v[228:231]\n"
"s_add_u32 s84, s94, 10240\n"
"v_mfma_f32_16x16x32_bf16 v[232:235], a[120:123], a[8:11], v[232:235]\n"
"v_mfma_f32_16x16x32_bf16 v[236:239], a[124:127], a[8:11], v[236:239]\n"
"v_mfma_f32_16x16x32_bf16 v[252:255], a[124:127], a[12:15], v[252:255]\n"
"buffer_load_dwordx4 a[60:63], v27, s[44:47], s84 offen sc1\n"
"v_mfma_f32_16x16x32_bf16 v[248:251], a[120:123], a[12:15], v[248:251]\n"
"v_mfma_f32_16x16x32_bf16 v[244:247], a[116:119], a[12:15], v[244:247]\n"
"v_mfma_f32_16x16x32_bf16 v[240:243], a[112:115], a[12:15], v[240:243]\n"
"s_waitcnt vmcnt(8)\n"
"v_mfma_f32_16x16x32_bf16 v[192:195], a[128:131], a[16:19], v[192:195]\n"
"s_add_u32 s84, s94, 11264\n"
"v_mfma_f32_16x16x32_bf16 v[196:199], a[132:135], a[16:19], v[196:199]\n"
"buffer_load_dwordx4 a[64:67], v24, s[44:47], s84 offen sc1\n"
"v_mfma_f32_16x16x32_bf16 v[200:203], a[136:139], a[16:19], v[200:203]\n"
"s_add_u32 s84, s94, 11264\n"
"v_mfma_f32_16x16x32_bf16 v[204:207], a[140:143], a[16:19], v[204:207]\n"
"v_mfma_f32_16x16x32_bf16 v[220:223], a[140:143], a[20:23], v[220:223]\n"
"buffer_load_dwordx4 a[68:71], v25, s[44:47], s84 offen sc1\n"
"v_mfma_f32_16x16x32_bf16 v[216:219], a[136:139], a[20:23], v[216:219]\n"
"s_add_u32 s84, s94, 11264\n"
"v_mfma_f32_16x16x32_bf16 v[212:215], a[132:135], a[20:23], v[212:215]\n"
"v_mfma_f32_16x16x32_bf16 v[208:211], a[128:131], a[20:23], v[208:211]\n"
"v_mfma_f32_16x16x32_bf16 v[224:227], a[128:131], a[24:27], v[224:227]\n"
"buffer_load_dwordx4 a[72:75], v26, s[44:47], s84 offen sc1\n"
"v_mfma_f32_16x16x32_bf16 v[228:231], a[132:135], a[24:27], v[228:231]\n"
"s_add_u32 s84, s94, 11264\n"
"v_mfma_f32_16x16x32_bf16 v[232:235], a[136:139], a[24:27], v[232:235]\n"
"v_mfma_f32_16x16x32_bf16 v[236:239], a[140:143], a[24:27], v[236:239]\n"
"v_mfma_f32_16x16x32_bf16 v[252:255], a[140:143], a[28:31], v[252:255]\n"
"buffer_load_dwordx4 a[76:79], v27, s[44:47], s84 offen sc1\n"
"v_mfma_f32_16x16x32_bf16 v[248:251], a[136:139], a[28:31], v[248:251]\n"
"v_mfma_f32_16x16x32_bf16 v[244:247], a[132:135], a[28:31], v[244:247]\n"
"s_nop 0\n"
"PG18_%=:\n"
"s_cbranch_execz PADRET_%=\n"
"v_mfma_f32_16x16x32_bf16 v[240:243], a[128:131], a[28:31], v[240:243]\n"
"s_waitcnt vmcnt(8)\n"
"v_mfma_f32_16x16x32_bf16 v[192:195], a[144:147], a[32:35], v[192:195]\n"
"s_add_u32 s84, s94, 12288\n"
"v_mfma_f32_16x16x32_bf16 v[196:199], a[148:151], a[32:35], v[196:199]\n"
"buffer_load_dwordx4 a[80:83], v24, s[44:47], s84 offen sc1\n"
"v_mfma_f32_16x16x32_bf16 v[200:203], a[152:155], a[32:35], v[200:203]\n"
"s_add_u32 s84, s94, 12288\n"
"v_mfma_f32_16x16x32_bf16 v[204:207], a[156:159], a[32:35], v[204:207]\n"
"v_mfma_f32_16x16x32_bf16 v[220:223], a[156:159], a[36:39], v[220:223]\n"
"buffer_load_dwordx4 a[84:87], v25, s[44:47], s84 offen sc1\n"
"v_mfma_f32_16x16x32_bf16 v[216:219], a[152:155], a[36:39], v[216:219]\n"
"s_add_u32 s84, s94, 12288\n"
"v_mfma_f32_16x16x32_bf16 v[212:215], a[148:151], a[36:39], v[212:215]\n"
"v_mfma_f32_16x16x32_bf16 v[208:211], a[144:147], a[36:39], v[208:211]\n"
"v_mfma_f32_16x16x32_bf16 v[224:227], a[144:147], a[40:43], v[224:227]\n"
"buffer_load_dwordx4 a[88:91], v26, s[44:47], s84 offen sc1\n"
"v_mfma_f32_16x16x32_bf16 v[228:231], a[148:151], a[40:43], v[228:231]\n"
"s_add_u32 s84, s94, 12288\n"
"v_mfma_f32_16x16x32_bf16 v[232:235], a[152:155], a[40:43], v[232:235]\n"
"v_mfma_f32_16x16x32_bf16 v[236:239], a[156:159], a[40:43], v[236:239]\n"
"v_mfma_f32_16x16x32_bf16 v[252:255], a[156:159], a[44:47], v[252:255]\n"
"buffer_load_dwordx4 a[92:95], v27, s[44:47], s84 offen sc1\n"
"v_mfma_f32_16x16x32_bf16 v[248:251], a[152:155], a[44:47], v[248:251]\n"
"v_mfma_f32_16x16x32_bf16 v[244:247], a[148:151], a[44:47], v[244:247]\n"
"v_mfma_f32_16x16x32_bf16 v[240:243], a[144:147], a[44:47], v[240:243]\n"
"s_waitcnt vmcnt(8)\n"
"v_mfma_f32_16x16x32_bf16 v[192:195], a[160:163], a[48:51], v[192:195]\n"
"s_add_u32 s84, s94, 13312\n"
"v_mfma_f32_16x16x32_bf16 v[196:199], a[164:167], a[48:51], v[196:199]\n"
"buffer_load_dwordx4 a[100:103], v25, s[44:47], s84 offen sc1\n"
"v_mfma_f32_16x16x32_bf16 v[200:203], a[168:171], a[48:51], v[200:203]\n"
"s_add_u32 s84, s94, 14336\n"
"v_mfma_f32_16x16x32_bf16 v[204:207], a[172:175], a[48:51], v[204:207]\n"
"v_mfma_f32_16x16x32_bf16 v[220:223], a[172:175], a[52:55], v[220:223]\n"
"buffer_load_dwordx4 a[4:7], v25, s[44:47], s84 offen sc1\n"
"v_mfma_f32_16x16x32_bf16 v[216:219], a[168:171], a[52:55], v[216:219]\n"
"s_add_u32 s84, s94, 15360\n"
"v_mfma_f32_16x16x32_bf16 v[212:215], a[164:167], a[52:55], v[212:215]\n"
"v_mfma_f32_16x16x32_bf16 v[208:211], a[160:163], a[52:55], v[208:211]\n"
"v_mfma_f32_16x16x32_bf16 v[224:227], a[160:163], a[56:59], v[224:227]\n"
"buffer_load_dwordx4 a[20:23], v25, s[44:47], s84 offen sc1\n"
"v_mfma_f32_16x16x32_bf16 v[228:231], a[164:167], a[56:59], v[228:231]\n"
"s_add_u32 s84, s94, 13312\n"
"v_mfma_f32_16x16x32_bf16 v[232:235], a[168:171], a[56:59], v[232:235]\n"
"v_mfma_f32_16x16x32_bf16 v[236:239], a[172:175], a[56:59], v[236:239]\n"
"v_mfma_f32_16x16x32_bf16 v[252:255], a[172:175], a[60:63], v[252:255]\n"
"buffer_load_dwordx4 a[104:107], v26, s[44:47], s84 offen sc1\n"
"v_mfma_f32_16x16x32_bf16 v[248:251], a[168:171], a[60:63], v[248:251]\n"
"v_mfma_f32_16x16x32_bf16 v[244:247], a[164:167], a[60:63], v[244:247]\n"
"v_mfma_f32_16x16x32_bf16 v[240:243], a[160:163], a[60:63], v[240:243]\n"
"s_waitcnt lgkmcnt(0)\n"
"s_waitcnt vmcnt(8)\n"
"v_mfma_f32_16x16x32_bf16 v[192:195], a[176:179], a[64:67], v[192:195]\n"
"s_add_u32 s84, s94, 14336\n"
"v_mfma_f32_16x16x32_bf16 v[196:199], a[180:183], a[64:67], v[196:199]\n"
"buffer_load_dwordx4 a[8:11], v26, s[44:47], s84 offen sc1\n"
"v_mfma_f32_16x16x32_bf16 v[200:203], a[184:187], a[64:67], v[200:203]\n"
"s_add_u32 s84, s94, 15360\n"
"v_mfma_f32_16x16x32_bf16 v[204:207], a[188:191], a[64:67], v[204:207]\n"
"v_mfma_f32_16x16x32_bf16 v[220:223], a[188:191], a[68:71], v[220:223]\n"
"buffer_load_dwordx4 a[24:27], v26, s[44:47], s84 offen sc1\n"
"v_mfma_f32_16x16x32_bf16 v[216:219], a[184:187], a[68:71], v[216:219]\n"
"s_add_u32 s84, s94, 13312\n"
"v_mfma_f32_16x16x32_bf16 v[212:215], a[180:183], a[68:71], v[212:215]\n"
"v_mfma_f32_16x16x32_bf16 v[208:211], a[176:179], a[68:71], v[208:211]\n"
"v_mfma_f32_16x16x32_bf16 v[224:227], a[176:179], a[72:75], v[224:227]\n"
"buffer_load_dwordx4 a[108:111], v27, s[44:47], s84 offen sc1\n"
"v_mfma_f32_16x16x32_bf16 v[228:231], a[180:183], a[72:75], v[228:231]\n"
"s_add_u32 s84, s94, 14336\n"
"v_mfma_f32_16x16x32_bf16 v[232:235], a[184:187], a[72:75], v[232:235]\n"
"v_mfma_f32_16x16x32_bf16 v[236:239], a[188:191], a[72:75], v[236:239]\n"
"v_mfma_f32_16x16x32_bf16 v[252:255], a[188:191], a[76:79], v[252:255]\n"
"buffer_load_dwordx4 a[12:15], v27, s[44:47], s84 offen sc1\n"
"v_mfma_f32_16x16x32_bf16 v[248:251], a[184:187], a[76:79], v[248:251]\n"
"v_mfma_f32_16x16x32_bf16 v[244:247], a[180:183], a[76:79], v[244:247]\n"
"v_mfma_f32_16x16x32_bf16 v[240:243], a[176:179], a[76:79], v[240:243]\n"
"s_waitcnt vmcnt(8)\n"
"v_mfma_f32_16x16x32_bf16 v[192:195], a[192:195], a[80:83], v[192:195]\n"
"s_add_u32 s84, s94, 15360\n"
"v_mfma_f32_16x16x32_bf16 v[196:199], a[196:199], a[80:83], v[196:199]\n"
"buffer_load_dwordx4 a[28:31], v27, s[44:47], s84 offen sc1\n"
"v_mfma_f32_16x16x32_bf16 v[200:203], a[200:203], a[80:83], v[200:203]\n"
"s_add_u32 s84, s94, 13312\n"
"v_mfma_f32_16x16x32_bf16 v[204:207], a[204:207], a[80:83], v[204:207]\n"
"v_mfma_f32_16x16x32_bf16 v[220:223], a[204:207], a[84:87], v[220:223]\n"
"buffer_load_dwordx4 a[96:99], v24, s[44:47], s84 offen sc1\n"
"v_mfma_f32_16x16x32_bf16 v[216:219], a[200:203], a[84:87], v[216:219]\n"
"s_add_u32 s84, s94, 14336\n"
"v_mfma_f32_16x16x32_bf16 v[212:215], a[196:199], a[84:87], v[212:215]\n"
"v_mfma_f32_16x16x32_bf16 v[208:211], a[192:195], a[84:87], v[208:211]\n"
"v_mfma_f32_16x16x32_bf16 v[224:227], a[192:195], a[88:91], v[224:227]\n"
"buffer_load_dwordx4 a[0:3], v24, s[44:47], s84 offen sc1\n"
"v_mfma_f32_16x16x32_bf16 v[228:231], a[196:199], a[88:91], v[228:231]\n"
"s_add_u32 s84, s94, 15360\n"
"v_mfma_f32_16x16x32_bf16 v[232:235], a[200:203], a[88:91], v[232:235]\n"
"v_mfma_f32_16x16x32_bf16 v[236:239], a[204:207], a[88:91], v[236:239]\n"
"v_mfma_f32_16x16x32_bf16 v[252:255], a[204:207], a[92:95], v[252:255]\n"
"buffer_load_dwordx4 a[16:19], v24, s[44:47], s84 offen sc1\n"
"v_mfma_f32_16x16x32_bf16 v[248:251], a[200:203], a[92:95], v[248:251]\n"
"v_mfma_f32_16x16x32_bf16 v[244:247], a[196:199], a[92:95], v[244:247]\n"
"v_mfma_f32_16x16x32_bf16 v[240:243], a[192:195], a[92:95], v[240:243]\n"
"s_waitcnt vmcnt(9)\n"
"v_mfma_f32_16x16x32_bf16 v[208:211], a[208:211], a[100:103], v[208:211]\n"
"v_mfma_f32_16x16x32_bf16 v[208:211], a[224:227], a[4:7], v[208:211]\n"
"v_mfma_f32_16x16x32_bf16 v[208:211], a[240:243], a[20:23], v[208:211]\n"
"v_mfma_f32_16x16x32_bf16 v[212:215], a[244:247], a[20:23], v[212:215]\n"
"v_mfma_f32_16x16x32_bf16 v[212:215], a[228:231], a[4:7], v[212:215]\n"
"v_mfma_f32_16x16x32_bf16 v[212:215], a[212:215], a[100:103], v[212:215]\n"
"v_mfma_f32_16x16x32_bf16 v[216:219], a[216:219], a[100:103], v[216:219]\n"
"v_mfma_f32_16x16x32_bf16 v[216:219], a[232:235], a[4:7], v[216:219]\n"
"v_mfma_f32_16x16x32_bf16 v[216:219], a[248:251], a[20:23], v[216:219]\n"
"v_mfma_f32_16x16x32_bf16 v[220:223], a[252:255], a[20:23], v[220:223]\n"
"v_mfma_f32_16x16x32_bf16 v[220:223], a[236:239], a[4:7], v[220:223]\n"
"v_mfma_f32_16x16x32_bf16 v[220:223], a[220:223], a[100:103], v[220:223]\n"
"s_waitcnt vmcnt(6)\n"
"v_mfma_f32_16x16x32_bf16 v[224:227], a[208:211], a[104:107], v[224:227]\n"
"s_add_u32 m0, s91, 65536\n"
"s_add_u32 s85, s72, 524288\n"
"v_mfma_f32_16x16x32_bf16 v[224:227], a[224:227], a[8:11], v[224:227]\n"
"buffer_load_dwordx4 v44, s[40:43], s85 offen offset:0 lds\n"
"v_mfma_f32_16x16x32_bf16 v[224:227], a[240:243], a[24:27], v[224:227]\n"
"ds_write_b128 v40, v[208:211] offset:0\n"
"v_mfma_f32_16x16x32_bf16 v[228:231], a[244:247], a[24:27], v[228:231]\n"
"buffer_load_dwordx4 v45, s[40:43], s85 offen offset:1024 lds\n"
"v_mfma_f32_16x16x32_bf16 v[228:231], a[228:231], a[8:11], v[228:231]\n"
"v_mfma_f32_16x16x32_bf16 v[228:231], a[212:215], a[104:107], v[228:231]\n"
"ds_write_b128 v40, v[212:215] offset:1024\n"
"v_mfma_f32_16x16x32_bf16 v[232:235], a[216:219], a[104:107], v[232:235]\n"
"buffer_load_dwordx4 v46, s[40:43], s85 offen offset:2048 lds\n"
"v_mfma_f32_16x16x32_bf16 v[232:235], a[232:235], a[8:11], v[232:235]\n"
"v_mfma_f32_16x16x32_bf16 v[232:235], a[248:251], a[24:27], v[232:235]\n"
"ds_write_b128 v40, v[216:219] offset:2048\n"
"v_mfma_f32_16x16x32_bf16 v[236:239], a[252:255], a[24:27], v[236:239]\n"
"buffer_load_dwordx4 v47, s[40:43], s85 offen offset:3072 lds\n"
"v_mfma_f32_16x16x32_bf16 v[236:239], a[236:239], a[8:11], v[236:239]\n"
"v_mfma_f32_16x16x32_bf16 v[236:239], a[220:223], a[104:107], v[236:239]\n"
"ds_write_b128 v40, v[220:223] offset:3072\n"
"s_waitcnt vmcnt(7)\n"
"v_mfma_f32_16x16x32_bf16 v[240:243], a[208:211], a[108:111], v[240:243]\n"
"v_mfma_f32_16x16x32_bf16 v[240:243], a[224:227], a[12:15], v[240:243]\n"
"v_mfma_f32_16x16x32_bf16 v[240:243], a[240:243], a[28:31], v[240:243]\n"
"ds_write_b128 v41, v[224:227] offset:0\n"
"v_mfma_f32_16x16x32_bf16 v[244:247], a[244:247], a[28:31], v[244:247]\n"
"v_mfma_f32_16x16x32_bf16 v[244:247], a[228:231], a[12:15], v[244:247]\n"
"v_mfma_f32_16x16x32_bf16 v[244:247], a[212:215], a[108:111], v[244:247]\n"
"ds_write_b128 v41, v[228:231] offset:1024\n"
"v_mfma_f32_16x16x32_bf16 v[248:251], a[216:219], a[108:111], v[248:251]\n"
"v_mfma_f32_16x16x32_bf16 v[248:251], a[232:235], a[12:15], v[248:251]\n"
"v_mfma_f32_16x16x32_bf16 v[248:251], a[248:251], a[28:31], v[248:251]\n"
"ds_write_b128 v41, v[232:235] offset:2048\n"
"v_mfma_f32_16x16x32_bf16 v[252:255], a[252:255], a[28:31], v[252:255]\n"
"v_mfma_f32_16x16x32_bf16 v[252:255], a[236:239], a[12:15], v[252:255]\n"
"v_mfma_f32_16x16x32_bf16 v[252:255], a[220:223], a[108:111], v[252:255]\n"
"ds_write_b128 v41, v[236:239] offset:3072\n"
"s_waitcnt vmcnt(4)\n"
"v_mfma_f32_16x16x32_bf16 v[192:195], a[208:211], a[96:99], v[192:195]\n"
"s_add_u32 m0, s91, 69632\n"
"s_add_u32 s85, s72, 655360\n"
"v_mfma_f32_16x16x32_bf16 v[192:195], a[224:227], a[0:3], v[192:195]\n"
"buffer_load_dwordx4 v44, s[40:43], s85 offen offset:0 lds\n"
"v_mfma_f32_16x16x32_bf16 v[192:195], a[240:243], a[16:19], v[192:195]\n"
"ds_write_b128 v42, v[240:243] offset:0\n"
"v_mfma_f32_16x16x32_bf16 v[196:199], a[244:247], a[16:19], v[196:199]\n"
"buffer_load_dwordx4 v45, s[40:43], s85 offen offset:1024 lds\n"
"v_mfma_f32_16x16x32_bf16 v[196:199], a[228:231], a[0:3], v[196:199]\n"
"v_mfma_f32_16x16x32_bf16 v[196:199], a[212:215], a[96:99], v[196:199]\n"
"ds_write_b128 v42, v[244:247] offset:1024\n"
"v_mfma_f32_16x16x32_bf16 v[200:203], a[216:219], a[96:99], v[200:203]\n"
"buffer_load_dwordx4 v46, s[40:43], s85 offen offset:2048 lds\n"
"v_mfma_f32_16x16x32_bf16 v[200:203], a[232:235], a[0:3], v[200:203]\n"
"v_mfma_f32_16x16x32_bf16 v[200:203], a[248:251], a[16:19], v[200:203]\n"
"ds_write_b128 v42, v[248:251] offset:2048\n"
"v_mfma_f32_16x16x32_bf16 v[204:207], a[252:255], a[16:19], v[204:207]\n"
"buffer_load_dwordx4 v47, s[40:43], s85 offen offset:3072 lds\n"
"v_mfma_f32_16x16x32_bf16 v[204:207], a[236:239], a[0:3], v[204:207]\n"
"v_mfma_f32_16x16x32_bf16 v[204:207], a[220:223], a[96:99], v[204:207]\n"
"ds_write_b128 v42, v[252:255] offset:3072\n"
"v_xor_b32 v40, 8192, v40\n"
"v_xor_b32 v41, 16384, v41\n"
"v_xor_b32 v42, 24576, v42\n"
"s_waitcnt lgkmcnt(0)\n"
"s_barrier\n"
"ds_read_b128 v[208:211], v40 offset:0\n"
"ds_read_b128 v[212:215], v40 offset:1024\n"
"ds_read_b128 v[216:219], v40 offset:2048\n"
"ds_read_b128 v[220:223], v40 offset:3072\n"
"ds_read_b128 v[224:227], v41 offset:0\n"
"ds_read_b128 v[228:231], v41 offset:1024\n"
"ds_read_b128 v[232:235], v41 offset:2048\n"
"ds_read_b128 v[236:239], v41 offset:3072\n"
"ds_read_b128 v[240:243], v42 offset:0\n"
"ds_read_b128 v[244:247], v42 offset:1024\n"
"ds_read_b128 v[248:251], v42 offset:2048\n"
"ds_read_b128 v[252:255], v42 offset:3072\n"
"s_add_u32 s86, s68, 1\n"
"v_mov_b32 v28, s86\n"
"s_mov_b32 s86, 917504\n"
"s_cmp_eq_u32 s81, 0\n"
"s_cselect_b64 exec, 1, 0\n"
"buffer_store_dword v28, off, s[44:47], s86 sc1\n"
"s_mov_b64 exec, -1\n"
"v_lshlrev_b32 v160, 16, v32\n"
"v_and_b32 v161, 0xffff0000, v32\n"
"v_lshlrev_b32 v162, 16, v33\n"
"v_and_b32 v163, 0xffff0000, v33\n"
"v_lshlrev_b32 v164, 16, v34\n"
"v_and_b32 v165, 0xffff0000, v34\n"
"v_lshlrev_b32 v166, 16, v35\n"
"v_and_b32 v167, 0xffff0000, v35\n"
"v_lshlrev_b32 v168, 16, v36\n"
"v_and_b32 v169, 0xffff0000, v36\n"
"v_lshlrev_b32 v170, 16, v37\n"
"v_and_b32 v171, 0xffff0000, v37\n"
"v_lshlrev_b32 v172, 16, v38\n"
"v_and_b32 v173, 0xffff0000, v38\n"
"v_lshlrev_b32 v174, 16, v39\n"
"v_and_b32 v175, 0xffff0000, v39\n"
"v_pk_add_f32 v[192:193], v[192:193], v[160:161]\n"
"v_pk_add_f32 v[194:195], v[194:195], v[162:163]\n"
"v_pk_add_f32 v[196:197], v[196:197], v[164:165]\n"
"v_pk_add_f32 v[198:199], v[198:199], v[166:167]\n"
"v_pk_add_f32 v[200:201], v[200:201], v[168:169]\n"
"v_pk_add_f32 v[202:203], v[202:203], v[170:171]\n"
"v_pk_add_f32 v[204:205], v[204:205], v[172:173]\n"
"v_pk_add_f32 v[206:207], v[206:207], v[174:175]\n"
"s_lshl_b32 s86, s77, 2\n"
"s_add_u32 s86, s86, s61\n"
"s_mul_i32 s86, s86, 57344\n"
"s_lshl_b32 s87, s78, 11\n"
"s_add_u32 s86, s86, s87\n"
"s_add_u32 s86, s86, 67108864\n"
"v_mov_b32 v176, 0xc0135761\n"
"v_mov_b32 v178, 0xbdd2d3e8\n"
"s_lshl_b32 s87, s76, 2\n"
"s_add_u32 s87, s87, s61\n"
"s_waitcnt lgkmcnt(8)\n"
"v_pk_add_f32 v[192:193], v[192:193], v[208:209]\n"
"v_pk_add_f32 v[194:195], v[194:195], v[210:211]\n"
"v_pk_add_f32 v[196:197], v[196:197], v[212:213]\n"
"v_pk_add_f32 v[198:199], v[198:199], v[214:215]\n"
"v_pk_add_f32 v[200:201], v[200:201], v[216:217]\n"
"v_pk_add_f32 v[202:203], v[202:203], v[218:219]\n"
"v_pk_add_f32 v[204:205], v[204:205], v[220:221]\n"
"v_pk_add_f32 v[206:207], v[206:207], v[222:223]\n"
"s_waitcnt lgkmcnt(4)\n"
"v_pk_add_f32 v[192:193], v[192:193], v[224:225]\n"
"v_pk_add_f32 v[194:195], v[194:195], v[226:227]\n"
"v_pk_add_f32 v[196:197], v[196:197], v[228:229]\n"
"v_pk_add_f32 v[198:199], v[198:199], v[230:231]\n"
"v_pk_add_f32 v[200:201], v[200:201], v[232:233]\n"
"v_pk_add_f32 v[202:203], v[202:203], v[234:235]\n"
"v_pk_add_f32 v[204:205], v[204:205], v[236:237]\n"
"v_pk_add_f32 v[206:207], v[206:207], v[238:239]\n"
"s_waitcnt lgkmcnt(0)\n"
"v_pk_add_f32 v[192:193], v[192:193], v[240:241]\n"
"v_pk_add_f32 v[194:195], v[194:195], v[242:243]\n"
"v_pk_add_f32 v[196:197], v[196:197], v[244:245]\n"
"v_pk_add_f32 v[198:199], v[198:199], v[246:247]\n"
"v_pk_add_f32 v[200:201], v[200:201], v[248:249]\n"
"v_pk_add_f32 v[202:203], v[202:203], v[250:251]\n"
"v_pk_add_f32 v[204:205], v[204:205], v[252:253]\n"
"v_pk_add_f32 v[206:207], v[206:207], v[254:255]\n"
"s_barrier\n"
"v_pk_mul_f32 v[160:161], v[192:193], v[192:193]\n"
"v_pk_mul_f32 v[162:163], v[194:195], v[194:195]\n"
"v_pk_mul_f32 v[164:165], v[196:197], v[196:197]\n"
"v_pk_mul_f32 v[166:167], v[198:199], v[198:199]\n"
"v_pk_mul_f32 v[168:169], v[200:201], v[200:201]\n"
"v_pk_mul_f32 v[170:171], v[202:203], v[202:203]\n"
"v_pk_mul_f32 v[172:173], v[204:205], v[204:205]\n"
"v_pk_mul_f32 v[174:175], v[206:207], v[206:207]\n"
"v_pk_fma_f32 v[160:161], v[160:161], v[178:179], v[176:177] op_sel_hi:[1,0,0]\n"
"v_pk_fma_f32 v[162:163], v[162:163], v[178:179], v[176:177] op_sel_hi:[1,0,0]\n"
"v_pk_fma_f32 v[164:165], v[164:165], v[178:179], v[176:177] op_sel_hi:[1,0,0]\n"
"v_pk_fma_f32 v[166:167], v[166:167], v[178:179], v[176:177] op_sel_hi:[1,0,0]\n"
"v_pk_fma_f32 v[168:169], v[168:169], v[178:179], v[176:177] op_sel_hi:[1,0,0]\n"
"v_pk_fma_f32 v[170:171], v[170:171], v[178:179], v[176:177] op_sel_hi:[1,0,0]\n"
"v_pk_fma_f32 v[172:173], v[172:173], v[178:179], v[176:177] op_sel_hi:[1,0,0]\n"
"v_pk_fma_f32 v[174:175], v[174:175], v[178:179], v[176:177] op_sel_hi:[1,0,0]\n"
"v_pk_mul_f32 v[160:161], v[160:161], v[192:193]\n"
"v_pk_mul_f32 v[162:163], v[162:163], v[194:195]\n"
"v_pk_mul_f32 v[164:165], v[164:165], v[196:197]\n"
"v_pk_mul_f32 v[166:167], v[166:167], v[198:199]\n"
"v_pk_mul_f32 v[168:169], v[168:169], v[200:201]\n"
"v_pk_mul_f32 v[170:171], v[170:171], v[202:203]\n"
"v_pk_mul_f32 v[172:173], v[172:173], v[204:205]\n"
"v_pk_mul_f32 v[174:175], v[174:175], v[206:207]\n"
"v_exp_f32 v160, v160\n"
"v_exp_f32 v161, v161\n"
"v_exp_f32 v162, v162\n"
"v_exp_f32 v163, v163\n"
"v_exp_f32 v164, v164\n"
"v_exp_f32 v165, v165\n"
"v_exp_f32 v166, v166\n"
"v_exp_f32 v167, v167\n"
"v_exp_f32 v168, v168\n"
"v_exp_f32 v169, v169\n"
"v_exp_f32 v170, v170\n"
"v_exp_f32 v171, v171\n"
"v_exp_f32 v172, v172\n"
"v_exp_f32 v173, v173\n"
"v_exp_f32 v174, v174\n"
"v_exp_f32 v175, v175\n"
"v_pk_add_f32 v[160:161], v[160:161], 1.0 op_sel_hi:[1,0]\n"
"v_pk_add_f32 v[162:163], v[162:163], 1.0 op_sel_hi:[1,0]\n"
"v_pk_add_f32 v[164:165], v[164:165], 1.0 op_sel_hi:[1,0]\n"
"v_pk_add_f32 v[166:167], v[166:167], 1.0 op_sel_hi:[1,0]\n"
"v_pk_add_f32 v[168:169], v[168:169], 1.0 op_sel_hi:[1,0]\n"
"v_pk_add_f32 v[170:171], v[170:171], 1.0 op_sel_hi:[1,0]\n"
"v_pk_add_f32 v[172:173], v[172:173], 1.0 op_sel_hi:[1,0]\n"
"v_pk_add_f32 v[174:175], v[174:175], 1.0 op_sel_hi:[1,0]\n"
"v_rcp_f32 v160, v160\n"
"v_rcp_f32 v161, v161\n"
"v_rcp_f32 v162, v162\n"
"v_rcp_f32 v163, v163\n"
"v_rcp_f32 v164, v164\n"
"v_rcp_f32 v165, v165\n"
"v_rcp_f32 v166, v166\n"
"v_rcp_f32 v167, v167\n"
"v_rcp_f32 v168, v168\n"
"v_rcp_f32 v169, v169\n"
"v_rcp_f32 v170, v170\n"
"v_rcp_f32 v171, v171\n"
"v_rcp_f32 v172, v172\n"
"v_rcp_f32 v173, v173\n"
"v_rcp_f32 v174, v174\n"
"v_rcp_f32 v175, v175\n"
"v_pk_mul_f32 v[192:193], v[160:161], v[192:193]\n"
"v_pk_mul_f32 v[194:195], v[162:163], v[194:195]\n"
"v_pk_mul_f32 v[196:197], v[164:165], v[196:197]\n"
"v_pk_mul_f32 v[198:199], v[166:167], v[198:199]\n"
"v_pk_mul_f32 v[200:201], v[168:169], v[200:201]\n"
"v_pk_mul_f32 v[202:203], v[170:171], v[202:203]\n"
"v_pk_mul_f32 v[204:205], v[172:173], v[204:205]\n"
"v_pk_mul_f32 v[206:207], v[174:175], v[206:207]\n"
"v_cvt_pk_bf16_f32 v192, v192, v193\n"
"v_cvt_pk_bf16_f32 v193, v194, v195\n"
"v_cvt_pk_bf16_f32 v194, v196, v197\n"
"v_cvt_pk_bf16_f32 v195, v198, v199\n"
"v_cvt_pk_bf16_f32 v196, v200, v201\n"
"v_cvt_pk_bf16_f32 v197, v202, v203\n"
"v_cvt_pk_bf16_f32 v198, v204, v205\n"
"v_cvt_pk_bf16_f32 v199, v206, v207\n"
"buffer_store_dwordx4 v[192:195], v1, s[44:47], s86 offen offset:0 sc1\n"
"buffer_store_dwordx4 v[196:199], v1, s[44:47], s86 offen offset:1024 sc1\n"
"s_add_u32 m0, s91, 131072\n"
"s_add_u32 s85, s72, 786432\n"
"buffer_load_dwordx4 v44, s[40:43], s85 offen offset:0 lds\n"
"buffer_load_dwordx4 v45, s[40:43], s85 offen offset:1024 lds\n"
"buffer_load_dwordx4 v46, s[40:43], s85 offen offset:2048 lds\n"
"buffer_load_dwordx4 v47, s[40:43], s85 offen offset:3072 lds\n"
"s_add_u32 m0, s91, 135168\n"
"s_add_u32 s85, s93, 917504\n"
"buffer_load_dwordx4 v44, s[40:43], s85 offen offset:0 lds\n"
"buffer_load_dwordx4 v45, s[40:43], s85 offen offset:1024 lds\n"
"buffer_load_dwordx4 v46, s[40:43], s85 offen offset:2048 lds\n"
"buffer_load_dwordx4 v47, s[40:43], s85 offen offset:3072 lds\n"
"s_add_u32 m0, s91, 32768\n"
"s_add_u32 s85, s93, 1048576\n"
"buffer_load_dwordx4 v44, s[40:43], s85 offen offset:0 lds\n"
"buffer_load_dwordx4 v45, s[40:43], s85 offen offset:1024 lds\n"
"buffer_load_dwordx4 v46, s[40:43], s85 offen offset:2048 lds\n"
"buffer_load_dwordx4 v47, s[40:43], s85 offen offset:3072 lds\n"
"s_waitcnt vmcnt(12)\n"
"s_lshl_b32 s99, s87, 7\n"
"s_add_u32 s99, s99, 524288\n"
"v_mov_b32 v28, s64\n"
"v_mov_b32 v29, s65\n"
"s_mov_b64 exec, 1\n"
"buffer_store_dwordx2 v[28:29], off, s[44:47], s99 sc1\n"
"s_mov_b64 exec, -1\n"
"G2P_47_%=:\n"
"v_min_u32 v40, 27, v0\n"
"v_lshrrev_b32 v41, 2, v40\n"
"v_and_b32 v40, 3, v40\n"
"v_lshlrev_b32 v41, 12, v41\n"
"v_lshl_add_u32 v40, v40, 7, v41\n"
"s_mul_i32 s86, s61, 56\n"
"s_add_u32 s86, s86, s74\n"
"s_lshl_b32 s86, s86, 9\n"
"s_add_u32 s86, s86, 524288\n"
"v_add_u32 v40, s86, v40\n"
"s_lshl_b32 s89, s73, 1\n"
"s_add_u32 s89, s89, s66\n"
"s_lshl_b32 s86, s89, 4\n"
"s_and_b32 s87, s74, 3\n"
"s_add_u32 s86, s86, s87\n"
"s_lshl_b32 s86, s86, 7\n"
"s_add_u32 s86, s86, 393216\n"
"v_and_b32 v41, 3, v0\n"
"v_lshl_add_u32 v41, v41, 9, s86\n"
"v_cmp_gt_u32 vcc, 32, v0\n"
"s_nop 1\n"
"v_cndmask_b32 v16, v41, v40, vcc\n"
"s_add_u32 m0, s91, 36864\n"
"s_add_u32 s85, s93, 1179648\n"
"buffer_load_dwordx4 v44, s[40:43], s85 offen offset:0 lds\n"
"buffer_load_dwordx4 v45, s[40:43], s85 offen offset:1024 lds\n"
"buffer_load_dwordx4 v46, s[40:43], s85 offen offset:2048 lds\n"
"buffer_load_dwordx4 v47, s[40:43], s85 offen offset:3072 lds\n"
"s_add_u32 m0, s91, 0\n"
"s_add_u32 s85, s93, 1310720\n"
"buffer_load_dwordx4 v44, s[40:43], s85 offen offset:0 lds\n"
"buffer_load_dwordx4 v45, s[40:43], s85 offen offset:1024 lds\n"
"buffer_load_dwordx4 v46, s[40:43], s85 offen offset:2048 lds\n"
"buffer_load_dwordx4 v47, s[40:43], s85 offen offset:3072 lds\n"
"s_add_u32 m0, s91, 4096\n"
"s_add_u32 s85, s93, 1441792\n"
"buffer_load_dwordx4 v44, s[40:43], s85 offen offset:0 lds\n"
"buffer_load_dwordx4 v45, s[40:43], s85 offen offset:1024 lds\n"
"buffer_load_dwordx4 v46, s[40:43], s85 offen offset:2048 lds\n"
"buffer_load_dwordx4 v47, s[40:43], s85 offen offset:3072 lds\n"
"s_add_u32 m0, s91, 98304\n"
"s_add_u32 s85, s93, 1572864\n"
"buffer_load_dwordx4 v44, s[40:43], s85 offen offset:0 lds\n"
"buffer_load_dwordx4 v45, s[40:43], s85 offen offset:1024 lds\n"
"buffer_load_dwordx4 v46, s[40:43], s85 offen offset:2048 lds\n"
"buffer_load_dwordx4 v47, s[40:43], s85 offen offset:3072 lds\n"
"s_add_u32 m0, s91, 102400\n"
"s_add_u32 s85, s93, 1703936\n"
"buffer_load_dwordx4 v44, s[40:43], s85 offen offset:0 lds\n"
"buffer_load_dwordx4 v45, s[40:43], s85 offen offset:1024 lds\n"
"buffer_load_dwordx4 v46, s[40:43], s85 offen offset:2048 lds\n"
"buffer_load_dwordx4 v47, s[40:43], s85 offen offset:3072 lds\n"
"v_add_u32 v24, 0, v1\n"
"v_add_u32 v25, 57344, v1\n"
"v_add_u32 v26, 114688, v1\n"
"v_add_u32 v27, 172032, v1\n"
"v_mov_b32 v44, 0\n"
"s_lshl_b32 s86, s73, 7\n"
"s_lshl_b32 s87, s61, 5\n"
"s_add_u32 s86, s86, s87\n"
"v_lshlrev_b32 v40, 1, v2\n"
"buffer_load_short_d16_hi v44, v40, s[56:59], s86 offen\n"
"v_lshlrev_b32 v40, 2, v3\n"
"v_sub_u32 v40, v2, v40\n"
"s_lshl_b32 s86, s67, 2\n"
"v_cmp_gt_u32 vcc, 4, v40\n"
"v_add_u32 v40, s86, v40\n"
"s_nop 0\n"
"v_cndmask_b32 v40, 64, v40, vcc\n"
"v_mov_b32 v41, 0x3f80\n"
"v_mov_b32 v42, 0x3f800000\n"
"v_cmp_eq_u32 vcc, 0, v40\n"
"s_nop 1\n"
"v_cndmask_b32 v36, 0, v41, vcc\n"
"v_cmp_eq_u32 vcc, 1, v40\n"
"s_nop 1\n"
"v_cndmask_b32 v36, v36, v42, vcc\n"
"v_cmp_eq_u32 vcc, 2, v40\n"
"s_nop 1\n"
"v_cndmask_b32 v37, 0, v41, vcc\n"
"v_cmp_eq_u32 vcc, 3, v40\n"
"s_nop 1\n"
"v_cndmask_b32 v37, v37, v42, vcc\n"
"v_cmp_eq_u32 vcc, 4, v40\n"
"s_nop 1\n"
"v_cndmask_b32 v38, 0, v41, vcc\n"
"v_cmp_eq_u32 vcc, 5, v40\n"
"s_nop 1\n"
"v_cndmask_b32 v38, v38, v42, vcc\n"
"v_cmp_eq_u32 vcc, 6, v40\n"
"s_nop 1\n"
"v_cndmask_b32 v39, 0, v41, vcc\n"
"v_cmp_eq_u32 vcc, 7, v40\n"
"s_nop 1\n"
"v_cndmask_b32 v39, v39, v42, vcc\n"
"s_mul_i32 s90, s74, 229376\n"
"s_mul_i32 s86, s61, 14336\n"
"s_add_u32 s90, s90, s86\n"
"s_add_u32 s90, s90, 67108864\n"
"s_and_b32 s86, s73, 0\n"
"s_mul_i32 s86, s86, 7168\n"
"s_sub_u32 s94, s90, s86\n"
"s_add_u32 s90, s90, s86\n"
"s_lshl_b32 s89, s89, 10\n"
"s_lshl_b32 s86, s74, 18\n"
"s_add_u32 s89, s89, s86\n"
"s_add_u32 s89, s89, 65011712\n"
"buffer_load_dwordx2 v[20:21], v16, s[44:47], 0 offen sc1\n"
"s_waitcnt vmcnt(0)\n"
"v_cmp_ne_u64 vcc, s[64:65], v[20:21]\n"
"s_nop 1\n"
"s_cbranch_vccz FOK_50_%=\n"
"FSP_51_%=:\n"
"s_cmp_eq_u32 s69, 0\n"
"s_cbranch_scc1 FOK_50_%=\n"
"s_sub_u32 s69, s69, 1\n"
"s_sleep 2\n"
"s_mov_b64 exec, vcc\n"
"buffer_load_dwordx2 v[20:21], v16, s[44:47], 0 offen sc1\n"
"s_mov_b64 exec, -1\n"
"s_waitcnt vmcnt(0)\n"
"v_cmp_ne_u64 vcc, s[64:65], v[20:21]\n"
"s_nop 1\n"
"s_cbranch_vccnz FSP_51_%=\n"
"FOK_50_%=:\n"
"s_add_u32 s84, s90, 0\n"
"buffer_load_dwordx4 a[0:3], v24, s[44:47], s84 offen sc1\n"
"s_add_u32 s84, s90, 0\n"
"buffer_load_dwordx4 a[4:7], v25, s[44:47], s84 offen sc1\n"
"s_add_u32 s84, s90, 0\n"
"buffer_load_dwordx4 a[8:11], v26, s[44:47], s84 offen sc1\n"
"s_add_u32 s84, s90, 0\n"
"buffer_load_dwordx4 a[12:15], v27, s[44:47], s84 offen sc1\n"
"s_add_u32 s84, s90, 1024\n"
"buffer_load_dwordx4 a[16:19], v24, s[44:47], s84 offen sc1\n"
"s_add_u32 s84, s90, 1024\n"
"buffer_load_dwordx4 a[20:23], v25, s[44:47], s84 offen sc1\n"
"s_add_u32 s84, s90, 1024\n"
"buffer_load_dwordx4 a[24:27], v26, s[44:47], s84 offen sc1\n"
"s_add_u32 s84, s90, 1024\n"
"buffer_load_dwordx4 a[28:31], v27, s[44:47], s84 offen sc1\n"
"s_add_u32 s84, s90, 2048\n"
"buffer_load_dwordx4 a[32:35], v24, s[44:47], s84 offen sc1\n"
"s_add_u32 s84, s90, 2048\n"
"buffer_load_dwordx4 a[36:39], v25, s[44:47], s84 offen sc1\n"
"s_add_u32 s84, s90, 2048\n"
"buffer_load_dwordx4 a[40:43], v26, s[44:47], s84 offen sc1\n"
"s_add_u32 s84, s90, 2048\n"
"buffer_load_dwordx4 a[44:47], v27, s[44:47], s84 offen sc1\n"
"s_add_u32 s86, s91, 65536\n"
"v_add_u32 v40, s86, v1\n"
"s_add_u32 s86, s91, 69632\n"
"v_add_u32 v41, s86, v1\n"
"s_add_u32 s86, s91, 131072\n"
"v_add_u32 v42, s86, v1\n"
"s_waitcnt vmcnt(8)\n"
"v_mfma_f32_16x16x32_bf16 v[192:195], a[0:3], v[48:51], 0\n"
"s_add_u32 s84, s90, 3072\n"
"v_mfma_f32_16x16x32_bf16 v[208:211], a[0:3], v[52:55], 0\n"
"buffer_load_dwordx4 a[48:51], v24, s[44:47], s84 offen sc1\n"
"v_mfma_f32_16x16x32_bf16 v[224:227], a[0:3], v[56:59], 0\n"
"s_add_u32 s84, s90, 3072\n"
"v_mfma_f32_16x16x32_bf16 v[240:243], a[0:3], v[60:63], 0\n"
"v_mfma_f32_16x16x32_bf16 v[244:247], a[4:7], v[60:63], 0\n"
"buffer_load_dwordx4 a[52:55], v25, s[44:47], s84 offen sc1\n"
"v_mfma_f32_16x16x32_bf16 v[228:231], a[4:7], v[56:59], 0\n"
"s_add_u32 s84, s90, 3072\n"
"v_mfma_f32_16x16x32_bf16 v[212:215], a[4:7], v[52:55], 0\n"
"v_mfma_f32_16x16x32_bf16 v[196:199], a[4:7], v[48:51], 0\n"
"v_mfma_f32_16x16x32_bf16 v[200:203], a[8:11], v[48:51], 0\n"
"buffer_load_dwordx4 a[56:59], v26, s[44:47], s84 offen sc1\n"
"v_mfma_f32_16x16x32_bf16 v[216:219], a[8:11], v[52:55], 0\n"
"s_add_u32 s84, s90, 3072\n"
"v_mfma_f32_16x16x32_bf16 v[232:235], a[8:11], v[56:59], 0\n"
"v_mfma_f32_16x16x32_bf16 v[248:251], a[8:11], v[60:63], 0\n"
"v_mfma_f32_16x16x32_bf16 v[252:255], a[12:15], v[60:63], 0\n"
"buffer_load_dwordx4 a[60:63], v27, s[44:47], s84 offen sc1\n"
"v_mfma_f32_16x16x32_bf16 v[236:239], a[12:15], v[56:59], 0\n"
"v_mfma_f32_16x16x32_bf16 v[220:223], a[12:15], v[52:55], 0\n"
"v_mfma_f32_16x16x32_bf16 v[204:207], a[12:15], v[48:51], 0\n"
"s_waitcnt vmcnt(8)\n"
"v_mfma_f32_16x16x32_bf16 v[192:195], a[16:19], v[64:67], v[192:195]\n"
"s_add_u32 s84, s90, 4096\n"
"v_mfma_f32_16x16x32_bf16 v[208:211], a[16:19], v[68:71], v[208:211]\n"
"buffer_load_dwordx4 a[64:67], v24, s[44:47], s84 offen sc1\n"
"v_mfma_f32_16x16x32_bf16 v[224:227], a[16:19], v[72:75], v[224:227]\n"
"s_add_u32 s84, s90, 4096\n"
"v_mfma_f32_16x16x32_bf16 v[240:243], a[16:19], v[76:79], v[240:243]\n"
"v_mfma_f32_16x16x32_bf16 v[244:247], a[20:23], v[76:79], v[244:247]\n"
"buffer_load_dwordx4 a[68:71], v25, s[44:47], s84 offen sc1\n"
"v_mfma_f32_16x16x32_bf16 v[228:231], a[20:23], v[72:75], v[228:231]\n"
"s_add_u32 s84, s90, 4096\n"
"v_mfma_f32_16x16x32_bf16 v[212:215], a[20:23], v[68:71], v[212:215]\n"
"v_mfma_f32_16x16x32_bf16 v[196:199], a[20:23], v[64:67], v[196:199]\n"
"v_mfma_f32_16x16x32_bf16 v[200:203], a[24:27], v[64:67], v[200:203]\n"
"buffer_load_dwordx4 a[72:75], v26, s[44:47], s84 offen sc1\n"
"v_mfma_f32_16x16x32_bf16 v[216:219], a[24:27], v[68:71], v[216:219]\n"
"s_add_u32 s84, s90, 4096\n"
"v_mfma_f32_16x16x32_bf16 v[232:235], a[24:27], v[72:75], v[232:235]\n"
"v_mfma_f32_16x16x32_bf16 v[248:251], a[24:27], v[76:79], v[248:251]\n"
"v_mfma_f32_16x16x32_bf16 v[252:255], a[28:31], v[76:79], v[252:255]\n"
"buffer_load_dwordx4 a[76:79], v27, s[44:47], s84 offen sc1\n"
"v_mfma_f32_16x16x32_bf16 v[236:239], a[28:31], v[72:75], v[236:239]\n"
"v_mfma_f32_16x16x32_bf16 v[220:223], a[28:31], v[68:71], v[220:223]\n"
"v_mfma_f32_16x16x32_bf16 v[204:207], a[28:31], v[64:67], v[204:207]\n"
"s_waitcnt vmcnt(8)\n"
"v_mfma_f32_16x16x32_bf16 v[192:195], a[32:35], v[80:83], v[192:195]\n"
"s_add_u32 s84, s90, 5120\n"
"ds_read_b64_tr_b16 v[160:161], v8 offset:0\n"
"ds_read_b64_tr_b16 v[162:163], v8 offset:2048\n"
"v_mfma_f32_16x16x32_bf16 v[208:211], a[32:35], v[84:87], v[208:211]\n"
"ds_read_b64_tr_b16 v[164:165], v9 offset:0\n"
"ds_read_b64_tr_b16 v[166:167], v9 offset:2048\n"
"v_mfma_f32_16x16x32_bf16 v[224:227], a[32:35], v[88:91], v[224:227]\n"
"ds_read_b64_tr_b16 v[168:169], v10 offset:0\n"
"ds_read_b64_tr_b16 v[170:171], v10 offset:2048\n"
"v_mfma_f32_16x16x32_bf16 v[240:243], a[32:35], v[92:95], v[240:243]\n"
"ds_read_b64_tr_b16 v[172:173], v11 offset:0\n"
"ds_read_b64_tr_b16 v[174:175], v11 offset:2048\n"
"v_mfma_f32_16x16x32_bf16 v[244:247], a[36:39], v[92:95], v[244:247]\n"
"buffer_load_dwordx4 a[80:83], v24, s[44:47], s84 offen sc1\n"
"s_nop 0\n"
"PG19_%=:\n"
"s_cbranch_execz PADRET_%=\n"
"v_mfma_f32_16x16x32_bf16 v[228:231], a[36:39], v[88:91], v[228:231]\n"
"s_add_u32 s84, s90, 5120\n"
"v_mfma_f32_16x16x32_bf16 v[212:215], a[36:39], v[84:87], v[212:215]\n"
"buffer_load_dwordx4 a[84:87], v25, s[44:47], s84 offen sc1\n"
"v_mfma_f32_16x16x32_bf16 v[196:199], a[36:39], v[80:83], v[196:199]\n"
"s_add_u32 s84, s90, 5120\n"
"v_mfma_f32_16x16x32_bf16 v[200:203], a[40:43], v[80:83], v[200:203]\n"
"buffer_load_dwordx4 a[88:91], v26, s[44:47], s84 offen sc1\n"
"v_mfma_f32_16x16x32_bf16 v[216:219], a[40:43], v[84:87], v[216:219]\n"
"s_add_u32 s84, s90, 5120\n"
"v_mfma_f32_16x16x32_bf16 v[232:235], a[40:43], v[88:91], v[232:235]\n"
"v_mfma_f32_16x16x32_bf16 v[248:251], a[40:43], v[92:95], v[248:251]\n"
"v_mfma_f32_16x16x32_bf16 v[252:255], a[44:47], v[92:95], v[252:255]\n"
"buffer_load_dwordx4 a[92:95], v27, s[44:47], s84 offen sc1\n"
"v_mfma_f32_16x16x32_bf16 v[236:239], a[44:47], v[88:91], v[236:239]\n"
"v_mfma_f32_16x16x32_bf16 v[220:223], a[44:47], v[84:87], v[220:223]\n"
"v_mfma_f32_16x16x32_bf16 v[204:207], a[44:47], v[80:83], v[204:207]\n"
"s_waitcnt vmcnt(8)\n"
"v_mfma_f32_16x16x32_bf16 v[192:195], a[48:51], v[96:99], v[192:195]\n"
"s_add_u32 s84, s90, 6144\n"
"ds_read_b64_tr_b16 v[176:177], v8 offset:4096\n"
"ds_read_b64_tr_b16 v[178:179], v8 offset:6144\n"
"v_mfma_f32_16x16x32_bf16 v[208:211], a[48:51], v[100:103], v[208:211]\n"
"ds_read_b64_tr_b16 v[180:181], v9 offset:4096\n"
"ds_read_b64_tr_b16 v[182:183], v9 offset:6144\n"
"v_mfma_f32_16x16x32_bf16 v[224:227], a[48:51], v[104:107], v[224:227]\n"
"ds_read_b64_tr_b16 v[184:185], v10 offset:4096\n"
"ds_read_b64_tr_b16 v[186:187], v10 offset:6144\n"
"v_mfma_f32_16x16x32_bf16 v[240:243], a[48:51], v[108:111], v[240:243]\n"
"ds_read_b64_tr_b16 v[188:189], v11 offset:4096\n"
"ds_read_b64_tr_b16 v[190:191], v11 offset:6144\n"
"v_mfma_f32_16x16x32_bf16 v[244:247], a[52:55], v[108:111], v[244:247]\n"
"buffer_load_dwordx4 a[96:99], v24, s[44:47], s84 offen sc1\n"
"v_mfma_f32_16x16x32_bf16 v[228:231], a[52:55], v[104:107], v[228:231]\n"
"s_add_u32 s84, s90, 6144\n"
"v_mfma_f32_16x16x32_bf16 v[212:215], a[52:55], v[100:103], v[212:215]\n"
"buffer_load_dwordx4 a[100:103], v25, s[44:47], s84 offen sc1\n"
"v_mfma_f32_16x16x32_bf16 v[196:199], a[52:55], v[96:99], v[196:199]\n"
"s_add_u32 s84, s90, 6144\n"
"v_mfma_f32_16x16x32_bf16 v[200:203], a[56:59], v[96:99], v[200:203]\n"
"buffer_load_dwordx4 a[104:107], v26, s[44:47], s84 offen sc1\n"
"v_mfma_f32_16x16x32_bf16 v[216:219], a[56:59], v[100:103], v[216:219]\n"
"s_add_u32 s84, s90, 6144\n"
"v_mfma_f32_16x16x32_bf16 v[232:235], a[56:59], v[104:107], v[232:235]\n"
"v_mfma_f32_16x16x32_bf16 v[248:251], a[56:59], v[108:111], v[248:251]\n"
"v_mfma_f32_16x16x32_bf16 v[252:255], a[60:63], v[108:111], v[252:255]\n"
"buffer_load_dwordx4 a[108:111], v27, s[44:47], s84 offen sc1\n"
"v_mfma_f32_16x16x32_bf16 v[236:239], a[60:63], v[104:107], v[236:239]\n"
"v_mfma_f32_16x16x32_bf16 v[220:223], a[60:63], v[100:103], v[220:223]\n"
"v_mfma_f32_16x16x32_bf16 v[204:207], a[60:63], v[96:99], v[204:207]\n"
"s_waitcnt vmcnt(8)\n"
"s_waitcnt lgkmcnt(14)\n"
"v_mfma_f32_16x16x32_bf16 v[192:195], a[64:67], v[160:163], v[192:195]\n"
"s_add_u32 s84, s94, 7168\n"
"ds_read_b64_tr_b16 a[192:193], v12 offset:0\n"
"ds_read_b64_tr_b16 a[194:195], v12 offset:2048\n"
"s_waitcnt lgkmcnt(14)\n"
"v_mfma_f32_16x16x32_bf16 v[208:211], a[64:67], v[164:167], v[208:211]\n"
"s_waitcnt lgkmcnt(12)\n"
"ds_read_b64_tr_b16 a[196:197], v13 offset:0\n"
"ds_read_b64_tr_b16 a[198:199], v13 offset:2048\n"
"v_mfma_f32_16x16x32_bf16 v[224:227], a[64:67], v[168:171], v[224:227]\n"
"s_waitcnt lgkmcnt(12)\n"
"ds_read_b64_tr_b16 a[200:201], v14 offset:0\n"
"ds_read_b64_tr_b16 a[202:203], v14 offset:2048\n"
"v_mfma_f32_16x16x32_bf16 v[240:243], a[64:67], v[172:175], v[240:243]\n"
"ds_read_b64_tr_b16 a[204:205], v15 offset:0\n"
"ds_read_b64_tr_b16 a[206:207], v15 offset:2048\n"
"v_mfma_f32_16x16x32_bf16 v[244:247], a[68:71], v[172:175], v[244:247]\n"
"buffer_load_dwordx4 a[0:3], v24, s[44:47], s84 offen sc1\n"
"v_mfma_f32_16x16x32_bf16 v[228:231], a[68:71], v[168:171], v[228:231]\n"
"s_add_u32 s84, s94, 7168\n"
"v_mfma_f32_16x16x32_bf16 v[212:215], a[68:71], v[164:167], v[212:215]\n"
"buffer_load_dwordx4 a[4:7], v25, s[44:47], s84 offen sc1\n"
"v_mfma_f32_16x16x32_bf16 v[196:199], a[68:71], v[160:163], v[196:199]\n"
"s_add_u32 s84, s94, 7168\n"
"v_mfma_f32_16x16x32_bf16 v[200:203], a[72:75], v[160:163], v[200:203]\n"
"buffer_load_dwordx4 a[8:11], v26, s[44:47], s84 offen sc1\n"
"v_mfma_f32_16x16x32_bf16 v[216:219], a[72:75], v[164:167], v[216:219]\n"
"s_add_u32 s84, s94, 7168\n"
"v_mfma_f32_16x16x32_bf16 v[232:235], a[72:75], v[168:171], v[232:235]\n"
"v_mfma_f32_16x16x32_bf16 v[248:251], a[72:75], v[172:175], v[248:251]\n"
"v_mfma_f32_16x16x32_bf16 v[252:255], a[76:79], v[172:175], v[252:255]\n"
"buffer_load_dwordx4 a[12:15], v27, s[44:47], s84 offen sc1\n"
"v_mfma_f32_16x16x32_bf16 v[236:239], a[76:79], v[168:171], v[236:239]\n"
"v_mfma_f32_16x16x32_bf16 v[220:223], a[76:79], v[164:167], v[220:223]\n"
"v_mfma_f32_16x16x32_bf16 v[204:207], a[76:79], v[160:163], v[204:207]\n"
"s_waitcnt vmcnt(8)\n"
"s_waitcnt lgkmcnt(14)\n"
"v_mfma_f32_16x16x32_bf16 v[192:195], a[80:83], v[176:179], v[192:195]\n"
"s_add_u32 s84, s94, 8192\n"
"ds_read_b64_tr_b16 a[208:209], v12 offset:4096\n"
"ds_read_b64_tr_b16 a[210:211], v12 offset:6144\n"
"s_waitcnt lgkmcnt(14)\n"
"v_mfma_f32_16x16x32_bf16 v[208:211], a[80:83], v[180:183], v[208:211]\n"
"s_waitcnt lgkmcnt(12)\n"
"ds_read_b64_tr_b16 a[212:213], v13 offset:4096\n"
"ds_read_b64_tr_b16 a[214:215], v13 offset:6144\n"
"v_mfma_f32_16x16x32_bf16 v[224:227], a[80:83], v[184:187], v[224:227]\n"
"s_waitcnt lgkmcnt(12)\n"
"ds_read_b64_tr_b16 a[216:217], v14 offset:4096\n"
"ds_read_b64_tr_b16 a[218:219], v14 offset:6144\n"
"v_mfma_f32_16x16x32_bf16 v[240:243], a[80:83], v[188:191], v[240:243]\n"
"ds_read_b64_tr_b16 a[220:221], v15 offset:4096\n"
"ds_read_b64_tr_b16 a[222:223], v15 offset:6144\n"
"v_mfma_f32_16x16x32_bf16 v[244:247], a[84:87], v[188:191], v[244:247]\n"
"buffer_load_dwordx4 a[16:19], v24, s[44:47], s84 offen sc1\n"
"v_mfma_f32_16x16x32_bf16 v[228:231], a[84:87], v[184:187], v[228:231]\n"
"s_add_u32 s84, s94, 8192\n"
"v_mfma_f32_16x16x32_bf16 v[212:215], a[84:87], v[180:183], v[212:215]\n"
"buffer_load_dwordx4 a[20:23], v25, s[44:47], s84 offen sc1\n"
"v_mfma_f32_16x16x32_bf16 v[196:199], a[84:87], v[176:179], v[196:199]\n"
"s_add_u32 s84, s94, 8192\n"
"v_mfma_f32_16x16x32_bf16 v[200:203], a[88:91], v[176:179], v[200:203]\n"
"buffer_load_dwordx4 a[24:27], v26, s[44:47], s84 offen sc1\n"
"v_mfma_f32_16x16x32_bf16 v[216:219], a[88:91], v[180:183], v[216:219]\n"
"s_add_u32 s84, s94, 8192\n"
"v_mfma_f32_16x16x32_bf16 v[232:235], a[88:91], v[184:187], v[232:235]\n"
"buffer_load_dwordx4 a[28:31], v27, s[44:47], s84 offen sc1\n"
"v_mfma_f32_16x16x32_bf16 v[248:251], a[88:91], v[188:191], v[248:251]\n"
"s_add_u32 s84, s89, 0\n"
"v_mfma_f32_16x16x32_bf16 v[252:255], a[92:95], v[188:191], v[252:255]\n"
"buffer_load_dwordx4 a[240:243], v1, s[44:47], s84 offen sc1\n"
"v_mfma_f32_16x16x32_bf16 v[236:239], a[92:95], v[184:187], v[236:239]\n"
"s_add_u32 s84, s89, 65536\n"
"v_mfma_f32_16x16x32_bf16 v[220:223], a[92:95], v[180:183], v[220:223]\n"
"buffer_load_dwordx4 a[244:247], v1, s[44:47], s84 offen sc1\n"
"v_mfma_f32_16x16x32_bf16 v[204:207], a[92:95], v[176:179], v[204:207]\n"
"s_add_u32 s84, s89, 131072\n"
"buffer_load_dwordx4 a[248:251], v1, s[44:47], s84 offen sc1\n"
"s_add_u32 s84, s89, 196608\n"
"buffer_load_dwordx4 a[252:255], v1, s[44:47], s84 offen sc1\n"
"s_waitcnt vmcnt(12)\n"
"s_waitcnt lgkmcnt(14)\n"
"v_mfma_f32_16x16x32_bf16 v[192:195], a[96:99], a[192:195], v[192:195]\n"
"s_add_u32 s84, s94, 9216\n"
"ds_read_b64_tr_b16 a[224:225], v4 offset:32768\n"
"ds_read_b64_tr_b16 a[226:227], v4 offset:34816\n"
"s_waitcnt lgkmcnt(14)\n"
"v_mfma_f32_16x16x32_bf16 v[208:211], a[96:99], a[196:199], v[208:211]\n"
"s_waitcnt lgkmcnt(12)\n"
"ds_read_b64_tr_b16 a[228:229], v5 offset:32768\n"
"ds_read_b64_tr_b16 a[230:231], v5 offset:34816\n"
"v_mfma_f32_16x16x32_bf16 v[224:227], a[96:99], a[200:203], v[224:227]\n"
"s_waitcnt lgkmcnt(12)\n"
"ds_read_b64_tr_b16 a[232:233], v6 offset:32768\n"
"ds_read_b64_tr_b16 a[234:235], v6 offset:34816\n"
"v_mfma_f32_16x16x32_bf16 v[240:243], a[96:99], a[204:207], v[240:243]\n"
"ds_read_b64_tr_b16 a[236:237], v7 offset:32768\n"
"ds_read_b64_tr_b16 a[238:239], v7 offset:34816\n"
"v_mfma_f32_16x16x32_bf16 v[244:247], a[100:103], a[204:207], v[244:247]\n"
"buffer_load_dwordx4 a[32:35], v24, s[44:47], s84 offen sc1\n"
"v_mfma_f32_16x16x32_bf16 v[228:231], a[100:103], a[200:203], v[228:231]\n"
"s_add_u32 s84, s94, 9216\n"
"v_mfma_f32_16x16x32_bf16 v[212:215], a[100:103], a[196:199], v[212:215]\n"
"buffer_load_dwordx4 a[36:39], v25, s[44:47], s84 offen sc1\n"
"v_mfma_f32_16x16x32_bf16 v[196:199], a[100:103], a[192:195], v[196:199]\n"
"s_add_u32 s84, s94, 9216\n"
"v_mfma_f32_16x16x32_bf16 v[200:203], a[104:107], a[192:195], v[200:203]\n"
"buffer_load_dwordx4 a[40:43], v26, s[44:47], s84 offen sc1\n"
"v_mfma_f32_16x16x32_bf16 v[216:219], a[104:107], a[196:199], v[216:219]\n"
"s_add_u32 s84, s94, 9216\n"
"v_mfma_f32_16x16x32_bf16 v[232:235], a[104:107], a[200:203], v[232:235]\n"
"v_mfma_f32_16x16x32_bf16 v[248:251], a[104:107], a[204:207], v[248:251]\n"
"v_mfma_f32_16x16x32_bf16 v[252:255], a[108:111], a[204:207], v[252:255]\n"
"buffer_load_dwordx4 a[44:47], v27, s[44:47], s84 offen sc1\n"
"v_mfma_f32_16x16x32_bf16 v[236:239], a[108:111], a[200:203], v[236:239]\n"
"v_mfma_f32_16x16x32_bf16 v[220:223], a[108:111], a[196:199], v[220:223]\n"
"v_mfma_f32_16x16x32_bf16 v[204:207], a[108:111], a[192:195], v[204:207]\n"
"s_waitcnt vmcnt(12)\n"
"s_waitcnt lgkmcnt(14)\n"
"v_mfma_f32_16x16x32_bf16 v[192:195], a[0:3], a[208:211], v[192:195]\n"
"s_add_u32 s84, s94, 10240\n"
"ds_read_b64_tr_b16 v[160:161], v4 offset:36864\n"
"ds_read_b64_tr_b16 v[162:163], v4 offset:38912\n"
"s_waitcnt lgkmcnt(14)\n"
"v_mfma_f32_16x16x32_bf16 v[208:211], a[0:3], a[212:215], v[208:211]\n"
"s_waitcnt lgkmcnt(12)\n"
"ds_read_b64_tr_b16 v[164:165], v5 offset:36864\n"
"ds_read_b64_tr_b16 v[166:167], v5 offset:38912\n"
"v_mfma_f32_16x16x32_bf16 v[224:227], a[0:3], a[216:219], v[224:227]\n"
"s_waitcnt lgkmcnt(12)\n"
"ds_read_b64_tr_b16 v[168:169], v6 offset:36864\n"
"ds_read_b64_tr_b16 v[170:171], v6 offset:38912\n"
"v_mfma_f32_16x16x32_bf16 v[240:243], a[0:3], a[220:223], v[240:243]\n"
"ds_read_b64_tr_b16 v[172:173], v7 offset:36864\n"
"ds_read_b64_tr_b16 v[174:175], v7 offset:38912\n"
"v_mfma_f32_16x16x32_bf16 v[244:247], a[4:7], a[220:223], v[244:247]\n"
"buffer_load_dwordx4 a[48:51], v24, s[44:47], s84 offen sc1\n"
"v_mfma_f32_16x16x32_bf16 v[228:231], a[4:7], a[216:219], v[228:231]\n"
"s_add_u32 s84, s94, 10240\n"
"v_mfma_f32_16x16x32_bf16 v[212:215], a[4:7], a[212:215], v[212:215]\n"
"buffer_load_dwordx4 a[52:55], v25, s[44:47], s84 offen sc1\n"
"v_mfma_f32_16x16x32_bf16 v[196:199], a[4:7], a[208:211], v[196:199]\n"
"s_add_u32 s84, s94, 10240\n"
"v_mfma_f32_16x16x32_bf16 v[200:203], a[8:11], a[208:211], v[200:203]\n"
"buffer_load_dwordx4 a[56:59], v26, s[44:47], s84 offen sc1\n"
"v_mfma_f32_16x16x32_bf16 v[216:219], a[8:11], a[212:215], v[216:219]\n"
"s_add_u32 s84, s94, 10240\n"
"v_mfma_f32_16x16x32_bf16 v[232:235], a[8:11], a[216:219], v[232:235]\n"
"v_mfma_f32_16x16x32_bf16 v[248:251], a[8:11], a[220:223], v[248:251]\n"
"v_mfma_f32_16x16x32_bf16 v[252:255], a[12:15], a[220:223], v[252:255]\n"
"buffer_load_dwordx4 a[60:63], v27, s[44:47], s84 offen sc1\n"
"v_mfma_f32_16x16x32_bf16 v[236:239], a[12:15], a[216:219], v[236:239]\n"
"v_mfma_f32_16x16x32_bf16 v[220:223], a[12:15], a[212:215], v[220:223]\n"
"v_mfma_f32_16x16x32_bf16 v[204:207], a[12:15], a[208:211], v[204:207]\n"
"s_waitcnt vmcnt(12)\n"
"s_waitcnt lgkmcnt(14)\n"
"v_mfma_f32_16x16x32_bf16 v[192:195], a[16:19], a[224:227], v[192:195]\n"
"s_add_u32 s84, s94, 11264\n"
"ds_read_b64_tr_b16 v[176:177], v4 offset:0\n"
"ds_read_b64_tr_b16 v[178:179], v4 offset:2048\n"
"s_waitcnt lgkmcnt(14)\n"
"v_mfma_f32_16x16x32_bf16 v[208:211], a[16:19], a[228:231], v[208:211]\n"
"s_waitcnt lgkmcnt(12)\n"
"ds_read_b64_tr_b16 v[180:181], v5 offset:0\n"
"ds_read_b64_tr_b16 v[182:183], v5 offset:2048\n"
"v_mfma_f32_16x16x32_bf16 v[224:227], a[16:19], a[232:235], v[224:227]\n"
"s_waitcnt lgkmcnt(12)\n"
"ds_read_b64_tr_b16 v[184:185], v6 offset:0\n"
"ds_read_b64_tr_b16 v[186:187], v6 offset:2048\n"
"v_mfma_f32_16x16x32_bf16 v[240:243], a[16:19], a[236:239], v[240:243]\n"
"ds_read_b64_tr_b16 v[188:189], v7 offset:0\n"
"ds_read_b64_tr_b16 v[190:191], v7 offset:2048\n"
"v_mfma_f32_16x16x32_bf16 v[244:247], a[20:23], a[236:239], v[244:247]\n"
"ds_read_b64_tr_b16 a[192:193], v4 offset:4096\n"
"ds_read_b64_tr_b16 a[194:195], v4 offset:6144\n"
"v_mfma_f32_16x16x32_bf16 v[228:231], a[20:23], a[232:235], v[228:231]\n"
"ds_read_b64_tr_b16 a[196:197], v5 offset:4096\n"
"ds_read_b64_tr_b16 a[198:199], v5 offset:6144\n"
"v_mfma_f32_16x16x32_bf16 v[212:215], a[20:23], a[228:231], v[212:215]\n"
"ds_read_b64_tr_b16 a[200:201], v6 offset:4096\n"
"ds_read_b64_tr_b16 a[202:203], v6 offset:6144\n"
"v_mfma_f32_16x16x32_bf16 v[196:199], a[20:23], a[224:227], v[196:199]\n"
"ds_read_b64_tr_b16 a[204:205], v7 offset:4096\n"
"ds_read_b64_tr_b16 a[206:207], v7 offset:6144\n"
"v_mfma_f32_16x16x32_bf16 v[200:203], a[24:27], a[224:227], v[200:203]\n"
"buffer_load_dwordx4 a[64:67], v24, s[44:47], s84 offen sc1\n"
"v_mfma_f32_16x16x32_bf16 v[216:219], a[24:27], a[228:231], v[216:219]\n"
"s_add_u32 s84, s94, 12288\n"
"v_mfma_f32_16x16x32_bf16 v[232:235], a[24:27], a[232:235], v[232:235]\n"
"buffer_load_dwordx4 a[80:83], v24, s[44:47], s84 offen sc1\n"
"v_mfma_f32_16x16x32_bf16 v[248:251], a[24:27], a[236:239], v[248:251]\n"
"s_add_u32 s84, s94, 13312\n"
"v_mfma_f32_16x16x32_bf16 v[252:255], a[28:31], a[236:239], v[252:255]\n"
"buffer_load_dwordx4 a[96:99], v24, s[44:47], s84 offen sc1\n"
"v_mfma_f32_16x16x32_bf16 v[236:239], a[28:31], a[232:235], v[236:239]\n"
"s_add_u32 s84, s94, 11264\n"
"v_mfma_f32_16x16x32_bf16 v[220:223], a[28:31], a[228:231], v[220:223]\n"
"buffer_load_dwordx4 a[68:71], v25, s[44:47], s84 offen sc1\n"
"v_mfma_f32_16x16x32_bf16 v[204:207], a[28:31], a[224:227], v[204:207]\n"
"s_waitcnt vmcnt(8)\n"
"s_waitcnt lgkmcnt(15)\n"
"v_mfma_f32_16x16x32_bf16 v[192:195], a[32:35], v[160:163], v[192:195]\n"
"s_add_u32 s84, s94, 12288\n"
"ds_read_b64_tr_b16 a[208:209], v8 offset:32768\n"
"ds_read_b64_tr_b16 a[210:211], v8 offset:34816\n"
"s_waitcnt lgkmcnt(15)\n"
"v_mfma_f32_16x16x32_bf16 v[208:211], a[32:35], v[164:167], v[208:211]\n"
"s_waitcnt lgkmcnt(15)\n"
"ds_read_b64_tr_b16 a[212:213], v9 offset:32768\n"
"ds_read_b64_tr_b16 a[214:215], v9 offset:34816\n"
"v_mfma_f32_16x16x32_bf16 v[224:227], a[32:35], v[168:171], v[224:227]\n"
"s_waitcnt lgkmcnt(15)\n"
"ds_read_b64_tr_b16 a[216:217], v10 offset:32768\n"
"ds_read_b64_tr_b16 a[218:219], v10 offset:34816\n"
"v_mfma_f32_16x16x32_bf16 v[240:243], a[32:35], v[172:175], v[240:243]\n"
"ds_read_b64_tr_b16 a[220:221], v11 offset:32768\n"
"ds_read_b64_tr_b16 a[222:223], v11 offset:34816\n"
"v_mfma_f32_16x16x32_bf16 v[244:247], a[36:39], v[172:175], v[244:247]\n"
"ds_read_b64_tr_b16 a[224:225], v8 offset:36864\n"
"ds_read_b64_tr_b16 a[226:227], v8 offset:38912\n"
"v_mfma_f32_16x16x32_bf16 v[228:231], a[36:39], v[168:171], v[228:231]\n"
"ds_read_b64_tr_b16 a[228:229], v9 offset:36864\n"
"ds_read_b64_tr_b16 a[230:231], v9 offset:38912\n"
"v_mfma_f32_16x16x32_bf16 v[212:215], a[36:39], v[164:167], v[212:215]\n"
"ds_read_b64_tr_b16 a[232:233], v10 offset:36864\n"
"ds_read_b64_tr_b16 a[234:235], v10 offset:38912\n"
"v_mfma_f32_16x16x32_bf16 v[196:199], a[36:39], v[160:163], v[196:199]\n"
"ds_read_b64_tr_b16 a[236:237], v11 offset:36864\n"
"ds_read_b64_tr_b16 a[238:239], v11 offset:38912\n"
"v_mfma_f32_16x16x32_bf16 v[200:203], a[40:43], v[160:163], v[200:203]\n"
"buffer_load_dwordx4 a[84:87], v25, s[44:47], s84 offen sc1\n"
"v_mfma_f32_16x16x32_bf16 v[216:219], a[40:43], v[164:167], v[216:219]\n"
"s_add_u32 s84, s94, 13312\n"
"v_mfma_f32_16x16x32_bf16 v[232:235], a[40:43], v[168:171], v[232:235]\n"
"buffer_load_dwordx4 a[100:103], v25, s[44:47], s84 offen sc1\n"
"v_mfma_f32_16x16x32_bf16 v[248:251], a[40:43], v[172:175], v[248:251]\n"
"s_add_u32 s84, s94, 11264\n"
"v_mfma_f32_16x16x32_bf16 v[252:255], a[44:47], v[172:175], v[252:255]\n"
"buffer_load_dwordx4 a[72:75], v26, s[44:47], s84 offen sc1\n"
"v_mfma_f32_16x16x32_bf16 v[236:239], a[44:47], v[168:171], v[236:239]\n"
"s_add_u32 s84, s94, 12288\n"
"v_mfma_f32_16x16x32_bf16 v[220:223], a[44:47], v[164:167], v[220:223]\n"
"buffer_load_dwordx4 a[88:91], v26, s[44:47], s84 offen sc1\n"
"v_mfma_f32_16x16x32_bf16 v[204:207], a[44:47], v[160:163], v[204:207]\n"
"s_waitcnt vmcnt(8)\n"
"s_waitcnt lgkmcnt(15)\n"
"v_mfma_f32_16x16x32_bf16 v[192:195], a[48:51], v[176:179], v[192:195]\n"
"s_add_u32 s84, s94, 13312\n"
"s_waitcnt lgkmcnt(15)\n"
"v_mfma_f32_16x16x32_bf16 v[208:211], a[48:51], v[180:183], v[208:211]\n"
"buffer_load_dwordx4 a[104:107], v26, s[44:47], s84 offen sc1\n"
"s_waitcnt lgkmcnt(15)\n"
"v_mfma_f32_16x16x32_bf16 v[224:227], a[48:51], v[184:187], v[224:227]\n"
"s_add_u32 s84, s94, 11264\n"
"s_waitcnt lgkmcnt(15)\n"
"v_mfma_f32_16x16x32_bf16 v[240:243], a[48:51], v[188:191], v[240:243]\n"
"v_mfma_f32_16x16x32_bf16 v[244:247], a[52:55], v[188:191], v[244:247]\n"
"buffer_load_dwordx4 a[76:79], v27, s[44:47], s84 offen sc1\n"
"v_mfma_f32_16x16x32_bf16 v[228:231], a[52:55], v[184:187], v[228:231]\n"
"s_add_u32 s84, s94, 12288\n"
"v_mfma_f32_16x16x32_bf16 v[212:215], a[52:55], v[180:183], v[212:215]\n"
"v_mfma_f32_16x16x32_bf16 v[196:199], a[52:55], v[176:179], v[196:199]\n"
"v_mfma_f32_16x16x32_bf16 v[200:203], a[56:59], v[176:179], v[200:203]\n"
"buffer_load_dwordx4 a[92:95], v27, s[44:47], s84 offen sc1\n"
"v_mfma_f32_16x16x32_bf16 v[216:219], a[56:59], v[180:183], v[216:219]\n"
"s_add_u32 s84, s94, 13312\n"
"v_mfma_f32_16x16x32_bf16 v[232:235], a[56:59], v[184:187], v[232:235]\n"
"v_mfma_f32_16x16x32_bf16 v[248:251], a[56:59], v[188:191], v[248:251]\n"
"v_mfma_f32_16x16x32_bf16 v[252:255], a[60:63], v[188:191], v[252:255]\n"
"buffer_load_dwordx4 a[108:111], v27, s[44:47], s84 offen sc1\n"
"v_mfma_f32_16x16x32_bf16 v[236:239], a[60:63], v[184:187], v[236:239]\n"
"v_mfma_f32_16x16x32_bf16 v[220:223], a[60:63], v[180:183], v[220:223]\n"
"v_mfma_f32_16x16x32_bf16 v[204:207], a[60:63], v[176:179], v[204:207]\n"
"s_waitcnt vmcnt(9)\n"
"s_waitcnt lgkmcnt(15)\n"
"v_mfma_f32_16x16x32_bf16 v[192:195], a[64:67], a[192:195], v[192:195]\n"
"s_waitcnt lgkmcnt(14)\n"
"v_mfma_f32_16x16x32_bf16 v[192:195], a[80:83], a[208:211], v[192:195]\n"
"s_waitcnt lgkmcnt(6)\n"
"v_mfma_f32_16x16x32_bf16 v[192:195], a[96:99], a[224:227], v[192:195]\n"
"s_waitcnt lgkmcnt(4)\n"
"v_mfma_f32_16x16x32_bf16 v[208:211], a[96:99], a[228:231], v[208:211]\n"
"v_mfma_f32_16x16x32_bf16 v[208:211], a[80:83], a[212:215], v[208:211]\n"
"v_mfma_f32_16x16x32_bf16 v[208:211], a[64:67], a[196:199], v[208:211]\n"
"v_mfma_f32_16x16x32_bf16 v[224:227], a[64:67], a[200:203], v[224:227]\n"
"v_mfma_f32_16x16x32_bf16 v[224:227], a[80:83], a[216:219], v[224:227]\n"
"s_waitcnt lgkmcnt(2)\n"
"v_mfma_f32_16x16x32_bf16 v[224:227], a[96:99], a[232:235], v[224:227]\n"
"s_waitcnt lgkmcnt(0)\n"
"v_mfma_f32_16x16x32_bf16 v[240:243], a[96:99], a[236:239], v[240:243]\n"
"v_mfma_f32_16x16x32_bf16 v[240:243], a[80:83], a[220:223], v[240:243]\n"
"v_mfma_f32_16x16x32_bf16 v[240:243], a[64:67], a[204:207], v[240:243]\n"
"s_waitcnt vmcnt(6)\n"
"v_mfma_f32_16x16x32_bf16 v[196:199], a[68:71], a[192:195], v[196:199]\n"
"v_mfma_f32_16x16x32_bf16 v[196:199], a[84:87], a[208:211], v[196:199]\n"
"v_mfma_f32_16x16x32_bf16 v[196:199], a[100:103], a[224:227], v[196:199]\n"
"ds_write_b128 v40, v[208:211] offset:0\n"
"v_mfma_f32_16x16x32_bf16 v[212:215], a[100:103], a[228:231], v[212:215]\n"
"v_mfma_f32_16x16x32_bf16 v[212:215], a[84:87], a[212:215], v[212:215]\n"
"v_mfma_f32_16x16x32_bf16 v[212:215], a[68:71], a[196:199], v[212:215]\n"
"ds_write_b128 v41, v[224:227] offset:0\n"
"v_mfma_f32_16x16x32_bf16 v[228:231], a[68:71], a[200:203], v[228:231]\n"
"v_mfma_f32_16x16x32_bf16 v[228:231], a[84:87], a[216:219], v[228:231]\n"
"v_mfma_f32_16x16x32_bf16 v[228:231], a[100:103], a[232:235], v[228:231]\n"
"ds_write_b128 v42, v[240:243] offset:0\n"
"v_mfma_f32_16x16x32_bf16 v[244:247], a[100:103], a[236:239], v[244:247]\n"
"v_mfma_f32_16x16x32_bf16 v[244:247], a[84:87], a[220:223], v[244:247]\n"
"v_mfma_f32_16x16x32_bf16 v[244:247], a[68:71], a[204:207], v[244:247]\n"
"s_waitcnt vmcnt(3)\n"
"v_mfma_f32_16x16x32_bf16 v[200:203], a[72:75], a[192:195], v[200:203]\n"
"v_mfma_f32_16x16x32_bf16 v[200:203], a[88:91], a[208:211], v[200:203]\n"
"v_mfma_f32_16x16x32_bf16 v[200:203], a[104:107], a[224:227], v[200:203]\n"
"ds_write_b128 v40, v[212:215] offset:1024\n"
"v_mfma_f32_16x16x32_bf16 v[216:219], a[104:107], a[228:231], v[216:219]\n"
"v_mfma_f32_16x16x32_bf16 v[216:219], a[88:91], a[212:215], v[216:219]\n"
"v_mfma_f32_16x16x32_bf16 v[216:219], a[72:75], a[196:199], v[216:219]\n"
"ds_write_b128 v41, v[228:231] offset:1024\n"
"v_mfma_f32_16x16x32_bf16 v[232:235], a[72:75], a[200:203], v[232:235]\n"
"v_mfma_f32_16x16x32_bf16 v[232:235], a[88:91], a[216:219], v[232:235]\n"
"v_mfma_f32_16x16x32_bf16 v[232:235], a[104:107], a[232:235], v[232:235]\n"
"ds_write_b128 v42, v[244:247] offset:1024\n"
"v_mfma_f32_16x16x32_bf16 v[248:251], a[104:107], a[236:239], v[248:251]\n"
"v_mfma_f32_16x16x32_bf16 v[248:251], a[88:91], a[220:223], v[248:251]\n"
"v_mfma_f32_16x16x32_bf16 v[248:251], a[72:75], a[204:207], v[248:251]\n"
"s_waitcnt vmcnt(0)\n"
"v_mfma_f32_16x16x32_bf16 v[204:207], a[76:79], a[192:195], v[204:207]\n"
"v_mfma_f32_16x16x32_bf16 v[204:207], a[92:95], a[208:211], v[204:207]\n"
"v_mfma_f32_16x16x32_bf16 v[204:207], a[108:111], a[224:227], v[204:207]\n"
"ds_write_b128 v40, v[216:219] offset:2048\n"
"v_mfma_f32_16x16x32_bf16 v[220:223], a[108:111], a[228:231], v[220:223]\n"
"v_mfma_f32_16x16x32_bf16 v[220:223], a[92:95], a[212:215], v[220:223]\n"
"v_mfma_f32_16x16x32_bf16 v[220:223], a[76:79], a[196:199], v[220:223]\n"
"ds_write_b128 v41, v[232:235] offset:2048\n"
"v_mfma_f32_16x16x32_bf16 v[236:239], a[76:79], a[200:203], v[236:239]\n"
"v_mfma_f32_16x16x32_bf16 v[236:239], a[92:95], a[216:219], v[236:239]\n"
"v_mfma_f32_16x16x32_bf16 v[236:239], a[108:111], a[232:235], v[236:239]\n"
"ds_write_b128 v42, v[248:251] offset:2048\n"
"v_mfma_f32_16x16x32_bf16 v[252:255], a[108:111], a[236:239], v[252:255]\n"
"v_mfma_f32_16x16x32_bf16 v[252:255], a[92:95], a[220:223], v[252:255]\n"
"v_mfma_f32_16x16x32_bf16 v[252:255], a[76:79], a[204:207], v[252:255]\n"
"v_mfma_f32_16x16x32_bf16 v[192:195], a[240:243], v[36:39], v[192:195]\n"
"v_mfma_f32_16x16x32_bf16 v[196:199], a[244:247], v[36:39], v[196:199]\n"
"v_mfma_f32_16x16x32_bf16 v[200:203], a[248:251], v[36:39], v[200:203]\n"
"v_mfma_f32_16x16x32_bf16 v[204:207], a[252:255], v[36:39], v[204:207]\n"
"ds_write_b128 v40, v[220:223] offset:3072\n"
"ds_write_b128 v41, v[236:239] offset:3072\n"
"s_nop 1\n"
"ds_write_b128 v42, v[252:255] offset:3072\n"
"v_xor_b32 v40, 8192, v40\n"
"v_xor_b32 v41, 16384, v41\n"
"v_xor_b32 v42, 24576, v42\n"
"s_waitcnt lgkmcnt(0)\n"
"s_barrier\n"
"ds_read_b128 v[208:211], v40 offset:0\n"
"ds_read_b128 v[212:215], v40 offset:1024\n"
"ds_read_b128 v[216:219], v40 offset:2048\n"
"ds_read_b128 v[220:223], v40 offset:3072\n"
"ds_read_b128 v[224:227], v41 offset:0\n"
"ds_read_b128 v[228:231], v41 offset:1024\n"
"ds_read_b128 v[232:235], v41 offset:2048\n"
"ds_read_b128 v[236:239], v41 offset:3072\n"
"ds_read_b128 v[240:243], v42 offset:0\n"
"ds_read_b128 v[244:247], v42 offset:1024\n"
"ds_read_b128 v[248:251], v42 offset:2048\n"
"ds_read_b128 v[252:255], v42 offset:3072\n"
"v_pk_add_f32 v[192:193], v[192:193], v[44:45] op_sel_hi:[1,0]\n"
"v_pk_add_f32 v[194:195], v[194:195], v[44:45] op_sel_hi:[1,0]\n"
"v_pk_add_f32 v[196:197], v[196:197], v[44:45] op_sel_hi:[1,0]\n"
"v_pk_add_f32 v[198:199], v[198:199], v[44:45] op_sel_hi:[1,0]\n"
"v_pk_add_f32 v[200:201], v[200:201], v[44:45] op_sel_hi:[1,0]\n"
"v_pk_add_f32 v[202:203], v[202:203], v[44:45] op_sel_hi:[1,0]\n"
"v_pk_add_f32 v[204:205], v[204:205], v[44:45] op_sel_hi:[1,0]\n"
"v_pk_add_f32 v[206:207], v[206:207], v[44:45] op_sel_hi:[1,0]\n"
"s_lshl_b32 s86, s73, 16\n"
"s_lshl_b32 s87, s61, 14\n"
"s_add_u32 s86, s86, s87\n"
"s_lshl_b32 s87, s74, 7\n"
"s_add_u32 s86, s86, s87\n"
"v_lshlrev_b32 v23, 10, v2\n"
"v_lshl_add_u32 v23, v3, 4, v23\n"
"s_waitcnt lgkmcnt(8)\n"
"v_pk_add_f32 v[192:193], v[192:193], v[208:209]\n"
"v_pk_add_f32 v[194:195], v[194:195], v[210:211]\n"
"v_pk_add_f32 v[196:197], v[196:197], v[212:213]\n"
"v_pk_add_f32 v[198:199], v[198:199], v[214:215]\n"
"v_pk_add_f32 v[200:201], v[200:201], v[216:217]\n"
"v_pk_add_f32 v[202:203], v[202:203], v[218:219]\n"
"v_pk_add_f32 v[204:205], v[204:205], v[220:221]\n"
"v_pk_add_f32 v[206:207], v[206:207], v[222:223]\n"
"s_waitcnt lgkmcnt(4)\n"
"v_pk_add_f32 v[192:193], v[192:193], v[224:225]\n"
"v_pk_add_f32 v[194:195], v[194:195], v[226:227]\n"
"v_pk_add_f32 v[196:197], v[196:197], v[228:229]\n"
"v_pk_add_f32 v[198:199], v[198:199], v[230:231]\n"
"v_pk_add_f32 v[200:201], v[200:201], v[232:233]\n"
"v_pk_add_f32 v[202:203], v[202:203], v[234:235]\n"
"v_pk_add_f32 v[204:205], v[204:205], v[236:237]\n"
"v_pk_add_f32 v[206:207], v[206:207], v[238:239]\n"
"s_waitcnt lgkmcnt(0)\n"
"v_pk_add_f32 v[192:193], v[192:193], v[240:241]\n"
"v_pk_add_f32 v[194:195], v[194:195], v[242:243]\n"
"v_pk_add_f32 v[196:197], v[196:197], v[244:245]\n"
"v_pk_add_f32 v[198:199], v[198:199], v[246:247]\n"
"v_pk_add_f32 v[200:201], v[200:201], v[248:249]\n"
"v_pk_add_f32 v[202:203], v[202:203], v[250:251]\n"
"v_pk_add_f32 v[204:205], v[204:205], v[252:253]\n"
"v_pk_add_f32 v[206:207], v[206:207], v[254:255]\n"
"v_cvt_pk_bf16_f32 v192, v192, v193\n"
"v_cvt_pk_bf16_f32 v193, v194, v195\n"
"v_cvt_pk_bf16_f32 v194, v196, v197\n"
"v_cvt_pk_bf16_f32 v195, v198, v199\n"
"v_cvt_pk_bf16_f32 v196, v200, v201\n"
"v_cvt_pk_bf16_f32 v197, v202, v203\n"
"v_cvt_pk_bf16_f32 v198, v204, v205\n"
"v_cvt_pk_bf16_f32 v199, v206, v207\n"
"buffer_store_dwordx4 v[192:195], v23, s[48:51], s86 offen offset:0 sc1\n"
"buffer_store_dwordx4 v[196:199], v23, s[48:51], s86 offen offset:64 sc1\n"
"s_branch END_52_%=\n"
"US_12_%=:\n"
"s_mov_b32 s93, 0\n"
"v_mov_b32 v176, 0\n"
"v_mov_b32 v177, 0\n"
"v_mov_b32 v178, 0\n"
"v_mov_b32 v179, 0\n"
"v_mov_b32 v180, 0\n"
"v_mov_b32 v181, 0\n"
"v_mov_b32 v182, 0\n"
"v_mov_b32 v183, 0\n"
"v_mov_b32 v184, 0\n"
"v_mov_b32 v185, 0\n"
"v_mov_b32 v186, 0\n"
"v_mov_b32 v187, 0\n"
"v_mov_b32 v188, 0\n"
"v_mov_b32 v189, 0\n"
"v_mov_b32 v190, 0\n"
"v_mov_b32 v191, 0\n"
"v_lshlrev_b32 v96, 2, v3\n"
"v_sub_u32 v96, v2, v96\n"
"s_lshl_b32 s87, s66, 4\n"
"v_add_u32 v96, s87, v96\n"
"v_cvt_f32_i32 v96, v96\n"
"v_add_f32 v96, 0.5, v96\n"
"v_mul_f32 v96, 0x71800000, v96\n"
"s_cmp_eq_u32 s67, 0\n"
"s_cselect_b64 vcc, -1, 0\n"
"v_cndmask_b32 v97, v188, v176, vcc\n"
"v_cndmask_b32 v98, v176, v188, vcc\n"
"v_add_f32 v99, 0x00000000, v96\n"
"v_min_f32 v44, v99, v97\n"
"v_min_f32 v52, v99, v98\n"
"v_add_f32 v99, 0xf1800000, v96\n"
"v_min_f32 v45, v99, v97\n"
"v_min_f32 v53, v99, v98\n"
"v_add_f32 v99, 0xf2000000, v96\n"
"v_min_f32 v46, v99, v97\n"
"v_min_f32 v54, v99, v98\n"
"v_add_f32 v99, 0xf2400000, v96\n"
"v_min_f32 v47, v99, v97\n"
"v_min_f32 v55, v99, v98\n"
"v_add_f32 v99, 0xf3800000, v96\n"
"v_min_f32 v48, v99, v97\n"
"v_min_f32 v56, v99, v98\n"
"v_add_f32 v99, 0xf3880000, v96\n"
"v_min_f32 v49, v99, v97\n"
"v_min_f32 v57, v99, v98\n"
"v_add_f32 v99, 0xf3900000, v96\n"
"v_min_f32 v50, v99, v97\n"
"v_min_f32 v58, v99, v98\n"
"v_add_f32 v99, 0xf3980000, v96\n"
"v_min_f32 v51, v99, v97\n"
"v_min_f32 v59, v99, v98\n"
"s_branch US_12_%=r\n"
"SAFE0_%=:\n"
"s_waitcnt vmcnt(0) lgkmcnt(0)\n"
"s_barrier\n"
"s_lshl_b32 s99, s60, 7\n"
"s_add_u32 s99, s99, 163840\n"
"v_mov_b32 v28, s64\n"
"v_mov_b32 v29, s65\n"
"s_cmp_eq_u32 s61, 0\n"
"s_cselect_b64 exec, 1, 0\n"
"buffer_store_dwordx2 v[28:29], off, s[44:47], s99 sc1\n"
"s_mov_b64 exec, -1\n"
"s_mov_b32 s58, 0\n"
"s_mov_b32 s93, 1\n"
"s_and_b32 s82, s60, 0xc7\n"
"s_lshl_b32 s82, s82, 7\n"
"s_add_u32 s82, s82, 163840\n"
"v_and_b32 v102, 7, v0\n"
"v_lshl_add_u32 v102, v102, 10, s82\n"
"buffer_load_dwordx2 v[28:29], v102, s[44:47], 0 offen sc1\n"
"s_waitcnt vmcnt(0)\n"
"v_cmp_ne_u64 vcc, s[64:65], v[28:29]\n"
"s_nop 1\n"
"s_cbranch_vccz FOK_35_%=\n"
"FSP_36_%=:\n"
"s_cmp_eq_u32 s69, 0\n"
"s_cbranch_scc1 FOK_35_%=\n"
"s_sub_u32 s69, s69, 1\n"
"s_sleep 2\n"
"s_mov_b64 exec, vcc\n"
"buffer_load_dwordx2 v[28:29], v102, s[44:47], 0 offen sc1\n"
"s_mov_b64 exec, -1\n"
"s_waitcnt vmcnt(0)\n"
"v_cmp_ne_u64 vcc, s[64:65], v[28:29]\n"
"s_nop 1\n"
"s_cbranch_vccnz FSP_36_%=\n"
"FOK_35_%=:\n"
"s_lshl_b32 s82, s70, 2\n"
"s_add_u32 s82, s82, 3\n"
"v_lshlrev_b32 v102, 2, v3\n"
"v_sub_u32 v102, v2, v102\n"
"s_lshl_b32 s83, s66, 4\n"
"s_sub_u32 s83, s83, 96\n"
"v_add_u32 v102, s83, v102\n"
"s_lshl_b32 s83, s67, 6\n"
"v_add_u32 v100, s83, v102\n"
"v_subrev_u32 v101, s83, v102\n"
"v_cvt_f32_i32 v100, v100\n"
"v_add_f32 v100, 0.5, v100\n"
"v_mul_f32 v100, 0x71800000, v100\n"
"v_cvt_f32_i32 v101, v101\n"
"v_add_f32 v101, 0.5, v101\n"
"v_mul_f32 v101, 0x71800000, v101\n"
"v_mov_b32 v104, 0xff61b1e6\n"
"v_mov_b32 v105, 0xff61b1e6\n"
"v_mov_b32 v106, 0xff61b1e6\n"
"v_mov_b32 v107, 0xff61b1e6\n"
"SFL_34_%=:\n"
"s_lshl_b32 s83, s82, 14\n"
"s_add_u32 s83, s83, s94\n"
"s_add_u32 s84, s83, 256\n"
"buffer_load_dwordx4 a[192:195], v24, s[44:47], s83 offen offset:0 sc1\n"
"buffer_load_dwordx4 a[196:199], v25, s[44:47], s83 offen offset:1024 sc1\n"
"buffer_load_dwordx4 a[200:203], v26, s[44:47], s83 offen offset:2048 sc1\n"
"buffer_load_dwordx4 a[204:207], v27, s[44:47], s83 offen offset:3072 sc1\n"
"buffer_load_dwordx4 a[208:211], v24, s[44:47], s84 offen offset:0 sc1\n"
"s_nop 0\n"
"PG20_%=:\n"
"s_cbranch_execz PADRET_%=\n"
"buffer_load_dwordx4 a[212:215], v25, s[44:47], s84 offen offset:1024 sc1\n"
"buffer_load_dwordx4 a[216:219], v26, s[44:47], s84 offen offset:2048 sc1\n"
"buffer_load_dwordx4 a[220:223], v27, s[44:47], s84 offen offset:3072 sc1\n"
"v_add_f32 v128, 0x00000000, v100\n"
"v_min_f32 v128, 0, v128\n"
"v_add_f32 v129, 0xf1800000, v100\n"
"v_min_f32 v129, 0, v129\n"
"v_add_f32 v130, 0xf2000000, v100\n"
"v_min_f32 v130, 0, v130\n"
"v_add_f32 v131, 0xf2400000, v100\n"
"v_min_f32 v131, 0, v131\n"
"v_add_f32 v132, 0xf3800000, v100\n"
"v_min_f32 v132, 0, v132\n"
"v_add_f32 v133, 0xf3880000, v100\n"
"v_min_f32 v133, 0, v133\n"
"v_add_f32 v134, 0xf3900000, v100\n"
"v_min_f32 v134, 0, v134\n"
"v_add_f32 v135, 0xf3980000, v100\n"
"v_min_f32 v135, 0, v135\n"
"v_add_f32 v136, 0x74000000, v100\n"
"v_min_f32 v136, 0, v136\n"
"v_add_f32 v137, 0x73f80000, v100\n"
"v_min_f32 v137, 0, v137\n"
"v_add_f32 v138, 0x73f00000, v100\n"
"v_min_f32 v138, 0, v138\n"
"v_add_f32 v139, 0x73e80000, v100\n"
"v_min_f32 v139, 0, v139\n"
"v_add_f32 v140, 0x73800000, v100\n"
"v_min_f32 v140, 0, v140\n"
"v_add_f32 v141, 0x73700000, v100\n"
"v_min_f32 v141, 0, v141\n"
"v_add_f32 v142, 0x73600000, v100\n"
"v_min_f32 v142, 0, v142\n"
"v_add_f32 v143, 0x73500000, v100\n"
"v_min_f32 v143, 0, v143\n"
"v_add_f32 v144, 0x74800000, v101\n"
"v_min_f32 v144, 0, v144\n"
"v_add_f32 v145, 0x747c0000, v101\n"
"v_min_f32 v145, 0, v145\n"
"v_add_f32 v146, 0x74780000, v101\n"
"v_min_f32 v146, 0, v146\n"
"v_add_f32 v147, 0x74740000, v101\n"
"v_min_f32 v147, 0, v147\n"
"v_add_f32 v148, 0x74400000, v101\n"
"v_min_f32 v148, 0, v148\n"
"v_add_f32 v149, 0x743c0000, v101\n"
"v_min_f32 v149, 0, v149\n"
"v_add_f32 v150, 0x74380000, v101\n"
"v_min_f32 v150, 0, v150\n"
"v_add_f32 v151, 0x74340000, v101\n"
"v_min_f32 v151, 0, v151\n"
"v_add_f32 v152, 0x74c00000, v101\n"
"v_min_f32 v152, 0, v152\n"
"v_add_f32 v153, 0x74be0000, v101\n"
"v_min_f32 v153, 0, v153\n"
"v_add_f32 v154, 0x74bc0000, v101\n"
"v_min_f32 v154, 0, v154\n"
"v_add_f32 v155, 0x74ba0000, v101\n"
"v_min_f32 v155, 0, v155\n"
"v_add_f32 v156, 0x74a00000, v101\n"
"v_min_f32 v156, 0, v156\n"
"v_add_f32 v157, 0x749e0000, v101\n"
"v_min_f32 v157, 0, v157\n"
"v_add_f32 v158, 0x749c0000, v101\n"
"v_min_f32 v158, 0, v158\n"
"v_add_f32 v159, 0x749a0000, v101\n"
"v_min_f32 v159, 0, v159\n"
"s_waitcnt vmcnt(0)\n"
"v_mfma_f32_16x16x32_bf16 v[128:131], a[192:195], a[64:67], v[128:131]\n"
"v_mfma_f32_16x16x32_bf16 v[128:131], a[196:199], a[68:71], v[128:131]\n"
"v_mfma_f32_16x16x32_bf16 v[128:131], a[200:203], a[72:75], v[128:131]\n"
"v_mfma_f32_16x16x32_bf16 v[128:131], a[204:207], a[76:79], v[128:131]\n"
"v_mfma_f32_16x16x32_bf16 v[132:135], a[220:223], a[76:79], v[132:135]\n"
"v_mfma_f32_16x16x32_bf16 v[132:135], a[216:219], a[72:75], v[132:135]\n"
"v_mfma_f32_16x16x32_bf16 v[132:135], a[212:215], a[68:71], v[132:135]\n"
"v_mfma_f32_16x16x32_bf16 v[132:135], a[208:211], a[64:67], v[132:135]\n"
"v_mfma_f32_16x16x32_bf16 v[140:143], a[208:211], a[80:83], v[140:143]\n"
"v_mfma_f32_16x16x32_bf16 v[140:143], a[212:215], a[84:87], v[140:143]\n"
"v_mfma_f32_16x16x32_bf16 v[140:143], a[216:219], a[88:91], v[140:143]\n"
"v_mfma_f32_16x16x32_bf16 v[140:143], a[220:223], a[92:95], v[140:143]\n"
"v_mfma_f32_16x16x32_bf16 v[136:139], a[204:207], a[92:95], v[136:139]\n"
"v_mfma_f32_16x16x32_bf16 v[136:139], a[200:203], a[88:91], v[136:139]\n"
"v_mfma_f32_16x16x32_bf16 v[136:139], a[196:199], a[84:87], v[136:139]\n"
"v_mfma_f32_16x16x32_bf16 v[136:139], a[192:195], a[80:83], v[136:139]\n"
"v_mfma_f32_16x16x32_bf16 v[144:147], a[192:195], a[96:99], v[144:147]\n"
"v_mfma_f32_16x16x32_bf16 v[144:147], a[196:199], a[100:103], v[144:147]\n"
"v_mfma_f32_16x16x32_bf16 v[144:147], a[200:203], a[104:107], v[144:147]\n"
"v_mfma_f32_16x16x32_bf16 v[144:147], a[204:207], a[108:111], v[144:147]\n"
"v_mfma_f32_16x16x32_bf16 v[148:151], a[220:223], a[108:111], v[148:151]\n"
"v_mfma_f32_16x16x32_bf16 v[148:151], a[216:219], a[104:107], v[148:151]\n"
"v_mfma_f32_16x16x32_bf16 v[148:151], a[212:215], a[100:103], v[148:151]\n"
"v_mfma_f32_16x16x32_bf16 v[148:151], a[208:211], a[96:99], v[148:151]\n"
"v_mfma_f32_16x16x32_bf16 v[156:159], a[208:211], a[112:115], v[156:159]\n"
"v_mfma_f32_16x16x32_bf16 v[156:159], a[212:215], a[116:119], v[156:159]\n"
"v_mfma_f32_16x16x32_bf16 v[156:159], a[216:219], a[120:123], v[156:159]\n"
"v_mfma_f32_16x16x32_bf16 v[156:159], a[220:223], a[124:127], v[156:159]\n"
"v_mfma_f32_16x16x32_bf16 v[152:155], a[204:207], a[124:127], v[152:155]\n"
"v_mfma_f32_16x16x32_bf16 v[152:155], a[200:203], a[120:123], v[152:155]\n"
"v_mfma_f32_16x16x32_bf16 v[152:155], a[196:199], a[116:119], v[152:155]\n"
"v_mfma_f32_16x16x32_bf16 v[152:155], a[192:195], a[112:115], v[152:155]\n"
"v_max3_f32 v104, v128, v129, v104\n"
"v_max3_f32 v104, v130, v131, v104\n"
"v_max3_f32 v104, v132, v133, v104\n"
"v_max3_f32 v104, v134, v135, v104\n"
"v_max3_f32 v105, v136, v137, v105\n"
"v_max3_f32 v105, v138, v139, v105\n"
"v_max3_f32 v105, v140, v141, v105\n"
"v_max3_f32 v105, v142, v143, v105\n"
"v_max3_f32 v106, v144, v145, v106\n"
"v_max3_f32 v106, v146, v147, v106\n"
"v_max3_f32 v106, v148, v149, v106\n"
"v_max3_f32 v106, v150, v151, v106\n"
"v_max3_f32 v107, v152, v153, v107\n"
"v_max3_f32 v107, v154, v155, v107\n"
"v_max3_f32 v107, v156, v157, v107\n"
"v_max3_f32 v107, v158, v159, v107\n"
"v_add_f32 v100, 0x74000000, v100\n"
"v_add_f32 v101, 0x74000000, v101\n"
"s_sub_u32 s82, s82, 1\n"
"s_cbranch_scc0 SFL_34_%=\n"
"v_mov_b32 v102, v104\n"
"s_nop 1\n"
"v_permlane16_swap_b32 v104, v102\n"
"v_max_f32 v104, v104, v102\n"
"v_mov_b32 v102, v104\n"
"s_nop 1\n"
"v_permlane32_swap_b32 v104, v102\n"
"v_max_f32 v104, v104, v102\n"
"v_sub_f32 v176, 0, v104\n"
"v_sub_f32 v177, 0, v104\n"
"v_sub_f32 v178, 0, v104\n"
"v_sub_f32 v179, 0, v104\n"
"v_mov_b32 v102, v105\n"
"s_nop 1\n"
"v_permlane16_swap_b32 v105, v102\n"
"v_max_f32 v105, v105, v102\n"
"v_mov_b32 v102, v105\n"
"s_nop 1\n"
"v_permlane32_swap_b32 v105, v102\n"
"v_max_f32 v105, v105, v102\n"
"v_sub_f32 v180, 0, v105\n"
"v_sub_f32 v181, 0, v105\n"
"v_sub_f32 v182, 0, v105\n"
"v_sub_f32 v183, 0, v105\n"
"v_mov_b32 v102, v106\n"
"s_nop 1\n"
"v_permlane16_swap_b32 v106, v102\n"
"v_max_f32 v106, v106, v102\n"
"v_mov_b32 v102, v106\n"
"s_nop 1\n"
"v_permlane32_swap_b32 v106, v102\n"
"v_max_f32 v106, v106, v102\n"
"v_sub_f32 v184, 0, v106\n"
"v_sub_f32 v185, 0, v106\n"
"v_sub_f32 v186, 0, v106\n"
"v_sub_f32 v187, 0, v106\n"
"v_mov_b32 v102, v107\n"
"s_nop 1\n"
"v_permlane16_swap_b32 v107, v102\n"
"v_max_f32 v107, v107, v102\n"
"v_mov_b32 v102, v107\n"
"s_nop 1\n"
"v_permlane32_swap_b32 v107, v102\n"
"v_max_f32 v107, v107, v102\n"
"v_sub_f32 v188, 0, v107\n"
"v_sub_f32 v189, 0, v107\n"
"v_sub_f32 v190, 0, v107\n"
"v_sub_f32 v191, 0, v107\n"
"v_lshlrev_b32 v96, 2, v3\n"
"v_sub_u32 v96, v2, v96\n"
"s_lshl_b32 s87, s66, 4\n"
"v_add_u32 v96, s87, v96\n"
"v_cvt_f32_i32 v96, v96\n"
"v_add_f32 v96, 0.5, v96\n"
"v_mul_f32 v96, 0x71800000, v96\n"
"s_cmp_eq_u32 s67, 0\n"
"s_cselect_b64 vcc, -1, 0\n"
"v_cndmask_b32 v97, v188, v176, vcc\n"
"v_cndmask_b32 v98, v176, v188, vcc\n"
"v_add_f32 v99, 0x00000000, v96\n"
"v_min_f32 v44, v99, v97\n"
"v_min_f32 v52, v99, v98\n"
"v_add_f32 v99, 0xf1800000, v96\n"
"v_min_f32 v45, v99, v97\n"
"v_min_f32 v53, v99, v98\n"
"v_add_f32 v99, 0xf2000000, v96\n"
"v_min_f32 v46, v99, v97\n"
"v_min_f32 v54, v99, v98\n"
"v_add_f32 v99, 0xf2400000, v96\n"
"v_min_f32 v47, v99, v97\n"
"v_min_f32 v55, v99, v98\n"
"v_add_f32 v99, 0xf3800000, v96\n"
"v_min_f32 v48, v99, v97\n"
"v_min_f32 v56, v99, v98\n"
"v_add_f32 v99, 0xf3880000, v96\n"
"v_min_f32 v49, v99, v97\n"
"v_min_f32 v57, v99, v98\n"
"v_add_f32 v99, 0xf3900000, v96\n"
"v_min_f32 v50, v99, v97\n"
"v_min_f32 v58, v99, v98\n"
"v_add_f32 v99, 0xf3980000, v96\n"
"v_min_f32 v51, v99, v97\n"
"v_min_f32 v59, v99, v98\n"
"s_lshl_b32 s78, s67, 8\n"
"s_add_u32 s78, s78, s94\n"
"s_lshl_b32 s87, s66, 14\n"
"s_sub_u32 s79, s78, s87\n"
"s_add_u32 s79, s79, 49152\n"
"s_add_u32 s78, s78, s87\n"
"s_add_u32 s80, s78, 512\n"
"s_add_u32 s81, s79, 512\n"
"s_mov_b32 s92, 65536\n"
"s_movk_i32 s5, 512\n"
"s_lshl_b32 s76, s61, 12\n"
"s_add_u32 s77, s76, 65536\n"
"s_mov_b64 s[20:21], s[44:45]\n"
"s_mov_b32 s22, s46\n"
"s_mov_b64 s[24:25], s[44:45]\n"
"s_mov_b32 s26, s46\n"
"s_mov_b32 s74, s70\n"
"s_mov_b32 s75, s71\n"
"s_mov_b64 s[72:73], s[78:79]\n"
"s_add_u32 s82, s70, 1\n"
"s_and_b32 s82, s82, 0\n"
"s_lshl_b32 s82, s82, 15\n"
"s_add_u32 s76, s76, s82\n"
"s_add_u32 s77, s77, s82\n"
"s_lshl_b32 s83, s67, 13\n"
"s_add_u32 s82, s82, s83\n"
"v_add_u32 v8, s82, v1\n"
"v_add_u32 v9, 0x10000, v8\n"
"s_add_u32 m0, s76, 0\n"
"s_nop 0\n"
"buffer_load_dwordx4 v24, s[20:23], s78 offen offset:0 sc1 lds\n"
"buffer_load_dwordx4 v25, s[20:23], s78 offen offset:1024 sc1 lds\n"
"buffer_load_dwordx4 v26, s[20:23], s78 offen offset:2048 sc1 lds\n"
"buffer_load_dwordx4 v27, s[20:23], s78 offen offset:3072 sc1 lds\n"
"s_add_u32 m0, s76, 16384\n"
"s_nop 0\n"
"buffer_load_dwordx4 v24, s[20:23], s79 offen offset:0 sc1 lds\n"
"buffer_load_dwordx4 v25, s[20:23], s79 offen offset:1024 sc1 lds\n"
"buffer_load_dwordx4 v26, s[20:23], s79 offen offset:2048 sc1 lds\n"
"buffer_load_dwordx4 v27, s[20:23], s79 offen offset:3072 sc1 lds\n"
"s_add_u32 m0, s77, 0\n"
"s_nop 0\n"
"buffer_load_dwordx4 v24, s[24:27], s80 offen offset:0 sc1 lds\n"
"buffer_load_dwordx4 v25, s[24:27], s80 offen offset:1024 sc1 lds\n"
"buffer_load_dwordx4 v26, s[24:27], s80 offen offset:2048 sc1 lds\n"
"buffer_load_dwordx4 v27, s[24:27], s80 offen offset:3072 sc1 lds\n"
"s_add_u32 m0, s77, 16384\n"
"s_nop 0\n"
"buffer_load_dwordx4 v24, s[24:27], s81 offen offset:0 sc1 lds\n"
"buffer_load_dwordx4 v25, s[24:27], s81 offen offset:1024 sc1 lds\n"
"buffer_load_dwordx4 v26, s[24:27], s81 offen offset:2048 sc1 lds\n"
"buffer_load_dwordx4 v27, s[24:27], s81 offen offset:3072 sc1 lds\n"
"s_add_u32 s78, s78, s92\n"
"s_add_u32 s79, s79, s92\n"
"s_xor_b32 s76, s76, 0x8000\n"
"s_sub_u32 s74, s74, 1\n"
"s_cselect_b64 s[78:79], s[72:73], s[78:79]\n"
"s_cselect_b64 s[72:73], s[16:17], s[72:73]\n"
"s_cselect_b32 s74, s75, s74\n"
"s_cselect_b32 s75, 64, s75\n"
"s_cmp_gt_u32 s74, 32\n"
"s_cselect_b64 s[20:21], s[32:33], s[20:21]\n"
"s_cselect_b32 s22, s34, s22\n"
"s_cselect_b32 s92, 262144, s92\n"
"s_cselect_b32 s5, 524288, s5\n"
"s_add_u32 m0, s76, 0\n"
"s_nop 0\n"
"buffer_load_dwordx4 v24, s[20:23], s78 offen offset:0 sc1 lds\n"
"buffer_load_dwordx4 v25, s[20:23], s78 offen offset:1024 sc1 lds\n"
"buffer_load_dwordx4 v26, s[20:23], s78 offen offset:2048 sc1 lds\n"
"buffer_load_dwordx4 v27, s[20:23], s78 offen offset:3072 sc1 lds\n"
"s_add_u32 m0, s76, 16384\n"
"s_nop 0\n"
"buffer_load_dwordx4 v24, s[20:23], s79 offen offset:0 sc1 lds\n"
"buffer_load_dwordx4 v25, s[20:23], s79 offen offset:1024 sc1 lds\n"
"buffer_load_dwordx4 v26, s[20:23], s79 offen offset:2048 sc1 lds\n"
"buffer_load_dwordx4 v27, s[20:23], s79 offen offset:3072 sc1 lds\n"
"s_add_u32 s80, s78, s5\n"
"s_add_u32 s81, s79, s5\n"
"s_mov_b64 s[24:25], s[20:21]\n"
"s_mov_b32 s26, s22\n"
"s_xor_b32 s77, s77, 0x8000\n"
"s_add_u32 s78, s78, s92\n"
"s_add_u32 s79, s79, s92\n"
"s_xor_b32 s76, s76, 0x8000\n"
"s_sub_u32 s74, s74, 1\n"
"s_cselect_b64 s[78:79], s[72:73], s[78:79]\n"
"s_cselect_b64 s[72:73], s[16:17], s[72:73]\n"
"s_cselect_b32 s74, s75, s74\n"
"s_cselect_b32 s75, 64, s75\n"
"s_cmp_gt_u32 s74, 32\n"
"s_cselect_b64 s[20:21], s[32:33], s[20:21]\n"
"s_cselect_b32 s22, s34, s22\n"
"s_cselect_b32 s92, 262144, s92\n"
"s_cselect_b32 s5, 524288, s5\n"
"s_waitcnt vmcnt(0)\n"
"s_barrier\n"
"ds_read_b128 a[224:227], v8 offset:0\n"
"ds_read_b128 a[228:231], v8 offset:1024\n"
"ds_read_b128 a[232:235], v8 offset:2048\n"
"ds_read_b128 a[236:239], v8 offset:3072\n"
"ds_read_b128 a[240:243], v8 offset:4096\n"
"ds_read_b128 a[244:247], v8 offset:5120\n"
"ds_read_b128 a[248:251], v8 offset:6144\n"
"ds_read_b128 a[252:255], v8 offset:7168\n"
"s_waitcnt lgkmcnt(0)\n"
"s_barrier\n"
"s_add_u32 m0, s76, 0\n"
"s_nop 0\n"
"buffer_load_dwordx4 v24, s[20:23], s78 offen offset:0 sc1 lds\n"
"buffer_load_dwordx4 v25, s[20:23], s78 offen offset:1024 sc1 lds\n"
"buffer_load_dwordx4 v26, s[20:23], s78 offen offset:2048 sc1 lds\n"
"buffer_load_dwordx4 v27, s[20:23], s78 offen offset:3072 sc1 lds\n"
"s_mov_b32 s82, 983040\n"
"s_mov_b32 s83, 983040\n"
"s_mov_b32 s84, 983040\n"
"s_mov_b32 s86, 983040\n"
"s_branch BLKS_%=\n"
"SAFE_%=:\n"
"s_waitcnt vmcnt(0) lgkmcnt(0)\n"
"s_barrier\n"
"s_lshl_b32 s99, s60, 7\n"
"s_add_u32 s99, s99, 163840\n"
"v_mov_b32 v28, s64\n"
"v_mov_b32 v29, s65\n"
"s_cmp_eq_u32 s61, 0\n"
"s_cselect_b64 exec, 1, 0\n"
"buffer_store_dwordx2 v[28:29], off, s[44:47], s99 sc1\n"
"s_mov_b64 exec, -1\n"
"s_mov_b32 s58, 0\n"
"s_mov_b32 s93, 1\n"
"s_and_b32 s82, s60, 0xc7\n"
"s_lshl_b32 s82, s82, 7\n"
"s_add_u32 s82, s82, 163840\n"
"v_and_b32 v102, 7, v0\n"
"v_lshl_add_u32 v102, v102, 10, s82\n"
"buffer_load_dwordx2 v[28:29], v102, s[44:47], 0 offen sc1\n"
"s_waitcnt vmcnt(0)\n"
"v_cmp_ne_u64 vcc, s[64:65], v[28:29]\n"
"s_nop 1\n"
"s_cbranch_vccz FOK_38_%=\n"
"FSP_39_%=:\n"
"s_cmp_eq_u32 s69, 0\n"
"s_cbranch_scc1 FOK_38_%=\n"
"s_sub_u32 s69, s69, 1\n"
"s_sleep 2\n"
"s_mov_b64 exec, vcc\n"
"buffer_load_dwordx2 v[28:29], v102, s[44:47], 0 offen sc1\n"
"s_mov_b64 exec, -1\n"
"s_waitcnt vmcnt(0)\n"
"v_cmp_ne_u64 vcc, s[64:65], v[28:29]\n"
"s_nop 1\n"
"s_cbranch_vccnz FSP_39_%=\n"
"FOK_38_%=:\n"
"s_lshl_b32 s82, s71, 2\n"
"s_add_u32 s82, s82, 3\n"
"v_lshlrev_b32 v102, 2, v3\n"
"v_sub_u32 v102, v2, v102\n"
"s_lshl_b32 s83, s66, 4\n"
"s_sub_u32 s83, s83, 96\n"
"v_add_u32 v102, s83, v102\n"
"s_lshl_b32 s83, s67, 6\n"
"v_add_u32 v100, s83, v102\n"
"v_subrev_u32 v101, s83, v102\n"
"v_cvt_f32_i32 v100, v100\n"
"v_add_f32 v100, 0.5, v100\n"
"v_mul_f32 v100, 0x71800000, v100\n"
"v_cvt_f32_i32 v101, v101\n"
"v_add_f32 v101, 0.5, v101\n"
"v_mul_f32 v101, 0x71800000, v101\n"
"v_mov_b32 v104, 0xff61b1e6\n"
"v_mov_b32 v105, 0xff61b1e6\n"
"v_mov_b32 v106, 0xff61b1e6\n"
"v_mov_b32 v107, 0xff61b1e6\n"
"SFL_37_%=:\n"
"s_lshl_b32 s83, s82, 14\n"
"s_add_u32 s83, s83, s94\n"
"s_add_u32 s84, s83, 256\n"
"buffer_load_dwordx4 a[192:195], v24, s[44:47], s83 offen offset:0 sc1\n"
"buffer_load_dwordx4 a[196:199], v25, s[44:47], s83 offen offset:1024 sc1\n"
"buffer_load_dwordx4 a[200:203], v26, s[44:47], s83 offen offset:2048 sc1\n"
"buffer_load_dwordx4 a[204:207], v27, s[44:47], s83 offen offset:3072 sc1\n"
"buffer_load_dwordx4 a[208:211], v24, s[44:47], s84 offen offset:0 sc1\n"
"buffer_load_dwordx4 a[212:215], v25, s[44:47], s84 offen offset:1024 sc1\n"
"buffer_load_dwordx4 a[216:219], v26, s[44:47], s84 offen offset:2048 sc1\n"
"buffer_load_dwordx4 a[220:223], v27, s[44:47], s84 offen offset:3072 sc1\n"
"v_add_f32 v128, 0x00000000, v100\n"
"v_min_f32 v128, 0, v128\n"
"v_add_f32 v129, 0xf1800000, v100\n"
"v_min_f32 v129, 0, v129\n"
"v_add_f32 v130, 0xf2000000, v100\n"
"v_min_f32 v130, 0, v130\n"
"v_add_f32 v131, 0xf2400000, v100\n"
"v_min_f32 v131, 0, v131\n"
"v_add_f32 v132, 0xf3800000, v100\n"
"v_min_f32 v132, 0, v132\n"
"v_add_f32 v133, 0xf3880000, v100\n"
"v_min_f32 v133, 0, v133\n"
"v_add_f32 v134, 0xf3900000, v100\n"
"v_min_f32 v134, 0, v134\n"
"v_add_f32 v135, 0xf3980000, v100\n"
"v_min_f32 v135, 0, v135\n"
"v_add_f32 v136, 0x74000000, v100\n"
"v_min_f32 v136, 0, v136\n"
"v_add_f32 v137, 0x73f80000, v100\n"
"v_min_f32 v137, 0, v137\n"
"v_add_f32 v138, 0x73f00000, v100\n"
"v_min_f32 v138, 0, v138\n"
"v_add_f32 v139, 0x73e80000, v100\n"
"v_min_f32 v139, 0, v139\n"
"v_add_f32 v140, 0x73800000, v100\n"
"v_min_f32 v140, 0, v140\n"
"v_add_f32 v141, 0x73700000, v100\n"
"v_min_f32 v141, 0, v141\n"
"v_add_f32 v142, 0x73600000, v100\n"
"v_min_f32 v142, 0, v142\n"
"v_add_f32 v143, 0x73500000, v100\n"
"v_min_f32 v143, 0, v143\n"
"v_add_f32 v144, 0x74800000, v101\n"
"v_min_f32 v144, 0, v144\n"
"v_add_f32 v145, 0x747c0000, v101\n"
"v_min_f32 v145, 0, v145\n"
"v_add_f32 v146, 0x74780000, v101\n"
"v_min_f32 v146, 0, v146\n"
"v_add_f32 v147, 0x74740000, v101\n"
"v_min_f32 v147, 0, v147\n"
"v_add_f32 v148, 0x74400000, v101\n"
"v_min_f32 v148, 0, v148\n"
"v_add_f32 v149, 0x743c0000, v101\n"
"v_min_f32 v149, 0, v149\n"
"v_add_f32 v150, 0x74380000, v101\n"
"v_min_f32 v150, 0, v150\n"
"v_add_f32 v151, 0x74340000, v101\n"
"v_min_f32 v151, 0, v151\n"
"v_add_f32 v152, 0x74c00000, v101\n"
"v_min_f32 v152, 0, v152\n"
"v_add_f32 v153, 0x74be0000, v101\n"
"v_min_f32 v153, 0, v153\n"
"v_add_f32 v154, 0x74bc0000, v101\n"
"v_min_f32 v154, 0, v154\n"
"v_add_f32 v155, 0x74ba0000, v101\n"
"v_min_f32 v155, 0, v155\n"
"v_add_f32 v156, 0x74a00000, v101\n"
"v_min_f32 v156, 0, v156\n"
"v_add_f32 v157, 0x749e0000, v101\n"
"v_min_f32 v157, 0, v157\n"
"v_add_f32 v158, 0x749c0000, v101\n"
"v_min_f32 v158, 0, v158\n"
"v_add_f32 v159, 0x749a0000, v101\n"
"v_min_f32 v159, 0, v159\n"
"s_waitcnt vmcnt(0)\n"
"v_mfma_f32_16x16x32_bf16 v[128:131], a[192:195], a[128:131], v[128:131]\n"
"v_mfma_f32_16x16x32_bf16 v[128:131], a[196:199], a[132:135], v[128:131]\n"
"v_mfma_f32_16x16x32_bf16 v[128:131], a[200:203], a[136:139], v[128:131]\n"
"v_mfma_f32_16x16x32_bf16 v[128:131], a[204:207], a[140:143], v[128:131]\n"
"v_mfma_f32_16x16x32_bf16 v[132:135], a[220:223], a[140:143], v[132:135]\n"
"v_mfma_f32_16x16x32_bf16 v[132:135], a[216:219], a[136:139], v[132:135]\n"
"v_mfma_f32_16x16x32_bf16 v[132:135], a[212:215], a[132:135], v[132:135]\n"
"v_mfma_f32_16x16x32_bf16 v[132:135], a[208:211], a[128:131], v[132:135]\n"
"v_mfma_f32_16x16x32_bf16 v[140:143], a[208:211], a[144:147], v[140:143]\n"
"v_mfma_f32_16x16x32_bf16 v[140:143], a[212:215], a[148:151], v[140:143]\n"
"v_mfma_f32_16x16x32_bf16 v[140:143], a[216:219], a[152:155], v[140:143]\n"
"v_mfma_f32_16x16x32_bf16 v[140:143], a[220:223], a[156:159], v[140:143]\n"
"v_mfma_f32_16x16x32_bf16 v[136:139], a[204:207], a[156:159], v[136:139]\n"
"v_mfma_f32_16x16x32_bf16 v[136:139], a[200:203], a[152:155], v[136:139]\n"
"v_mfma_f32_16x16x32_bf16 v[136:139], a[196:199], a[148:151], v[136:139]\n"
"v_mfma_f32_16x16x32_bf16 v[136:139], a[192:195], a[144:147], v[136:139]\n"
"v_mfma_f32_16x16x32_bf16 v[144:147], a[192:195], a[160:163], v[144:147]\n"
"v_mfma_f32_16x16x32_bf16 v[144:147], a[196:199], a[164:167], v[144:147]\n"
"v_mfma_f32_16x16x32_bf16 v[144:147], a[200:203], a[168:171], v[144:147]\n"
"v_mfma_f32_16x16x32_bf16 v[144:147], a[204:207], a[172:175], v[144:147]\n"
"v_mfma_f32_16x16x32_bf16 v[148:151], a[220:223], a[172:175], v[148:151]\n"
"v_mfma_f32_16x16x32_bf16 v[148:151], a[216:219], a[168:171], v[148:151]\n"
"v_mfma_f32_16x16x32_bf16 v[148:151], a[212:215], a[164:167], v[148:151]\n"
"v_mfma_f32_16x16x32_bf16 v[148:151], a[208:211], a[160:163], v[148:151]\n"
"v_mfma_f32_16x16x32_bf16 v[156:159], a[208:211], a[176:179], v[156:159]\n"
"v_mfma_f32_16x16x32_bf16 v[156:159], a[212:215], a[180:183], v[156:159]\n"
"v_mfma_f32_16x16x32_bf16 v[156:159], a[216:219], a[184:187], v[156:159]\n"
"v_mfma_f32_16x16x32_bf16 v[156:159], a[220:223], a[188:191], v[156:159]\n"
"v_mfma_f32_16x16x32_bf16 v[152:155], a[204:207], a[188:191], v[152:155]\n"
"v_mfma_f32_16x16x32_bf16 v[152:155], a[200:203], a[184:187], v[152:155]\n"
"v_mfma_f32_16x16x32_bf16 v[152:155], a[196:199], a[180:183], v[152:155]\n"
"v_mfma_f32_16x16x32_bf16 v[152:155], a[192:195], a[176:179], v[152:155]\n"
"v_max3_f32 v104, v128, v129, v104\n"
"v_max3_f32 v104, v130, v131, v104\n"
"v_max3_f32 v104, v132, v133, v104\n"
"v_max3_f32 v104, v134, v135, v104\n"
"v_max3_f32 v105, v136, v137, v105\n"
"v_max3_f32 v105, v138, v139, v105\n"
"v_max3_f32 v105, v140, v141, v105\n"
"v_max3_f32 v105, v142, v143, v105\n"
"v_max3_f32 v106, v144, v145, v106\n"
"v_max3_f32 v106, v146, v147, v106\n"
"v_max3_f32 v106, v148, v149, v106\n"
"v_max3_f32 v106, v150, v151, v106\n"
"v_max3_f32 v107, v152, v153, v107\n"
"v_max3_f32 v107, v154, v155, v107\n"
"v_max3_f32 v107, v156, v157, v107\n"
"v_max3_f32 v107, v158, v159, v107\n"
"v_add_f32 v100, 0x74000000, v100\n"
"v_add_f32 v101, 0x74000000, v101\n"
"s_sub_u32 s82, s82, 1\n"
"s_cbranch_scc0 SFL_37_%=\n"
"v_mov_b32 v102, v104\n"
"s_nop 1\n"
"v_permlane16_swap_b32 v104, v102\n"
"v_max_f32 v104, v104, v102\n"
"v_mov_b32 v102, v104\n"
"s_nop 1\n"
"v_permlane32_swap_b32 v104, v102\n"
"v_max_f32 v104, v104, v102\n"
"v_sub_f32 v176, 0, v104\n"
"v_sub_f32 v177, 0, v104\n"
"v_sub_f32 v178, 0, v104\n"
"v_sub_f32 v179, 0, v104\n"
"v_mov_b32 v102, v105\n"
"s_nop 1\n"
"v_permlane16_swap_b32 v105, v102\n"
"v_max_f32 v105, v105, v102\n"
"v_mov_b32 v102, v105\n"
"s_nop 1\n"
"v_permlane32_swap_b32 v105, v102\n"
"v_max_f32 v105, v105, v102\n"
"v_sub_f32 v180, 0, v105\n"
"v_sub_f32 v181, 0, v105\n"
"v_sub_f32 v182, 0, v105\n"
"v_sub_f32 v183, 0, v105\n"
"v_mov_b32 v102, v106\n"
"s_nop 1\n"
"v_permlane16_swap_b32 v106, v102\n"
"v_max_f32 v106, v106, v102\n"
"v_mov_b32 v102, v106\n"
"s_nop 1\n"
"v_permlane32_swap_b32 v106, v102\n"
"v_max_f32 v106, v106, v102\n"
"v_sub_f32 v184, 0, v106\n"
"v_sub_f32 v185, 0, v106\n"
"v_sub_f32 v186, 0, v106\n"
"v_sub_f32 v187, 0, v106\n"
"v_mov_b32 v102, v107\n"
"s_nop 1\n"
"v_permlane16_swap_b32 v107, v102\n"
"v_max_f32 v107, v107, v102\n"
"v_mov_b32 v102, v107\n"
"s_nop 1\n"
"v_permlane32_swap_b32 v107, v102\n"
"v_max_f32 v107, v107, v102\n"
"v_sub_f32 v188, 0, v107\n"
"v_sub_f32 v189, 0, v107\n"
"v_sub_f32 v190, 0, v107\n"
"v_sub_f32 v191, 0, v107\n"
"v_lshlrev_b32 v96, 2, v3\n"
"v_sub_u32 v96, v2, v96\n"
"s_lshl_b32 s87, s66, 4\n"
"v_add_u32 v96, s87, v96\n"
"v_cvt_f32_i32 v96, v96\n"
"v_add_f32 v96, 0.5, v96\n"
"v_mul_f32 v96, 0x71800000, v96\n"
"s_cmp_eq_u32 s67, 0\n"
"s_cselect_b64 vcc, -1, 0\n"
"v_cndmask_b32 v97, v188, v176, vcc\n"
"v_cndmask_b32 v98, v176, v188, vcc\n"
"v_add_f32 v99, 0x00000000, v96\n"
"v_min_f32 v44, v99, v97\n"
"v_min_f32 v52, v99, v98\n"
"v_add_f32 v99, 0xf1800000, v96\n"
"v_min_f32 v45, v99, v97\n"
"v_min_f32 v53, v99, v98\n"
"v_add_f32 v99, 0xf2000000, v96\n"
"v_min_f32 v46, v99, v97\n"
"v_min_f32 v54, v99, v98\n"
"v_add_f32 v99, 0xf2400000, v96\n"
"v_min_f32 v47, v99, v97\n"
"v_min_f32 v55, v99, v98\n"
"v_add_f32 v99, 0xf3800000, v96\n"
"v_min_f32 v48, v99, v97\n"
"v_min_f32 v56, v99, v98\n"
"v_add_f32 v99, 0xf3880000, v96\n"
"v_min_f32 v49, v99, v97\n"
"v_min_f32 v57, v99, v98\n"
"v_add_f32 v99, 0xf3900000, v96\n"
"v_min_f32 v50, v99, v97\n"
"v_min_f32 v58, v99, v98\n"
"v_add_f32 v99, 0xf3980000, v96\n"
"v_min_f32 v51, v99, v97\n"
"v_min_f32 v59, v99, v98\n"
"s_lshl_b32 s78, s67, 8\n"
"s_add_u32 s78, s78, s94\n"
"s_lshl_b32 s87, s66, 14\n"
"s_sub_u32 s79, s78, s87\n"
"s_add_u32 s79, s79, 49152\n"
"s_add_u32 s78, s78, s87\n"
"s_add_u32 s80, s78, 512\n"
"s_add_u32 s81, s79, 512\n"
"s_mov_b32 s92, 65536\n"
"s_movk_i32 s5, 512\n"
"s_lshl_b32 s76, s61, 12\n"
"s_add_u32 s77, s76, 65536\n"
"s_mov_b64 s[20:21], s[44:45]\n"
"s_mov_b32 s22, s46\n"
"s_mov_b64 s[24:25], s[44:45]\n"
"s_mov_b32 s26, s46\n"
"s_mov_b32 s74, s71\n"
"s_mov_b32 s75, 64\n"
"s_mov_b64 s[72:73], s[16:17]\n"
"s_add_u32 s82, s70, 1\n"
"s_and_b32 s82, s82, 1\n"
"s_lshl_b32 s82, s82, 15\n"
"s_add_u32 s76, s76, s82\n"
"s_add_u32 s77, s77, s82\n"
"s_lshl_b32 s83, s67, 13\n"
"s_add_u32 s82, s82, s83\n"
"v_add_u32 v8, s82, v1\n"
"v_add_u32 v9, 0x10000, v8\n"
"s_add_u32 m0, s76, 0\n"
"s_nop 0\n"
"buffer_load_dwordx4 v24, s[20:23], s78 offen offset:0 sc1 lds\n"
"buffer_load_dwordx4 v25, s[20:23], s78 offen offset:1024 sc1 lds\n"
"buffer_load_dwordx4 v26, s[20:23], s78 offen offset:2048 sc1 lds\n"
"buffer_load_dwordx4 v27, s[20:23], s78 offen offset:3072 sc1 lds\n"
"s_add_u32 m0, s76, 16384\n"
"s_nop 0\n"
"buffer_load_dwordx4 v24, s[20:23], s79 offen offset:0 sc1 lds\n"
"buffer_load_dwordx4 v25, s[20:23], s79 offen offset:1024 sc1 lds\n"
"buffer_load_dwordx4 v26, s[20:23], s79 offen offset:2048 sc1 lds\n"
"buffer_load_dwordx4 v27, s[20:23], s79 offen offset:3072 sc1 lds\n"
"s_add_u32 m0, s77, 0\n"
"s_nop 0\n"
"buffer_load_dwordx4 v24, s[24:27], s80 offen offset:0 sc1 lds\n"
"buffer_load_dwordx4 v25, s[24:27], s80 offen offset:1024 sc1 lds\n"
"buffer_load_dwordx4 v26, s[24:27], s80 offen offset:2048 sc1 lds\n"
"buffer_load_dwordx4 v27, s[24:27], s80 offen offset:3072 sc1 lds\n"
"s_add_u32 m0, s77, 16384\n"
"s_nop 0\n"
"buffer_load_dwordx4 v24, s[24:27], s81 offen offset:0 sc1 lds\n"
"buffer_load_dwordx4 v25, s[24:27], s81 offen offset:1024 sc1 lds\n"
"buffer_load_dwordx4 v26, s[24:27], s81 offen offset:2048 sc1 lds\n"
"buffer_load_dwordx4 v27, s[24:27], s81 offen offset:3072 sc1 lds\n"
"s_add_u32 s78, s78, s92\n"
"s_add_u32 s79, s79, s92\n"
"s_xor_b32 s76, s76, 0x8000\n"
"s_sub_u32 s74, s74, 1\n"
"s_cselect_b64 s[78:79], s[72:73], s[78:79]\n"
"s_cselect_b64 s[72:73], s[16:17], s[72:73]\n"
"s_cselect_b32 s74, s75, s74\n"
"s_cselect_b32 s75, 64, s75\n"
"s_cmp_gt_u32 s74, 32\n"
"s_cselect_b64 s[20:21], s[32:33], s[20:21]\n"
"s_cselect_b32 s22, s34, s22\n"
"s_cselect_b32 s92, 262144, s92\n"
"s_cselect_b32 s5, 524288, s5\n"
"s_add_u32 m0, s76, 0\n"
"s_nop 0\n"
"buffer_load_dwordx4 v24, s[20:23], s78 offen offset:0 sc1 lds\n"
"buffer_load_dwordx4 v25, s[20:23], s78 offen offset:1024 sc1 lds\n"
"buffer_load_dwordx4 v26, s[20:23], s78 offen offset:2048 sc1 lds\n"
"buffer_load_dwordx4 v27, s[20:23], s78 offen offset:3072 sc1 lds\n"
"s_add_u32 m0, s76, 16384\n"
"s_nop 0\n"
"buffer_load_dwordx4 v24, s[20:23], s79 offen offset:0 sc1 lds\n"
"buffer_load_dwordx4 v25, s[20:23], s79 offen offset:1024 sc1 lds\n"
"buffer_load_dwordx4 v26, s[20:23], s79 offen offset:2048 sc1 lds\n"
"buffer_load_dwordx4 v27, s[20:23], s79 offen offset:3072 sc1 lds\n"
"s_add_u32 s80, s78, s5\n"
"s_add_u32 s81, s79, s5\n"
"s_mov_b64 s[24:25], s[20:21]\n"
"s_mov_b32 s26, s22\n"
"s_xor_b32 s77, s77, 0x8000\n"
"s_add_u32 s78, s78, s92\n"
"s_add_u32 s79, s79, s92\n"
"s_xor_b32 s76, s76, 0x8000\n"
"s_sub_u32 s74, s74, 1\n"
"s_cselect_b64 s[78:79], s[72:73], s[78:79]\n"
"s_cselect_b64 s[72:73], s[16:17], s[72:73]\n"
"s_cselect_b32 s74, s75, s74\n"
"s_cselect_b32 s75, 64, s75\n"
"s_cmp_gt_u32 s74, 32\n"
"s_cselect_b64 s[20:21], s[32:33], s[20:21]\n"
"s_cselect_b32 s22, s34, s22\n"
"s_cselect_b32 s92, 262144, s92\n"
"s_cselect_b32 s5, 524288, s5\n"
"s_waitcnt vmcnt(0)\n"
"s_barrier\n"
"ds_read_b128 a[224:227], v8 offset:0\n"
"ds_read_b128 a[228:231], v8 offset:1024\n"
"ds_read_b128 a[232:235], v8 offset:2048\n"
"ds_read_b128 a[236:239], v8 offset:3072\n"
"ds_read_b128 a[240:243], v8 offset:4096\n"
"ds_read_b128 a[244:247], v8 offset:5120\n"
"ds_read_b128 a[248:251], v8 offset:6144\n"
"ds_read_b128 a[252:255], v8 offset:7168\n"
"s_waitcnt lgkmcnt(0)\n"
"s_barrier\n"
"s_mov_b32 s82, 983040\n"
"s_mov_b32 s83, 983040\n"
"s_mov_b32 s84, 983040\n"
"s_mov_b32 s86, 983040\n"
"s_branch BLKB_%=\n"
"IDLE_%=:\n"
"s_cbranch_execz PADRET_%=\n"
"s_waitcnt vmcnt(0)\n"
"s_xor_b32 s86, s61, 0\n"
"s_lshl_b32 s86, s86, 5\n"
"v_xor_b32 v4, s86, v30\n"
"v_add_u32 v8, 65536, v4\n"
"v_add_u32 v12, 131072, v4\n"
"s_xor_b32 s86, s61, 1\n"
"s_lshl_b32 s86, s86, 5\n"
"v_xor_b32 v5, s86, v30\n"
"v_add_u32 v9, 65536, v5\n"
"PG21_%=:\n"
"s_cbranch_execz PADRET_%=\n"
"v_add_u32 v13, 131072, v5\n"
"s_xor_b32 s86, s61, 2\n"
"s_lshl_b32 s86, s86, 5\n"
"v_xor_b32 v6, s86, v30\n"
"v_add_u32 v10, 65536, v6\n"
"v_add_u32 v14, 131072, v6\n"
"s_xor_b32 s86, s61, 3\n"
"s_lshl_b32 s86, s86, 5\n"
"v_xor_b32 v7, s86, v30\n"
"v_add_u32 v11, 65536, v7\n"
"v_add_u32 v15, 131072, v7\n"
"ds_read_b64_tr_b16 v[48:49], v4 offset:4096\n"
"ds_read_b64_tr_b16 v[50:51], v4 offset:6144\n"
"ds_read_b64_tr_b16 v[52:53], v5 offset:4096\n"
"ds_read_b64_tr_b16 v[54:55], v5 offset:6144\n"
"ds_read_b64_tr_b16 v[56:57], v6 offset:4096\n"
"ds_read_b64_tr_b16 v[58:59], v6 offset:6144\n"
"ds_read_b64_tr_b16 v[60:61], v7 offset:4096\n"
"ds_read_b64_tr_b16 v[62:63], v7 offset:6144\n"
"ds_read_b64_tr_b16 v[64:65], v8 offset:32768\n"
"ds_read_b64_tr_b16 v[66:67], v8 offset:34816\n"
"ds_read_b64_tr_b16 v[68:69], v9 offset:32768\n"
"ds_read_b64_tr_b16 v[70:71], v9 offset:34816\n"
"ds_read_b64_tr_b16 v[72:73], v10 offset:32768\n"
"ds_read_b64_tr_b16 v[74:75], v10 offset:34816\n"
"ds_read_b64_tr_b16 v[76:77], v11 offset:32768\n"
"ds_read_b64_tr_b16 v[78:79], v11 offset:34816\n"
"ds_read_b64_tr_b16 v[80:81], v8 offset:0\n"
"ds_read_b64_tr_b16 v[82:83], v8 offset:2048\n"
"ds_read_b64_tr_b16 v[84:85], v9 offset:0\n"
"ds_read_b64_tr_b16 v[86:87], v9 offset:2048\n"
"ds_read_b64_tr_b16 v[88:89], v10 offset:0\n"
"ds_read_b64_tr_b16 v[90:91], v10 offset:2048\n"
"ds_read_b64_tr_b16 v[92:93], v11 offset:0\n"
"ds_read_b64_tr_b16 v[94:95], v11 offset:2048\n"
"ds_read_b64_tr_b16 v[96:97], v8 offset:4096\n"
"ds_read_b64_tr_b16 v[98:99], v8 offset:6144\n"
"ds_read_b64_tr_b16 v[100:101], v9 offset:4096\n"
"ds_read_b64_tr_b16 v[102:103], v9 offset:6144\n"
"ds_read_b64_tr_b16 v[104:105], v10 offset:4096\n"
"ds_read_b64_tr_b16 v[106:107], v10 offset:6144\n"
"ds_read_b64_tr_b16 v[108:109], v11 offset:4096\n"
"ds_read_b64_tr_b16 v[110:111], v11 offset:6144\n"
"s_waitcnt lgkmcnt(0)\n"
"s_add_u32 m0, s91, 65536\n"
"s_add_u32 s85, s72, 524288\n"
"buffer_load_dwordx4 v44, s[40:43], s85 offen offset:0 lds\n"
"buffer_load_dwordx4 v45, s[40:43], s85 offen offset:1024 lds\n"
"buffer_load_dwordx4 v46, s[40:43], s85 offen offset:2048 lds\n"
"buffer_load_dwordx4 v47, s[40:43], s85 offen offset:3072 lds\n"
"s_add_u32 m0, s91, 69632\n"
"s_add_u32 s85, s72, 655360\n"
"buffer_load_dwordx4 v44, s[40:43], s85 offen offset:0 lds\n"
"buffer_load_dwordx4 v45, s[40:43], s85 offen offset:1024 lds\n"
"buffer_load_dwordx4 v46, s[40:43], s85 offen offset:2048 lds\n"
"buffer_load_dwordx4 v47, s[40:43], s85 offen offset:3072 lds\n"
"s_add_u32 m0, s91, 131072\n"
"s_add_u32 s85, s72, 786432\n"
"buffer_load_dwordx4 v44, s[40:43], s85 offen offset:0 lds\n"
"buffer_load_dwordx4 v45, s[40:43], s85 offen offset:1024 lds\n"
"buffer_load_dwordx4 v46, s[40:43], s85 offen offset:2048 lds\n"
"buffer_load_dwordx4 v47, s[40:43], s85 offen offset:3072 lds\n"
"s_add_u32 m0, s91, 135168\n"
"s_add_u32 s85, s93, 917504\n"
"buffer_load_dwordx4 v44, s[40:43], s85 offen offset:0 lds\n"
"buffer_load_dwordx4 v45, s[40:43], s85 offen offset:1024 lds\n"
"buffer_load_dwordx4 v46, s[40:43], s85 offen offset:2048 lds\n"
"buffer_load_dwordx4 v47, s[40:43], s85 offen offset:3072 lds\n"
"s_add_u32 m0, s91, 32768\n"
"s_add_u32 s85, s93, 1048576\n"
"buffer_load_dwordx4 v44, s[40:43], s85 offen offset:0 lds\n"
"buffer_load_dwordx4 v45, s[40:43], s85 offen offset:1024 lds\n"
"buffer_load_dwordx4 v46, s[40:43], s85 offen offset:2048 lds\n"
"buffer_load_dwordx4 v47, s[40:43], s85 offen offset:3072 lds\n"
"s_branch G2P_47_%=\n"
"KEND_%=:\n"
"END_52_%=:\n"
  :: [ka]"s"(ka), [bid]"s"(bid), [wid]"s"(wid), [lds]"s"((unsigned)(size_t)lds)
  : "v0","v1","v2","v3","v4","v5","v6","v7","v8","v9","v10","v11","v12","v13","v14","v15","v16","v17","v18","v19","v20","v21","v22","v23","v24","v25","v26","v27","v28","v29","v30","v31","v32","v33","v34","v35","v36","v37","v38","v39","v40","v41","v42","v43","v44","v45","v46","v47","v48","v49","v50","v51","v52","v53","v54","v55","v56","v57","v58","v59","v60","v61","v62","v63","v64","v65","v66","v67","v68","v69","v70","v71","v72","v73","v74","v75","v76","v77","v78","v79","v80","v81","v82","v83","v84","v85","v86","v87","v88","v89","v90","v91","v92","v93","v94","v95","v96","v97","v98","v99","v100","v101","v102","v103","v104","v105","v106","v107","v108","v109","v110","v111","v112","v113","v114","v115","v116","v117","v118","v119","v120","v121","v122","v123","v124","v125","v126","v127","v128","v129","v130","v131","v132","v133","v134","v135","v136","v137","v138","v139","v140","v141","v142","v143","v144","v145","v146","v147","v148","v149","v150","v151","v152","v153","v154","v155","v156","v157","v158","v159","v160","v161","v162","v163","v164","v165","v166","v167","v168","v169","v170","v171","v172","v173","v174","v175","v176","v177","v178","v179","v180","v181","v182","v183","v184","v185","v186","v187","v188","v189","v190","v191","v192","v193","v194","v195","v196","v197","v198","v199","v200","v201","v202","v203","v204","v205","v206","v207","v208","v209","v210","v211","v212","v213","v214","v215","v216","v217","v218","v219","v220","v221","v222","v223","v224","v225","v226","v227","v228","v229","v230","v231","v232","v233","v234","v235","v236","v237","v238","v239","v240","v241","v242","v243","v244","v245","v246","v247","v248","v249","v250","v251","v252","v253","v254","v255","a0","a1","a2","a3","a4","a5","a6","a7","a8","a9","a10","a11","a12","a13","a14","a15","a16","a17","a18","a19","a20","a21","a22","a23","a24","a25","a26","a27","a28","a29","a30","a31","a32","a33","a34","a35","a36","a37","a38","a39","a40","a41","a42","a43","a44","a45","a46","a47","a48","a49","a50","a51","a52","a53","a54","a55","a56","a57","a58","a59","a60","a61","a62","a63","a64","a65","a66","a67","a68","a69","a70","a71","a72","a73","a74","a75","a76","a77","a78","a79","a80","a81","a82","a83","a84","a85","a86","a87","a88","a89","a90","a91","a92","a93","a94","a95","a96","a97","a98","a99","a100","a101","a102","a103","a104","a105","a106","a107","a108","a109","a110","a111","a112","a113","a114","a115","a116","a117","a118","a119","a120","a121","a122","a123","a124","a125","a126","a127","a128","a129","a130","a131","a132","a133","a134","a135","a136","a137","a138","a139","a140","a141","a142","a143","a144","a145","a146","a147","a148","a149","a150","a151","a152","a153","a154","a155","a156","a157","a158","a159","a160","a161","a162","a163","a164","a165","a166","a167","a168","a169","a170","a171","a172","a173","a174","a175","a176","a177","a178","a179","a180","a181","a182","a183","a184","a185","a186","a187","a188","a189","a190","a191","a192","a193","a194","a195","a196","a197","a198","a199","a200","a201","a202","a203","a204","a205","a206","a207","a208","a209","a210","a211","a212","a213","a214","a215","a216","a217","a218","a219","a220","a221","a222","a223","a224","a225","a226","a227","a228","a229","a230","a231","a232","a233","a234","a235","a236","a237","a238","a239","a240","a241","a242","a243","a244","a245","a246","a247","a248","a249","a250","a251","a252","a253","a254","a255","s5","s6","s7","s8","s9","s10","s11","s12","s13","s14","s15","s16","s17","s18","s19","s20","s21","s22","s23","s24","s25","s26","s27","s28","s29","s30","s31","s32","s33","s34","s35","s36","s37","s38","s39","s40","s41","s42","s43","s44","s45","s46","s47","s48","s49","s50","s51","s52","s53","s54","s55","s56","s57","s58","s59","s60","s61","s62","s63","s64","s65","s66","s67","s68","s69","s70","s71","s72","s73","s74","s75","s76","s77","s78","s79","s80","s81","s82","s83","s84","s85","s86","s87","s88","s89","s90","s91","s92","s93","s94","s95","s96","s97","s98","s99","s100","s101","vcc","scc","memory");
}
extern "C" void kernel_launch(void* const* d_in, const int* in_sizes, int n_in, void* d_out, int out_size, void* d_ws, size_t ws_size, hipStream_t stream) {
  const void* p[12] = {d_in[0], d_in[1], d_in[2], d_in[3], d_in[4], d_in[5], d_in[7], d_in[9], d_ws, d_out, d_in[8], d_in[10]};
  const unsigned n[12] = {2097152u, 2097152u, 4194304u, 4194304u, 4194304u, 4194304u, 7340032u, 7340032u, 69206016u, 2097152u, 3584u, 4096u};
  Args a;
  for (int i = 0; i < 12; ++i) { a.b[i].base = p[i]; a.b[i].bytes = n[i]; a.b[i].fmt = 0x20000u; }
  fused_block<<<256, 256, 0, stream>>>(a);
}
```
